```python
import torch

def _softmax(x, axis=-1):
    return torch.softmax(x, dim=axis)
def _gelu(x):
    return torch.nn.functional.gelu(x, approximate="tanh")
def _silu(x):
    return torch.nn.functional.silu(x)
def _sigmoid(x):
    return torch.sigmoid(x)
def _layernorm(x, axis=-1, eps=1e-5):
    m = x.mean(dim=axis, keepdim=True)
    v = x.var(dim=axis, keepdim=True, unbiased=False)
    return (x - m) / torch.sqrt(v + eps)
def _scatter_add(tgt, idx, src, axis=0):
    return tgt.clone().index_add_(axis, idx, src)
def _segment_sum(src, seg, n):
    out = torch.zeros((n,) + src.shape[1:], dtype=src.dtype)
    return out.index_add_(0, seg, src)
def _pad(x, pw):
    flat = tuple(v for pair in reversed(pw) for v in pair)
    return torch.nn.functional.pad(x, flat)

kernel_name = 'concat_heads__gqa_block_cached__compressed_kv_cla__decode'

def setup_inputs():

    _w = (lambda a: (lambda t: t.to(torch.bfloat16) if t.is_floating_point() else t)(torch.as_tensor(a)))
    def _A_setup():
        B, Sq, D = 2, 1, 512
        Hq, Hkv, Sk, Dh = 32, 8, 8192, 128
        assert Hq % Hkv == 0, (Hq, Hkv)
        return {
            "x": _w((lambda *s: torch.randn(*s, dtype=torch.bfloat16) * 1.6)(B, Sq, D)),
            "k_cache": _w((lambda *s: torch.randn(*s, dtype=torch.bfloat16) * 1.6)(B, Hkv, Sk, Dh)),
            "v_cache": _w((lambda *s: torch.randn(*s, dtype=torch.bfloat16) * 0.05)(B, Hkv, Sk, Dh)),
            "wq": _w((lambda *s: torch.randn(*s, dtype=torch.bfloat16) * 1.6)(D, Hq * Dh) * (D ** -0.5)),
            "wo": _w((lambda *s: torch.randn(*s, dtype=torch.bfloat16) * 0.05)(Hq * Dh, D) * ((Hq * Dh) ** -0.5)),
        }
    _A_in = _A_setup()

    def _B_setup():
        B, H, Sq, Sk, D, M = 2, 32, 1, 8192, 128, 512
        Dc = 4 * D
        return {
            "x": _w((lambda *s: torch.randn(*s, dtype=torch.bfloat16) * 3.2)(B, Sq, M)),
            "c_kv": _w((lambda *s: torch.randn(*s, dtype=torch.bfloat16) * 0.1)(B, Sk, Dc)),
            "w_q": _w((lambda *s: torch.randn(*s, dtype=torch.bfloat16) * 6.4)(M, H, D) * (M ** -0.5)),
            "w_uk": _w((lambda *s: torch.randn(*s, dtype=torch.bfloat16) * 6.4)(Dc, H, D) * (Dc ** -0.5)),
            "w_uv": _w((lambda *s: torch.randn(*s, dtype=torch.bfloat16) * 0.1)(Dc, H, D) * (Dc ** -0.5)),
            "w_o": _w((lambda *s: torch.randn(*s, dtype=torch.bfloat16) * 0.1)(H, D, M) * ((H * D) ** -0.5)),
        }
    _B_in = _B_setup()

    return {
        "A_x": _A_in["x"],
        "A_k_cache": _A_in["k_cache"],
        "A_v_cache": _A_in["v_cache"],
        "A_wq": _A_in["wq"],
        "A_wo": _A_in["wo"],
        "B_x": _B_in["x"],
        "B_c_kv": _B_in["c_kv"],
        "B_w_q": _B_in["w_q"],
        "B_w_uk": _B_in["w_uk"],
        "B_w_uv": _B_in["w_uv"],
        "B_w_o": _B_in["w_o"],
    }

def reference(A_x, A_k_cache, A_v_cache, A_wq, A_wo, B_x, B_c_kv, B_w_q, B_w_uk, B_w_uv, B_w_o):
    def _A_ref(x, k_cache, v_cache, wq, wo):
        B, Sq, D = x.shape
        Hq, Dh = 32, 128
        Hkv = k_cache.shape[1]
        rep = Hq // Hkv
        q = (lambda x, *d: x.permute(*d))((x @ wq).reshape(B, Sq, Hq, Dh), 0, 2, 1, 3)
        k = (lambda x, n, axis: torch.repeat_interleave(x, n, dim=axis))(k_cache, rep, axis=1)
        v = (lambda x, n, axis: torch.repeat_interleave(x, n, dim=axis))(v_cache, rep, axis=1)
        scores = torch.einsum("bhqd,bhkd->bhqk", q, k) * (Dh ** -0.5)
        p = _softmax(scores, axis=-1)
        o = torch.einsum("bhqk,bhkd->bhqd", p, v)
        return (lambda x, *d: x.permute(*d))(o, 0, 2, 1, 3).reshape(B, Sq, Hq * Dh) @ wo

    def _B_ref(x, c_kv, w_q, w_uk, w_uv, w_o):
        B, Sq, M = x.shape[0], x.shape[1], x.shape[2]
        Sk = c_kv.shape[1]
        H, D = w_q.shape[1], w_q.shape[2]
        q = torch.einsum("bqm,mhd->bhqd", x, w_q)
        k = torch.einsum("bkc,chd->bhkd", c_kv, w_uk)
        v = torch.einsum("bkc,chd->bhkd", c_kv, w_uv)
        scores = torch.einsum("bhqd,bhkd->bhqk", q, k) * (D ** -0.5)
        off = Sk - Sq
        qi = torch.arange(Sq)[:, None]
        kj = torch.arange(Sk)[None, :]
        cmask = kj <= (qi + off)
        scores = torch.where(cmask[None, None], scores, -1e9)
        p = _softmax(scores, axis=-1)
        ctx = torch.einsum("bhqk,bhkd->bhqd", p, v)
        return torch.einsum("bhqd,hdm->bqm", ctx, w_o)

    a = _A_ref(A_x, A_k_cache, A_v_cache, A_wq, A_wo)
    b = _B_ref(B_x, B_c_kv, B_w_q, B_w_uk, B_w_uv, B_w_o)
    return torch.concatenate([a, b], axis=-1)
```

```cpp
#include <hip/hip_runtime.h>
#include <hip/hip_bf16.h>
#include <stdint.h>

typedef unsigned short u16;
__device__ __forceinline__ float bf2f(u16 b){ return __uint_as_float(((unsigned)b)<<16); }
__device__ __forceinline__ u16 f2bf(float f){ unsigned u=__float_as_uint(f); u += 0x7FFFu + ((u>>16)&1u); return (u16)(u>>16); }
__device__ __forceinline__ float rbf(float f){ return bf2f(f2bf(f)); }

__global__ void k_q(const u16* xA, const u16* wA, const u16* xB, const u16* wB, float* qA, float* qB){
  int n = blockIdx.x*256+threadIdx.x; int which = blockIdx.y;
  const u16* x = which? xB: xA; const u16* w = which? wB: wA; float* q = which? qB: qA;
  float a0=0,a1=0;
  for(int m=0;m<512;m++){ float wv=bf2f(w[(size_t)m*4096+n]); a0+=bf2f(x[m])*wv; a1+=bf2f(x[512+m])*wv; }
  q[n]=rbf(a0); q[4096+n]=rbf(a1);
}
__global__ void k_qabs(const float* qB, const u16* wuk, float* qabs){
  int c = threadIdx.x + (blockIdx.x&1)*256; int h = blockIdx.x>>1;
  float a0=0,a1=0;
  for(int d=0;d<128;d++){ float wv=bf2f(wuk[(size_t)c*4096+h*128+d]); a0+=wv*qB[h*128+d]; a1+=wv*qB[4096+h*128+d]; }
  qabs[(0*32+h)*512+c]=a0; qabs[(1*32+h)*512+c]=a1;
}
__global__ void k_scoreA(const float* qA, const u16* K, float* S){
  int k = blockIdx.x*256+threadIdx.x; int h=blockIdx.y; int b=blockIdx.z;
  const u16* kp = K + (((size_t)b*8+h/4)*8192+k)*128; const float* q=qA+b*4096+h*128;
  float a=0; for(int d=0;d<128;d++) a+=q[d]*bf2f(kp[d]);
  S[((size_t)b*32+h)*8192+k]=a*0.08838834764831845f;
}
__global__ void k_scoreB(const float* qabs, const u16* C, float* S){
  int k = blockIdx.x*256+threadIdx.x; int h=blockIdx.y; int b=blockIdx.z;
  const u16* kp = C + ((size_t)b*8192+k)*512; const float* q=qabs+(b*32+h)*512;
  float a=0; for(int d=0;d<512;d++) a+=q[d]*bf2f(kp[d]);
  S[((size_t)b*32+h)*8192+k]=a*0.08838834764831845f;
}
__global__ void k_softmax(float* S){
  __shared__ float red[256];
  float* s=S+(size_t)blockIdx.x*8192; float m=-INFINITY;
  for(int i=threadIdx.x;i<8192;i+=256) m=fmaxf(m,s[i]);
  red[threadIdx.x]=m; __syncthreads();
  for(int o=128;o>0;o>>=1){ if(threadIdx.x<o) red[threadIdx.x]=fmaxf(red[threadIdx.x],red[threadIdx.x+o]); __syncthreads(); }
  m=red[0]; __syncthreads();
  float l=0; for(int i=threadIdx.x;i<8192;i+=256){ float e=__expf(s[i]-m); s[i]=e; l+=e; }
  red[threadIdx.x]=l; __syncthreads();
  for(int o=128;o>0;o>>=1){ if(threadIdx.x<o) red[threadIdx.x]+=red[threadIdx.x+o]; __syncthreads(); }
  l=red[0];
  for(int i=threadIdx.x;i<8192;i+=256) s[i]/=l;
}
__global__ void k_pvA(const float* P, const u16* V, float* o){
  __shared__ float red[8][128];
  int h=blockIdx.x, b=blockIdx.y; int d=threadIdx.x&127, g=threadIdx.x>>7;
  const float* p=P+((size_t)b*32+h)*8192; const u16* v=V+(((size_t)b*8+h/4)*8192)*128;
  float a=0; for(int k=g;k<8192;k+=8) a+=p[k]*bf2f(v[(size_t)k*128+d]);
  red[g][d]=a; __syncthreads();
  if(g==0){ float s=0; for(int i=0;i<8;i++) s+=red[i][d]; o[(b*32+h)*128+d]=s; }
}
__global__ void k_pvB(const float* P, const u16* C, float* o){
  __shared__ float red[2][512];
  int h=blockIdx.x, b=blockIdx.y; int d=threadIdx.x&511, g=threadIdx.x>>9;
  const float* p=P+((size_t)b*32+h)*8192; const u16* v=C+((size_t)b*8192)*512;
  float a=0; for(int k=g;k<8192;k+=2) a+=p[k]*bf2f(v[(size_t)k*512+d]);
  red[g][d]=a; __syncthreads();
  if(g==0){ o[(b*32+h)*512+d]=red[0][d]+red[1][d]; }
}
__global__ void k_uv(const float* olat, const u16* wuv, float* ctx){
  int h=blockIdx.x,b=blockIdx.y,d=threadIdx.x; float a=0;
  for(int c=0;c<512;c++) a+=olat[(b*32+h)*512+c]*bf2f(wuv[(size_t)c*4096+h*128+d]);
  ctx[(b*32+h)*128+d]=a;
}
__global__ void k_out(const float* oA, const u16* woA, const float* oB, const u16* woB, u16* out){
  int n=threadIdx.x + (blockIdx.x&7)*64; int which=(blockIdx.x>>3)&1; int b=blockIdx.y;
  const float* o=(which?oB:oA)+b*4096; const u16* w=which?woB:woA; float a=0;
  for(int k=0;k<4096;k++) a+=rbf(o[k])*bf2f(w[(size_t)k*512+n]);
  out[b*1024+which*512+n]=f2bf(a);
}

extern "C" void kernel_launch(void* const* d_in, const int* in_sizes, int n_in,
                              void* d_out, int out_size, void* d_ws, size_t ws_size,
                              hipStream_t stream) {
  const u16* Ax=(const u16*)d_in[0]; const u16* K=(const u16*)d_in[1]; const u16* V=(const u16*)d_in[2];
  const u16* wq=(const u16*)d_in[3]; const u16* wo=(const u16*)d_in[4];
  const u16* Bx=(const u16*)d_in[5]; const u16* C=(const u16*)d_in[6]; const u16* w_q=(const u16*)d_in[7];
  const u16* wuk=(const u16*)d_in[8]; const u16* wuv=(const u16*)d_in[9]; const u16* w_o=(const u16*)d_in[10];
  float* ws=(float*)d_ws;
  float* qA=ws; float* qB=ws+8192; float* qabs=ws+16384; float* SA=ws+65536; float* SB=SA+2*32*8192;
  float* oA=SB+2*32*8192; float* olat=oA+8192; float* ctx=olat+2*32*512;
  k_q<<<dim3(16,2),256,0,stream>>>(Ax,wq,Bx,w_q,qA,qB);
  k_qabs<<<64,256,0,stream>>>(qB,wuk,qabs);
  k_scoreA<<<dim3(32,32,2),256,0,stream>>>(qA,K,SA);
  k_scoreB<<<dim3(32,32,2),256,0,stream>>>(qabs,C,SB);
  k_softmax<<<128,256,0,stream>>>(SA);
  k_pvA<<<dim3(32,2),1024,0,stream>>>(SA,V,oA);
  k_pvB<<<dim3(32,2),1024,0,stream>>>(SB,C,olat);
  k_uv<<<dim3(32,2),128,0,stream>>>(olat,wuv,ctx);
  k_out<<<dim3(16,2),64,0,stream>>>(oA,wo,ctx,w_o,(u16*)d_out);
}
```

```cpp
#include <hip/hip_runtime.h>
#include <stdint.h>
#pragma clang diagnostic ignored "-Wint-to-pointer-cast"

#define GAS __attribute__((address_space(1)))
#define LAS __attribute__((address_space(3)))
typedef unsigned v4u __attribute__((ext_vector_type(4)));
typedef unsigned v2u __attribute__((ext_vector_type(2)));
typedef float f4 __attribute__((ext_vector_type(4)));
typedef float f2 __attribute__((ext_vector_type(2)));
typedef float f16v __attribute__((ext_vector_type(16)));
typedef short s4 __attribute__((ext_vector_type(4)));
typedef __bf16 b8 __attribute__((ext_vector_type(8)));
typedef __bf16 b2 __attribute__((ext_vector_type(2)));

#define WIN_A 24
#define WB 6
#define CODE_KB 20
#define QGO 20

namespace {

constexpr int NB_CU = 64, NSPLIT_A = 12, NSLOT = 15, NCONS = 7;
constexpr float SC = 0.08838834764831845f * 1.4426950408889634f;
constexpr int SPIN_CAP = 1 << 18;

constexpr unsigned FS = 64;
constexpr unsigned WS_E = 0;
constexpr unsigned WS_FQ = 4096;
constexpr unsigned WS_FP = WS_FQ + 64 * FS;
constexpr unsigned WS_QG = 65536;
constexpr unsigned WS_QAB = WS_QG + 262144;
constexpr unsigned WS_PBM = WS_QAB + 65536;
constexpr unsigned WS_PAM = WS_PBM + 16384;
constexpr unsigned WS_CT = WS_PAM + 16384;
constexpr unsigned WS_PAO = WS_CT + 131072;
constexpr unsigned WS_PB = WS_PAO + 786432;
constexpr unsigned WS_F = WS_PB + 2097152;
constexpr unsigned WS_END = WS_F + 2097152;

constexpr unsigned LDS_BYTES = 163840;
constexpr unsigned SCR = 131072;
constexpr unsigned A_MISC = NSLOT * 8192;
constexpr unsigned A_LANDED = A_MISC, A_QRDY = A_MISC + 4, A_DONE = A_MISC + 64;
constexpr unsigned A_QIMG = A_MISC + 256;
constexpr unsigned A_ML = A_MISC + 1280;
constexpr unsigned A_OIMG = A_MISC + 1536;
constexpr unsigned A_XIMG = A_MISC + 2048, A_DUMMY = A_MISC + 3072;
constexpr unsigned SLOT0 = 0, SLOT1 = 65536;
constexpr unsigned B_QBI = SCR, B_QAS = SCR + 1024;
constexpr unsigned B_MX = SCR, B_AL = SCR + 512, B_LS = SCR + 640, B_P = SCR + 2048;
constexpr unsigned B_RED = SCR + 8192, B_OL = SCR + 24576, B_OIMG = SCR + 26624, B_FIN = SCR + 27648;
constexpr unsigned B_T = SLOT1, B_OU = SLOT1 + 32768;
constexpr unsigned B_XIMG = SLOT0 + 63 * 1024, B_DUMMY = SLOT0 + 62 * 1024;

__device__ __forceinline__ v4u lds128(unsigned a) { return *(const LAS v4u*)a; }
__device__ __forceinline__ void sto128(unsigned a, v4u v) { *(LAS v4u*)a = v; }
__device__ __forceinline__ void sto64(unsigned a, v2u v) { *(LAS v2u*)a = v; }
__device__ __forceinline__ void sto32(unsigned a, unsigned v) { *(LAS unsigned*)a = v; }
__device__ __forceinline__ float ldsf(unsigned a) { return *(const LAS float*)a; }
__device__ __forceinline__ void stof(unsigned a, float v) { *(LAS float*)a = v; }
__device__ __forceinline__ v2u trd(unsigned a) { return __builtin_bit_cast(v2u, __builtin_amdgcn_ds_read_tr16_b64_v4i16((LAS s4*)a)); }
__device__ __forceinline__ v4u trd2(unsigned a0, unsigned a1) { const v2u x = trd(a0), y = trd(a1); return (v4u){x.x, x.y, y.x, y.y}; }
__device__ __forceinline__ f4 mfma32(v4u a, v4u b, f4 c) {
  return __builtin_amdgcn_mfma_f32_16x16x32_bf16(__builtin_bit_cast(b8, a), __builtin_bit_cast(b8, b), c, 0, 0, 0); }
__device__ __forceinline__ f4 mfma16(v2u a, v2u b, f4 c) {
  return __builtin_amdgcn_mfma_f32_16x16x16bf16_1k(__builtin_bit_cast(s4, a), __builtin_bit_cast(s4, b), c, 0, 0, 0); }
__device__ __forceinline__ f16v mfmaL(v4u a, v4u b, f16v c) {
  return __builtin_amdgcn_mfma_f32_32x32x16_bf16(__builtin_bit_cast(b8, a), __builtin_bit_cast(b8, b), c, 0, 0, 0); }
__device__ __forceinline__ unsigned pk(float lo, float hi) { return __builtin_bit_cast(unsigned, __builtin_convertvector((f2){lo, hi}, b2)); }
__device__ __forceinline__ float asf(unsigned u) { return __builtin_bit_cast(float, u); }
__device__ __forceinline__ unsigned asu(float f) { return __builtin_bit_cast(unsigned, f); }
__device__ __forceinline__ float ex2(float x) { return __builtin_amdgcn_exp2f(x); }
template <int CTRL> __device__ __forceinline__ float dpp(float x) {
  return __builtin_bit_cast(float, __builtin_amdgcn_mov_dpp(__builtin_bit_cast(int, x), CTRL, 0xf, 0xf, true)); }
constexpr int XOR1 = 0xB1, XOR2 = 0x4E, ROR4 = 0x124, ROR8 = 0x128;
__device__ __forceinline__ float x16max(float x) { auto s = __builtin_amdgcn_permlane16_swap(asu(x), asu(x), false, false); return fmaxf(asf(s[0]), asf(s[1])); }
__device__ __forceinline__ float x32max(float x) { auto s = __builtin_amdgcn_permlane32_swap(asu(x), asu(x), false, false); return fmaxf(asf(s[0]), asf(s[1])); }
__device__ __forceinline__ float x16sum(float x) { auto s = __builtin_amdgcn_permlane16_swap(asu(x), asu(x), false, false); return asf(s[0]) + asf(s[1]); }
__device__ __forceinline__ float x32sum(float x) { auto s = __builtin_amdgcn_permlane32_swap(asu(x), asu(x), false, false); return asf(s[0]) + asf(s[1]); }
__device__ __forceinline__ float row16max(float x) { x = fmaxf(x, dpp<XOR1>(x)); x = fmaxf(x, dpp<XOR2>(x)); x = fmaxf(x, dpp<ROR4>(x)); return fmaxf(x, dpp<ROR8>(x)); }
__device__ __forceinline__ float row16sum(float x) { x += dpp<XOR1>(x); x += dpp<XOR2>(x); x += dpp<ROR4>(x); return x + dpp<ROR8>(x); }

template <int IMM> __device__ __forceinline__ void dma(const void* sbase, unsigned voff, unsigned lds) {
  asm volatile("s_mov_b32 m0, %2\n\ts_nop 0\n\tglobal_load_lds_dwordx4 %0, %1 offset:%3 nt" :: "v"(voff), "s"(sbase), "s"(lds), "n"(IMM) : "memory");
}
template <int N> __device__ __forceinline__ void vmwait() { asm volatile("s_waitcnt vmcnt(%0)" :: "n"(N) : "memory"); }
__device__ __forceinline__ void bar() { asm volatile("s_waitcnt lgkmcnt(0)\n\ts_barrier" ::: "memory"); }
__device__ __forceinline__ void lgk0() { asm volatile("s_waitcnt lgkmcnt(0)" ::: "memory"); }
__device__ __forceinline__ void cfence() { asm volatile("" ::: "memory"); }
__device__ __forceinline__ unsigned lds_peek(unsigned a) { return __builtin_amdgcn_readfirstlane(*(volatile LAS unsigned*)a); }
__device__ __forceinline__ void lds_wait_ge(unsigned a, unsigned want) {
  _Pragma("nounroll") for (int it = 0; it < SPIN_CAP * 8; ++it) { if (lds_peek(a) >= want) break; __builtin_amdgcn_s_sleep(1); }
  cfence();
}
typedef __amdgpu_buffer_rsrc_t rsrc_t;
constexpr int SC1 = 16;
__device__ __forceinline__ v4u gld128(rsrc_t r, unsigned off) { return __builtin_amdgcn_raw_buffer_load_b128(r, off, 0, SC1); }
__device__ __forceinline__ v2u gld64(rsrc_t r, unsigned off) { return __builtin_amdgcn_raw_buffer_load_b64(r, off, 0, SC1); }
__device__ __forceinline__ unsigned gld32(rsrc_t r, unsigned off) { return __builtin_amdgcn_raw_buffer_load_b32(r, off, 0, SC1); }
__device__ __forceinline__ void gst128(rsrc_t r, unsigned off, v4u v) { __builtin_amdgcn_raw_buffer_store_b128(v, r, off, 0, SC1); }
__device__ __forceinline__ void gst64(rsrc_t r, unsigned off, v2u v) { __builtin_amdgcn_raw_buffer_store_b64(v, r, off, 0, SC1); }
__device__ __forceinline__ void gst32(rsrc_t r, unsigned off, unsigned v) { __builtin_amdgcn_raw_buffer_store_b32(v, r, off, 0, SC1); }
__device__ __forceinline__ bool tagok(v4u v, unsigned tag) { return v.y == tag && v.w == tag; }

#define STAMP(i) do {} while (0)
#define STAMP_DECL do {} while (0)
#define STAMP_OUT() do {} while (0)

__device__ __forceinline__ void q_unit(unsigned ximg, int nb, int lane, int mat, int cb, int kh, rsrc_t rs, unsigned tag) {
  const int g = lane >> 4, c16 = lane & 15, q = c16 >> 2, p = lane & 3;
  const int f2_ = ((q >> 1) & 1) | ((g & 1) << 1);
  const unsigned a0 = SCR + (8 * g + q) * 128 + (((2 * nb + (p >> 1)) ^ (f2_ << 1)) << 4) + (p & 1) * 8;
  v4u wf[8], xf[8];
#pragma unroll
  for (int s = 0; s < 8; ++s) { wf[s] = trd2(a0 + s * 4096, a0 + s * 4096 + 512); xf[s] = lds128(ximg + (c16 & 1) * 512 + g * 16 + s * 64); }
  __builtin_amdgcn_sched_barrier(0);
  f4 d = {0.f, 0.f, 0.f, 0.f};
#pragma unroll
  for (int s = 0; s < 8; ++s) d = mfma32(wf[s], xf[s], d);
  const int pr = (c16 >> 1) & 1;
  const float v0 = pr ? d[2] : d[0], v1 = pr ? d[3] : d[1];
  if (c16 < 4) gst128(rs, WS_QG + ((((mat * 2 + kh) * 2 + (c16 & 1)) * 4096) + 64 * cb + 16 * nb + 4 * g + 2 * pr) * 8, (v4u){asu(v0), tag, asu(v1), tag});
}

__device__ __forceinline__ void out_unit(unsigned ou, unsigned oimg, int w, int lane, int mat, int pidx, rsrc_t rs, unsigned tag) {
  const int g = lane >> 4, c16 = lane & 15, q = c16 >> 2, p = lane & 3;
  const int f3 = q | ((g & 1) << 2);
  const v4u ob = lds128(oimg + (c16 & 1) * 64 + g * 16);
  f4 d[4];
  v4u wf[4];
#pragma unroll
  for (int tt = 0; tt < 4; ++tt) {
    const unsigned a0 = ou + (8 * g + q) * 1024 + (((2 * (4 * w + tt) + (p >> 1)) ^ (f3 << 1)) << 4) + (p & 1) * 8;
    wf[tt] = trd2(a0, a0 + 4096);
  }
  __builtin_amdgcn_sched_barrier(0);
#pragma unroll
  for (int tt = 0; tt < 4; ++tt) d[tt] = mfma32(wf[tt], ob, (f4){0.f, 0.f, 0.f, 0.f});
  const int tt = (c16 >> 1) & 3, pr = c16 >> 3;
  const f4 e = tt == 0 ? d[0] : tt == 1 ? d[1] : tt == 2 ? d[2] : d[3];
  const float v0 = pr ? e[2] : e[0], v1 = pr ? e[3] : e[1];
  const int n = 64 * w + 16 * tt + 4 * g + 2 * pr;
  gst128(rs, WS_F + ((((mat * 64 + (n >> 3)) * 128 + pidx) * 16) + (c16 & 1) * 8 + (n & 7)) * 8, (v4u){asu(v0), tag, asu(v1), tag});
}

__device__ __forceinline__ void gather_ctx(unsigned oimg, int w, int lane, int h, int dq, rsrc_t rs, unsigned tag) {
  const int b = lane >> 5, pr = (lane >> 4) & 1, s = lane & 15;
  const unsigned off = WS_CT + (((h * 2 + (s & 1)) * 2 + b) * 128 + 32 * dq + 4 * w + 2 * pr) * 8;
  v4u v;
  _Pragma("nounroll") for (int it = 0; it < SPIN_CAP; ++it) { cfence(); v = gld128(rs, off); if (__all(tagok(v, tag))) break; __builtin_amdgcn_s_sleep(2); }
  const float o0 = asf(v.x) + dpp<XOR1>(asf(v.x)), o1 = asf(v.z) + dpp<XOR1>(asf(v.z));
  if (s == 0) sto32(oimg + b * 64 + (4 * w + 2 * pr) * 2, pk(o0, o1));
}

}

__global__ void __launch_bounds__(512, 2) fused_decode(
    const char* __restrict__ Ax, const char* __restrict__ Kc, const char* __restrict__ Vc, const char* __restrict__ Wq, const char* __restrict__ Wo,
    const char* __restrict__ Bx, const char* __restrict__ Ckv, const char* __restrict__ Bwq, const char* __restrict__ Wuk, const char* __restrict__ Wuv,
    const char* __restrict__ Bwo, unsigned short* __restrict__ out, char* __restrict__ ws) {
  const int bid = blockIdx.x, tid = threadIdx.x, lane = tid & 63, wave = __builtin_amdgcn_readfirstlane(tid >> 6);
  const int g = lane >> 4, c16 = lane & 15;
  STAMP_DECL;
  STAMP(0);
  const rsrc_t rs = __builtin_amdgcn_make_buffer_rsrc(ws, 0, WS_END, 0x00020000);
  asm volatile("" :: "s"(Ax), "s"(Kc), "s"(Vc), "s"(Wq), "s"(Wo), "s"(Bx), "s"(Ckv), "s"(Bwq), "s"(Wuk), "s"(Wuv), "s"(Bwo), "s"(out), "s"(ws));
  unsigned tag;
#define LOAD_TAG() asm volatile("s_load_dword %0, %1, 0x0" : "=s"(tag) : "s"(ws) : "memory")
#define WAIT_TAG() do { asm volatile("s_waitcnt lgkmcnt(0)" : "+s"(tag) :: "memory"); tag += 1u; } while (0)
  const int qmat = bid & 1, qkh = (bid >> 1) & 1, qcb = bid >> 2;
  const char* qW = (qmat ? Bwq : Wq) + (size_t)(256 * qkh) * 8192 + qcb * 128;
  const char* qx = (qmat ? Bx : Ax) + 512 * qkh;
  const unsigned vx = (lane >> 5) * 1024 + (lane & 31) * 16;
  unsigned long long pc;
  asm volatile("s_getpc_b64 %0" : "=s"(pc));
  const char* codep = (const char*)((pc & ~255ull) + (unsigned)(((bid >> 3) % CODE_KB) * 1024));
  const unsigned vq0 = (lane >> 3) * 8192 + (((lane & 7) ^ (((lane >> 4) & 1) << 1)) << 4);

  if (bid >= NB_CU) {
    const int iA = bid - NB_CU, pair = iA & 15, split = iA >> 4, b = pair >> 3, kvh = pair & 7;
    const int nblk = split < 8 ? 43 : 42, blk0 = split < 8 ? 43 * split : 344 + 42 * (split - 8);
    const int omat = iA < 64 ? 1 : 0, ounit = iA < 64 ? 64 + iA : iA - 64;
    if (wave == 0) {
      __builtin_amdgcn_s_setprio(3);
      constexpr int W = WIN_A, IST = (W - 1) & 3;
      static_assert(W >= 4 && W <= 32, "window");
      int pub = 0, p = 0;
#define PUBLISH(val) do { const int v_ = (val); if (v_ > pub) { pub = v_; if (lane == 0) *(volatile LAS unsigned*)A_LANDED = (unsigned)v_; } } while (0)
#define PACE(i) do { vmwait<W - 1>(); if ((i) == IST) PUBLISH((4 * p + IST + 1 - W) / 4); } while (0)
      if (lane < 32) sto32(A_MISC + lane * 4, 0u);
      dma<0>(qx, vx, A_XIMG);
      dma<0>(codep, lane * 16, A_DUMMY);
#pragma unroll
      for (int u = 0; u < 4; ++u) dma<0>(qW + (size_t)u * 65536, vq0 ^ ((u & 1) << 6), SCR + u * 1024);
      bar();
#pragma nounroll
      for (p = 1; p < 8; ++p) {
#pragma unroll
        for (int i = 0; i < 4; ++i) dma<0>(qW + (size_t)(4 * p + i) * 65536, vq0 ^ ((i & 1) << 6), SCR + (4 * p + i) * 1024);
      }
      LOAD_TAG();
      STAMP(9);
      const char* kp = Kc + ((size_t)pair * 8192 + blk0 * 16) * 256;
      const char* vp = Vc + ((size_t)pair * 8192 + blk0 * 16) * 256;
      const unsigned vk0 = g * 256 + ((c16 ^ g) << 4), vv0 = g * 256 + ((c16 ^ (g << 1)) << 4);
      dma<0>(kp, vk0, 0); dma<1024>(kp, vk0 ^ 64, 0); dma<2048>(kp, vk0 ^ 128, 0); dma<3072>(kp, vk0 ^ 192, 0);
      dma<0>(vp, vv0, 4096); dma<1024>(vp, vv0 ^ 128, 4096); dma<2048>(vp, vv0, 4096); dma<3072>(vp, vv0 ^ 128, 4096);
      kp += 4096; vp += 4096;
      vmwait<39>();
      STAMP(10);
      vmwait<8>();
      PUBLISH(8);
      STAMP(11);
      p = 10;
      int slot = 1;
      for (int j = 1; j < nblk; ++j) {
        if (j >= NSLOT) {
          const unsigned need = j - NSLOT + 1;
          if (lds_peek(A_DONE + slot * 4) < need) { vmwait<0>(); PUBLISH(p); lds_wait_ge(A_DONE + slot * 4, need); }
        }
        const unsigned sb = slot * 8192;
        PACE(0); dma<0>(kp, vk0, sb); PACE(1); dma<1024>(kp, vk0 ^ 64, sb); PACE(2); dma<2048>(kp, vk0 ^ 128, sb); PACE(3); dma<3072>(kp, vk0 ^ 192, sb);
        ++p;
        PACE(0); dma<0>(vp, vv0, sb + 4096); PACE(1); dma<1024>(vp, vv0 ^ 128, sb + 4096); PACE(2); dma<2048>(vp, vv0, sb + 4096); PACE(3); dma<3072>(vp, vv0 ^ 128, sb + 4096);
        ++p;
        kp += 4096; vp += 4096;
        slot = slot == NSLOT - 1 ? 0 : slot + 1;
      }
      lds_wait_ge(A_QRDY, 1u);
      const char* op = (omat ? Bwo : Wo) + (size_t)ounit * 32768;
#pragma unroll
      for (int k = 0; k < 32; ++k) {
        if (k < W) { vmwait<W - 1>(); if ((k & 3) == IST) PUBLISH(p + (k + 1 - W) / 4); }
        dma<0>(op + k * 1024, (lane ^ (((k & 3) | (((k >> 3) & 1) << 2)) << 1)) << 4, SCR + k * 1024);
      }
      __builtin_amdgcn_s_setprio(0);
      WAIT_TAG();
      STAMP(5);
#undef PACE
#undef PUBLISH
    } else {
      bar();
      LOAD_TAG();
      WAIT_TAG();
      if (wave <= 4) {
        lds_wait_ge(A_LANDED, 8u);
        STAMP(1);
        q_unit(A_XIMG, wave - 1, lane, qmat, qcb, qkh, rs, tag);
        lgk0();
        if (lane == 0) __hip_atomic_fetch_add((LAS unsigned*)(A_MISC + 8), 1u, __ATOMIC_RELAXED, __HIP_MEMORY_SCOPE_WORKGROUP);
        STAMP(2);
      }
      if (wave == 5) {
        lds_wait_ge(A_LANDED, (unsigned)QGO);
        v4u v[8];
        const unsigned off = WS_QG + (b * 4096 + kvh * 512 + 2 * lane) * 8;
        _Pragma("nounroll") for (int it = 0; it < SPIN_CAP; ++it) {
          cfence();
          bool ok = true;
#pragma unroll
          for (int k = 0; k < 8; ++k) { v[k] = gld128(rs, off + (k & 1) * 65536 + (k >> 1) * 1024); ok &= tagok(v[k], tag); }
          if (__all(ok)) break;
          __builtin_amdgcn_s_sleep(8);
        }
#pragma unroll
        for (int hh = 0; hh < 4; ++hh) sto32(A_QIMG + hh * 256 + lane * 4, pk(asf(v[2 * hh].x) + asf(v[2 * hh + 1].x), asf(v[2 * hh].z) + asf(v[2 * hh + 1].z)));
        lds_wait_ge(A_MISC + 8, 4u);
        lgk0();
        if (lane == 0) *(volatile LAS unsigned*)A_QRDY = 1u;
      }
      lds_wait_ge(A_QRDY, 1u);
      STAMP(3);
      v4u qf[4];
#pragma unroll
      for (int s = 0; s < 4; ++s) qf[s] = lds128(A_QIMG + (c16 & 3) * 256 + s * 64 + g * 16);
      const unsigned ko = c16 * 256 + ((g ^ c16) << 4);
      const int vr = 4 * g + (c16 >> 2);
      const unsigned vo = 4096 + vr * 256 + ((((lane & 3) >> 1) ^ ((vr & 7) << 1)) << 4) + (lane & 1) * 8;
      float m = -INFINITY, l = 0.f;
      f4 acc[8];
#pragma unroll
      for (int t = 0; t < 8; ++t) acc[t] = (f4){0.f, 0.f, 0.f, 0.f};
      int slot = wave - 1, j = wave - 1;
      for (; j < nblk; j += NCONS) {
        const unsigned sb = slot * 8192;
        lds_wait_ge(A_LANDED, 9u + 2 * j);
        v4u kf[4];
#pragma unroll
        for (int s = 0; s < 4; ++s) kf[s] = lds128(sb + (ko ^ (s << 6)));
        __builtin_amdgcn_sched_barrier(0);
        f4 s4_ = {0.f, 0.f, 0.f, 0.f};
#pragma unroll
        for (int s = 0; s < 4; ++s) s4_ = mfma32(kf[s], qf[s], s4_);
        float mx = fmaxf(fmaxf(s4_[0], s4_[1]), fmaxf(s4_[2], s4_[3]));
        mx = x32max(x16max(mx));
        const float mn = fmaxf(m, mx), al = ex2((m - mn) * SC), mc = -mn * SC;
        const float p0 = ex2(fmaf(s4_[0], SC, mc)), p1 = ex2(fmaf(s4_[1], SC, mc)), p2 = ex2(fmaf(s4_[2], SC, mc)), p3 = ex2(fmaf(s4_[3], SC, mc));
        l = fmaf(l, al, (p0 + p1) + (p2 + p3));
        m = mn;
        const v2u pb = {pk(p0, p1), pk(p2, p3)};
#pragma unroll
        for (int t = 0; t < 8; ++t) acc[t] *= al;
        lds_wait_ge(A_LANDED, 10u + 2 * j);
        v2u vf[8];
#pragma unroll
        for (int t = 0; t < 8; ++t) vf[t] = trd(sb + (vo ^ (t << 5)));
        lgk0();
        if (lane == 0) *(volatile LAS unsigned*)(A_DONE + slot * 4) = (unsigned)(j + 1);
#pragma unroll
        for (int t = 0; t < 8; ++t) acc[t] = mfma16(vf[t], pb, acc[t]);
        if (j + NCONS < nblk) { slot += NCONS; if (slot >= NSLOT) slot -= NSLOT; }
      }
      STAMP(4);
      l = x32sum(x16sum(l));
      if (c16 < 4) {
#pragma unroll
        for (int t = 0; t < 8; ++t) sto128(slot * 8192 + c16 * 512 + t * 64 + g * 16, __builtin_bit_cast(v4u, acc[t]));
        if (g == 0) sto64(A_ML + ((wave - 1) * 4 + c16) * 8, (v2u){asu(m * SC), asu(l)});
      }
    }
    bar();
    if (wave >= 4) {
      const int hh = wave - 4;
      float mw[NCONS], lw[NCONS];
      f2 ow[NCONS];
#pragma unroll
      for (int c = 0; c < NCONS; ++c) {
        const int jl = c + ((nblk - 1 - c) / NCONS) * NCONS;
        const f2 t = *(const LAS f2*)(A_ML + (c * 4 + hh) * 8);
        mw[c] = t.x; lw[c] = t.y;
        ow[c] = *(const LAS f2*)((jl % NSLOT) * 8192 + hh * 512 + lane * 8);
      }
      float M = mw[0];
#pragma unroll
      for (int c = 1; c < NCONS; ++c) M = fmaxf(M, mw[c]);
      float L = 0.f, o0 = 0.f, o1 = 0.f;
#pragma unroll
      for (int c = 0; c < NCONS; ++c) { const float wt = ex2(mw[c] - M); L = fmaf(wt, lw[c], L); o0 = fmaf(wt, ow[c].x, o0); o1 = fmaf(wt, ow[c].y, o1); }
      const int rec = (b * 32 + kvh * 4 + hh) * NSPLIT_A + split;
      gst128(rs, WS_PAO + rec * 1024 + lane * 16, (v4u){asu(o0), tag, asu(o1), tag});
      if (lane == 0) gst128(rs, WS_PAM + rec * 16, (v4u){asu(M), tag, asu(L), tag});
    }
    STAMP(6);
    const int oh = ounit >> 2, odq = ounit & 3;
    if (omat) {
      gather_ctx(A_OIMG, wave, lane, oh, odq, rs, tag);
    } else {
      const int gb = lane >> 5, pr = (lane >> 4) & 1, s = c16 < NSPLIT_A ? c16 : NSPLIT_A - 1;
      const int rec = (gb * 32 + oh) * NSPLIT_A + s;
      v4u v, ml;
      _Pragma("nounroll") for (int it = 0; it < SPIN_CAP; ++it) {
        cfence();
        v = gld128(rs, WS_PAO + rec * 1024 + (32 * odq + 4 * wave + 2 * pr) * 8);
        ml = gld128(rs, WS_PAM + rec * 16);
        if (__all(tagok(v, tag) && tagok(ml, tag))) break;
        __builtin_amdgcn_s_sleep(1);
      }
      const float M = row16max(asf(ml.x));
      const float wt = c16 < NSPLIT_A ? ex2(asf(ml.x) - M) : 0.f;
      const float L = row16sum(wt * asf(ml.z)), o0 = row16sum(wt * asf(v.x)), o1 = row16sum(wt * asf(v.z));
      const float inv = 1.f / L;
      if (c16 == 0) sto32(A_OIMG + gb * 64 + (4 * wave + 2 * pr) * 2, pk(o0 * inv, o1 * inv));
    }
    vmwait<0>();
    STAMP(7);
    bar();
    out_unit(SCR, A_OIMG, wave, lane, omat, ounit, rs, tag);
    STAMP(8);
    STAMP_OUT();
  } else {
    const int b = bid >> 5, sp = bid & 31, h = bid >> 1, ch = bid & 1, w = wave;
    const char* uk = Wuk + (size_t)(256 * ch + 32 * w) * 8192 + h * 256;
    const char* uv = Wuv + (size_t)(256 * ch + 32 * w) * 8192 + h * 256;
    const char* cp = Ckv + ((size_t)b * 8192 + 256 * sp + 8 * w) * 1024;
    const unsigned vuk = g * 8192 + ((c16 ^ g) << 4), vuv = g * 8192 + ((c16 ^ (g << 1)) << 4);
    const unsigned vc0 = (lane ^ ((w & 1) ? 3 : 0)) << 4, vc1 = (lane ^ ((w & 1) ? 1 : 2)) << 4;
#define LD_UK(u) dma<0>(uk + (size_t)(u) * 32768, vuk ^ (((u) & 3) << 6), SLOT1 + (32 * w + 4 * (u)) * 256)
#define LD_UV(u) dma<0>(uv + (size_t)(u) * 32768, vuv ^ ((((u) >> 1) & 1) << 7), SLOT0 + (32 * w + 4 * (u)) * 256)
#define LD_C(rho, u) dma<((u) & 3) * 1024>(cp + (size_t)(rho) * 65536 + ((u) >> 2) * 4096, (((u) >> 2) ? vc1 : vc0) ^ (((u) & 3) << 6), ((rho) & 1) * 65536 + w * 8192 + ((u) >> 2) * 4096)
    if (w == 7) { dma<0>(qx, vx, B_XIMG); dma<0>(codep, lane * 16, B_DUMMY); }
#pragma unroll
    for (int i = 0; i < 4; ++i) dma<0>(qW + (size_t)(4 * w + i) * 65536, vq0 ^ ((i & 1) << 6), SCR + (4 * w + i) * 1024);
    asm volatile("s_barrier" ::: "memory");
    LD_UK(0); LD_UK(1);
    LOAD_TAG();
    vmwait<2>();
    WAIT_TAG();
    bar();
    STAMP(1);
    LD_UK(2); LD_UK(3);
    if (WB >= 6) { LD_UK(4); LD_UK(5); }
    v4u qg[4];
    if (w >= 4) {
      q_unit(B_XIMG, w - 4, lane, qmat, qcb, qkh, rs, tag);
    } else if (w == 0) {
      const unsigned off = WS_QG + (4 * 4096 + h * 128 + 2 * lane) * 8;
      _Pragma("nounroll") for (int it = 0; it < SPIN_CAP; ++it) {
        cfence();
        bool ok = true;
#pragma unroll
        for (int k = 0; k < 4; ++k) { qg[k] = gld128(rs, off + (k & 1) * 65536 + (k >> 1) * 32768); ok &= tagok(qg[k], tag); }
        if (__all(ok)) break;
        __builtin_amdgcn_s_sleep(2);
      }
    }
    if (WB < 6) { vmwait<WB - 2>(); LD_UK(4); LD_UK(5); }
    vmwait<WB - 2>(); LD_UK(6); LD_UK(7);
    vmwait<WB - 2>(); LD_C(0, 0); LD_C(0, 1);
    vmwait<WB - 2>(); LD_C(0, 2); LD_C(0, 3);
    bar();
    if (w == 0) {
      sto32(B_QBI + lane * 4, pk(asf(qg[0].x) + asf(qg[1].x), asf(qg[0].z) + asf(qg[1].z)));
      sto32(B_QBI + 256 + lane * 4, pk(asf(qg[2].x) + asf(qg[3].x), asf(qg[2].z) + asf(qg[3].z)));
    }
    vmwait<(WB - 2 < 4 ? WB - 2 : 4)>();
    bar();
    STAMP(2);
    LD_C(0, 4); LD_C(0, 5);
    {
      const unsigned a0 = SLOT1 + (32 * w + c16) * 256 + ((g ^ c16) << 4);
      v4u qb[4], u0[4], u1[4];
#pragma unroll
      for (int s = 0; s < 4; ++s) { qb[s] = lds128(B_QBI + (c16 & 1) * 256 + s * 64 + g * 16); u0[s] = lds128(a0 ^ (s << 6)); u1[s] = lds128((a0 ^ (s << 6)) + 4096); }
      __builtin_amdgcn_sched_barrier(0);
      f4 d0 = {0.f, 0.f, 0.f, 0.f}, d1 = d0;
#pragma unroll
      for (int s = 0; s < 4; ++s) { d0 = mfma32(u0[s], qb[s], d0); d1 = mfma32(u1[s], qb[s], d1); }
      const int rb = (c16 >> 1) & 1;
      const f4 e = rb ? d1 : d0;
      if (c16 < 4) sto64(B_QAS + (c16 & 1) * 512 + (32 * w + 16 * rb + 4 * g) * 2, (v2u){pk(e[0], e[1]), pk(e[2], e[3])});
    }
    bar();
    if (w == 0) {
      gst128(rs, WS_QAB + ((lane >> 5) * 32 + h) * 1024 + ch * 512 + (lane & 31) * 16, lds128(B_QAS + lane * 16));
      vmwait<0>();
      if (lane == 0) gst32(rs, WS_FQ + bid * FS, tag);
    }
    STAMP(3);
    vmwait<WB - 2>(); LD_C(0, 6); LD_C(0, 7);
    vmwait<WB - 2>(); LD_C(1, 0); LD_C(1, 1);
    vmwait<WB - 2>(); LD_C(1, 2); LD_C(1, 3);
    if (w == 0) {
      _Pragma("nounroll") for (int it = 0; it < SPIN_CAP; ++it) { cfence(); if (__all(gld32(rs, WS_FQ + lane * FS) == tag)) break; __builtin_amdgcn_s_sleep(2); }
    }
    bar();
    STAMP(4);
    {
      v4u t[4];
#pragma unroll
      for (int i = 0; i < 4; ++i) t[i] = gld128(rs, WS_QAB + (b * 32 + 4 * w + i) * 1024 + lane * 16);
#pragma unroll
      for (int i = 0; i < 4; ++i) sto128(SCR + (4 * w + i) * 1024 + ((lane ^ ((4 * w + i) & 15)) << 4), t[i]);
    }
    LD_C(1, 4); LD_C(1, 5); LD_C(1, 6); LD_C(1, 7);
    bar();
    const int kb4 = w & 3, hg = w >> 2;
    v4u qf[16];
#pragma unroll
    for (int s = 0; s < 16; ++s) qf[s] = lds128(SCR + (16 * hg + c16) * 1024 + ((((4 * s) & 15) ^ g ^ c16) << 4) + (s >> 2) * 256);
    STAMP(5);
    const unsigned fk = ((c16 & 3) << 2) | ((0x78u >> (2 * (c16 >> 2))) & 3u);
    const unsigned ka = (16 * kb4 + c16) * 1024 + ((g ^ fk) << 4);
    const int h2 = lane >> 5, blk = g & 1, qq = c16 >> 2, pp = lane & 3;
    const unsigned ft0 = (qq << 2) | (h2 ? 3u : 0u), ft1 = (qq << 2) | (h2 ? 1u : 2u);
    const unsigned cbase = 8 * w + 2 * blk + (pp >> 1);
    const unsigned va00 = (8 * h2 + qq) * 1024 + ((cbase ^ ft0) << 4) + (pp & 1) * 8, va01 = (8 * h2 + 4 + qq) * 1024 + ((cbase ^ ft1) << 4) + (pp & 1) * 8;
    float m = -INFINITY, l = 0.f;
    f16v acc0, acc1;
#pragma unroll
    for (int i = 0; i < 16; ++i) { acc0[i] = 0.f; acc1[i] = 0.f; }
    vmwait<8>();
#pragma nounroll
    for (int rho = 0; rho < 4; ++rho) {
      const unsigned sb = (rho & 1) * 65536;
      bar();
      f4 s4_ = {0.f, 0.f, 0.f, 0.f};
      {
        v4u kf[16];
#pragma unroll
        for (int s = 0; s < 16; ++s) kf[s] = lds128(sb + (ka ^ ((s & 3) << 6)) + (s >> 2) * 256);
        __builtin_amdgcn_sched_barrier(0);
#pragma unroll
        for (int s = 0; s < 16; ++s) s4_ = mfma32(kf[s], qf[s], s4_);
      }
      float mx = fmaxf(fmaxf(s4_[0], s4_[1]), fmaxf(s4_[2], s4_[3]));
      mx = x32max(x16max(mx));
      if (g == 0) stof(B_MX + (kb4 * 32 + 16 * hg + c16) * 4, mx);
      bar();
      const unsigned ma = B_MX + (16 * hg + c16) * 4;
      const float mn = fmaxf(fmaxf(m, fmaxf(ldsf(ma), ldsf(ma + 128))), fmaxf(ldsf(ma + 256), ldsf(ma + 384)));
      const float al = ex2((m - mn) * SC), mc = -mn * SC;
      const float p0 = ex2(fmaf(s4_[0], SC, mc)), p1 = ex2(fmaf(s4_[1], SC, mc)), p2 = ex2(fmaf(s4_[2], SC, mc)), p3 = ex2(fmaf(s4_[3], SC, mc));
      l = fmaf(l, al, (p0 + p1) + (p2 + p3));
      m = mn;
      sto64(B_P + kb4 * 1024 + (16 * hg + c16) * 32 + g * 8, (v2u){pk(p0, p1), pk(p2, p3)});
      if (kb4 == 0 && g == 0) stof(B_AL + (16 * hg + c16) * 4, al);
      v4u c0[4], c1[4];
#pragma unroll
      for (int ks = 0; ks < 4; ++ks) {
        c0[ks] = trd2(sb + ks * 16384 + va00, sb + ks * 16384 + va01);
        c1[ks] = trd2(sb + ks * 16384 + (va00 ^ 64), sb + ks * 16384 + (va01 ^ 64));
      }
      bar();
      if (rho == 2) { LD_UV(0); LD_UV(1); LD_UV(2); LD_UV(3); LD_UV(4); LD_UV(5); }
      else if (rho != 3) { LD_C(rho + 2, 0); LD_C(rho + 2, 1); LD_C(rho + 2, 2); LD_C(rho + 2, 3); LD_C(rho + 2, 4); LD_C(rho + 2, 5); }
      const float alp = ldsf(B_AL + (lane & 31) * 4);
      v4u pf[4];
#pragma unroll
      for (int ks = 0; ks < 4; ++ks) pf[ks] = lds128(B_P + ks * 1024 + (lane & 31) * 32 + h2 * 16);
      __builtin_amdgcn_sched_barrier(0);
      acc0 *= alp; acc1 *= alp;
#pragma unroll
      for (int ks = 0; ks < 4; ++ks) { acc0 = mfmaL(c0[ks], pf[ks], acc0); acc1 = mfmaL(c1[ks], pf[ks], acc1); }
      if (rho == 2) { LD_UV(6); LD_UV(7); } else if (rho != 3) { LD_C(rho + 2, 6); LD_C(rho + 2, 7); }
      if (rho != 3) vmwait<8>();
    }
    l = x32sum(x16sum(l));
    if (g == 0) stof(B_LS + (kb4 * 32 + 16 * hg + c16) * 4, l);
    {
      const unsigned tb = B_T + w * 4096, n32 = lane & 31;
#pragma unroll
      for (int q4 = 0; q4 < 4; ++q4) {
        sto64(tb + n32 * 128 + ((q4 ^ (n32 & 7)) << 4) + h2 * 8, (v2u){pk(acc0[4 * q4], acc0[4 * q4 + 1]), pk(acc0[4 * q4 + 2], acc0[4 * q4 + 3])});
        sto64(tb + n32 * 128 + (((4 + q4) ^ (n32 & 7)) << 4) + h2 * 8, (v2u){pk(acc1[4 * q4], acc1[4 * q4 + 1]), pk(acc1[4 * q4 + 2], acc1[4 * q4 + 3])});
      }
      lgk0();
      const int hr = lane >> 3, ck = lane & 7;
#pragma unroll
      for (int i = 0; i < 4; ++i)
        gst128(rs, WS_PB + ((b * 32 + sp) * 32 + 8 * i + hr) * 1024 + w * 128 + ck * 16, lds128(tb + (8 * i + hr) * 128 + ((ck ^ hr) << 4)));
    }
    bar();
    if (kb4 == 0 && g == 0) {
      const unsigned la = B_LS + (16 * hg + c16) * 4;
      const float L = (ldsf(la) + ldsf(la + 128)) + (ldsf(la + 256) + ldsf(la + 384));
      gst64(rs, WS_PBM + ((b * 32 + sp) * 32 + 16 * hg + c16) * 8, (v2u){asu(m * SC), asu(L)});
    }
    vmwait<0>();
    bar();
    if (w == 0 && lane == 0) gst32(rs, WS_FP + bid * FS, tag);
    STAMP(10);
    {
      const char* op = Bwo + (size_t)bid * 32768 + w * 4096;
      const unsigned fo = ((w >> 1) & 1) << 3;
      dma<0>(op, (lane ^ fo) << 4, B_OU + w * 4096); dma<1024>(op, (lane ^ (fo | 2u)) << 4, B_OU + w * 4096);
      dma<2048>(op, (lane ^ (fo | 4u)) << 4, B_OU + w * 4096); dma<3072>(op, (lane ^ (fo | 6u)) << 4, B_OU + w * 4096);
    }
    if (w == 0) {
      _Pragma("nounroll") for (int it = 0; it < SPIN_CAP; ++it) { cfence(); if (__all(gld32(rs, WS_FP + lane * FS) == tag)) break; __builtin_amdgcn_s_sleep(4); }
    }
    bar();
    STAMP(11);
    {
      const int gb = lane >> 5, s = lane & 31;
      const v2u ml = gld64(rs, WS_PBM + ((gb * 32 + s) * 32 + h) * 8);
      v4u o[4];
#pragma unroll
      for (int i = 0; i < 4; ++i) o[i] = gld128(rs, WS_PB + ((gb * 32 + 4 * w + i) * 32 + h) * 1024 + ch * 512 + s * 16);
      const float M = x16max(row16max(asf(ml.x)));
      const float wt = ex2(asf(ml.x) - M);
      const float L = x16sum(row16sum(wt * asf(ml.y)));
      const float wn = wt / L;
      float a[8];
#pragma unroll
      for (int k = 0; k < 8; ++k) a[k] = 0.f;
#pragma unroll
      for (int i = 0; i < 4; ++i) {
        const float wi = asf(__builtin_amdgcn_ds_bpermute((gb * 32 + 4 * w + i) * 4, asu(wn)));
#pragma unroll
        for (int k = 0; k < 4; ++k) { a[2 * k] = fmaf(wi, asf(o[i][k] << 16), a[2 * k]); a[2 * k + 1] = fmaf(wi, asf(o[i][k] & 0xffff0000u), a[2 * k + 1]); }
      }
      sto128(B_RED + w * 2048 + lane * 32, (v4u){asu(a[0]), asu(a[1]), asu(a[2]), asu(a[3])});
      sto128(B_RED + w * 2048 + lane * 32 + 16, (v4u){asu(a[4]), asu(a[5]), asu(a[6]), asu(a[7])});
    }
    bar();
    if (tid < 256) {
      float s0 = 0.f, s1 = 0.f;
#pragma unroll
      for (int i = 0; i < 8; ++i) { const f2 t = *(const LAS f2*)(B_RED + i * 2048 + tid * 8); s0 += t.x; s1 += t.y; }
      sto32(B_OL + tid * 4, pk(s0, s1));
    }
    bar();
    {
      const int f3 = qq | ((g & 1) << 2);
      const unsigned a0 = SLOT0 + (8 * g + qq) * 256 + (((2 * w + (pp >> 1)) ^ (f3 << 1)) << 4) + (pp & 1) * 8;
      v4u wf[8], of[8];
#pragma unroll
      for (int s = 0; s < 8; ++s) { wf[s] = trd2(a0 + s * 8192, a0 + s * 8192 + 1024); of[s] = lds128(B_OL + (c16 & 1) * 512 + s * 64 + g * 16); }
      __builtin_amdgcn_sched_barrier(0);
      f4 d = {0.f, 0.f, 0.f, 0.f};
#pragma unroll
      for (int s = 0; s < 8; ++s) d = mfma32(wf[s], of[s], d);
      const int pr = (c16 >> 1) & 1;
      const float v0 = pr ? d[2] : d[0], v1 = pr ? d[3] : d[1];
      if (c16 < 4) gst128(rs, WS_CT + (((h * 2 + ch) * 2 + (c16 & 1)) * 128 + 16 * w + 4 * g + 2 * pr) * 8, (v4u){asu(v0), tag, asu(v1), tag});
    }
    STAMP(12);
    gather_ctx(B_OIMG, w, lane, bid >> 2, bid & 3, rs, tag);
    vmwait<0>();
    bar();
    out_unit(B_OU, B_OIMG, w, lane, 1, bid, rs, tag);
    STAMP(13);
    {
      const unsigned off = WS_PAM + (bid * NSPLIT_A + (bid % NSPLIT_A)) * 16;
      _Pragma("nounroll") for (int it = 0; it < SPIN_CAP; ++it) { cfence(); if (__all(gld64(rs, off).y == tag)) break; __builtin_amdgcn_s_sleep(8); }
    }
    STAMP(14);
    v4u f[4];
    _Pragma("nounroll") for (int it = 0; it < SPIN_CAP; ++it) {
      cfence();
      bool ok = true;
#pragma unroll
      for (int k = 0; k < 4; ++k) { f[k] = gld128(rs, WS_F + ((k >> 1) * 64 + bid) * 16384 + (k & 1) * 8192 + tid * 16); ok &= tagok(f[k], tag); }
      if (__all(ok)) break;
      __builtin_amdgcn_s_sleep(1);
    }
    float r[4] = {asf(f[0].x) + asf(f[1].x), asf(f[0].z) + asf(f[1].z), asf(f[2].x) + asf(f[3].x), asf(f[2].z) + asf(f[3].z)};
#pragma unroll
    for (int k = 0; k < 4; ++k) { r[k] += dpp<ROR8>(r[k]); r[k] = x32sum(x16sum(r[k])); }
    if (lane < 8) { sto64(B_FIN + w * 128 + lane * 8, (v2u){asu(r[0]), asu(r[1])}); sto64(B_FIN + w * 128 + 64 + lane * 8, (v2u){asu(r[2]), asu(r[3])}); }
    bar();
    if (tid < 16) {
      float s0 = 0.f, s1 = 0.f;
#pragma unroll
      for (int i = 0; i < 8; ++i) { const f2 t = *(const LAS f2*)(B_FIN + i * 128 + tid * 8); s0 += t.x; s1 += t.y; }
      const int mat = tid >> 3, ob = (tid >> 2) & 1, pr = tid & 3;
      *(GAS unsigned*)((GAS char*)out + (ob * 1024 + mat * 512 + 8 * bid + 2 * pr) * 2) = pk(s0, s1);
      if (bid == 0 && tid == 0) gst32(rs, WS_E, tag);
    }
    STAMP(15);
    STAMP_OUT();
  }
}

extern "C" void kernel_launch(void* const* d_in, const int* in_sizes, int n_in, void* d_out, int out_size, void* d_ws, size_t ws_size,
                              hipStream_t stream) {
  (void)in_sizes; (void)n_in; (void)out_size; (void)ws_size;
  (void)hipFuncSetAttribute((const void*)fused_decode, hipFuncAttributeMaxDynamicSharedMemorySize, LDS_BYTES);
  fused_decode<<<256, 512, LDS_BYTES, stream>>>(
      (const char*)d_in[0], (const char*)d_in[1], (const char*)d_in[2], (const char*)d_in[3], (const char*)d_in[4], (const char*)d_in[5],
      (const char*)d_in[6], (const char*)d_in[7], (const char*)d_in[8], (const char*)d_in[9], (const char*)d_in[10], (unsigned short*)d_out,
      (char*)d_ws);
}
```

```cpp
#define DBG_TS
#define PROBE 0
#define PROLE 0
#define PSTAT 0
#define PZERO 1
#include <hip/hip_runtime.h>
#include <stdint.h>
#pragma clang diagnostic ignored "-Wint-to-pointer-cast"

#define GAS __attribute__((address_space(1)))
#define LAS __attribute__((address_space(3)))
typedef unsigned v4u __attribute__((ext_vector_type(4)));
typedef unsigned v2u __attribute__((ext_vector_type(2)));
typedef float f4 __attribute__((ext_vector_type(4)));
typedef float f2 __attribute__((ext_vector_type(2)));
typedef float f16v __attribute__((ext_vector_type(16)));
typedef short s4 __attribute__((ext_vector_type(4)));
typedef __bf16 b8 __attribute__((ext_vector_type(8)));
typedef __bf16 b2 __attribute__((ext_vector_type(2)));

#define WIN_A 24
#define WB 6
#define CODE_KB 20
#define QGO 20

namespace {

constexpr int NB_CU = 64, NSPLIT_A = 12, NSLOT = 15, NCONS = 7;
constexpr float SC = 0.08838834764831845f * 1.4426950408889634f;
constexpr int SPIN_CAP = 1 << 18;

constexpr unsigned FS = 64;
constexpr unsigned WS_E = 0;
constexpr unsigned WS_FQ = 4096;
constexpr unsigned WS_FP = WS_FQ + 64 * FS;
constexpr unsigned WS_QG = 65536;
constexpr unsigned WS_QAB = WS_QG + 262144;
constexpr unsigned WS_PBM = WS_QAB + 65536;
constexpr unsigned WS_PAM = WS_PBM + 16384;
constexpr unsigned WS_CT = WS_PAM + 16384;
constexpr unsigned WS_PAO = WS_CT + 131072;
constexpr unsigned WS_PB = WS_PAO + 786432;
constexpr unsigned WS_F = WS_PB + 2097152;
constexpr unsigned WS_END = WS_F + 2097152;
constexpr unsigned WS_TS = 8u << 20;

constexpr unsigned LDS_BYTES = 163840;
constexpr unsigned SCR = 131072;
constexpr unsigned A_MISC = NSLOT * 8192;
constexpr unsigned A_LANDED = A_MISC, A_QRDY = A_MISC + 4, A_DONE = A_MISC + 64;
constexpr unsigned A_QIMG = A_MISC + 256;
constexpr unsigned A_ML = A_MISC + 1280;
constexpr unsigned A_OIMG = A_MISC + 1536;
constexpr unsigned A_XIMG = A_MISC + 2048, A_DUMMY = A_MISC + 3072;
constexpr unsigned SLOT0 = 0, SLOT1 = 65536;
constexpr unsigned B_QBI = SCR, B_QAS = SCR + 1024;
constexpr unsigned B_MX = SCR, B_AL = SCR + 512, B_LS = SCR + 640, B_P = SCR + 2048;
constexpr unsigned B_RED = SCR + 8192, B_OL = SCR + 24576, B_OIMG = SCR + 26624, B_FIN = SCR + 27648;
constexpr unsigned B_T = SLOT1, B_OU = SLOT1 + 32768;
constexpr unsigned B_XIMG = SLOT0 + 63 * 1024, B_DUMMY = SLOT0 + 62 * 1024;

__device__ __forceinline__ v4u lds128(unsigned a) { return *(const LAS v4u*)a; }
__device__ __forceinline__ void sto128(unsigned a, v4u v) { *(LAS v4u*)a = v; }
__device__ __forceinline__ void sto64(unsigned a, v2u v) { *(LAS v2u*)a = v; }
__device__ __forceinline__ void sto32(unsigned a, unsigned v) { *(LAS unsigned*)a = v; }
__device__ __forceinline__ float ldsf(unsigned a) { return *(const LAS float*)a; }
__device__ __forceinline__ void stof(unsigned a, float v) { *(LAS float*)a = v; }
__device__ __forceinline__ v2u trd(unsigned a) { return __builtin_bit_cast(v2u, __builtin_amdgcn_ds_read_tr16_b64_v4i16((LAS s4*)a)); }
__device__ __forceinline__ v4u trd2(unsigned a0, unsigned a1) { const v2u x = trd(a0), y = trd(a1); return (v4u){x.x, x.y, y.x, y.y}; }
__device__ __forceinline__ f4 mfma32(v4u a, v4u b, f4 c) {
  return __builtin_amdgcn_mfma_f32_16x16x32_bf16(__builtin_bit_cast(b8, a), __builtin_bit_cast(b8, b), c, 0, 0, 0); }
__device__ __forceinline__ f4 mfma16(v2u a, v2u b, f4 c) {
  return __builtin_amdgcn_mfma_f32_16x16x16bf16_1k(__builtin_bit_cast(s4, a), __builtin_bit_cast(s4, b), c, 0, 0, 0); }
__device__ __forceinline__ f16v mfmaL(v4u a, v4u b, f16v c) {
  return __builtin_amdgcn_mfma_f32_32x32x16_bf16(__builtin_bit_cast(b8, a), __builtin_bit_cast(b8, b), c, 0, 0, 0); }
__device__ __forceinline__ unsigned pk(float lo, float hi) { return __builtin_bit_cast(unsigned, __builtin_convertvector((f2){lo, hi}, b2)); }
__device__ __forceinline__ float asf(unsigned u) { return __builtin_bit_cast(float, u); }
__device__ __forceinline__ unsigned asu(float f) { return __builtin_bit_cast(unsigned, f); }
__device__ __forceinline__ float ex2(float x) { return __builtin_amdgcn_exp2f(x); }
template <int CTRL> __device__ __forceinline__ float dpp(float x) {
  return __builtin_bit_cast(float, __builtin_amdgcn_mov_dpp(__builtin_bit_cast(int, x), CTRL, 0xf, 0xf, true)); }
constexpr int XOR1 = 0xB1, XOR2 = 0x4E, ROR4 = 0x124, ROR8 = 0x128;
__device__ __forceinline__ float x16max(float x) { auto s = __builtin_amdgcn_permlane16_swap(asu(x), asu(x), false, false); return fmaxf(asf(s[0]), asf(s[1])); }
__device__ __forceinline__ float x32max(float x) { auto s = __builtin_amdgcn_permlane32_swap(asu(x), asu(x), false, false); return fmaxf(asf(s[0]), asf(s[1])); }
__device__ __forceinline__ float x16sum(float x) { auto s = __builtin_amdgcn_permlane16_swap(asu(x), asu(x), false, false); return asf(s[0]) + asf(s[1]); }
__device__ __forceinline__ float x32sum(float x) { auto s = __builtin_amdgcn_permlane32_swap(asu(x), asu(x), false, false); return asf(s[0]) + asf(s[1]); }
__device__ __forceinline__ float row16max(float x) { x = fmaxf(x, dpp<XOR1>(x)); x = fmaxf(x, dpp<XOR2>(x)); x = fmaxf(x, dpp<ROR4>(x)); return fmaxf(x, dpp<ROR8>(x)); }
__device__ __forceinline__ float row16sum(float x) { x += dpp<XOR1>(x); x += dpp<XOR2>(x); x += dpp<ROR4>(x); return x + dpp<ROR8>(x); }

template <int IMM> __device__ __forceinline__ void dma(const void* sbase, unsigned voff, unsigned lds) {
  asm volatile("s_mov_b32 m0, %2\n\ts_nop 0\n\tglobal_load_lds_dwordx4 %0, %1 offset:%3 nt" :: "v"(voff), "s"(sbase), "s"(lds), "n"(IMM) : "memory");
}
template <int N> __device__ __forceinline__ void vmwait() { asm volatile("s_waitcnt vmcnt(%0)" :: "n"(N) : "memory"); }
__device__ __forceinline__ void bar() { asm volatile("s_waitcnt lgkmcnt(0)\n\ts_barrier" ::: "memory"); }
__device__ __forceinline__ void lgk0() { asm volatile("s_waitcnt lgkmcnt(0)" ::: "memory"); }
__device__ __forceinline__ void cfence() { asm volatile("" ::: "memory"); }
__device__ __forceinline__ unsigned lds_peek(unsigned a) { return __builtin_amdgcn_readfirstlane(*(volatile LAS unsigned*)a); }
__device__ __forceinline__ void lds_wait_ge(unsigned a, unsigned want) {
  _Pragma("nounroll") for (int it = 0; it < SPIN_CAP * 8; ++it) { if (lds_peek(a) >= want) break; __builtin_amdgcn_s_sleep(1); }
  cfence();
}
typedef __amdgpu_buffer_rsrc_t rsrc_t;
constexpr int SC1 = 16;
__device__ __forceinline__ v4u gld128(rsrc_t r, unsigned off) { return __builtin_amdgcn_raw_buffer_load_b128(r, off, 0, SC1); }
__device__ __forceinline__ v2u gld64(rsrc_t r, unsigned off) { return __builtin_amdgcn_raw_buffer_load_b64(r, off, 0, SC1); }
__device__ __forceinline__ unsigned gld32(rsrc_t r, unsigned off) { return __builtin_amdgcn_raw_buffer_load_b32(r, off, 0, SC1); }
__device__ __forceinline__ void gst128(rsrc_t r, unsigned off, v4u v) { __builtin_amdgcn_raw_buffer_store_b128(v, r, off, 0, SC1); }
__device__ __forceinline__ void gst64(rsrc_t r, unsigned off, v2u v) { __builtin_amdgcn_raw_buffer_store_b64(v, r, off, 0, SC1); }
__device__ __forceinline__ void gst32(rsrc_t r, unsigned off, unsigned v) { __builtin_amdgcn_raw_buffer_store_b32(v, r, off, 0, SC1); }
__device__ __forceinline__ bool tagok(v4u v, unsigned tag) { return v.y == tag && v.w == tag; }

#define STAMP(i) do { unsigned long long t_; asm volatile("s_memrealtime %0\n\ts_waitcnt lgkmcnt(0)" : "=s"(t_) :: "memory"); tsv[i] = (unsigned)t_; } while (0)
#define STAMP_DECL unsigned tsv[16] = {0,0,0,0,0,0,0,0,0,0,0,0,0,0,0,0}
#define STAMP_OUT() do { if (lane == 0) gst128(rs2, WS_TS + (blockIdx.x * 8 + wave) * 16, (v4u){tsv[0], tag, tsv[PROBE], tag}); \
  if (blockIdx.x == 0 && wave == 0) { unsigned mn = 0xffffffffu; float mx = 0.f, sm = 0.f, cnt = 0.f; unsigned ref = tsv[0] - 1000u; \
    for (int i_ = 0; i_ < 32; ++i_) { v4u v_; for (int it = 0; it < SPIN_CAP; ++it) { cfence(); v_ = gld128(rs2, WS_TS + (i_ * 64 + lane) * 16); if (__all(tagok(v_, tag))) break; } \
      mn = min(mn, v_.x - ref); const bool role_ = PROLE ? (i_ < 8) : (i_ >= 8); if (v_.z != 0u && role_) { const float d_ = (float)(v_.z - ref); mx = fmaxf(mx, d_); sm += d_; cnt += 1.f; } } \
    float mnf = -(float)mn; mnf = -x32max(x16max(row16max(mnf))); mx = x32max(x16max(row16max(mx))); sm = x32sum(x16sum(row16sum(sm))); cnt = x32sum(x16sum(row16sum(cnt))); \
    const float X = PZERO ? 0.f : (PSTAT ? sm / cnt : mx) - mnf; const unsigned until = ref + (unsigned)(mnf + 6000.f + 8.f * X); \
    for (int it = 0; it < 100000000; ++it) { unsigned long long t_; asm volatile("s_memrealtime %0\n\ts_waitcnt lgkmcnt(0)" : "=s"(t_) :: "memory"); if ((int)((unsigned)t_ - __builtin_amdgcn_readfirstlane(until)) >= 0) break; } } } while (0)

__device__ __forceinline__ void q_unit(unsigned ximg, int nb, int lane, int mat, int cb, int kh, rsrc_t rs, unsigned tag) {
  const int g = lane >> 4, c16 = lane & 15, q = c16 >> 2, p = lane & 3;
  const int f2_ = ((q >> 1) & 1) | ((g & 1) << 1);
  const unsigned a0 = SCR + (8 * g + q) * 128 + (((2 * nb + (p >> 1)) ^ (f2_ << 1)) << 4) + (p & 1) * 8;
  v4u wf[8], xf[8];
#pragma unroll
  for (int s = 0; s < 8; ++s) { wf[s] = trd2(a0 + s * 4096, a0 + s * 4096 + 512); xf[s] = lds128(ximg + (c16 & 1) * 512 + g * 16 + s * 64); }
  __builtin_amdgcn_sched_barrier(0);
  f4 d = {0.f, 0.f, 0.f, 0.f};
#pragma unroll
  for (int s = 0; s < 8; ++s) d = mfma32(wf[s], xf[s], d);
  const int pr = (c16 >> 1) & 1;
  const float v0 = pr ? d[2] : d[0], v1 = pr ? d[3] : d[1];
  if (c16 < 4) gst128(rs, WS_QG + ((((mat * 2 + kh) * 2 + (c16 & 1)) * 4096) + 64 * cb + 16 * nb + 4 * g + 2 * pr) * 8, (v4u){asu(v0), tag, asu(v1), tag});
}

__device__ __forceinline__ void out_unit(unsigned ou, unsigned oimg, int w, int lane, int mat, int pidx, rsrc_t rs, unsigned tag) {
  const int g = lane >> 4, c16 = lane & 15, q = c16 >> 2, p = lane & 3;
  const int f3 = q | ((g & 1) << 2);
  const v4u ob = lds128(oimg + (c16 & 1) * 64 + g * 16);
  f4 d[4];
  v4u wf[4];
#pragma unroll
  for (int tt = 0; tt < 4; ++tt) {
    const unsigned a0 = ou + (8 * g + q) * 1024 + (((2 * (4 * w + tt) + (p >> 1)) ^ (f3 << 1)) << 4) + (p & 1) * 8;
    wf[tt] = trd2(a0, a0 + 4096);
  }
  __builtin_amdgcn_sched_barrier(0);
#pragma unroll
  for (int tt = 0; tt < 4; ++tt) d[tt] = mfma32(wf[tt], ob, (f4){0.f, 0.f, 0.f, 0.f});
  const int tt = (c16 >> 1) & 3, pr = c16 >> 3;
  const f4 e = tt == 0 ? d[0] : tt == 1 ? d[1] : tt == 2 ? d[2] : d[3];
  const float v0 = pr ? e[2] : e[0], v1 = pr ? e[3] : e[1];
  const int n = 64 * w + 16 * tt + 4 * g + 2 * pr;
  gst128(rs, WS_F + ((((mat * 64 + (n >> 3)) * 128 + pidx) * 16) + (c16 & 1) * 8 + (n & 7)) * 8, (v4u){asu(v0), tag, asu(v1), tag});
}

__device__ __forceinline__ void gather_ctx(unsigned oimg, int w, int lane, int h, int dq, rsrc_t rs, unsigned tag) {
  const int b = lane >> 5, pr = (lane >> 4) & 1, s = lane & 15;
  const unsigned off = WS_CT + (((h * 2 + (s & 1)) * 2 + b) * 128 + 32 * dq + 4 * w + 2 * pr) * 8;
  v4u v;
  _Pragma("nounroll") for (int it = 0; it < SPIN_CAP; ++it) { cfence(); v = gld128(rs, off); if (__all(tagok(v, tag))) break; __builtin_amdgcn_s_sleep(2); }
  const float o0 = asf(v.x) + dpp<XOR1>(asf(v.x)), o1 = asf(v.z) + dpp<XOR1>(asf(v.z));
  if (s == 0) sto32(oimg + b * 64 + (4 * w + 2 * pr) * 2, pk(o0, o1));
}

}

__global__ void __launch_bounds__(512, 2) fused_decode(
    const char* __restrict__ Ax, const char* __restrict__ Kc, const char* __restrict__ Vc, const char* __restrict__ Wq, const char* __restrict__ Wo,
    const char* __restrict__ Bx, const char* __restrict__ Ckv, const char* __restrict__ Bwq, const char* __restrict__ Wuk, const char* __restrict__ Wuv,
    const char* __restrict__ Bwo, unsigned short* __restrict__ out, char* __restrict__ ws) {
  const int bid = blockIdx.x, tid = threadIdx.x, lane = tid & 63, wave = __builtin_amdgcn_readfirstlane(tid >> 6);
  const int g = lane >> 4, c16 = lane & 15;
  STAMP_DECL;
  STAMP(0);
  const rsrc_t rs = __builtin_amdgcn_make_buffer_rsrc(ws, 0, WS_END, 0x00020000);
  const rsrc_t rs2 = __builtin_amdgcn_make_buffer_rsrc(ws, 0, 16u << 20, 0x00020000);
  asm volatile("" :: "s"(Ax), "s"(Kc), "s"(Vc), "s"(Wq), "s"(Wo), "s"(Bx), "s"(Ckv), "s"(Bwq), "s"(Wuk), "s"(Wuv), "s"(Bwo), "s"(out), "s"(ws));
  unsigned tag;
#define LOAD_TAG() asm volatile("s_load_dword %0, %1, 0x0" : "=s"(tag) : "s"(ws) : "memory")
#define WAIT_TAG() do { asm volatile("s_waitcnt lgkmcnt(0)" : "+s"(tag) :: "memory"); tag += 1u; } while (0)
  const int qmat = bid & 1, qkh = (bid >> 1) & 1, qcb = bid >> 2;
  const char* qW = (qmat ? Bwq : Wq) + (size_t)(256 * qkh) * 8192 + qcb * 128;
  const char* qx = (qmat ? Bx : Ax) + 512 * qkh;
  const unsigned vx = (lane >> 5) * 1024 + (lane & 31) * 16;
  unsigned long long pc;
  asm volatile("s_getpc_b64 %0" : "=s"(pc));
  const char* codep = (const char*)((pc & ~255ull) + (unsigned)(((bid >> 3) % CODE_KB) * 1024));
  const unsigned vq0 = (lane >> 3) * 8192 + (((lane & 7) ^ (((lane >> 4) & 1) << 1)) << 4);

  if (bid >= NB_CU) {
    const int iA = bid - NB_CU, pair = iA & 15, split = iA >> 4, b = pair >> 3, kvh = pair & 7;
    const int nblk = split < 8 ? 43 : 42, blk0 = split < 8 ? 43 * split : 344 + 42 * (split - 8);
    const int omat = iA < 64 ? 1 : 0, ounit = iA < 64 ? 64 + iA : iA - 64;
    if (wave == 0) {
      __builtin_amdgcn_s_setprio(3);
      constexpr int W = WIN_A, IST = (W - 1) & 3;
      static_assert(W >= 4 && W <= 32, "window");
      int pub = 0, p = 0;
#define PUBLISH(val) do { const int v_ = (val); if (v_ > pub) { pub = v_; if (lane == 0) *(volatile LAS unsigned*)A_LANDED = (unsigned)v_; } } while (0)
#define PACE(i) do { vmwait<W - 1>(); if ((i) == IST) PUBLISH((4 * p + IST + 1 - W) / 4); } while (0)
      if (lane < 32) sto32(A_MISC + lane * 4, 0u);
      dma<0>(qx, vx, A_XIMG);
      dma<0>(codep, lane * 16, A_DUMMY);
#pragma unroll
      for (int u = 0; u < 4; ++u) dma<0>(qW + (size_t)u * 65536, vq0 ^ ((u & 1) << 6), SCR + u * 1024);
      bar();
#pragma nounroll
      for (p = 1; p < 8; ++p) {
#pragma unroll
        for (int i = 0; i < 4; ++i) dma<0>(qW + (size_t)(4 * p + i) * 65536, vq0 ^ ((i & 1) << 6), SCR + (4 * p + i) * 1024);
      }
      LOAD_TAG();
      STAMP(9);
      const char* kp = Kc + ((size_t)pair * 8192 + blk0 * 16) * 256;
      const char* vp = Vc + ((size_t)pair * 8192 + blk0 * 16) * 256;
      const unsigned vk0 = g * 256 + ((c16 ^ g) << 4), vv0 = g * 256 + ((c16 ^ (g << 1)) << 4);
      dma<0>(kp, vk0, 0); dma<1024>(kp, vk0 ^ 64, 0); dma<2048>(kp, vk0 ^ 128, 0); dma<3072>(kp, vk0 ^ 192, 0);
      dma<0>(vp, vv0, 4096); dma<1024>(vp, vv0 ^ 128, 4096); dma<2048>(vp, vv0, 4096); dma<3072>(vp, vv0 ^ 128, 4096);
      kp += 4096; vp += 4096;
      vmwait<39>();
      STAMP(10);
      vmwait<8>();
      PUBLISH(8);
      STAMP(11);
      p = 10;
      int slot = 1;
      for (int j = 1; j < nblk; ++j) {
        if (j >= NSLOT) {
          const unsigned need = j - NSLOT + 1;
          if (lds_peek(A_DONE + slot * 4) < need) { vmwait<0>(); PUBLISH(p); lds_wait_ge(A_DONE + slot * 4, need); }
        }
        const unsigned sb = slot * 8192;
        PACE(0); dma<0>(kp, vk0, sb); PACE(1); dma<1024>(kp, vk0 ^ 64, sb); PACE(2); dma<2048>(kp, vk0 ^ 128, sb); PACE(3); dma<3072>(kp, vk0 ^ 192, sb);
        ++p;
        PACE(0); dma<0>(vp, vv0, sb + 4096); PACE(1); dma<1024>(vp, vv0 ^ 128, sb + 4096); PACE(2); dma<2048>(vp, vv0, sb + 4096); PACE(3); dma<3072>(vp, vv0 ^ 128, sb + 4096);
        ++p;
        kp += 4096; vp += 4096;
        slot = slot == NSLOT - 1 ? 0 : slot + 1;
      }
      lds_wait_ge(A_QRDY, 1u);
      const char* op = (omat ? Bwo : Wo) + (size_t)ounit * 32768;
#pragma unroll
      for (int k = 0; k < 32; ++k) {
        if (k < W) { vmwait<W - 1>(); if ((k & 3) == IST) PUBLISH(p + (k + 1 - W) / 4); }
        dma<0>(op + k * 1024, (lane ^ (((k & 3) | (((k >> 3) & 1) << 2)) << 1)) << 4, SCR + k * 1024);
      }
      __builtin_amdgcn_s_setprio(0);
      WAIT_TAG();
      STAMP(5);
#undef PACE
#undef PUBLISH
    } else {
      bar();
      LOAD_TAG();
      WAIT_TAG();
      if (wave <= 4) {
        lds_wait_ge(A_LANDED, 8u);
        STAMP(1);
        q_unit(A_XIMG, wave - 1, lane, qmat, qcb, qkh, rs, tag);
        lgk0();
        if (lane == 0) __hip_atomic_fetch_add((LAS unsigned*)(A_MISC + 8), 1u, __ATOMIC_RELAXED, __HIP_MEMORY_SCOPE_WORKGROUP);
        STAMP(2);
      }
      if (wave == 5) {
        lds_wait_ge(A_LANDED, (unsigned)QGO);
        v4u v[8];
        const unsigned off = WS_QG + (b * 4096 + kvh * 512 + 2 * lane) * 8;
        _Pragma("nounroll") for (int it = 0; it < SPIN_CAP; ++it) {
          cfence();
          bool ok = true;
#pragma unroll
          for (int k = 0; k < 8; ++k) { v[k] = gld128(rs, off + (k & 1) * 65536 + (k >> 1) * 1024); ok &= tagok(v[k], tag); }
          if (__all(ok)) break;
          __builtin_amdgcn_s_sleep(8);
        }
#pragma unroll
        for (int hh = 0; hh < 4; ++hh) sto32(A_QIMG + hh * 256 + lane * 4, pk(asf(v[2 * hh].x) + asf(v[2 * hh + 1].x), asf(v[2 * hh].z) + asf(v[2 * hh + 1].z)));
        lds_wait_ge(A_MISC + 8, 4u);
        lgk0();
        if (lane == 0) *(volatile LAS unsigned*)A_QRDY = 1u;
      }
      lds_wait_ge(A_QRDY, 1u);
      STAMP(3);
      v4u qf[4];
#pragma unroll
      for (int s = 0; s < 4; ++s) qf[s] = lds128(A_QIMG + (c16 & 3) * 256 + s * 64 + g * 16);
      const unsigned ko = c16 * 256 + ((g ^ c16) << 4);
      const int vr = 4 * g + (c16 >> 2);
      const unsigned vo = 4096 + vr * 256 + ((((lane & 3) >> 1) ^ ((vr & 7) << 1)) << 4) + (lane & 1) * 8;
      float m = -INFINITY, l = 0.f;
      f4 acc[8];
#pragma unroll
      for (int t = 0; t < 8; ++t) acc[t] = (f4){0.f, 0.f, 0.f, 0.f};
      int slot = wave - 1, j = wave - 1;
      for (; j < nblk; j += NCONS) {
        const unsigned sb = slot * 8192;
        lds_wait_ge(A_LANDED, 9u + 2 * j);
        v4u kf[4];
#pragma unroll
        for (int s = 0; s < 4; ++s) kf[s] = lds128(sb + (ko ^ (s << 6)));
        __builtin_amdgcn_sched_barrier(0);
        f4 s4_ = {0.f, 0.f, 0.f, 0.f};
#pragma unroll
        for (int s = 0; s < 4; ++s) s4_ = mfma32(kf[s], qf[s], s4_);
        float mx = fmaxf(fmaxf(s4_[0], s4_[1]), fmaxf(s4_[2], s4_[3]));
        mx = x32max(x16max(mx));
        const float mn = fmaxf(m, mx), al = ex2((m - mn) * SC), mc = -mn * SC;
        const float p0 = ex2(fmaf(s4_[0], SC, mc)), p1 = ex2(fmaf(s4_[1], SC, mc)), p2 = ex2(fmaf(s4_[2], SC, mc)), p3 = ex2(fmaf(s4_[3], SC, mc));
        l = fmaf(l, al, (p0 + p1) + (p2 + p3));
        m = mn;
        const v2u pb = {pk(p0, p1), pk(p2, p3)};
#pragma unroll
        for (int t = 0; t < 8; ++t) acc[t] *= al;
        lds_wait_ge(A_LANDED, 10u + 2 * j);
        v2u vf[8];
#pragma unroll
        for (int t = 0; t < 8; ++t) vf[t] = trd(sb + (vo ^ (t << 5)));
        lgk0();
        if (lane == 0) *(volatile LAS unsigned*)(A_DONE + slot * 4) = (unsigned)(j + 1);
#pragma unroll
        for (int t = 0; t < 8; ++t) acc[t] = mfma16(vf[t], pb, acc[t]);
        if (j + NCONS < nblk) { slot += NCONS; if (slot >= NSLOT) slot -= NSLOT; }
      }
      STAMP(4);
      l = x32sum(x16sum(l));
      if (c16 < 4) {
#pragma unroll
        for (int t = 0; t < 8; ++t) sto128(slot * 8192 + c16 * 512 + t * 64 + g * 16, __builtin_bit_cast(v4u, acc[t]));
        if (g == 0) sto64(A_ML + ((wave - 1) * 4 + c16) * 8, (v2u){asu(m * SC), asu(l)});
      }
    }
    bar();
    if (wave >= 4) {
      const int hh = wave - 4;
      float mw[NCONS], lw[NCONS];
      f2 ow[NCONS];
#pragma unroll
      for (int c = 0; c < NCONS; ++c) {
        const int jl = c + ((nblk - 1 - c) / NCONS) * NCONS;
        const f2 t = *(const LAS f2*)(A_ML + (c * 4 + hh) * 8);
        mw[c] = t.x; lw[c] = t.y;
        ow[c] = *(const LAS f2*)((jl % NSLOT) * 8192 + hh * 512 + lane * 8);
      }
      float M = mw[0];
#pragma unroll
      for (int c = 1; c < NCONS; ++c) M = fmaxf(M, mw[c]);
      float L = 0.f, o0 = 0.f, o1 = 0.f;
#pragma unroll
      for (int c = 0; c < NCONS; ++c) { const float wt = ex2(mw[c] - M); L = fmaf(wt, lw[c], L); o0 = fmaf(wt, ow[c].x, o0); o1 = fmaf(wt, ow[c].y, o1); }
      const int rec = (b * 32 + kvh * 4 + hh) * NSPLIT_A + split;
      gst128(rs, WS_PAO + rec * 1024 + lane * 16, (v4u){asu(o0), tag, asu(o1), tag});
      if (lane == 0) gst128(rs, WS_PAM + rec * 16, (v4u){asu(M), tag, asu(L), tag});
    }
    STAMP(6);
    const int oh = ounit >> 2, odq = ounit & 3;
    if (omat) {
      gather_ctx(A_OIMG, wave, lane, oh, odq, rs, tag);
    } else {
      const int gb = lane >> 5, pr = (lane >> 4) & 1, s = c16 < NSPLIT_A ? c16 : NSPLIT_A - 1;
      const int rec = (gb * 32 + oh) * NSPLIT_A + s;
      v4u v, ml;
      _Pragma("nounroll") for (int it = 0; it < SPIN_CAP; ++it) {
        cfence();
        v = gld128(rs, WS_PAO + rec * 1024 + (32 * odq + 4 * wave + 2 * pr) * 8);
        ml = gld128(rs, WS_PAM + rec * 16);
        if (__all(tagok(v, tag) && tagok(ml, tag))) break;
        __builtin_amdgcn_s_sleep(1);
      }
      const float M = row16max(asf(ml.x));
      const float wt = c16 < NSPLIT_A ? ex2(asf(ml.x) - M) : 0.f;
      const float L = row16sum(wt * asf(ml.z)), o0 = row16sum(wt * asf(v.x)), o1 = row16sum(wt * asf(v.z));
      const float inv = 1.f / L;
      if (c16 == 0) sto32(A_OIMG + gb * 64 + (4 * wave + 2 * pr) * 2, pk(o0 * inv, o1 * inv));
    }
    vmwait<0>();
    STAMP(7);
    bar();
    out_unit(SCR, A_OIMG, wave, lane, omat, ounit, rs, tag);
    STAMP(8);
    STAMP_OUT();
  } else {
    const int b = bid >> 5, sp = bid & 31, h = bid >> 1, ch = bid & 1, w = wave;
    const char* uk = Wuk + (size_t)(256 * ch + 32 * w) * 8192 + h * 256;
    const char* uv = Wuv + (size_t)(256 * ch + 32 * w) * 8192 + h * 256;
    const char* cp = Ckv + ((size_t)b * 8192 + 256 * sp + 8 * w) * 1024;
    const unsigned vuk = g * 8192 + ((c16 ^ g) << 4), vuv = g * 8192 + ((c16 ^ (g << 1)) << 4);
    const unsigned vc0 = (lane ^ ((w & 1) ? 3 : 0)) << 4, vc1 = (lane ^ ((w & 1) ? 1 : 2)) << 4;
#define LD_UK(u) dma<0>(uk + (size_t)(u) * 32768, vuk ^ (((u) & 3) << 6), SLOT1 + (32 * w + 4 * (u)) * 256)
#define LD_UV(u) dma<0>(uv + (size_t)(u) * 32768, vuv ^ ((((u) >> 1) & 1) << 7), SLOT0 + (32 * w + 4 * (u)) * 256)
#define LD_C(rho, u) dma<((u) & 3) * 1024>(cp + (size_t)(rho) * 65536 + ((u) >> 2) * 4096, (((u) >> 2) ? vc1 : vc0) ^ (((u) & 3) << 6), ((rho) & 1) * 65536 + w * 8192 + ((u) >> 2) * 4096)
    if (w == 7) { dma<0>(qx, vx, B_XIMG); dma<0>(codep, lane * 16, B_DUMMY); }
#pragma unroll
    for (int i = 0; i < 4; ++i) dma<0>(qW + (size_t)(4 * w + i) * 65536, vq0 ^ ((i & 1) << 6), SCR + (4 * w + i) * 1024);
    asm volatile("s_barrier" ::: "memory");
    LD_UK(0); LD_UK(1);
    LOAD_TAG();
    vmwait<2>();
    WAIT_TAG();
    bar();
    STAMP(1);
    LD_UK(2); LD_UK(3);
    if (WB >= 6) { LD_UK(4); LD_UK(5); }
    v4u qg[4];
    if (w >= 4) {
      q_unit(B_XIMG, w - 4, lane, qmat, qcb, qkh, rs, tag);
    } else if (w == 0) {
      const unsigned off = WS_QG + (4 * 4096 + h * 128 + 2 * lane) * 8;
      _Pragma("nounroll") for (int it = 0; it < SPIN_CAP; ++it) {
        cfence();
        bool ok = true;
#pragma unroll
        for (int k = 0; k < 4; ++k) { qg[k] = gld128(rs, off + (k & 1) * 65536 + (k >> 1) * 32768); ok &= tagok(qg[k], tag); }
        if (__all(ok)) break;
        __builtin_amdgcn_s_sleep(2);
      }
    }
    if (WB < 6) { vmwait<WB - 2>(); LD_UK(4); LD_UK(5); }
    vmwait<WB - 2>(); LD_UK(6); LD_UK(7);
    vmwait<WB - 2>(); LD_C(0, 0); LD_C(0, 1);
    vmwait<WB - 2>(); LD_C(0, 2); LD_C(0, 3);
    bar();
    if (w == 0) {
      sto32(B_QBI + lane * 4, pk(asf(qg[0].x) + asf(qg[1].x), asf(qg[0].z) + asf(qg[1].z)));
      sto32(B_QBI + 256 + lane * 4, pk(asf(qg[2].x) + asf(qg[3].x), asf(qg[2].z) + asf(qg[3].z)));
    }
    vmwait<(WB - 2 < 4 ? WB - 2 : 4)>();
    bar();
    STAMP(2);
    LD_C(0, 4); LD_C(0, 5);
    {
      const unsigned a0 = SLOT1 + (32 * w + c16) * 256 + ((g ^ c16) << 4);
      v4u qb[4], u0[4], u1[4];
#pragma unroll
      for (int s = 0; s < 4; ++s) { qb[s] = lds128(B_QBI + (c16 & 1) * 256 + s * 64 + g * 16); u0[s] = lds128(a0 ^ (s << 6)); u1[s] = lds128((a0 ^ (s << 6)) + 4096); }
      __builtin_amdgcn_sched_barrier(0);
      f4 d0 = {0.f, 0.f, 0.f, 0.f}, d1 = d0;
#pragma unroll
      for (int s = 0; s < 4; ++s) { d0 = mfma32(u0[s], qb[s], d0); d1 = mfma32(u1[s], qb[s], d1); }
      const int rb = (c16 >> 1) & 1;
      const f4 e = rb ? d1 : d0;
      if (c16 < 4) sto64(B_QAS + (c16 & 1) * 512 + (32 * w + 16 * rb + 4 * g) * 2, (v2u){pk(e[0], e[1]), pk(e[2], e[3])});
    }
    bar();
    if (w == 0) {
      gst128(rs, WS_QAB + ((lane >> 5) * 32 + h) * 1024 + ch * 512 + (lane & 31) * 16, lds128(B_QAS + lane * 16));
      vmwait<0>();
      if (lane == 0) gst32(rs, WS_FQ + bid * FS, tag);
    }
    STAMP(3);
    vmwait<WB - 2>(); LD_C(0, 6); LD_C(0, 7);
    vmwait<WB - 2>(); LD_C(1, 0); LD_C(1, 1);
    vmwait<WB - 2>(); LD_C(1, 2); LD_C(1, 3);
    if (w == 0) {
      _Pragma("nounroll") for (int it = 0; it < SPIN_CAP; ++it) { cfence(); if (__all(gld32(rs, WS_FQ + lane * FS) == tag)) break; __builtin_amdgcn_s_sleep(2); }
    }
    bar();
    STAMP(4);
    {
      v4u t[4];
#pragma unroll
      for (int i = 0; i < 4; ++i) t[i] = gld128(rs, WS_QAB + (b * 32 + 4 * w + i) * 1024 + lane * 16);
#pragma unroll
      for (int i = 0; i < 4; ++i) sto128(SCR + (4 * w + i) * 1024 + ((lane ^ ((4 * w + i) & 15)) << 4), t[i]);
    }
    LD_C(1, 4); LD_C(1, 5); LD_C(1, 6); LD_C(1, 7);
    bar();
    const int kb4 = w & 3, hg = w >> 2;
    v4u qf[16];
#pragma unroll
    for (int s = 0; s < 16; ++s) qf[s] = lds128(SCR + (16 * hg + c16) * 1024 + ((((4 * s) & 15) ^ g ^ c16) << 4) + (s >> 2) * 256);
    STAMP(5);
    const unsigned fk = ((c16 & 3) << 2) | ((0x78u >> (2 * (c16 >> 2))) & 3u);
    const unsigned ka = (16 * kb4 + c16) * 1024 + ((g ^ fk) << 4);
    const int h2 = lane >> 5, blk = g & 1, qq = c16 >> 2, pp = lane & 3;
    const unsigned ft0 = (qq << 2) | (h2 ? 3u : 0u), ft1 = (qq << 2) | (h2 ? 1u : 2u);
    const unsigned cbase = 8 * w + 2 * blk + (pp >> 1);
    const unsigned va00 = (8 * h2 + qq) * 1024 + ((cbase ^ ft0) << 4) + (pp & 1) * 8, va01 = (8 * h2 + 4 + qq) * 1024 + ((cbase ^ ft1) << 4) + (pp & 1) * 8;
    float m = -INFINITY, l = 0.f;
    f16v acc0, acc1;
#pragma unroll
    for (int i = 0; i < 16; ++i) { acc0[i] = 0.f; acc1[i] = 0.f; }
    vmwait<8>();
#pragma nounroll
    for (int rho = 0; rho < 4; ++rho) {
      const unsigned sb = (rho & 1) * 65536;
      bar();
      f4 s4_ = {0.f, 0.f, 0.f, 0.f};
      {
        v4u kf[16];
#pragma unroll
        for (int s = 0; s < 16; ++s) kf[s] = lds128(sb + (ka ^ ((s & 3) << 6)) + (s >> 2) * 256);
        __builtin_amdgcn_sched_barrier(0);
#pragma unroll
        for (int s = 0; s < 16; ++s) s4_ = mfma32(kf[s], qf[s], s4_);
      }
      float mx = fmaxf(fmaxf(s4_[0], s4_[1]), fmaxf(s4_[2], s4_[3]));
      mx = x32max(x16max(mx));
      if (g == 0) stof(B_MX + (kb4 * 32 + 16 * hg + c16) * 4, mx);
      bar();
      const unsigned ma = B_MX + (16 * hg + c16) * 4;
      const float mn = fmaxf(fmaxf(m, fmaxf(ldsf(ma), ldsf(ma + 128))), fmaxf(ldsf(ma + 256), ldsf(ma + 384)));
      const float al = ex2((m - mn) * SC), mc = -mn * SC;
      const float p0 = ex2(fmaf(s4_[0], SC, mc)), p1 = ex2(fmaf(s4_[1], SC, mc)), p2 = ex2(fmaf(s4_[2], SC, mc)), p3 = ex2(fmaf(s4_[3], SC, mc));
      l = fmaf(l, al, (p0 + p1) + (p2 + p3));
      m = mn;
      sto64(B_P + kb4 * 1024 + (16 * hg + c16) * 32 + g * 8, (v2u){pk(p0, p1), pk(p2, p3)});
      if (kb4 == 0 && g == 0) stof(B_AL + (16 * hg + c16) * 4, al);
      v4u c0[4], c1[4];
#pragma unroll
      for (int ks = 0; ks < 4; ++ks) {
        c0[ks] = trd2(sb + ks * 16384 + va00, sb + ks * 16384 + va01);
        c1[ks] = trd2(sb + ks * 16384 + (va00 ^ 64), sb + ks * 16384 + (va01 ^ 64));
      }
      bar();
      if (rho == 2) { LD_UV(0); LD_UV(1); LD_UV(2); LD_UV(3); LD_UV(4); LD_UV(5); }
      else if (rho != 3) { LD_C(rho + 2, 0); LD_C(rho + 2, 1); LD_C(rho + 2, 2); LD_C(rho + 2, 3); LD_C(rho + 2, 4); LD_C(rho + 2, 5); }
      const float alp = ldsf(B_AL + (lane & 31) * 4);
      v4u pf[4];
#pragma unroll
      for (int ks = 0; ks < 4; ++ks) pf[ks] = lds128(B_P + ks * 1024 + (lane & 31) * 32 + h2 * 16);
      __builtin_amdgcn_sched_barrier(0);
      acc0 *= alp; acc1 *= alp;
#pragma unroll
      for (int ks = 0; ks < 4; ++ks) { acc0 = mfmaL(c0[ks], pf[ks], acc0); acc1 = mfmaL(c1[ks], pf[ks], acc1); }
      if (rho == 0) STAMP(6); else if (rho == 1) STAMP(7); else if (rho == 2) STAMP(8); else STAMP(9);
      if (rho == 2) { LD_UV(6); LD_UV(7); } else if (rho != 3) { LD_C(rho + 2, 6); LD_C(rho + 2, 7); }
      if (rho != 3) vmwait<8>();
    }
    l = x32sum(x16sum(l));
    if (g == 0) stof(B_LS + (kb4 * 32 + 16 * hg + c16) * 4, l);
    {
      const unsigned tb = B_T + w * 4096, n32 = lane & 31;
#pragma unroll
      for (int q4 = 0; q4 < 4; ++q4) {
        sto64(tb + n32 * 128 + ((q4 ^ (n32 & 7)) << 4) + h2 * 8, (v2u){pk(acc0[4 * q4], acc0[4 * q4 + 1]), pk(acc0[4 * q4 + 2], acc0[4 * q4 + 3])});
        sto64(tb + n32 * 128 + (((4 + q4) ^ (n32 & 7)) << 4) + h2 * 8, (v2u){pk(acc1[4 * q4], acc1[4 * q4 + 1]), pk(acc1[4 * q4 + 2], acc1[4 * q4 + 3])});
      }
      lgk0();
      const int hr = lane >> 3, ck = lane & 7;
#pragma unroll
      for (int i = 0; i < 4; ++i)
        gst128(rs, WS_PB + ((b * 32 + sp) * 32 + 8 * i + hr) * 1024 + w * 128 + ck * 16, lds128(tb + (8 * i + hr) * 128 + ((ck ^ hr) << 4)));
    }
    bar();
    if (kb4 == 0 && g == 0) {
      const unsigned la = B_LS + (16 * hg + c16) * 4;
      const float L = (ldsf(la) + ldsf(la + 128)) + (ldsf(la + 256) + ldsf(la + 384));
      gst64(rs, WS_PBM + ((b * 32 + sp) * 32 + 16 * hg + c16) * 8, (v2u){asu(m * SC), asu(L)});
    }
    vmwait<0>();
    bar();
    if (w == 0 && lane == 0) gst32(rs, WS_FP + bid * FS, tag);
    STAMP(10);
    {
      const char* op = Bwo + (size_t)bid * 32768 + w * 4096;
      const unsigned fo = ((w >> 1) & 1) << 3;
      dma<0>(op, (lane ^ fo) << 4, B_OU + w * 4096); dma<1024>(op, (lane ^ (fo | 2u)) << 4, B_OU + w * 4096);
      dma<2048>(op, (lane ^ (fo | 4u)) << 4, B_OU + w * 4096); dma<3072>(op, (lane ^ (fo | 6u)) << 4, B_OU + w * 4096);
    }
    if (w == 0) {
      _Pragma("nounroll") for (int it = 0; it < SPIN_CAP; ++it) { cfence(); if (__all(gld32(rs, WS_FP + lane * FS) == tag)) break; __builtin_amdgcn_s_sleep(4); }
    }
    bar();
    STAMP(11);
    {
      const int gb = lane >> 5, s = lane & 31;
      const v2u ml = gld64(rs, WS_PBM + ((gb * 32 + s) * 32 + h) * 8);
      v4u o[4];
#pragma unroll
      for (int i = 0; i < 4; ++i) o[i] = gld128(rs, WS_PB + ((gb * 32 + 4 * w + i) * 32 + h) * 1024 + ch * 512 + s * 16);
      const float M = x16max(row16max(asf(ml.x)));
      const float wt = ex2(asf(ml.x) - M);
      const float L = x16sum(row16sum(wt * asf(ml.y)));
      const float wn = wt / L;
      float a[8];
#pragma unroll
      for (int k = 0; k < 8; ++k) a[k] = 0.f;
#pragma unroll
      for (int i = 0; i < 4; ++i) {
        const float wi = asf(__builtin_amdgcn_ds_bpermute((gb * 32 + 4 * w + i) * 4, asu(wn)));
#pragma unroll
        for (int k = 0; k < 4; ++k) { a[2 * k] = fmaf(wi, asf(o[i][k] << 16), a[2 * k]); a[2 * k + 1] = fmaf(wi, asf(o[i][k] & 0xffff0000u), a[2 * k + 1]); }
      }
      sto128(B_RED + w * 2048 + lane * 32, (v4u){asu(a[0]), asu(a[1]), asu(a[2]), asu(a[3])});
      sto128(B_RED + w * 2048 + lane * 32 + 16, (v4u){asu(a[4]), asu(a[5]), asu(a[6]), asu(a[7])});
    }
    bar();
    if (tid < 256) {
      float s0 = 0.f, s1 = 0.f;
#pragma unroll
      for (int i = 0; i < 8; ++i) { const f2 t = *(const LAS f2*)(B_RED + i * 2048 + tid * 8); s0 += t.x; s1 += t.y; }
      sto32(B_OL + tid * 4, pk(s0, s1));
    }
    bar();
    {
      const int f3 = qq | ((g & 1) << 2);
      const unsigned a0 = SLOT0 + (8 * g + qq) * 256 + (((2 * w + (pp >> 1)) ^ (f3 << 1)) << 4) + (pp & 1) * 8;
      v4u wf[8], of[8];
#pragma unroll
      for (int s = 0; s < 8; ++s) { wf[s] = trd2(a0 + s * 8192, a0 + s * 8192 + 1024); of[s] = lds128(B_OL + (c16 & 1) * 512 + s * 64 + g * 16); }
      __builtin_amdgcn_sched_barrier(0);
      f4 d = {0.f, 0.f, 0.f, 0.f};
#pragma unroll
      for (int s = 0; s < 8; ++s) d = mfma32(wf[s], of[s], d);
      const int pr = (c16 >> 1) & 1;
      const float v0 = pr ? d[2] : d[0], v1 = pr ? d[3] : d[1];
      if (c16 < 4) gst128(rs, WS_CT + (((h * 2 + ch) * 2 + (c16 & 1)) * 128 + 16 * w + 4 * g + 2 * pr) * 8, (v4u){asu(v0), tag, asu(v1), tag});
    }
    STAMP(12);
    gather_ctx(B_OIMG, w, lane, bid >> 2, bid & 3, rs, tag);
    vmwait<0>();
    bar();
    out_unit(B_OU, B_OIMG, w, lane, 1, bid, rs, tag);
    STAMP(13);
    {
      const unsigned off = WS_PAM + (bid * NSPLIT_A + (bid % NSPLIT_A)) * 16;
      _Pragma("nounroll") for (int it = 0; it < SPIN_CAP; ++it) { cfence(); if (__all(gld64(rs, off).y == tag)) break; __builtin_amdgcn_s_sleep(8); }
    }
    STAMP(14);
    v4u f[4];
    _Pragma("nounroll") for (int it = 0; it < SPIN_CAP; ++it) {
      cfence();
      bool ok = true;
#pragma unroll
      for (int k = 0; k < 4; ++k) { f[k] = gld128(rs, WS_F + ((k >> 1) * 64 + bid) * 16384 + (k & 1) * 8192 + tid * 16); ok &= tagok(f[k], tag); }
      if (__all(ok)) break;
      __builtin_amdgcn_s_sleep(1);
    }
    float r[4] = {asf(f[0].x) + asf(f[1].x), asf(f[0].z) + asf(f[1].z), asf(f[2].x) + asf(f[3].x), asf(f[2].z) + asf(f[3].z)};
#pragma unroll
    for (int k = 0; k < 4; ++k) { r[k] += dpp<ROR8>(r[k]); r[k] = x32sum(x16sum(r[k])); }
    if (lane < 8) { sto64(B_FIN + w * 128 + lane * 8, (v2u){asu(r[0]), asu(r[1])}); sto64(B_FIN + w * 128 + 64 + lane * 8, (v2u){asu(r[2]), asu(r[3])}); }
    bar();
    if (tid < 16) {
      float s0 = 0.f, s1 = 0.f;
#pragma unroll
      for (int i = 0; i < 8; ++i) { const f2 t = *(const LAS f2*)(B_FIN + i * 128 + tid * 8); s0 += t.x; s1 += t.y; }
      const int mat = tid >> 3, ob = (tid >> 2) & 1, pr = tid & 3;
      *(GAS unsigned*)((GAS char*)out + (ob * 1024 + mat * 512 + 8 * bid + 2 * pr) * 2) = pk(s0, s1);
      if (bid == 0 && tid == 0) gst32(rs, WS_E, tag);
    }
    STAMP(15);
    STAMP_OUT();
  }
}

extern "C" void kernel_launch(void* const* d_in, const int* in_sizes, int n_in, void* d_out, int out_size, void* d_ws, size_t ws_size,
                              hipStream_t stream) {
  (void)in_sizes; (void)n_in; (void)out_size; (void)ws_size;
  (void)hipFuncSetAttribute((const void*)fused_decode, hipFuncAttributeMaxDynamicSharedMemorySize, LDS_BYTES);
  fused_decode<<<256, 512, LDS_BYTES, stream>>>(
      (const char*)d_in[0], (const char*)d_in[1], (const char*)d_in[2], (const char*)d_in[3], (const char*)d_in[4], (const char*)d_in[5],
      (const char*)d_in[6], (const char*)d_in[7], (const char*)d_in[8], (const char*)d_in[9], (const char*)d_in[10], (unsigned short*)d_out,
      (char*)d_ws);
}
```

```cpp
#define DBG_TS
#define KST 12
#define PROBE 0
#define PROLE 0
#define PSTAT 0
#define PZERO 1
#include <hip/hip_runtime.h>
#include <stdint.h>
#pragma clang diagnostic ignored "-Wint-to-pointer-cast"

#define GAS __attribute__((address_space(1)))
#define LAS __attribute__((address_space(3)))
typedef unsigned v4u __attribute__((ext_vector_type(4)));
typedef unsigned v2u __attribute__((ext_vector_type(2)));
typedef float f4 __attribute__((ext_vector_type(4)));
typedef float f2 __attribute__((ext_vector_type(2)));
typedef float f16v __attribute__((ext_vector_type(16)));
typedef short s4 __attribute__((ext_vector_type(4)));
typedef __bf16 b8 __attribute__((ext_vector_type(8)));
typedef __bf16 b2 __attribute__((ext_vector_type(2)));

#define WIN_A 24
#define WB 6
#define CODE_KB 20
#define QGO 20

namespace {

constexpr int NB_CU = 64, NSPLIT_A = 12, NSLOT = 15, NCONS = 7;
constexpr float SC = 0.08838834764831845f * 1.4426950408889634f;
constexpr int SPIN_CAP = 1 << 18;

constexpr unsigned FS = 64;
constexpr unsigned WS_E = 0;
constexpr unsigned WS_FQ = 4096;
constexpr unsigned WS_FP = WS_FQ + 64 * FS;
constexpr unsigned WS_QG = 65536;
constexpr unsigned WS_QAB = WS_QG + 262144;
constexpr unsigned WS_PBM = WS_QAB + 65536;
constexpr unsigned WS_PAM = WS_PBM + 16384;
constexpr unsigned WS_CT = WS_PAM + 16384;
constexpr unsigned WS_PAO = WS_CT + 131072;
constexpr unsigned WS_PB = WS_PAO + 786432;
constexpr unsigned WS_F = WS_PB + 2097152;
constexpr unsigned WS_END = WS_F + 2097152;
constexpr unsigned WS_TS = 8u << 20;

constexpr unsigned LDS_BYTES = 163840;
constexpr unsigned SCR = 131072;
constexpr unsigned A_MISC = NSLOT * 8192;
constexpr unsigned A_LANDED = A_MISC, A_QRDY = A_MISC + 4, A_DONE = A_MISC + 64;
constexpr unsigned A_QIMG = A_MISC + 256;
constexpr unsigned A_ML = A_MISC + 1280;
constexpr unsigned A_OIMG = A_MISC + 1536;
constexpr unsigned A_XIMG = A_MISC + 2048, A_DUMMY = A_MISC + 3072;
constexpr unsigned SLOT0 = 0, SLOT1 = 65536;
constexpr unsigned B_QBI = SCR, B_QAS = SCR + 1024;
constexpr unsigned B_MX = SCR, B_AL = SCR + 512, B_LS = SCR + 640, B_P = SCR + 2048;
constexpr unsigned B_RED = SCR + 8192, B_OL = SCR + 24576, B_OIMG = SCR + 26624, B_FIN = SCR + 27648;
constexpr unsigned B_T = SLOT1, B_OU = SLOT1 + 32768;
constexpr unsigned B_XIMG = SLOT0 + 63 * 1024, B_DUMMY = SLOT0 + 62 * 1024;

__device__ __forceinline__ v4u lds128(unsigned a) { return *(const LAS v4u*)a; }
__device__ __forceinline__ void sto128(unsigned a, v4u v) { *(LAS v4u*)a = v; }
__device__ __forceinline__ void sto64(unsigned a, v2u v) { *(LAS v2u*)a = v; }
__device__ __forceinline__ void sto32(unsigned a, unsigned v) { *(LAS unsigned*)a = v; }
__device__ __forceinline__ float ldsf(unsigned a) { return *(const LAS float*)a; }
__device__ __forceinline__ void stof(unsigned a, float v) { *(LAS float*)a = v; }
__device__ __forceinline__ v2u trd(unsigned a) { return __builtin_bit_cast(v2u, __builtin_amdgcn_ds_read_tr16_b64_v4i16((LAS s4*)a)); }
__device__ __forceinline__ v4u trd2(unsigned a0, unsigned a1) { const v2u x = trd(a0), y = trd(a1); return (v4u){x.x, x.y, y.x, y.y}; }
__device__ __forceinline__ f4 mfma32(v4u a, v4u b, f4 c) {
  return __builtin_amdgcn_mfma_f32_16x16x32_bf16(__builtin_bit_cast(b8, a), __builtin_bit_cast(b8, b), c, 0, 0, 0); }
__device__ __forceinline__ f4 mfma16(v2u a, v2u b, f4 c) {
  return __builtin_amdgcn_mfma_f32_16x16x16bf16_1k(__builtin_bit_cast(s4, a), __builtin_bit_cast(s4, b), c, 0, 0, 0); }
__device__ __forceinline__ f16v mfmaL(v4u a, v4u b, f16v c) {
  return __builtin_amdgcn_mfma_f32_32x32x16_bf16(__builtin_bit_cast(b8, a), __builtin_bit_cast(b8, b), c, 0, 0, 0); }
__device__ __forceinline__ unsigned pk(float lo, float hi) { return __builtin_bit_cast(unsigned, __builtin_convertvector((f2){lo, hi}, b2)); }
__device__ __forceinline__ float asf(unsigned u) { return __builtin_bit_cast(float, u); }
__device__ __forceinline__ unsigned asu(float f) { return __builtin_bit_cast(unsigned, f); }
__device__ __forceinline__ float ex2(float x) { return __builtin_amdgcn_exp2f(x); }
template <int CTRL> __device__ __forceinline__ float dpp(float x) {
  return __builtin_bit_cast(float, __builtin_amdgcn_mov_dpp(__builtin_bit_cast(int, x), CTRL, 0xf, 0xf, true)); }
constexpr int XOR1 = 0xB1, XOR2 = 0x4E, ROR4 = 0x124, ROR8 = 0x128;
__device__ __forceinline__ float x16max(float x) { auto s = __builtin_amdgcn_permlane16_swap(asu(x), asu(x), false, false); return fmaxf(asf(s[0]), asf(s[1])); }
__device__ __forceinline__ float x32max(float x) { auto s = __builtin_amdgcn_permlane32_swap(asu(x), asu(x), false, false); return fmaxf(asf(s[0]), asf(s[1])); }
__device__ __forceinline__ float x16sum(float x) { auto s = __builtin_amdgcn_permlane16_swap(asu(x), asu(x), false, false); return asf(s[0]) + asf(s[1]); }
__device__ __forceinline__ float x32sum(float x) { auto s = __builtin_amdgcn_permlane32_swap(asu(x), asu(x), false, false); return asf(s[0]) + asf(s[1]); }
__device__ __forceinline__ float row16max(float x) { x = fmaxf(x, dpp<XOR1>(x)); x = fmaxf(x, dpp<XOR2>(x)); x = fmaxf(x, dpp<ROR4>(x)); return fmaxf(x, dpp<ROR8>(x)); }
__device__ __forceinline__ float row16sum(float x) { x += dpp<XOR1>(x); x += dpp<XOR2>(x); x += dpp<ROR4>(x); return x + dpp<ROR8>(x); }

template <int IMM> __device__ __forceinline__ void dma(const void* sbase, unsigned voff, unsigned lds) {
  asm volatile("s_mov_b32 m0, %2\n\ts_nop 0\n\tglobal_load_lds_dwordx4 %0, %1 offset:%3 nt" :: "v"(voff), "s"(sbase), "s"(lds), "n"(IMM) : "memory");
}
template <int N> __device__ __forceinline__ void vmwait() { asm volatile("s_waitcnt vmcnt(%0)" :: "n"(N) : "memory"); }
__device__ __forceinline__ void bar() { asm volatile("s_waitcnt lgkmcnt(0)\n\ts_barrier" ::: "memory"); }
__device__ __forceinline__ void lgk0() { asm volatile("s_waitcnt lgkmcnt(0)" ::: "memory"); }
__device__ __forceinline__ void cfence() { asm volatile("" ::: "memory"); }
__device__ __forceinline__ unsigned lds_peek(unsigned a) { return __builtin_amdgcn_readfirstlane(*(volatile LAS unsigned*)a); }
__device__ __forceinline__ void lds_wait_ge(unsigned a, unsigned want) {
  _Pragma("nounroll") for (int it = 0; it < SPIN_CAP * 8; ++it) { if (lds_peek(a) >= want) break; __builtin_amdgcn_s_sleep(1); }
  cfence();
}
typedef __amdgpu_buffer_rsrc_t rsrc_t;
constexpr int SC1 = 16;
__device__ __forceinline__ v4u gld128(rsrc_t r, unsigned off) { return __builtin_amdgcn_raw_buffer_load_b128(r, off, 0, SC1); }
__device__ __forceinline__ v2u gld64(rsrc_t r, unsigned off) { return __builtin_amdgcn_raw_buffer_load_b64(r, off, 0, SC1); }
__device__ __forceinline__ unsigned gld32(rsrc_t r, unsigned off) { return __builtin_amdgcn_raw_buffer_load_b32(r, off, 0, SC1); }
__device__ __forceinline__ void gst128(rsrc_t r, unsigned off, v4u v) { __builtin_amdgcn_raw_buffer_store_b128(v, r, off, 0, SC1); }
__device__ __forceinline__ void gst64(rsrc_t r, unsigned off, v2u v) { __builtin_amdgcn_raw_buffer_store_b64(v, r, off, 0, SC1); }
__device__ __forceinline__ void gst32(rsrc_t r, unsigned off, unsigned v) { __builtin_amdgcn_raw_buffer_store_b32(v, r, off, 0, SC1); }
__device__ __forceinline__ bool tagok(v4u v, unsigned tag) { return v.y == tag && v.w == tag; }

#define STAMP(i) do { unsigned long long t_; asm volatile("s_memrealtime %0\n\ts_waitcnt lgkmcnt(0)" : "=s"(t_) :: "memory"); tsv[i] = (unsigned)t_; } while (0)
#define STAMP_DECL unsigned tsv[16] = {0,0,0,0,0,0,0,0,0,0,0,0,0,0,0,0}
#define STAMP_OUT() do { if (lane == 0) gst128(rs2, WS_TS + (blockIdx.x * 8 + wave) * 16, (v4u){tsv[0], tag, tsv[PROBE], tag}); \
  if (blockIdx.x == 0 && wave == 0) { unsigned mn = 0xffffffffu; float mx = 0.f, sm = 0.f, cnt = 0.f; unsigned ref = tsv[0] - 1000u; \
    for (int i_ = 0; i_ < 32; ++i_) { v4u v_; for (int it = 0; it < SPIN_CAP; ++it) { cfence(); v_ = gld128(rs2, WS_TS + (i_ * 64 + lane) * 16); if (__all(tagok(v_, tag))) break; } \
      mn = min(mn, v_.x - ref); const bool role_ = PROLE ? (i_ < 8) : (i_ >= 8); if (v_.z != 0u && role_) { const float d_ = (float)(v_.z - ref); mx = fmaxf(mx, d_); sm += d_; cnt += 1.f; } } \
    float mnf = -(float)mn; mnf = -x32max(x16max(row16max(mnf))); mx = x32max(x16max(row16max(mx))); sm = x32sum(x16sum(row16sum(sm))); cnt = x32sum(x16sum(row16sum(cnt))); \
    const float X = PZERO ? 0.f : (PSTAT ? sm / cnt : mx) - mnf; const unsigned until = ref + (unsigned)(mnf + 6000.f + 8.f * X); \
    for (int it = 0; it < 100000000; ++it) { unsigned long long t_; asm volatile("s_memrealtime %0\n\ts_waitcnt lgkmcnt(0)" : "=s"(t_) :: "memory"); if ((int)((unsigned)t_ - __builtin_amdgcn_readfirstlane(until)) >= 0) break; } } } while (0)

__device__ __forceinline__ void q_unit(unsigned ximg, int nb, int lane, int mat, int cb, int kh, rsrc_t rs, unsigned tag) {
  const int g = lane >> 4, c16 = lane & 15, q = c16 >> 2, p = lane & 3;
  const int f2_ = ((q >> 1) & 1) | ((g & 1) << 1);
  const unsigned a0 = SCR + (8 * g + q) * 128 + (((2 * nb + (p >> 1)) ^ (f2_ << 1)) << 4) + (p & 1) * 8;
  v4u wf[8], xf[8];
#pragma unroll
  for (int s = 0; s < 8; ++s) { wf[s] = trd2(a0 + s * 4096, a0 + s * 4096 + 512); xf[s] = lds128(ximg + (c16 & 1) * 512 + g * 16 + s * 64); }
  __builtin_amdgcn_sched_barrier(0);
  f4 d = {0.f, 0.f, 0.f, 0.f};
#pragma unroll
  for (int s = 0; s < 8; ++s) d = mfma32(wf[s], xf[s], d);
  const int pr = (c16 >> 1) & 1;
  const float v0 = pr ? d[2] : d[0], v1 = pr ? d[3] : d[1];
  if (c16 < 4) gst128(rs, WS_QG + ((((mat * 2 + kh) * 2 + (c16 & 1)) * 4096) + 64 * cb + 16 * nb + 4 * g + 2 * pr) * 8, (v4u){asu(v0), tag, asu(v1), tag});
}

__device__ __forceinline__ void out_unit(unsigned ou, unsigned oimg, int w, int lane, int mat, int pidx, rsrc_t rs, unsigned tag) {
  const int g = lane >> 4, c16 = lane & 15, q = c16 >> 2, p = lane & 3;
  const int f3 = q | ((g & 1) << 2);
  const v4u ob = lds128(oimg + (c16 & 1) * 64 + g * 16);
  f4 d[4];
  v4u wf[4];
#pragma unroll
  for (int tt = 0; tt < 4; ++tt) {
    const unsigned a0 = ou + (8 * g + q) * 1024 + (((2 * (4 * w + tt) + (p >> 1)) ^ (f3 << 1)) << 4) + (p & 1) * 8;
    wf[tt] = trd2(a0, a0 + 4096);
  }
  __builtin_amdgcn_sched_barrier(0);
#pragma unroll
  for (int tt = 0; tt < 4; ++tt) d[tt] = mfma32(wf[tt], ob, (f4){0.f, 0.f, 0.f, 0.f});
  const int tt = (c16 >> 1) & 3, pr = c16 >> 3;
  const f4 e = tt == 0 ? d[0] : tt == 1 ? d[1] : tt == 2 ? d[2] : d[3];
  const float v0 = pr ? e[2] : e[0], v1 = pr ? e[3] : e[1];
  const int n = 64 * w + 16 * tt + 4 * g + 2 * pr;
  gst128(rs, WS_F + ((((mat * 64 + (n >> 3)) * 128 + pidx) * 16) + (c16 & 1) * 8 + (n & 7)) * 8, (v4u){asu(v0), tag, asu(v1), tag});
}

__device__ __forceinline__ void gather_ctx(unsigned oimg, int w, int lane, int h, int dq, rsrc_t rs, unsigned tag) {
  const int b = lane >> 5, pr = (lane >> 4) & 1, s = lane & 15;
  const unsigned off = WS_CT + (((h * 2 + (s & 1)) * 2 + b) * 128 + 32 * dq + 4 * w + 2 * pr) * 8;
  v4u v;
  _Pragma("nounroll") for (int it = 0; it < SPIN_CAP; ++it) { cfence(); v = gld128(rs, off); if (__all(tagok(v, tag))) break; __builtin_amdgcn_s_sleep(2); }
  const float o0 = asf(v.x) + dpp<XOR1>(asf(v.x)), o1 = asf(v.z) + dpp<XOR1>(asf(v.z));
  if (s == 0) sto32(oimg + b * 64 + (4 * w + 2 * pr) * 2, pk(o0, o1));
}

}

__global__ void __launch_bounds__(512, 2) fused_decode(
    const char* Ax_, const char* Kc_, const char* Vc_, const char* Wq_, const char* Wo_,
    const char* Bx_, const char* Ckv_, const char* Bwq_, const char* Wuk_, const char* Wuv_,
    const char* Bwo_, unsigned short* out_, char* ws_) {
  STAMP_DECL;
  STAMP(0);
  typedef unsigned long long u64x8 __attribute__((ext_vector_type(8)));
  typedef unsigned long long u64x4 __attribute__((ext_vector_type(4)));
  u64x8 ka0; u64x4 ka1; unsigned long long ka2;
  asm volatile("s_load_dwordx16 %0, %3, 0x0\n\ts_load_dwordx8 %1, %3, 0x40\n\ts_load_dwordx2 %2, %3, 0x60\n\ts_waitcnt lgkmcnt(0)" : "=&s"(ka0), "=&s"(ka1), "=&s"(ka2) : "s"(__builtin_amdgcn_kernarg_segment_ptr()) : "memory");
  STAMP(KST);
  const char* Ax = (const char*)ka0[0]; const char* Kc = (const char*)ka0[1]; const char* Vc = (const char*)ka0[2]; const char* Wq = (const char*)ka0[3]; const char* Wo = (const char*)ka0[4];
  const char* Bx = (const char*)ka0[5]; const char* Ckv = (const char*)ka0[6]; const char* Bwq = (const char*)ka0[7]; const char* Wuk = (const char*)ka1[0]; const char* Wuv = (const char*)ka1[1];
  const char* Bwo = (const char*)ka1[2]; unsigned short* out = (unsigned short*)ka1[3]; char* ws = (char*)ka2;
  const int bid = blockIdx.x, tid = threadIdx.x, lane = tid & 63, wave = __builtin_amdgcn_readfirstlane(tid >> 6);
  const int g = lane >> 4, c16 = lane & 15;
  const rsrc_t rs = __builtin_amdgcn_make_buffer_rsrc(ws, 0, WS_END, 0x00020000);
  const rsrc_t rs2 = __builtin_amdgcn_make_buffer_rsrc(ws, 0, 16u << 20, 0x00020000);
  unsigned tag;
#define LOAD_TAG() asm volatile("s_load_dword %0, %1, 0x0" : "=s"(tag) : "s"(ws) : "memory")
#define WAIT_TAG() do { asm volatile("s_waitcnt lgkmcnt(0)" : "+s"(tag) :: "memory"); tag += 1u; } while (0)
  const int qmat = bid & 1, qkh = (bid >> 1) & 1, qcb = bid >> 2;
  const char* qW = (qmat ? Bwq : Wq) + (size_t)(256 * qkh) * 8192 + qcb * 128;
  const char* qx = (qmat ? Bx : Ax) + 512 * qkh;
  const unsigned vx = (lane >> 5) * 1024 + (lane & 31) * 16;
  unsigned long long pc;
  asm volatile("s_getpc_b64 %0" : "=s"(pc));
  const char* codep = (const char*)((pc & ~255ull) + (unsigned)(((bid >> 3) % CODE_KB) * 1024));
  const unsigned vq0 = (lane >> 3) * 8192 + (((lane & 7) ^ (((lane >> 4) & 1) << 1)) << 4);

  if (bid >= NB_CU) {
    const int iA = bid - NB_CU, pair = iA & 15, split = iA >> 4, b = pair >> 3, kvh = pair & 7;
    const int nblk = split < 8 ? 43 : 42, blk0 = split < 8 ? 43 * split : 344 + 42 * (split - 8);
    const int omat = iA < 64 ? 1 : 0, ounit = iA < 64 ? 64 + iA : iA - 64;
    if (wave == 0) {
      __builtin_amdgcn_s_setprio(3);
      constexpr int W = WIN_A, IST = (W - 1) & 3;
      static_assert(W >= 4 && W <= 32, "window");
      int pub = 0, p = 0;
#define PUBLISH(val) do { const int v_ = (val); if (v_ > pub) { pub = v_; if (lane == 0) *(volatile LAS unsigned*)A_LANDED = (unsigned)v_; } } while (0)
#define PACE(i) do { vmwait<W - 1>(); if ((i) == IST) PUBLISH((4 * p + IST + 1 - W) / 4); } while (0)
      if (lane < 32) sto32(A_MISC + lane * 4, 0u);
      dma<0>(qx, vx, A_XIMG);
      dma<0>(codep, lane * 16, A_DUMMY);
#pragma unroll
      for (int u = 0; u < 4; ++u) dma<0>(qW + (size_t)u * 65536, vq0 ^ ((u & 1) << 6), SCR + u * 1024);
      bar();
#pragma nounroll
      for (p = 1; p < 8; ++p) {
#pragma unroll
        for (int i = 0; i < 4; ++i) dma<0>(qW + (size_t)(4 * p + i) * 65536, vq0 ^ ((i & 1) << 6), SCR + (4 * p + i) * 1024);
      }
      LOAD_TAG();
      STAMP(9);
      const char* kp = Kc + ((size_t)pair * 8192 + blk0 * 16) * 256;
      const char* vp = Vc + ((size_t)pair * 8192 + blk0 * 16) * 256;
      const unsigned vk0 = g * 256 + ((c16 ^ g) << 4), vv0 = g * 256 + ((c16 ^ (g << 1)) << 4);
      dma<0>(kp, vk0, 0); dma<1024>(kp, vk0 ^ 64, 0); dma<2048>(kp, vk0 ^ 128, 0); dma<3072>(kp, vk0 ^ 192, 0);
      dma<0>(vp, vv0, 4096); dma<1024>(vp, vv0 ^ 128, 4096); dma<2048>(vp, vv0, 4096); dma<3072>(vp, vv0 ^ 128, 4096);
      kp += 4096; vp += 4096;
      vmwait<39>();
      STAMP(10);
      vmwait<8>();
      PUBLISH(8);
      STAMP(11);
      p = 10;
      int slot = 1;
      for (int j = 1; j < nblk; ++j) {
        if (j >= NSLOT) {
          const unsigned need = j - NSLOT + 1;
          if (lds_peek(A_DONE + slot * 4) < need) { vmwait<0>(); PUBLISH(p); lds_wait_ge(A_DONE + slot * 4, need); }
        }
        const unsigned sb = slot * 8192;
        PACE(0); dma<0>(kp, vk0, sb); PACE(1); dma<1024>(kp, vk0 ^ 64, sb); PACE(2); dma<2048>(kp, vk0 ^ 128, sb); PACE(3); dma<3072>(kp, vk0 ^ 192, sb);
        ++p;
        PACE(0); dma<0>(vp, vv0, sb + 4096); PACE(1); dma<1024>(vp, vv0 ^ 128, sb + 4096); PACE(2); dma<2048>(vp, vv0, sb + 4096); PACE(3); dma<3072>(vp, vv0 ^ 128, sb + 4096);
        ++p;
        kp += 4096; vp += 4096;
        slot = slot == NSLOT - 1 ? 0 : slot + 1;
      }
      lds_wait_ge(A_QRDY, 1u);
      const char* op = (omat ? Bwo : Wo) + (size_t)ounit * 32768;
#pragma unroll
      for (int k = 0; k < 32; ++k) {
        if (k < W) { vmwait<W - 1>(); if ((k & 3) == IST) PUBLISH(p + (k + 1 - W) / 4); }
        dma<0>(op + k * 1024, (lane ^ (((k & 3) | (((k >> 3) & 1) << 2)) << 1)) << 4, SCR + k * 1024);
      }
      __builtin_amdgcn_s_setprio(0);
      WAIT_TAG();
      STAMP(5);
#undef PACE
#undef PUBLISH
    } else {
      bar();
      LOAD_TAG();
      WAIT_TAG();
      if (wave <= 4) {
        lds_wait_ge(A_LANDED, 8u);
        STAMP(1);
        q_unit(A_XIMG, wave - 1, lane, qmat, qcb, qkh, rs, tag);
        lgk0();
        if (lane == 0) __hip_atomic_fetch_add((LAS unsigned*)(A_MISC + 8), 1u, __ATOMIC_RELAXED, __HIP_MEMORY_SCOPE_WORKGROUP);
        STAMP(2);
      }
      if (wave == 5) {
        lds_wait_ge(A_LANDED, (unsigned)QGO);
        v4u v[8];
        const unsigned off = WS_QG + (b * 4096 + kvh * 512 + 2 * lane) * 8;
        _Pragma("nounroll") for (int it = 0; it < SPIN_CAP; ++it) {
          cfence();
          bool ok = true;
#pragma unroll
          for (int k = 0; k < 8; ++k) { v[k] = gld128(rs, off + (k & 1) * 65536 + (k >> 1) * 1024); ok &= tagok(v[k], tag); }
          if (__all(ok)) break;
          __builtin_amdgcn_s_sleep(8);
        }
#pragma unroll
        for (int hh = 0; hh < 4; ++hh) sto32(A_QIMG + hh * 256 + lane * 4, pk(asf(v[2 * hh].x) + asf(v[2 * hh + 1].x), asf(v[2 * hh].z) + asf(v[2 * hh + 1].z)));
        lds_wait_ge(A_MISC + 8, 4u);
        lgk0();
        if (lane == 0) *(volatile LAS unsigned*)A_QRDY = 1u;
      }
      lds_wait_ge(A_QRDY, 1u);
      STAMP(3);
      v4u qf[4];
#pragma unroll
      for (int s = 0; s < 4; ++s) qf[s] = lds128(A_QIMG + (c16 & 3) * 256 + s * 64 + g * 16);
      const unsigned ko = c16 * 256 + ((g ^ c16) << 4);
      const int vr = 4 * g + (c16 >> 2);
      const unsigned vo = 4096 + vr * 256 + ((((lane & 3) >> 1) ^ ((vr & 7) << 1)) << 4) + (lane & 1) * 8;
      float m = -INFINITY, l = 0.f;
      f4 acc[8];
#pragma unroll
      for (int t = 0; t < 8; ++t) acc[t] = (f4){0.f, 0.f, 0.f, 0.f};
      int slot = wave - 1, j = wave - 1;
      for (; j < nblk; j += NCONS) {
        const unsigned sb = slot * 8192;
        lds_wait_ge(A_LANDED, 9u + 2 * j);
        v4u kf[4];
#pragma unroll
        for (int s = 0; s < 4; ++s) kf[s] = lds128(sb + (ko ^ (s << 6)));
        __builtin_amdgcn_sched_barrier(0);
        f4 s4_ = {0.f, 0.f, 0.f, 0.f};
#pragma unroll
        for (int s = 0; s < 4; ++s) s4_ = mfma32(kf[s], qf[s], s4_);
        float mx = fmaxf(fmaxf(s4_[0], s4_[1]), fmaxf(s4_[2], s4_[3]));
        mx = x32max(x16max(mx));
        const float mn = fmaxf(m, mx), al = ex2((m - mn) * SC), mc = -mn * SC;
        const float p0 = ex2(fmaf(s4_[0], SC, mc)), p1 = ex2(fmaf(s4_[1], SC, mc)), p2 = ex2(fmaf(s4_[2], SC, mc)), p3 = ex2(fmaf(s4_[3], SC, mc));
        l = fmaf(l, al, (p0 + p1) + (p2 + p3));
        m = mn;
        const v2u pb = {pk(p0, p1), pk(p2, p3)};
#pragma unroll
        for (int t = 0; t < 8; ++t) acc[t] *= al;
        lds_wait_ge(A_LANDED, 10u + 2 * j);
        v2u vf[8];
#pragma unroll
        for (int t = 0; t < 8; ++t) vf[t] = trd(sb + (vo ^ (t << 5)));
        lgk0();
        if (lane == 0) *(volatile LAS unsigned*)(A_DONE + slot * 4) = (unsigned)(j + 1);
#pragma unroll
        for (int t = 0; t < 8; ++t) acc[t] = mfma16(vf[t], pb, acc[t]);
        if (j + NCONS < nblk) { slot += NCONS; if (slot >= NSLOT) slot -= NSLOT; }
      }
      STAMP(4);
      l = x32sum(x16sum(l));
      if (c16 < 4) {
#pragma unroll
        for (int t = 0; t < 8; ++t) sto128(slot * 8192 + c16 * 512 + t * 64 + g * 16, __builtin_bit_cast(v4u, acc[t]));
        if (g == 0) sto64(A_ML + ((wave - 1) * 4 + c16) * 8, (v2u){asu(m * SC), asu(l)});
      }
    }
    bar();
    if (wave >= 4) {
      const int hh = wave - 4;
      float mw[NCONS], lw[NCONS];
      f2 ow[NCONS];
#pragma unroll
      for (int c = 0; c < NCONS; ++c) {
        const int jl = c + ((nblk - 1 - c) / NCONS) * NCONS;
        const f2 t = *(const LAS f2*)(A_ML + (c * 4 + hh) * 8);
        mw[c] = t.x; lw[c] = t.y;
        ow[c] = *(const LAS f2*)((jl % NSLOT) * 8192 + hh * 512 + lane * 8);
      }
      float M = mw[0];
#pragma unroll
      for (int c = 1; c < NCONS; ++c) M = fmaxf(M, mw[c]);
      float L = 0.f, o0 = 0.f, o1 = 0.f;
#pragma unroll
      for (int c = 0; c < NCONS; ++c) { const float wt = ex2(mw[c] - M); L = fmaf(wt, lw[c], L); o0 = fmaf(wt, ow[c].x, o0); o1 = fmaf(wt, ow[c].y, o1); }
      const int rec = (b * 32 + kvh * 4 + hh) * NSPLIT_A + split;
      gst128(rs, WS_PAO + rec * 1024 + lane * 16, (v4u){asu(o0), tag, asu(o1), tag});
      if (lane == 0) gst128(rs, WS_PAM + rec * 16, (v4u){asu(M), tag, asu(L), tag});
    }
    STAMP(6);
    const int oh = ounit >> 2, odq = ounit & 3;
    if (omat) {
      gather_ctx(A_OIMG, wave, lane, oh, odq, rs, tag);
    } else {
      const int gb = lane >> 5, pr = (lane >> 4) & 1, s = c16 < NSPLIT_A ? c16 : NSPLIT_A - 1;
      const int rec = (gb * 32 + oh) * NSPLIT_A + s;
      v4u v, ml;
      _Pragma("nounroll") for (int it = 0; it < SPIN_CAP; ++it) {
        cfence();
        v = gld128(rs, WS_PAO + rec * 1024 + (32 * odq + 4 * wave + 2 * pr) * 8);
        ml = gld128(rs, WS_PAM + rec * 16);
        if (__all(tagok(v, tag) && tagok(ml, tag))) break;
        __builtin_amdgcn_s_sleep(1);
      }
      const float M = row16max(asf(ml.x));
      const float wt = c16 < NSPLIT_A ? ex2(asf(ml.x) - M) : 0.f;
      const float L = row16sum(wt * asf(ml.z)), o0 = row16sum(wt * asf(v.x)), o1 = row16sum(wt * asf(v.z));
      const float inv = 1.f / L;
      if (c16 == 0) sto32(A_OIMG + gb * 64 + (4 * wave + 2 * pr) * 2, pk(o0 * inv, o1 * inv));
    }
    vmwait<0>();
    STAMP(7);
    bar();
    out_unit(SCR, A_OIMG, wave, lane, omat, ounit, rs, tag);
    STAMP(8);
    STAMP_OUT();
  } else {
    const int b = bid >> 5, sp = bid & 31, h = bid >> 1, ch = bid & 1, w = wave;
    const char* uk = Wuk + (size_t)(256 * ch + 32 * w) * 8192 + h * 256;
    const char* uv = Wuv + (size_t)(256 * ch + 32 * w) * 8192 + h * 256;
    const char* cp = Ckv + ((size_t)b * 8192 + 256 * sp + 8 * w) * 1024;
    const unsigned vuk = g * 8192 + ((c16 ^ g) << 4), vuv = g * 8192 + ((c16 ^ (g << 1)) << 4);
    const unsigned vc0 = (lane ^ ((w & 1) ? 3 : 0)) << 4, vc1 = (lane ^ ((w & 1) ? 1 : 2)) << 4;
#define LD_UK(u) dma<0>(uk + (size_t)(u) * 32768, vuk ^ (((u) & 3) << 6), SLOT1 + (32 * w + 4 * (u)) * 256)
#define LD_UV(u) dma<0>(uv + (size_t)(u) * 32768, vuv ^ ((((u) >> 1) & 1) << 7), SLOT0 + (32 * w + 4 * (u)) * 256)
#define LD_C(rho, u) dma<((u) & 3) * 1024>(cp + (size_t)(rho) * 65536 + ((u) >> 2) * 4096, (((u) >> 2) ? vc1 : vc0) ^ (((u) & 3) << 6), ((rho) & 1) * 65536 + w * 8192 + ((u) >> 2) * 4096)
    if (w == 7) { dma<0>(qx, vx, B_XIMG); dma<0>(codep, lane * 16, B_DUMMY); }
#pragma unroll
    for (int i = 0; i < 4; ++i) dma<0>(qW + (size_t)(4 * w + i) * 65536, vq0 ^ ((i & 1) << 6), SCR + (4 * w + i) * 1024);
    asm volatile("s_barrier" ::: "memory");
    LD_UK(0); LD_UK(1);
    LOAD_TAG();
    vmwait<2>();
    WAIT_TAG();
    bar();
    STAMP(1);
    LD_UK(2); LD_UK(3);
    if (WB >= 6) { LD_UK(4); LD_UK(5); }
    v4u qg[4];
    if (w >= 4) {
      q_unit(B_XIMG, w - 4, lane, qmat, qcb, qkh, rs, tag);
    } else if (w == 0) {
      const unsigned off = WS_QG + (4 * 4096 + h * 128 + 2 * lane) * 8;
      _Pragma("nounroll") for (int it = 0; it < SPIN_CAP; ++it) {
        cfence();
        bool ok = true;
#pragma unroll
        for (int k = 0; k < 4; ++k) { qg[k] = gld128(rs, off + (k & 1) * 65536 + (k >> 1) * 32768); ok &= tagok(qg[k], tag); }
        if (__all(ok)) break;
        __builtin_amdgcn_s_sleep(2);
      }
    }
    if (WB < 6) { vmwait<WB - 2>(); LD_UK(4); LD_UK(5); }
    vmwait<WB - 2>(); LD_UK(6); LD_UK(7);
    vmwait<WB - 2>(); LD_C(0, 0); LD_C(0, 1);
    vmwait<WB - 2>(); LD_C(0, 2); LD_C(0, 3);
    bar();
    if (w == 0) {
      sto32(B_QBI + lane * 4, pk(asf(qg[0].x) + asf(qg[1].x), asf(qg[0].z) + asf(qg[1].z)));
      sto32(B_QBI + 256 + lane * 4, pk(asf(qg[2].x) + asf(qg[3].x), asf(qg[2].z) + asf(qg[3].z)));
    }
    vmwait<(WB - 2 < 4 ? WB - 2 : 4)>();
    bar();
    STAMP(2);
    LD_C(0, 4); LD_C(0, 5);
    {
      const unsigned a0 = SLOT1 + (32 * w + c16) * 256 + ((g ^ c16) << 4);
      v4u qb[4], u0[4], u1[4];
#pragma unroll
      for (int s = 0; s < 4; ++s) { qb[s] = lds128(B_QBI + (c16 & 1) * 256 + s * 64 + g * 16); u0[s] = lds128(a0 ^ (s << 6)); u1[s] = lds128((a0 ^ (s << 6)) + 4096); }
      __builtin_amdgcn_sched_barrier(0);
      f4 d0 = {0.f, 0.f, 0.f, 0.f}, d1 = d0;
#pragma unroll
      for (int s = 0; s < 4; ++s) { d0 = mfma32(u0[s], qb[s], d0); d1 = mfma32(u1[s], qb[s], d1); }
      const int rb = (c16 >> 1) & 1;
      const f4 e = rb ? d1 : d0;
      if (c16 < 4) sto64(B_QAS + (c16 & 1) * 512 + (32 * w + 16 * rb + 4 * g) * 2, (v2u){pk(e[0], e[1]), pk(e[2], e[3])});
    }
    bar();
    if (w == 0) {
      gst128(rs, WS_QAB + ((lane >> 5) * 32 + h) * 1024 + ch * 512 + (lane & 31) * 16, lds128(B_QAS + lane * 16));
      vmwait<0>();
      if (lane == 0) gst32(rs, WS_FQ + bid * FS, tag);
    }
    STAMP(3);
    vmwait<WB - 2>(); LD_C(0, 6); LD_C(0, 7);
    vmwait<WB - 2>(); LD_C(1, 0); LD_C(1, 1);
    vmwait<WB - 2>(); LD_C(1, 2); LD_C(1, 3);
    if (w == 0) {
      _Pragma("nounroll") for (int it = 0; it < SPIN_CAP; ++it) { cfence(); if (__all(gld32(rs, WS_FQ + lane * FS) == tag)) break; __builtin_amdgcn_s_sleep(2); }
    }
    bar();
    STAMP(4);
    {
      v4u t[4];
#pragma unroll
      for (int i = 0; i < 4; ++i) t[i] = gld128(rs, WS_QAB + (b * 32 + 4 * w + i) * 1024 + lane * 16);
#pragma unroll
      for (int i = 0; i < 4; ++i) sto128(SCR + (4 * w + i) * 1024 + ((lane ^ ((4 * w + i) & 15)) << 4), t[i]);
    }
    LD_C(1, 4); LD_C(1, 5); LD_C(1, 6); LD_C(1, 7);
    bar();
    const int kb4 = w & 3, hg = w >> 2;
    v4u qf[16];
#pragma unroll
    for (int s = 0; s < 16; ++s) qf[s] = lds128(SCR + (16 * hg + c16) * 1024 + ((((4 * s) & 15) ^ g ^ c16) << 4) + (s >> 2) * 256);
    STAMP(5);
    const unsigned fk = ((c16 & 3) << 2) | ((0x78u >> (2 * (c16 >> 2))) & 3u);
    const unsigned ka = (16 * kb4 + c16) * 1024 + ((g ^ fk) << 4);
    const int h2 = lane >> 5, blk = g & 1, qq = c16 >> 2, pp = lane & 3;
    const unsigned ft0 = (qq << 2) | (h2 ? 3u : 0u), ft1 = (qq << 2) | (h2 ? 1u : 2u);
    const unsigned cbase = 8 * w + 2 * blk + (pp >> 1);
    const unsigned va00 = (8 * h2 + qq) * 1024 + ((cbase ^ ft0) << 4) + (pp & 1) * 8, va01 = (8 * h2 + 4 + qq) * 1024 + ((cbase ^ ft1) << 4) + (pp & 1) * 8;
    float m = -INFINITY, l = 0.f;
    f16v acc0, acc1;
#pragma unroll
    for (int i = 0; i < 16; ++i) { acc0[i] = 0.f; acc1[i] = 0.f; }
    vmwait<8>();
#pragma nounroll
    for (int rho = 0; rho < 4; ++rho) {
      const unsigned sb = (rho & 1) * 65536;
      bar();
      f4 s4_ = {0.f, 0.f, 0.f, 0.f};
      {
        v4u kf[16];
#pragma unroll
        for (int s = 0; s < 16; ++s) kf[s] = lds128(sb + (ka ^ ((s & 3) << 6)) + (s >> 2) * 256);
        __builtin_amdgcn_sched_barrier(0);
#pragma unroll
        for (int s = 0; s < 16; ++s) s4_ = mfma32(kf[s], qf[s], s4_);
      }
      float mx = fmaxf(fmaxf(s4_[0], s4_[1]), fmaxf(s4_[2], s4_[3]));
      mx = x32max(x16max(mx));
      if (g == 0) stof(B_MX + (kb4 * 32 + 16 * hg + c16) * 4, mx);
      bar();
      const unsigned ma = B_MX + (16 * hg + c16) * 4;
      const float mn = fmaxf(fmaxf(m, fmaxf(ldsf(ma), ldsf(ma + 128))), fmaxf(ldsf(ma + 256), ldsf(ma + 384)));
      const float al = ex2((m - mn) * SC), mc = -mn * SC;
      const float p0 = ex2(fmaf(s4_[0], SC, mc)), p1 = ex2(fmaf(s4_[1], SC, mc)), p2 = ex2(fmaf(s4_[2], SC, mc)), p3 = ex2(fmaf(s4_[3], SC, mc));
      l = fmaf(l, al, (p0 + p1) + (p2 + p3));
      m = mn;
      sto64(B_P + kb4 * 1024 + (16 * hg + c16) * 32 + g * 8, (v2u){pk(p0, p1), pk(p2, p3)});
      if (kb4 == 0 && g == 0) stof(B_AL + (16 * hg + c16) * 4, al);
      v4u c0[4], c1[4];
#pragma unroll
      for (int ks = 0; ks < 4; ++ks) {
        c0[ks] = trd2(sb + ks * 16384 + va00, sb + ks * 16384 + va01);
        c1[ks] = trd2(sb + ks * 16384 + (va00 ^ 64), sb + ks * 16384 + (va01 ^ 64));
      }
      bar();
      if (rho == 2) { LD_UV(0); LD_UV(1); LD_UV(2); LD_UV(3); LD_UV(4); LD_UV(5); }
      else if (rho != 3) { LD_C(rho + 2, 0); LD_C(rho + 2, 1); LD_C(rho + 2, 2); LD_C(rho + 2, 3); LD_C(rho + 2, 4); LD_C(rho + 2, 5); }
      const float alp = ldsf(B_AL + (lane & 31) * 4);
      v4u pf[4];
#pragma unroll
      for (int ks = 0; ks < 4; ++ks) pf[ks] = lds128(B_P + ks * 1024 + (lane & 31) * 32 + h2 * 16);
      __builtin_amdgcn_sched_barrier(0);
      acc0 *= alp; acc1 *= alp;
#pragma unroll
      for (int ks = 0; ks < 4; ++ks) { acc0 = mfmaL(c0[ks], pf[ks], acc0); acc1 = mfmaL(c1[ks], pf[ks], acc1); }
      if (rho == 0) STAMP(6); else if (rho == 1) STAMP(7); else if (rho == 2) STAMP(8); else STAMP(9);
      if (rho == 2) { LD_UV(6); LD_UV(7); } else if (rho != 3) { LD_C(rho + 2, 6); LD_C(rho + 2, 7); }
      if (rho != 3) vmwait<8>();
    }
    l = x32sum(x16sum(l));
    if (g == 0) stof(B_LS + (kb4 * 32 + 16 * hg + c16) * 4, l);
    {
      const unsigned tb = B_T + w * 4096, n32 = lane & 31;
#pragma unroll
      for (int q4 = 0; q4 < 4; ++q4) {
        sto64(tb + n32 * 128 + ((q4 ^ (n32 & 7)) << 4) + h2 * 8, (v2u){pk(acc0[4 * q4], acc0[4 * q4 + 1]), pk(acc0[4 * q4 + 2], acc0[4 * q4 + 3])});
        sto64(tb + n32 * 128 + (((4 + q4) ^ (n32 & 7)) << 4) + h2 * 8, (v2u){pk(acc1[4 * q4], acc1[4 * q4 + 1]), pk(acc1[4 * q4 + 2], acc1[4 * q4 + 3])});
      }
      lgk0();
      const int hr = lane >> 3, ck = lane & 7;
#pragma unroll
      for (int i = 0; i < 4; ++i)
        gst128(rs, WS_PB + ((b * 32 + sp) * 32 + 8 * i + hr) * 1024 + w * 128 + ck * 16, lds128(tb + (8 * i + hr) * 128 + ((ck ^ hr) << 4)));
    }
    bar();
    if (kb4 == 0 && g == 0) {
      const unsigned la = B_LS + (16 * hg + c16) * 4;
      const float L = (ldsf(la) + ldsf(la + 128)) + (ldsf(la + 256) + ldsf(la + 384));
      gst64(rs, WS_PBM + ((b * 32 + sp) * 32 + 16 * hg + c16) * 8, (v2u){asu(m * SC), asu(L)});
    }
    vmwait<0>();
    bar();
    if (w == 0 && lane == 0) gst32(rs, WS_FP + bid * FS, tag);
    STAMP(10);
    {
      const char* op = Bwo + (size_t)bid * 32768 + w * 4096;
      const unsigned fo = ((w >> 1) & 1) << 3;
      dma<0>(op, (lane ^ fo) << 4, B_OU + w * 4096); dma<1024>(op, (lane ^ (fo | 2u)) << 4, B_OU + w * 4096);
      dma<2048>(op, (lane ^ (fo | 4u)) << 4, B_OU + w * 4096); dma<3072>(op, (lane ^ (fo | 6u)) << 4, B_OU + w * 4096);
    }
    if (w == 0) {
      _Pragma("nounroll") for (int it = 0; it < SPIN_CAP; ++it) { cfence(); if (__all(gld32(rs, WS_FP + lane * FS) == tag)) break; __builtin_amdgcn_s_sleep(4); }
    }
    bar();
    STAMP(11);
    {
      const int gb = lane >> 5, s = lane & 31;
      const v2u ml = gld64(rs, WS_PBM + ((gb * 32 + s) * 32 + h) * 8);
      v4u o[4];
#pragma unroll
      for (int i = 0; i < 4; ++i) o[i] = gld128(rs, WS_PB + ((gb * 32 + 4 * w + i) * 32 + h) * 1024 + ch * 512 + s * 16);
      const float M = x16max(row16max(asf(ml.x)));
      const float wt = ex2(asf(ml.x) - M);
      const float L = x16sum(row16sum(wt * asf(ml.y)));
      const float wn = wt / L;
      float a[8];
#pragma unroll
      for (int k = 0; k < 8; ++k) a[k] = 0.f;
#pragma unroll
      for (int i = 0; i < 4; ++i) {
        const float wi = asf(__builtin_amdgcn_ds_bpermute((gb * 32 + 4 * w + i) * 4, asu(wn)));
#pragma unroll
        for (int k = 0; k < 4; ++k) { a[2 * k] = fmaf(wi, asf(o[i][k] << 16), a[2 * k]); a[2 * k + 1] = fmaf(wi, asf(o[i][k] & 0xffff0000u), a[2 * k + 1]); }
      }
      sto128(B_RED + w * 2048 + lane * 32, (v4u){asu(a[0]), asu(a[1]), asu(a[2]), asu(a[3])});
      sto128(B_RED + w * 2048 + lane * 32 + 16, (v4u){asu(a[4]), asu(a[5]), asu(a[6]), asu(a[7])});
    }
    bar();
    if (tid < 256) {
      float s0 = 0.f, s1 = 0.f;
#pragma unroll
      for (int i = 0; i < 8; ++i) { const f2 t = *(const LAS f2*)(B_RED + i * 2048 + tid * 8); s0 += t.x; s1 += t.y; }
      sto32(B_OL + tid * 4, pk(s0, s1));
    }
    bar();
    {
      const int f3 = qq | ((g & 1) << 2);
      const unsigned a0 = SLOT0 + (8 * g + qq) * 256 + (((2 * w + (pp >> 1)) ^ (f3 << 1)) << 4) + (pp & 1) * 8;
      v4u wf[8], of[8];
#pragma unroll
      for (int s = 0; s < 8; ++s) { wf[s] = trd2(a0 + s * 8192, a0 + s * 8192 + 1024); of[s] = lds128(B_OL + (c16 & 1) * 512 + s * 64 + g * 16); }
      __builtin_amdgcn_sched_barrier(0);
      f4 d = {0.f, 0.f, 0.f, 0.f};
#pragma unroll
      for (int s = 0; s < 8; ++s) d = mfma32(wf[s], of[s], d);
      const int pr = (c16 >> 1) & 1;
      const float v0 = pr ? d[2] : d[0], v1 = pr ? d[3] : d[1];
      if (c16 < 4) gst128(rs, WS_CT + (((h * 2 + ch) * 2 + (c16 & 1)) * 128 + 16 * w + 4 * g + 2 * pr) * 8, (v4u){asu(v0), tag, asu(v1), tag});
    }
    STAMP(12);
    gather_ctx(B_OIMG, w, lane, bid >> 2, bid & 3, rs, tag);
    vmwait<0>();
    bar();
    out_unit(B_OU, B_OIMG, w, lane, 1, bid, rs, tag);
    STAMP(13);
    {
      const unsigned off = WS_PAM + (bid * NSPLIT_A + (bid % NSPLIT_A)) * 16;
      _Pragma("nounroll") for (int it = 0; it < SPIN_CAP; ++it) { cfence(); if (__all(gld64(rs, off).y == tag)) break; __builtin_amdgcn_s_sleep(8); }
    }
    STAMP(14);
    v4u f[4];
    _Pragma("nounroll") for (int it = 0; it < SPIN_CAP; ++it) {
      cfence();
      bool ok = true;
#pragma unroll
      for (int k = 0; k < 4; ++k) { f[k] = gld128(rs, WS_F + ((k >> 1) * 64 + bid) * 16384 + (k & 1) * 8192 + tid * 16); ok &= tagok(f[k], tag); }
      if (__all(ok)) break;
      __builtin_amdgcn_s_sleep(1);
    }
    float r[4] = {asf(f[0].x) + asf(f[1].x), asf(f[0].z) + asf(f[1].z), asf(f[2].x) + asf(f[3].x), asf(f[2].z) + asf(f[3].z)};
#pragma unroll
    for (int k = 0; k < 4; ++k) { r[k] += dpp<ROR8>(r[k]); r[k] = x32sum(x16sum(r[k])); }
    if (lane < 8) { sto64(B_FIN + w * 128 + lane * 8, (v2u){asu(r[0]), asu(r[1])}); sto64(B_FIN + w * 128 + 64 + lane * 8, (v2u){asu(r[2]), asu(r[3])}); }
    bar();
    if (tid < 16) {
      float s0 = 0.f, s1 = 0.f;
#pragma unroll
      for (int i = 0; i < 8; ++i) { const f2 t = *(const LAS f2*)(B_FIN + i * 128 + tid * 8); s0 += t.x; s1 += t.y; }
      const int mat = tid >> 3, ob = (tid >> 2) & 1, pr = tid & 3;
      *(GAS unsigned*)((GAS char*)out + (ob * 1024 + mat * 512 + 8 * bid + 2 * pr) * 2) = pk(s0, s1);
      if (bid == 0 && tid == 0) gst32(rs, WS_E, tag);
    }
    STAMP(15);
    STAMP_OUT();
  }
}

extern "C" void kernel_launch(void* const* d_in, const int* in_sizes, int n_in, void* d_out, int out_size, void* d_ws, size_t ws_size,
                              hipStream_t stream) {
  (void)in_sizes; (void)n_in; (void)out_size; (void)ws_size;
  (void)hipFuncSetAttribute((const void*)fused_decode, hipFuncAttributeMaxDynamicSharedMemorySize, LDS_BYTES);
  fused_decode<<<256, 512, LDS_BYTES, stream>>>(
      (const char*)d_in[0], (const char*)d_in[1], (const char*)d_in[2], (const char*)d_in[3], (const char*)d_in[4], (const char*)d_in[5],
      (const char*)d_in[6], (const char*)d_in[7], (const char*)d_in[8], (const char*)d_in[9], (const char*)d_in[10], (unsigned short*)d_out,
      (char*)d_ws);
}
```

```cpp
#define HX 0
#define DBG_TS
#define PROBE 0
#define PROLE 0
#define PSTAT 0
#define PZERO 1
#include <hip/hip_runtime.h>
#include <stdint.h>
#pragma clang diagnostic ignored "-Wint-to-pointer-cast"

#define GAS __attribute__((address_space(1)))
#define LAS __attribute__((address_space(3)))
typedef unsigned v4u __attribute__((ext_vector_type(4)));
typedef unsigned v2u __attribute__((ext_vector_type(2)));
typedef float f4 __attribute__((ext_vector_type(4)));
typedef float f2 __attribute__((ext_vector_type(2)));
typedef float f16v __attribute__((ext_vector_type(16)));
typedef short s4 __attribute__((ext_vector_type(4)));
typedef __bf16 b8 __attribute__((ext_vector_type(8)));
typedef __bf16 b2 __attribute__((ext_vector_type(2)));

#define WIN_A 24
#define WB 6
#define CODE_KB 20
#define QGO 20

namespace {

constexpr int NB_CU = 64, NSPLIT_A = 12, NSLOT = 15, NCONS = 7;
constexpr float SC = 0.08838834764831845f * 1.4426950408889634f;
constexpr int SPIN_CAP = 1 << 18;

constexpr unsigned FS = 64;
constexpr unsigned WS_E = 0;
constexpr unsigned WS_FQ = 4096;
constexpr unsigned WS_FP = WS_FQ + 64 * FS;
constexpr unsigned WS_QG = 65536;
constexpr unsigned WS_QAB = WS_QG + 262144;
constexpr unsigned WS_PBM = WS_QAB + 65536;
constexpr unsigned WS_PAM = WS_PBM + 16384;
constexpr unsigned WS_CT = WS_PAM + 16384;
constexpr unsigned WS_PAO = WS_CT + 131072;
constexpr unsigned WS_PB = WS_PAO + 786432;
constexpr unsigned WS_F = WS_PB + 2097152;
constexpr unsigned WS_END = WS_F + 2097152;
constexpr unsigned WS_TS = 8u << 20;

constexpr unsigned LDS_BYTES = 163840;
constexpr unsigned SCR = 131072;
constexpr unsigned A_MISC = NSLOT * 8192;
constexpr unsigned A_LANDED = A_MISC, A_QRDY = A_MISC + 4, A_DONE = A_MISC + 64;
constexpr unsigned A_QIMG = A_MISC + 256;
constexpr unsigned A_ML = A_MISC + 1280;
constexpr unsigned A_OIMG = A_MISC + 1536;
constexpr unsigned A_XIMG = A_MISC + 2048, A_DUMMY = A_MISC + 3072;
constexpr unsigned SLOT0 = 0, SLOT1 = 65536;
constexpr unsigned B_QBI = SCR, B_QAS = SCR + 1024;
constexpr unsigned B_MX = SCR, B_AL = SCR + 512, B_LS = SCR + 640, B_P = SCR + 2048;
constexpr unsigned B_RED = SCR + 8192, B_OL = SCR + 24576, B_OIMG = SCR + 26624, B_FIN = SCR + 27648;
constexpr unsigned B_T = SLOT1, B_OU = SLOT1 + 32768;
constexpr unsigned B_XIMG = SLOT0 + 63 * 1024, B_DUMMY = SLOT0 + 62 * 1024;

__device__ __forceinline__ v4u lds128(unsigned a) { return *(const LAS v4u*)a; }
__device__ __forceinline__ void sto128(unsigned a, v4u v) { *(LAS v4u*)a = v; }
__device__ __forceinline__ void sto64(unsigned a, v2u v) { *(LAS v2u*)a = v; }
__device__ __forceinline__ void sto32(unsigned a, unsigned v) { *(LAS unsigned*)a = v; }
__device__ __forceinline__ float ldsf(unsigned a) { return *(const LAS float*)a; }
__device__ __forceinline__ void stof(unsigned a, float v) { *(LAS float*)a = v; }
__device__ __forceinline__ v2u trd(unsigned a) { return __builtin_bit_cast(v2u, __builtin_amdgcn_ds_read_tr16_b64_v4i16((LAS s4*)a)); }
__device__ __forceinline__ v4u trd2(unsigned a0, unsigned a1) { const v2u x = trd(a0), y = trd(a1); return (v4u){x.x, x.y, y.x, y.y}; }
__device__ __forceinline__ f4 mfma32(v4u a, v4u b, f4 c) {
  return __builtin_amdgcn_mfma_f32_16x16x32_bf16(__builtin_bit_cast(b8, a), __builtin_bit_cast(b8, b), c, 0, 0, 0); }
__device__ __forceinline__ f4 mfma16(v2u a, v2u b, f4 c) {
  return __builtin_amdgcn_mfma_f32_16x16x16bf16_1k(__builtin_bit_cast(s4, a), __builtin_bit_cast(s4, b), c, 0, 0, 0); }
__device__ __forceinline__ f16v mfmaL(v4u a, v4u b, f16v c) {
  return __builtin_amdgcn_mfma_f32_32x32x16_bf16(__builtin_bit_cast(b8, a), __builtin_bit_cast(b8, b), c, 0, 0, 0); }
__device__ __forceinline__ unsigned pk(float lo, float hi) { return __builtin_bit_cast(unsigned, __builtin_convertvector((f2){lo, hi}, b2)); }
__device__ __forceinline__ float asf(unsigned u) { return __builtin_bit_cast(float, u); }
__device__ __forceinline__ unsigned asu(float f) { return __builtin_bit_cast(unsigned, f); }
__device__ __forceinline__ float ex2(float x) { return __builtin_amdgcn_exp2f(x); }
template <int CTRL> __device__ __forceinline__ float dpp(float x) {
  return __builtin_bit_cast(float, __builtin_amdgcn_mov_dpp(__builtin_bit_cast(int, x), CTRL, 0xf, 0xf, true)); }
constexpr int XOR1 = 0xB1, XOR2 = 0x4E, ROR4 = 0x124, ROR8 = 0x128;
__device__ __forceinline__ float x16max(float x) { auto s = __builtin_amdgcn_permlane16_swap(asu(x), asu(x), false, false); return fmaxf(asf(s[0]), asf(s[1])); }
__device__ __forceinline__ float x32max(float x) { auto s = __builtin_amdgcn_permlane32_swap(asu(x), asu(x), false, false); return fmaxf(asf(s[0]), asf(s[1])); }
__device__ __forceinline__ float x16sum(float x) { auto s = __builtin_amdgcn_permlane16_swap(asu(x), asu(x), false, false); return asf(s[0]) + asf(s[1]); }
__device__ __forceinline__ float x32sum(float x) { auto s = __builtin_amdgcn_permlane32_swap(asu(x), asu(x), false, false); return asf(s[0]) + asf(s[1]); }
__device__ __forceinline__ float row16max(float x) { x = fmaxf(x, dpp<XOR1>(x)); x = fmaxf(x, dpp<XOR2>(x)); x = fmaxf(x, dpp<ROR4>(x)); return fmaxf(x, dpp<ROR8>(x)); }
__device__ __forceinline__ float row16sum(float x) { x += dpp<XOR1>(x); x += dpp<XOR2>(x); x += dpp<ROR4>(x); return x + dpp<ROR8>(x); }

template <int IMM> __device__ __forceinline__ void dma(const void* sbase, unsigned voff, unsigned lds) {
  asm volatile("s_mov_b32 m0, %2\n\ts_nop 0\n\tglobal_load_lds_dwordx4 %0, %1 offset:%3 nt" :: "v"(voff), "s"(sbase), "s"(lds), "n"(IMM) : "memory");
}
template <int N> __device__ __forceinline__ void vmwait() { asm volatile("s_waitcnt vmcnt(%0)" :: "n"(N) : "memory"); }
__device__ __forceinline__ void bar() { asm volatile("s_waitcnt lgkmcnt(0)\n\ts_barrier" ::: "memory"); }
__device__ __forceinline__ void lgk0() { asm volatile("s_waitcnt lgkmcnt(0)" ::: "memory"); }
__device__ __forceinline__ void cfence() { asm volatile("" ::: "memory"); }
__device__ __forceinline__ unsigned lds_peek(unsigned a) { return __builtin_amdgcn_readfirstlane(*(volatile LAS unsigned*)a); }
__device__ __forceinline__ void lds_wait_ge(unsigned a, unsigned want) {
  _Pragma("nounroll") for (int it = 0; it < SPIN_CAP * 8; ++it) { if (lds_peek(a) >= want) break; __builtin_amdgcn_s_sleep(1); }
  cfence();
}
typedef __amdgpu_buffer_rsrc_t rsrc_t;
constexpr int SC1 = 16;
__device__ __forceinline__ v4u gld128(rsrc_t r, unsigned off) { return __builtin_amdgcn_raw_buffer_load_b128(r, off, 0, SC1); }
__device__ __forceinline__ v2u gld64(rsrc_t r, unsigned off) { return __builtin_amdgcn_raw_buffer_load_b64(r, off, 0, SC1); }
__device__ __forceinline__ unsigned gld32(rsrc_t r, unsigned off) { return __builtin_amdgcn_raw_buffer_load_b32(r, off, 0, SC1); }
__device__ __forceinline__ void gst128(rsrc_t r, unsigned off, v4u v) { __builtin_amdgcn_raw_buffer_store_b128(v, r, off, 0, SC1); }
__device__ __forceinline__ void gst64(rsrc_t r, unsigned off, v2u v) { __builtin_amdgcn_raw_buffer_store_b64(v, r, off, 0, SC1); }
__device__ __forceinline__ void gst32(rsrc_t r, unsigned off, unsigned v) { __builtin_amdgcn_raw_buffer_store_b32(v, r, off, 0, SC1); }
__device__ __forceinline__ bool tagok(v4u v, unsigned tag) { return v.y == tag && v.w == tag; }

#define STAMP(i) do { unsigned long long t_; asm volatile("s_memrealtime %0\n\ts_waitcnt lgkmcnt(0)" : "=s"(t_) :: "memory"); tsv[i] = (unsigned)t_; } while (0)
#define STAMP_DECL unsigned tsv[16] = {0,0,0,0,0,0,0,0,0,0,0,0,0,0,0,0}
#define STAMP_OUT() do { if (lane == 0) gst128(rs2, WS_TS + (blockIdx.x * 8 + wave) * 16, (v4u){tsv[0], tag, tsv[PROBE], tag}); \
  if (blockIdx.x == 0 && wave == 0) { unsigned mn = 0xffffffffu; float mx = 0.f, sm = 0.f, cnt = 0.f; unsigned ref = tsv[0] - 1000u; \
    for (int i_ = 0; i_ < 32; ++i_) { v4u v_; for (int it = 0; it < SPIN_CAP; ++it) { cfence(); v_ = gld128(rs2, WS_TS + (i_ * 64 + lane) * 16); if (__all(tagok(v_, tag))) break; } \
      mn = min(mn, v_.x - ref); const bool role_ = PROLE ? (i_ < 8) : (i_ >= 8); if (v_.z != 0u && role_) { const float d_ = (float)(v_.z - ref); mx = fmaxf(mx, d_); sm += d_; cnt += 1.f; } } \
    float mnf = -(float)mn; mnf = -x32max(x16max(row16max(mnf))); mx = x32max(x16max(row16max(mx))); sm = x32sum(x16sum(row16sum(sm))); cnt = x32sum(x16sum(row16sum(cnt))); \
    const float X = PZERO ? 0.f : (PSTAT ? sm / cnt : mx) - mnf; const unsigned T0 = *(volatile GAS unsigned*)((GAS char*)ws + (12u << 20)); const unsigned until = T0 + 6000u + HX * 8u * (ref + (unsigned)mnf - T0); \
    for (int it = 0; it < 100000000; ++it) { unsigned long long t_; asm volatile("s_memrealtime %0\n\ts_waitcnt lgkmcnt(0)" : "=s"(t_) :: "memory"); if ((int)((unsigned)t_ - __builtin_amdgcn_readfirstlane(until)) >= 0) break; } } } while (0)

__device__ __forceinline__ void q_unit(unsigned ximg, int nb, int lane, int mat, int cb, int kh, rsrc_t rs, unsigned tag) {
  const int g = lane >> 4, c16 = lane & 15, q = c16 >> 2, p = lane & 3;
  const int f2_ = ((q >> 1) & 1) | ((g & 1) << 1);
  const unsigned a0 = SCR + (8 * g + q) * 128 + (((2 * nb + (p >> 1)) ^ (f2_ << 1)) << 4) + (p & 1) * 8;
  v4u wf[8], xf[8];
#pragma unroll
  for (int s = 0; s < 8; ++s) { wf[s] = trd2(a0 + s * 4096, a0 + s * 4096 + 512); xf[s] = lds128(ximg + (c16 & 1) * 512 + g * 16 + s * 64); }
  __builtin_amdgcn_sched_barrier(0);
  f4 d = {0.f, 0.f, 0.f, 0.f};
#pragma unroll
  for (int s = 0; s < 8; ++s) d = mfma32(wf[s], xf[s], d);
  const int pr = (c16 >> 1) & 1;
  const float v0 = pr ? d[2] : d[0], v1 = pr ? d[3] : d[1];
  if (c16 < 4) gst128(rs, WS_QG + ((((mat * 2 + kh) * 2 + (c16 & 1)) * 4096) + 64 * cb + 16 * nb + 4 * g + 2 * pr) * 8, (v4u){asu(v0), tag, asu(v1), tag});
}

__device__ __forceinline__ void out_unit(unsigned ou, unsigned oimg, int w, int lane, int mat, int pidx, rsrc_t rs, unsigned tag) {
  const int g = lane >> 4, c16 = lane & 15, q = c16 >> 2, p = lane & 3;
  const int f3 = q | ((g & 1) << 2);
  const v4u ob = lds128(oimg + (c16 & 1) * 64 + g * 16);
  f4 d[4];
  v4u wf[4];
#pragma unroll
  for (int tt = 0; tt < 4; ++tt) {
    const unsigned a0 = ou + (8 * g + q) * 1024 + (((2 * (4 * w + tt) + (p >> 1)) ^ (f3 << 1)) << 4) + (p & 1) * 8;
    wf[tt] = trd2(a0, a0 + 4096);
  }
  __builtin_amdgcn_sched_barrier(0);
#pragma unroll
  for (int tt = 0; tt < 4; ++tt) d[tt] = mfma32(wf[tt], ob, (f4){0.f, 0.f, 0.f, 0.f});
  const int tt = (c16 >> 1) & 3, pr = c16 >> 3;
  const f4 e = tt == 0 ? d[0] : tt == 1 ? d[1] : tt == 2 ? d[2] : d[3];
  const float v0 = pr ? e[2] : e[0], v1 = pr ? e[3] : e[1];
  const int n = 64 * w + 16 * tt + 4 * g + 2 * pr;
  gst128(rs, WS_F + ((((mat * 64 + (n >> 3)) * 128 + pidx) * 16) + (c16 & 1) * 8 + (n & 7)) * 8, (v4u){asu(v0), tag, asu(v1), tag});
}

__device__ __forceinline__ void gather_ctx(unsigned oimg, int w, int lane, int h, int dq, rsrc_t rs, unsigned tag) {
  const int b = lane >> 5, pr = (lane >> 4) & 1, s = lane & 15;
  const unsigned off = WS_CT + (((h * 2 + (s & 1)) * 2 + b) * 128 + 32 * dq + 4 * w + 2 * pr) * 8;
  v4u v;
  _Pragma("nounroll") for (int it = 0; it < SPIN_CAP; ++it) { cfence(); v = gld128(rs, off); if (__all(tagok(v, tag))) break; __builtin_amdgcn_s_sleep(2); }
  const float o0 = asf(v.x) + dpp<XOR1>(asf(v.x)), o1 = asf(v.z) + dpp<XOR1>(asf(v.z));
  if (s == 0) sto32(oimg + b * 64 + (4 * w + 2 * pr) * 2, pk(o0, o1));
}

}

__global__ void __launch_bounds__(512, 2) fused_decode(
    const char* __restrict__ Ax, const char* __restrict__ Kc, const char* __restrict__ Vc, const char* __restrict__ Wq, const char* __restrict__ Wo,
    const char* __restrict__ Bx, const char* __restrict__ Ckv, const char* __restrict__ Bwq, const char* __restrict__ Wuk, const char* __restrict__ Wuv,
    const char* __restrict__ Bwo, unsigned short* __restrict__ out, char* __restrict__ ws) {
  const int bid = blockIdx.x, tid = threadIdx.x, lane = tid & 63, wave = __builtin_amdgcn_readfirstlane(tid >> 6);
  const int g = lane >> 4, c16 = lane & 15;
  STAMP_DECL;
  STAMP(0);
  const rsrc_t rs = __builtin_amdgcn_make_buffer_rsrc(ws, 0, WS_END, 0x00020000);
  const rsrc_t rs2 = __builtin_amdgcn_make_buffer_rsrc(ws, 0, 16u << 20, 0x00020000);
  asm volatile("" :: "s"(Ax), "s"(Kc), "s"(Vc), "s"(Wq), "s"(Wo), "s"(Bx), "s"(Ckv), "s"(Bwq), "s"(Wuk), "s"(Wuv), "s"(Bwo), "s"(out), "s"(ws));
  unsigned tag;
#define LOAD_TAG() asm volatile("s_load_dword %0, %1, 0x0" : "=s"(tag) : "s"(ws) : "memory")
#define WAIT_TAG() do { asm volatile("s_waitcnt lgkmcnt(0)" : "+s"(tag) :: "memory"); tag += 1u; } while (0)
  const int qmat = bid & 1, qkh = (bid >> 1) & 1, qcb = bid >> 2;
  const char* qW = (qmat ? Bwq : Wq) + (size_t)(256 * qkh) * 8192 + qcb * 128;
  const char* qx = (qmat ? Bx : Ax) + 512 * qkh;
  const unsigned vx = (lane >> 5) * 1024 + (lane & 31) * 16;
  unsigned long long pc;
  asm volatile("s_getpc_b64 %0" : "=s"(pc));
  const char* codep = (const char*)((pc & ~255ull) + (unsigned)(((bid >> 3) % CODE_KB) * 1024));
  const unsigned vq0 = (lane >> 3) * 8192 + (((lane & 7) ^ (((lane >> 4) & 1) << 1)) << 4);

  if (bid >= NB_CU) {
    const int iA = bid - NB_CU, pair = iA & 15, split = iA >> 4, b = pair >> 3, kvh = pair & 7;
    const int nblk = split < 8 ? 43 : 42, blk0 = split < 8 ? 43 * split : 344 + 42 * (split - 8);
    const int omat = iA < 64 ? 1 : 0, ounit = iA < 64 ? 64 + iA : iA - 64;
    if (wave == 0) {
      __builtin_amdgcn_s_setprio(3);
      constexpr int W = WIN_A, IST = (W - 1) & 3;
      static_assert(W >= 4 && W <= 32, "window");
      int pub = 0, p = 0;
#define PUBLISH(val) do { const int v_ = (val); if (v_ > pub) { pub = v_; if (lane == 0) *(volatile LAS unsigned*)A_LANDED = (unsigned)v_; } } while (0)
#define PACE(i) do { vmwait<W - 1>(); if ((i) == IST) PUBLISH((4 * p + IST + 1 - W) / 4); } while (0)
      if (lane < 32) sto32(A_MISC + lane * 4, 0u);
      dma<0>(qx, vx, A_XIMG);
      dma<0>(codep, lane * 16, A_DUMMY);
#pragma unroll
      for (int u = 0; u < 4; ++u) dma<0>(qW + (size_t)u * 65536, vq0 ^ ((u & 1) << 6), SCR + u * 1024);
      bar();
#pragma nounroll
      for (p = 1; p < 8; ++p) {
#pragma unroll
        for (int i = 0; i < 4; ++i) dma<0>(qW + (size_t)(4 * p + i) * 65536, vq0 ^ ((i & 1) << 6), SCR + (4 * p + i) * 1024);
      }
      LOAD_TAG();
      STAMP(9);
      const char* kp = Kc + ((size_t)pair * 8192 + blk0 * 16) * 256;
      const char* vp = Vc + ((size_t)pair * 8192 + blk0 * 16) * 256;
      const unsigned vk0 = g * 256 + ((c16 ^ g) << 4), vv0 = g * 256 + ((c16 ^ (g << 1)) << 4);
      dma<0>(kp, vk0, 0); dma<1024>(kp, vk0 ^ 64, 0); dma<2048>(kp, vk0 ^ 128, 0); dma<3072>(kp, vk0 ^ 192, 0);
      dma<0>(vp, vv0, 4096); dma<1024>(vp, vv0 ^ 128, 4096); dma<2048>(vp, vv0, 4096); dma<3072>(vp, vv0 ^ 128, 4096);
      kp += 4096; vp += 4096;
      vmwait<39>();
      STAMP(10);
      vmwait<8>();
      PUBLISH(8);
      STAMP(11);
      p = 10;
      int slot = 1;
      for (int j = 1; j < nblk; ++j) {
        if (j >= NSLOT) {
          const unsigned need = j - NSLOT + 1;
          if (lds_peek(A_DONE + slot * 4) < need) { vmwait<0>(); PUBLISH(p); lds_wait_ge(A_DONE + slot * 4, need); }
        }
        const unsigned sb = slot * 8192;
        PACE(0); dma<0>(kp, vk0, sb); PACE(1); dma<1024>(kp, vk0 ^ 64, sb); PACE(2); dma<2048>(kp, vk0 ^ 128, sb); PACE(3); dma<3072>(kp, vk0 ^ 192, sb);
        ++p;
        PACE(0); dma<0>(vp, vv0, sb + 4096); PACE(1); dma<1024>(vp, vv0 ^ 128, sb + 4096); PACE(2); dma<2048>(vp, vv0, sb + 4096); PACE(3); dma<3072>(vp, vv0 ^ 128, sb + 4096);
        ++p;
        kp += 4096; vp += 4096;
        slot = slot == NSLOT - 1 ? 0 : slot + 1;
      }
      lds_wait_ge(A_QRDY, 1u);
      const char* op = (omat ? Bwo : Wo) + (size_t)ounit * 32768;
#pragma unroll
      for (int k = 0; k < 32; ++k) {
        if (k < W) { vmwait<W - 1>(); if ((k & 3) == IST) PUBLISH(p + (k + 1 - W) / 4); }
        dma<0>(op + k * 1024, (lane ^ (((k & 3) | (((k >> 3) & 1) << 2)) << 1)) << 4, SCR + k * 1024);
      }
      __builtin_amdgcn_s_setprio(0);
      WAIT_TAG();
      STAMP(5);
#undef PACE
#undef PUBLISH
    } else {
      bar();
      LOAD_TAG();
      WAIT_TAG();
      if (wave <= 4) {
        lds_wait_ge(A_LANDED, 8u);
        STAMP(1);
        q_unit(A_XIMG, wave - 1, lane, qmat, qcb, qkh, rs, tag);
        lgk0();
        if (lane == 0) __hip_atomic_fetch_add((LAS unsigned*)(A_MISC + 8), 1u, __ATOMIC_RELAXED, __HIP_MEMORY_SCOPE_WORKGROUP);
        STAMP(2);
      }
      if (wave == 5) {
        lds_wait_ge(A_LANDED, (unsigned)QGO);
        v4u v[8];
        const unsigned off = WS_QG + (b * 4096 + kvh * 512 + 2 * lane) * 8;
        _Pragma("nounroll") for (int it = 0; it < SPIN_CAP; ++it) {
          cfence();
          bool ok = true;
#pragma unroll
          for (int k = 0; k < 8; ++k) { v[k] = gld128(rs, off + (k & 1) * 65536 + (k >> 1) * 1024); ok &= tagok(v[k], tag); }
          if (__all(ok)) break;
          __builtin_amdgcn_s_sleep(8);
        }
#pragma unroll
        for (int hh = 0; hh < 4; ++hh) sto32(A_QIMG + hh * 256 + lane * 4, pk(asf(v[2 * hh].x) + asf(v[2 * hh + 1].x), asf(v[2 * hh].z) + asf(v[2 * hh + 1].z)));
        lds_wait_ge(A_MISC + 8, 4u);
        lgk0();
        if (lane == 0) *(volatile LAS unsigned*)A_QRDY = 1u;
      }
      lds_wait_ge(A_QRDY, 1u);
      STAMP(3);
      v4u qf[4];
#pragma unroll
      for (int s = 0; s < 4; ++s) qf[s] = lds128(A_QIMG + (c16 & 3) * 256 + s * 64 + g * 16);
      const unsigned ko = c16 * 256 + ((g ^ c16) << 4);
      const int vr = 4 * g + (c16 >> 2);
      const unsigned vo = 4096 + vr * 256 + ((((lane & 3) >> 1) ^ ((vr & 7) << 1)) << 4) + (lane & 1) * 8;
      float m = -INFINITY, l = 0.f;
      f4 acc[8];
#pragma unroll
      for (int t = 0; t < 8; ++t) acc[t] = (f4){0.f, 0.f, 0.f, 0.f};
      int slot = wave - 1, j = wave - 1;
      for (; j < nblk; j += NCONS) {
        const unsigned sb = slot * 8192;
        lds_wait_ge(A_LANDED, 9u + 2 * j);
        v4u kf[4];
#pragma unroll
        for (int s = 0; s < 4; ++s) kf[s] = lds128(sb + (ko ^ (s << 6)));
        __builtin_amdgcn_sched_barrier(0);
        f4 s4_ = {0.f, 0.f, 0.f, 0.f};
#pragma unroll
        for (int s = 0; s < 4; ++s) s4_ = mfma32(kf[s], qf[s], s4_);
        float mx = fmaxf(fmaxf(s4_[0], s4_[1]), fmaxf(s4_[2], s4_[3]));
        mx = x32max(x16max(mx));
        const float mn = fmaxf(m, mx), al = ex2((m - mn) * SC), mc = -mn * SC;
        const float p0 = ex2(fmaf(s4_[0], SC, mc)), p1 = ex2(fmaf(s4_[1], SC, mc)), p2 = ex2(fmaf(s4_[2], SC, mc)), p3 = ex2(fmaf(s4_[3], SC, mc));
        l = fmaf(l, al, (p0 + p1) + (p2 + p3));
        m = mn;
        const v2u pb = {pk(p0, p1), pk(p2, p3)};
#pragma unroll
        for (int t = 0; t < 8; ++t) acc[t] *= al;
        lds_wait_ge(A_LANDED, 10u + 2 * j);
        v2u vf[8];
#pragma unroll
        for (int t = 0; t < 8; ++t) vf[t] = trd(sb + (vo ^ (t << 5)));
        lgk0();
        if (lane == 0) *(volatile LAS unsigned*)(A_DONE + slot * 4) = (unsigned)(j + 1);
#pragma unroll
        for (int t = 0; t < 8; ++t) acc[t] = mfma16(vf[t], pb, acc[t]);
        if (j + NCONS < nblk) { slot += NCONS; if (slot >= NSLOT) slot -= NSLOT; }
      }
      STAMP(4);
      l = x32sum(x16sum(l));
      if (c16 < 4) {
#pragma unroll
        for (int t = 0; t < 8; ++t) sto128(slot * 8192 + c16 * 512 + t * 64 + g * 16, __builtin_bit_cast(v4u, acc[t]));
        if (g == 0) sto64(A_ML + ((wave - 1) * 4 + c16) * 8, (v2u){asu(m * SC), asu(l)});
      }
    }
    bar();
    if (wave >= 4) {
      const int hh = wave - 4;
      float mw[NCONS], lw[NCONS];
      f2 ow[NCONS];
#pragma unroll
      for (int c = 0; c < NCONS; ++c) {
        const int jl = c + ((nblk - 1 - c) / NCONS) * NCONS;
        const f2 t = *(const LAS f2*)(A_ML + (c * 4 + hh) * 8);
        mw[c] = t.x; lw[c] = t.y;
        ow[c] = *(const LAS f2*)((jl % NSLOT) * 8192 + hh * 512 + lane * 8);
      }
      float M = mw[0];
#pragma unroll
      for (int c = 1; c < NCONS; ++c) M = fmaxf(M, mw[c]);
      float L = 0.f, o0 = 0.f, o1 = 0.f;
#pragma unroll
      for (int c = 0; c < NCONS; ++c) { const float wt = ex2(mw[c] - M); L = fmaf(wt, lw[c], L); o0 = fmaf(wt, ow[c].x, o0); o1 = fmaf(wt, ow[c].y, o1); }
      const int rec = (b * 32 + kvh * 4 + hh) * NSPLIT_A + split;
      gst128(rs, WS_PAO + rec * 1024 + lane * 16, (v4u){asu(o0), tag, asu(o1), tag});
      if (lane == 0) gst128(rs, WS_PAM + rec * 16, (v4u){asu(M), tag, asu(L), tag});
    }
    STAMP(6);
    const int oh = ounit >> 2, odq = ounit & 3;
    if (omat) {
      gather_ctx(A_OIMG, wave, lane, oh, odq, rs, tag);
    } else {
      const int gb = lane >> 5, pr = (lane >> 4) & 1, s = c16 < NSPLIT_A ? c16 : NSPLIT_A - 1;
      const int rec = (gb * 32 + oh) * NSPLIT_A + s;
      v4u v, ml;
      _Pragma("nounroll") for (int it = 0; it < SPIN_CAP; ++it) {
        cfence();
        v = gld128(rs, WS_PAO + rec * 1024 + (32 * odq + 4 * wave + 2 * pr) * 8);
        ml = gld128(rs, WS_PAM + rec * 16);
        if (__all(tagok(v, tag) && tagok(ml, tag))) break;
        __builtin_amdgcn_s_sleep(1);
      }
      const float M = row16max(asf(ml.x));
      const float wt = c16 < NSPLIT_A ? ex2(asf(ml.x) - M) : 0.f;
      const float L = row16sum(wt * asf(ml.z)), o0 = row16sum(wt * asf(v.x)), o1 = row16sum(wt * asf(v.z));
      const float inv = 1.f / L;
      if (c16 == 0) sto32(A_OIMG + gb * 64 + (4 * wave + 2 * pr) * 2, pk(o0 * inv, o1 * inv));
    }
    vmwait<0>();
    STAMP(7);
    bar();
    out_unit(SCR, A_OIMG, wave, lane, omat, ounit, rs, tag);
    STAMP(8);
    STAMP_OUT();
  } else {
    const int b = bid >> 5, sp = bid & 31, h = bid >> 1, ch = bid & 1, w = wave;
    const char* uk = Wuk + (size_t)(256 * ch + 32 * w) * 8192 + h * 256;
    const char* uv = Wuv + (size_t)(256 * ch + 32 * w) * 8192 + h * 256;
    const char* cp = Ckv + ((size_t)b * 8192 + 256 * sp + 8 * w) * 1024;
    const unsigned vuk = g * 8192 + ((c16 ^ g) << 4), vuv = g * 8192 + ((c16 ^ (g << 1)) << 4);
    const unsigned vc0 = (lane ^ ((w & 1) ? 3 : 0)) << 4, vc1 = (lane ^ ((w & 1) ? 1 : 2)) << 4;
#define LD_UK(u) dma<0>(uk + (size_t)(u) * 32768, vuk ^ (((u) & 3) << 6), SLOT1 + (32 * w + 4 * (u)) * 256)
#define LD_UV(u) dma<0>(uv + (size_t)(u) * 32768, vuv ^ ((((u) >> 1) & 1) << 7), SLOT0 + (32 * w + 4 * (u)) * 256)
#define LD_C(rho, u) dma<((u) & 3) * 1024>(cp + (size_t)(rho) * 65536 + ((u) >> 2) * 4096, (((u) >> 2) ? vc1 : vc0) ^ (((u) & 3) << 6), ((rho) & 1) * 65536 + w * 8192 + ((u) >> 2) * 4096)
    if (w == 7) { dma<0>(qx, vx, B_XIMG); dma<0>(codep, lane * 16, B_DUMMY); }
#pragma unroll
    for (int i = 0; i < 4; ++i) dma<0>(qW + (size_t)(4 * w + i) * 65536, vq0 ^ ((i & 1) << 6), SCR + (4 * w + i) * 1024);
    asm volatile("s_barrier" ::: "memory");
    LD_UK(0); LD_UK(1);
    LOAD_TAG();
    vmwait<2>();
    WAIT_TAG();
    bar();
    STAMP(1);
    LD_UK(2); LD_UK(3);
    if (WB >= 6) { LD_UK(4); LD_UK(5); }
    v4u qg[4];
    if (w >= 4) {
      q_unit(B_XIMG, w - 4, lane, qmat, qcb, qkh, rs, tag);
    } else if (w == 0) {
      const unsigned off = WS_QG + (4 * 4096 + h * 128 + 2 * lane) * 8;
      _Pragma("nounroll") for (int it = 0; it < SPIN_CAP; ++it) {
        cfence();
        bool ok = true;
#pragma unroll
        for (int k = 0; k < 4; ++k) { qg[k] = gld128(rs, off + (k & 1) * 65536 + (k >> 1) * 32768); ok &= tagok(qg[k], tag); }
        if (__all(ok)) break;
        __builtin_amdgcn_s_sleep(2);
      }
    }
    if (WB < 6) { vmwait<WB - 2>(); LD_UK(4); LD_UK(5); }
    vmwait<WB - 2>(); LD_UK(6); LD_UK(7);
    vmwait<WB - 2>(); LD_C(0, 0); LD_C(0, 1);
    vmwait<WB - 2>(); LD_C(0, 2); LD_C(0, 3);
    bar();
    if (w == 0) {
      sto32(B_QBI + lane * 4, pk(asf(qg[0].x) + asf(qg[1].x), asf(qg[0].z) + asf(qg[1].z)));
      sto32(B_QBI + 256 + lane * 4, pk(asf(qg[2].x) + asf(qg[3].x), asf(qg[2].z) + asf(qg[3].z)));
    }
    vmwait<(WB - 2 < 4 ? WB - 2 : 4)>();
    bar();
    STAMP(2);
    LD_C(0, 4); LD_C(0, 5);
    {
      const unsigned a0 = SLOT1 + (32 * w + c16) * 256 + ((g ^ c16) << 4);
      v4u qb[4], u0[4], u1[4];
#pragma unroll
      for (int s = 0; s < 4; ++s) { qb[s] = lds128(B_QBI + (c16 & 1) * 256 + s * 64 + g * 16); u0[s] = lds128(a0 ^ (s << 6)); u1[s] = lds128((a0 ^ (s << 6)) + 4096); }
      __builtin_amdgcn_sched_barrier(0);
      f4 d0 = {0.f, 0.f, 0.f, 0.f}, d1 = d0;
#pragma unroll
      for (int s = 0; s < 4; ++s) { d0 = mfma32(u0[s], qb[s], d0); d1 = mfma32(u1[s], qb[s], d1); }
      const int rb = (c16 >> 1) & 1;
      const f4 e = rb ? d1 : d0;
      if (c16 < 4) sto64(B_QAS + (c16 & 1) * 512 + (32 * w + 16 * rb + 4 * g) * 2, (v2u){pk(e[0], e[1]), pk(e[2], e[3])});
    }
    bar();
    if (w == 0) {
      gst128(rs, WS_QAB + ((lane >> 5) * 32 + h) * 1024 + ch * 512 + (lane & 31) * 16, lds128(B_QAS + lane * 16));
      vmwait<0>();
      if (lane == 0) gst32(rs, WS_FQ + bid * FS, tag);
    }
    STAMP(3);
    vmwait<WB - 2>(); LD_C(0, 6); LD_C(0, 7);
    vmwait<WB - 2>(); LD_C(1, 0); LD_C(1, 1);
    vmwait<WB - 2>(); LD_C(1, 2); LD_C(1, 3);
    if (w == 0) {
      _Pragma("nounroll") for (int it = 0; it < SPIN_CAP; ++it) { cfence(); if (__all(gld32(rs, WS_FQ + lane * FS) == tag)) break; __builtin_amdgcn_s_sleep(2); }
    }
    bar();
    STAMP(4);
    {
      v4u t[4];
#pragma unroll
      for (int i = 0; i < 4; ++i) t[i] = gld128(rs, WS_QAB + (b * 32 + 4 * w + i) * 1024 + lane * 16);
#pragma unroll
      for (int i = 0; i < 4; ++i) sto128(SCR + (4 * w + i) * 1024 + ((lane ^ ((4 * w + i) & 15)) << 4), t[i]);
    }
    LD_C(1, 4); LD_C(1, 5); LD_C(1, 6); LD_C(1, 7);
    bar();
    const int kb4 = w & 3, hg = w >> 2;
    v4u qf[16];
#pragma unroll
    for (int s = 0; s < 16; ++s) qf[s] = lds128(SCR + (16 * hg + c16) * 1024 + ((((4 * s) & 15) ^ g ^ c16) << 4) + (s >> 2) * 256);
    STAMP(5);
    const unsigned fk = ((c16 & 3) << 2) | ((0x78u >> (2 * (c16 >> 2))) & 3u);
    const unsigned ka = (16 * kb4 + c16) * 1024 + ((g ^ fk) << 4);
    const int h2 = lane >> 5, blk = g & 1, qq = c16 >> 2, pp = lane & 3;
    const unsigned ft0 = (qq << 2) | (h2 ? 3u : 0u), ft1 = (qq << 2) | (h2 ? 1u : 2u);
    const unsigned cbase = 8 * w + 2 * blk + (pp >> 1);
    const unsigned va00 = (8 * h2 + qq) * 1024 + ((cbase ^ ft0) << 4) + (pp & 1) * 8, va01 = (8 * h2 + 4 + qq) * 1024 + ((cbase ^ ft1) << 4) + (pp & 1) * 8;
    float m = -INFINITY, l = 0.f;
    f16v acc0, acc1;
#pragma unroll
    for (int i = 0; i < 16; ++i) { acc0[i] = 0.f; acc1[i] = 0.f; }
    vmwait<8>();
#pragma nounroll
    for (int rho = 0; rho < 4; ++rho) {
      const unsigned sb = (rho & 1) * 65536;
      bar();
      f4 s4_ = {0.f, 0.f, 0.f, 0.f};
      {
        v4u kf[16];
#pragma unroll
        for (int s = 0; s < 16; ++s) kf[s] = lds128(sb + (ka ^ ((s & 3) << 6)) + (s >> 2) * 256);
        __builtin_amdgcn_sched_barrier(0);
#pragma unroll
        for (int s = 0; s < 16; ++s) s4_ = mfma32(kf[s], qf[s], s4_);
      }
      float mx = fmaxf(fmaxf(s4_[0], s4_[1]), fmaxf(s4_[2], s4_[3]));
      mx = x32max(x16max(mx));
      if (g == 0) stof(B_MX + (kb4 * 32 + 16 * hg + c16) * 4, mx);
      bar();
      const unsigned ma = B_MX + (16 * hg + c16) * 4;
      const float mn = fmaxf(fmaxf(m, fmaxf(ldsf(ma), ldsf(ma + 128))), fmaxf(ldsf(ma + 256), ldsf(ma + 384)));
      const float al = ex2((m - mn) * SC), mc = -mn * SC;
      const float p0 = ex2(fmaf(s4_[0], SC, mc)), p1 = ex2(fmaf(s4_[1], SC, mc)), p2 = ex2(fmaf(s4_[2], SC, mc)), p3 = ex2(fmaf(s4_[3], SC, mc));
      l = fmaf(l, al, (p0 + p1) + (p2 + p3));
      m = mn;
      sto64(B_P + kb4 * 1024 + (16 * hg + c16) * 32 + g * 8, (v2u){pk(p0, p1), pk(p2, p3)});
      if (kb4 == 0 && g == 0) stof(B_AL + (16 * hg + c16) * 4, al);
      v4u c0[4], c1[4];
#pragma unroll
      for (int ks = 0; ks < 4; ++ks) {
        c0[ks] = trd2(sb + ks * 16384 + va00, sb + ks * 16384 + va01);
        c1[ks] = trd2(sb + ks * 16384 + (va00 ^ 64), sb + ks * 16384 + (va01 ^ 64));
      }
      bar();
      if (rho == 2) { LD_UV(0); LD_UV(1); LD_UV(2); LD_UV(3); LD_UV(4); LD_UV(5); }
      else if (rho != 3) { LD_C(rho + 2, 0); LD_C(rho + 2, 1); LD_C(rho + 2, 2); LD_C(rho + 2, 3); LD_C(rho + 2, 4); LD_C(rho + 2, 5); }
      const float alp = ldsf(B_AL + (lane & 31) * 4);
      v4u pf[4];
#pragma unroll
      for (int ks = 0; ks < 4; ++ks) pf[ks] = lds128(B_P + ks * 1024 + (lane & 31) * 32 + h2 * 16);
      __builtin_amdgcn_sched_barrier(0);
      acc0 *= alp; acc1 *= alp;
#pragma unroll
      for (int ks = 0; ks < 4; ++ks) { acc0 = mfmaL(c0[ks], pf[ks], acc0); acc1 = mfmaL(c1[ks], pf[ks], acc1); }
      if (rho == 0) STAMP(6); else if (rho == 1) STAMP(7); else if (rho == 2) STAMP(8); else STAMP(9);
      if (rho == 2) { LD_UV(6); LD_UV(7); } else if (rho != 3) { LD_C(rho + 2, 6); LD_C(rho + 2, 7); }
      if (rho != 3) vmwait<8>();
    }
    l = x32sum(x16sum(l));
    if (g == 0) stof(B_LS + (kb4 * 32 + 16 * hg + c16) * 4, l);
    {
      const unsigned tb = B_T + w * 4096, n32 = lane & 31;
#pragma unroll
      for (int q4 = 0; q4 < 4; ++q4) {
        sto64(tb + n32 * 128 + ((q4 ^ (n32 & 7)) << 4) + h2 * 8, (v2u){pk(acc0[4 * q4], acc0[4 * q4 + 1]), pk(acc0[4 * q4 + 2], acc0[4 * q4 + 3])});
        sto64(tb + n32 * 128 + (((4 + q4) ^ (n32 & 7)) << 4) + h2 * 8, (v2u){pk(acc1[4 * q4], acc1[4 * q4 + 1]), pk(acc1[4 * q4 + 2], acc1[4 * q4 + 3])});
      }
      lgk0();
      const int hr = lane >> 3, ck = lane & 7;
#pragma unroll
      for (int i = 0; i < 4; ++i)
        gst128(rs, WS_PB + ((b * 32 + sp) * 32 + 8 * i + hr) * 1024 + w * 128 + ck * 16, lds128(tb + (8 * i + hr) * 128 + ((ck ^ hr) << 4)));
    }
    bar();
    if (kb4 == 0 && g == 0) {
      const unsigned la = B_LS + (16 * hg + c16) * 4;
      const float L = (ldsf(la) + ldsf(la + 128)) + (ldsf(la + 256) + ldsf(la + 384));
      gst64(rs, WS_PBM + ((b * 32 + sp) * 32 + 16 * hg + c16) * 8, (v2u){asu(m * SC), asu(L)});
    }
    vmwait<0>();
    bar();
    if (w == 0 && lane == 0) gst32(rs, WS_FP + bid * FS, tag);
    STAMP(10);
    {
      const char* op = Bwo + (size_t)bid * 32768 + w * 4096;
      const unsigned fo = ((w >> 1) & 1) << 3;
      dma<0>(op, (lane ^ fo) << 4, B_OU + w * 4096); dma<1024>(op, (lane ^ (fo | 2u)) << 4, B_OU + w * 4096);
      dma<2048>(op, (lane ^ (fo | 4u)) << 4, B_OU + w * 4096); dma<3072>(op, (lane ^ (fo | 6u)) << 4, B_OU + w * 4096);
    }
    if (w == 0) {
      _Pragma("nounroll") for (int it = 0; it < SPIN_CAP; ++it) { cfence(); if (__all(gld32(rs, WS_FP + lane * FS) == tag)) break; __builtin_amdgcn_s_sleep(4); }
    }
    bar();
    STAMP(11);
    {
      const int gb = lane >> 5, s = lane & 31;
      const v2u ml = gld64(rs, WS_PBM + ((gb * 32 + s) * 32 + h) * 8);
      v4u o[4];
#pragma unroll
      for (int i = 0; i < 4; ++i) o[i] = gld128(rs, WS_PB + ((gb * 32 + 4 * w + i) * 32 + h) * 1024 + ch * 512 + s * 16);
      const float M = x16max(row16max(asf(ml.x)));
      const float wt = ex2(asf(ml.x) - M);
      const float L = x16sum(row16sum(wt * asf(ml.y)));
      const float wn = wt / L;
      float a[8];
#pragma unroll
      for (int k = 0; k < 8; ++k) a[k] = 0.f;
#pragma unroll
      for (int i = 0; i < 4; ++i) {
        const float wi = asf(__builtin_amdgcn_ds_bpermute((gb * 32 + 4 * w + i) * 4, asu(wn)));
#pragma unroll
        for (int k = 0; k < 4; ++k) { a[2 * k] = fmaf(wi, asf(o[i][k] << 16), a[2 * k]); a[2 * k + 1] = fmaf(wi, asf(o[i][k] & 0xffff0000u), a[2 * k + 1]); }
      }
      sto128(B_RED + w * 2048 + lane * 32, (v4u){asu(a[0]), asu(a[1]), asu(a[2]), asu(a[3])});
      sto128(B_RED + w * 2048 + lane * 32 + 16, (v4u){asu(a[4]), asu(a[5]), asu(a[6]), asu(a[7])});
    }
    bar();
    if (tid < 256) {
      float s0 = 0.f, s1 = 0.f;
#pragma unroll
      for (int i = 0; i < 8; ++i) { const f2 t = *(const LAS f2*)(B_RED + i * 2048 + tid * 8); s0 += t.x; s1 += t.y; }
      sto32(B_OL + tid * 4, pk(s0, s1));
    }
    bar();
    {
      const int f3 = qq | ((g & 1) << 2);
      const unsigned a0 = SLOT0 + (8 * g + qq) * 256 + (((2 * w + (pp >> 1)) ^ (f3 << 1)) << 4) + (pp & 1) * 8;
      v4u wf[8], of[8];
#pragma unroll
      for (int s = 0; s < 8; ++s) { wf[s] = trd2(a0 + s * 8192, a0 + s * 8192 + 1024); of[s] = lds128(B_OL + (c16 & 1) * 512 + s * 64 + g * 16); }
      __builtin_amdgcn_sched_barrier(0);
      f4 d = {0.f, 0.f, 0.f, 0.f};
#pragma unroll
      for (int s = 0; s < 8; ++s) d = mfma32(wf[s], of[s], d);
      const int pr = (c16 >> 1) & 1;
      const float v0 = pr ? d[2] : d[0], v1 = pr ? d[3] : d[1];
      if (c16 < 4) gst128(rs, WS_CT + (((h * 2 + ch) * 2 + (c16 & 1)) * 128 + 16 * w + 4 * g + 2 * pr) * 8, (v4u){asu(v0), tag, asu(v1), tag});
    }
    STAMP(12);
    gather_ctx(B_OIMG, w, lane, bid >> 2, bid & 3, rs, tag);
    vmwait<0>();
    bar();
    out_unit(B_OU, B_OIMG, w, lane, 1, bid, rs, tag);
    STAMP(13);
    {
      const unsigned off = WS_PAM + (bid * NSPLIT_A + (bid % NSPLIT_A)) * 16;
      _Pragma("nounroll") for (int it = 0; it < SPIN_CAP; ++it) { cfence(); if (__all(gld64(rs, off).y == tag)) break; __builtin_amdgcn_s_sleep(8); }
    }
    STAMP(14);
    v4u f[4];
    _Pragma("nounroll") for (int it = 0; it < SPIN_CAP; ++it) {
      cfence();
      bool ok = true;
#pragma unroll
      for (int k = 0; k < 4; ++k) { f[k] = gld128(rs, WS_F + ((k >> 1) * 64 + bid) * 16384 + (k & 1) * 8192 + tid * 16); ok &= tagok(f[k], tag); }
      if (__all(ok)) break;
      __builtin_amdgcn_s_sleep(1);
    }
    float r[4] = {asf(f[0].x) + asf(f[1].x), asf(f[0].z) + asf(f[1].z), asf(f[2].x) + asf(f[3].x), asf(f[2].z) + asf(f[3].z)};
#pragma unroll
    for (int k = 0; k < 4; ++k) { r[k] += dpp<ROR8>(r[k]); r[k] = x32sum(x16sum(r[k])); }
    if (lane < 8) { sto64(B_FIN + w * 128 + lane * 8, (v2u){asu(r[0]), asu(r[1])}); sto64(B_FIN + w * 128 + 64 + lane * 8, (v2u){asu(r[2]), asu(r[3])}); }
    bar();
    if (tid < 16) {
      float s0 = 0.f, s1 = 0.f;
#pragma unroll
      for (int i = 0; i < 8; ++i) { const f2 t = *(const LAS f2*)(B_FIN + i * 128 + tid * 8); s0 += t.x; s1 += t.y; }
      const int mat = tid >> 3, ob = (tid >> 2) & 1, pr = tid & 3;
      *(GAS unsigned*)((GAS char*)out + (ob * 1024 + mat * 512 + 8 * bid + 2 * pr) * 2) = pk(s0, s1);
      if (bid == 0 && tid == 0) gst32(rs, WS_E, tag);
    }
    STAMP(15);
    STAMP_OUT();
  }
}

__global__ void k_head(unsigned* p) {
  unsigned long long t_; asm volatile("s_memrealtime %0\n\ts_waitcnt lgkmcnt(0)" : "=s"(t_) :: "memory");
  if (threadIdx.x == 0) *p = (unsigned)t_;
}
extern "C" void kernel_launch(void* const* d_in, const int* in_sizes, int n_in, void* d_out, int out_size, void* d_ws, size_t ws_size,
                              hipStream_t stream) {
  (void)in_sizes; (void)n_in; (void)out_size; (void)ws_size;
  (void)hipFuncSetAttribute((const void*)fused_decode, hipFuncAttributeMaxDynamicSharedMemorySize, LDS_BYTES);
  k_head<<<1, 64, 0, stream>>>((unsigned*)((char*)d_ws + (12u << 20)));
  fused_decode<<<256, 512, LDS_BYTES, stream>>>(
      (const char*)d_in[0], (const char*)d_in[1], (const char*)d_in[2], (const char*)d_in[3], (const char*)d_in[4], (const char*)d_in[5],
      (const char*)d_in[6], (const char*)d_in[7], (const char*)d_in[8], (const char*)d_in[9], (const char*)d_in[10], (unsigned short*)d_out,
      (char*)d_ws);
}
```

```cpp
#define DBG_TS
#define PROBE 4
#define PROLE 0
#define PSTAT 1
#define PZERO 0
#include <hip/hip_runtime.h>
#include <stdint.h>
#pragma clang diagnostic ignored "-Wint-to-pointer-cast"

#define GAS __attribute__((address_space(1)))
#define LAS __attribute__((address_space(3)))
typedef unsigned v4u __attribute__((ext_vector_type(4)));
typedef unsigned v2u __attribute__((ext_vector_type(2)));
typedef float f4 __attribute__((ext_vector_type(4)));
typedef float f2 __attribute__((ext_vector_type(2)));
typedef float f16v __attribute__((ext_vector_type(16)));
typedef short s4 __attribute__((ext_vector_type(4)));
typedef __bf16 b8 __attribute__((ext_vector_type(8)));
typedef __bf16 b2 __attribute__((ext_vector_type(2)));

#define WIN_A 24
#define WB 6
#define CODE_KB 20
#define QGO 20

namespace {

constexpr int NB_CU = 64, NSPLIT_A = 12, NSLOT = 15, NCONS = 7;
constexpr float SC = 0.08838834764831845f * 1.4426950408889634f;
constexpr int SPIN_CAP = 1 << 18;

constexpr unsigned FS = 64;
constexpr unsigned WS_E = 0;
constexpr unsigned WS_FQ = 4096;
constexpr unsigned WS_FP = WS_FQ + 64 * FS;
constexpr unsigned WS_QG = 65536;
constexpr unsigned WS_QAB = WS_QG + 262144;
constexpr unsigned WS_PBM = WS_QAB + 65536;
constexpr unsigned WS_PAM = WS_PBM + 16384;
constexpr unsigned WS_CT = WS_PAM + 16384;
constexpr unsigned WS_PAO = WS_CT + 131072;
constexpr unsigned WS_PB = WS_PAO + 786432;
constexpr unsigned WS_F = WS_PB + 2097152;
constexpr unsigned WS_END = WS_F + 2097152;
constexpr unsigned WS_TS = 8u << 20;

constexpr unsigned LDS_BYTES = 163840;
constexpr unsigned SCR = 131072;
constexpr unsigned A_MISC = NSLOT * 8192;
constexpr unsigned A_LANDED = A_MISC, A_QRDY = A_MISC + 4, A_DONE = A_MISC + 64;
constexpr unsigned A_QIMG = A_MISC + 256;
constexpr unsigned A_ML = A_MISC + 1280;
constexpr unsigned A_OIMG = A_MISC + 1536;
constexpr unsigned A_XIMG = A_MISC + 2048, A_DUMMY = A_MISC + 3072;
constexpr unsigned SLOT0 = 0, SLOT1 = 65536;
constexpr unsigned B_QBI = SCR, B_QAS = SCR + 1024;
constexpr unsigned B_MX = SCR, B_AL = SCR + 512, B_LS = SCR + 640, B_P = SCR + 2048;
constexpr unsigned B_RED = SCR + 8192, B_OL = SCR + 24576, B_OIMG = SCR + 26624, B_FIN = SCR + 27648;
constexpr unsigned B_T = SLOT1, B_OU = SLOT1 + 32768;
constexpr unsigned B_XIMG = SLOT0 + 63 * 1024, B_DUMMY = SLOT0 + 62 * 1024;

__device__ __forceinline__ v4u lds128(unsigned a) { return *(const LAS v4u*)a; }
__device__ __forceinline__ void sto128(unsigned a, v4u v) { *(LAS v4u*)a = v; }
__device__ __forceinline__ void sto64(unsigned a, v2u v) { *(LAS v2u*)a = v; }
__device__ __forceinline__ void sto32(unsigned a, unsigned v) { *(LAS unsigned*)a = v; }
__device__ __forceinline__ float ldsf(unsigned a) { return *(const LAS float*)a; }
__device__ __forceinline__ void stof(unsigned a, float v) { *(LAS float*)a = v; }
__device__ __forceinline__ v2u trd(unsigned a) { return __builtin_bit_cast(v2u, __builtin_amdgcn_ds_read_tr16_b64_v4i16((LAS s4*)a)); }
__device__ __forceinline__ v4u trd2(unsigned a0, unsigned a1) { const v2u x = trd(a0), y = trd(a1); return (v4u){x.x, x.y, y.x, y.y}; }
__device__ __forceinline__ f4 mfma32(v4u a, v4u b, f4 c) {
  return __builtin_amdgcn_mfma_f32_16x16x32_bf16(__builtin_bit_cast(b8, a), __builtin_bit_cast(b8, b), c, 0, 0, 0); }
__device__ __forceinline__ f4 mfma16(v2u a, v2u b, f4 c) {
  return __builtin_amdgcn_mfma_f32_16x16x16bf16_1k(__builtin_bit_cast(s4, a), __builtin_bit_cast(s4, b), c, 0, 0, 0); }
__device__ __forceinline__ f16v mfmaL(v4u a, v4u b, f16v c) {
  return __builtin_amdgcn_mfma_f32_32x32x16_bf16(__builtin_bit_cast(b8, a), __builtin_bit_cast(b8, b), c, 0, 0, 0); }
__device__ __forceinline__ unsigned pk(float lo, float hi) { return __builtin_bit_cast(unsigned, __builtin_convertvector((f2){lo, hi}, b2)); }
__device__ __forceinline__ float asf(unsigned u) { return __builtin_bit_cast(float, u); }
__device__ __forceinline__ unsigned asu(float f) { return __builtin_bit_cast(unsigned, f); }
__device__ __forceinline__ float ex2(float x) { return __builtin_amdgcn_exp2f(x); }
template <int CTRL> __device__ __forceinline__ float dpp(float x) {
  return __builtin_bit_cast(float, __builtin_amdgcn_mov_dpp(__builtin_bit_cast(int, x), CTRL, 0xf, 0xf, true)); }
constexpr int XOR1 = 0xB1, XOR2 = 0x4E, ROR4 = 0x124, ROR8 = 0x128;
__device__ __forceinline__ float x16max(float x) { auto s = __builtin_amdgcn_permlane16_swap(asu(x), asu(x), false, false); return fmaxf(asf(s[0]), asf(s[1])); }
__device__ __forceinline__ float x32max(float x) { auto s = __builtin_amdgcn_permlane32_swap(asu(x), asu(x), false, false); return fmaxf(asf(s[0]), asf(s[1])); }
__device__ __forceinline__ float x16sum(float x) { auto s = __builtin_amdgcn_permlane16_swap(asu(x), asu(x), false, false); return asf(s[0]) + asf(s[1]); }
__device__ __forceinline__ float x32sum(float x) { auto s = __builtin_amdgcn_permlane32_swap(asu(x), asu(x), false, false); return asf(s[0]) + asf(s[1]); }
__device__ __forceinline__ float row16max(float x) { x = fmaxf(x, dpp<XOR1>(x)); x = fmaxf(x, dpp<XOR2>(x)); x = fmaxf(x, dpp<ROR4>(x)); return fmaxf(x, dpp<ROR8>(x)); }
__device__ __forceinline__ float row16sum(float x) { x += dpp<XOR1>(x); x += dpp<XOR2>(x); x += dpp<ROR4>(x); return x + dpp<ROR8>(x); }

template <int IMM> __device__ __forceinline__ void dma(const void* sbase, unsigned voff, unsigned lds) {
  asm volatile("s_mov_b32 m0, %2\n\ts_nop 0\n\tglobal_load_lds_dwordx4 %0, %1 offset:%3 nt" :: "v"(voff), "s"(sbase), "s"(lds), "n"(IMM) : "memory");
}
template <int N> __device__ __forceinline__ void vmwait() { asm volatile("s_waitcnt vmcnt(%0)" :: "n"(N) : "memory"); }
__device__ __forceinline__ void bar() { asm volatile("s_waitcnt lgkmcnt(0)\n\ts_barrier" ::: "memory"); }
__device__ __forceinline__ void lgk0() { asm volatile("s_waitcnt lgkmcnt(0)" ::: "memory"); }
__device__ __forceinline__ void cfence() { asm volatile("" ::: "memory"); }
__device__ __forceinline__ unsigned lds_peek(unsigned a) { return __builtin_amdgcn_readfirstlane(*(volatile LAS unsigned*)a); }
__device__ __forceinline__ void lds_wait_ge(unsigned a, unsigned want) {
  _Pragma("nounroll") for (int it = 0; it < SPIN_CAP * 8; ++it) { if (lds_peek(a) >= want) break; __builtin_amdgcn_s_sleep(1); }
  cfence();
}
typedef __amdgpu_buffer_rsrc_t rsrc_t;
constexpr int SC1 = 16;
__device__ __forceinline__ v4u gld128(rsrc_t r, unsigned off) { return __builtin_amdgcn_raw_buffer_load_b128(r, off, 0, SC1); }
__device__ __forceinline__ v2u gld64(rsrc_t r, unsigned off) { return __builtin_amdgcn_raw_buffer_load_b64(r, off, 0, SC1); }
__device__ __forceinline__ unsigned gld32(rsrc_t r, unsigned off) { return __builtin_amdgcn_raw_buffer_load_b32(r, off, 0, SC1); }
__device__ __forceinline__ void gst128(rsrc_t r, unsigned off, v4u v) { __builtin_amdgcn_raw_buffer_store_b128(v, r, off, 0, SC1); }
__device__ __forceinline__ void gst64(rsrc_t r, unsigned off, v2u v) { __builtin_amdgcn_raw_buffer_store_b64(v, r, off, 0, SC1); }
__device__ __forceinline__ void gst32(rsrc_t r, unsigned off, unsigned v) { __builtin_amdgcn_raw_buffer_store_b32(v, r, off, 0, SC1); }
__device__ __forceinline__ bool tagok(v4u v, unsigned tag) { return v.y == tag && v.w == tag; }

#define STAMP(i) do { unsigned long long t_; asm volatile("s_memrealtime %0\n\ts_waitcnt lgkmcnt(0)" : "=s"(t_) :: "memory"); tsv[i] = (unsigned)t_; } while (0)
#define STAMP_DECL unsigned tsv[16] = {0,0,0,0,0,0,0,0,0,0,0,0,0,0,0,0}
#define STAMP_OUT() do { if (lane == 0) gst128(rs2, WS_TS + (blockIdx.x * 8 + wave) * 16, (v4u){tsv[0], tag, tsv[PROBE], tag}); \
  if (blockIdx.x == 0 && wave == 0) { unsigned mn = 0xffffffffu; float mx = 0.f, sm = 0.f, cnt = 0.f; unsigned ref = tsv[0] - 1000u; \
    for (int i_ = 0; i_ < 32; ++i_) { v4u v_; for (int it = 0; it < SPIN_CAP; ++it) { cfence(); v_ = gld128(rs2, WS_TS + (i_ * 64 + lane) * 16); if (__all(tagok(v_, tag))) break; } \
      mn = min(mn, v_.x - ref); const bool role_ = PROLE ? (i_ < 8) : (i_ >= 8); if (v_.z != 0u && role_) { const float d_ = (float)(v_.z - ref); mx = fmaxf(mx, d_); sm += d_; cnt += 1.f; } } \
    float mnf = -(float)mn; mnf = -x32max(x16max(row16max(mnf))); mx = x32max(x16max(row16max(mx))); sm = x32sum(x16sum(row16sum(sm))); cnt = x32sum(x16sum(row16sum(cnt))); \
    const float X = PZERO ? 0.f : (PSTAT ? sm / cnt : mx) - mnf; const unsigned until = ref + (unsigned)(mnf + 6000.f + 8.f * X); \
    for (int it = 0; it < 100000000; ++it) { unsigned long long t_; asm volatile("s_memrealtime %0\n\ts_waitcnt lgkmcnt(0)" : "=s"(t_) :: "memory"); if ((int)((unsigned)t_ - __builtin_amdgcn_readfirstlane(until)) >= 0) break; } } } while (0)

__device__ __forceinline__ void q_unit(unsigned ximg, int nb, int lane, int mat, int cb, int kh, rsrc_t rs, unsigned tag) {
  const int g = lane >> 4, c16 = lane & 15, q = c16 >> 2, p = lane & 3;
  const int f2_ = ((q >> 1) & 1) | ((g & 1) << 1);
  const unsigned a0 = SCR + (8 * g + q) * 128 + (((2 * nb + (p >> 1)) ^ (f2_ << 1)) << 4) + (p & 1) * 8;
  v4u wf[8], xf[8];
#pragma unroll
  for (int s = 0; s < 8; ++s) { wf[s] = trd2(a0 + s * 4096, a0 + s * 4096 + 512); xf[s] = lds128(ximg + (c16 & 1) * 512 + g * 16 + s * 64); }
  __builtin_amdgcn_sched_barrier(0);
  f4 d = {0.f, 0.f, 0.f, 0.f};
#pragma unroll
  for (int s = 0; s < 8; ++s) d = mfma32(wf[s], xf[s], d);
  const int pr = (c16 >> 1) & 1;
  const float v0 = pr ? d[2] : d[0], v1 = pr ? d[3] : d[1];
  if (c16 < 4) gst128(rs, WS_QG + ((((mat * 2 + kh) * 2 + (c16 & 1)) * 4096) + 64 * cb + 16 * nb + 4 * g + 2 * pr) * 8, (v4u){asu(v0), tag, asu(v1), tag});
}

__device__ __forceinline__ void out_unit(unsigned ou, unsigned oimg, int w, int lane, int mat, int pidx, rsrc_t rs, unsigned tag) {
  const int g = lane >> 4, c16 = lane & 15, q = c16 >> 2, p = lane & 3;
  const int f3 = q | ((g & 1) << 2);
  const v4u ob = lds128(oimg + (c16 & 1) * 64 + g * 16);
  f4 d[4];
  v4u wf[4];
#pragma unroll
  for (int tt = 0; tt < 4; ++tt) {
    const unsigned a0 = ou + (8 * g + q) * 1024 + (((2 * (4 * w + tt) + (p >> 1)) ^ (f3 << 1)) << 4) + (p & 1) * 8;
    wf[tt] = trd2(a0, a0 + 4096);
  }
  __builtin_amdgcn_sched_barrier(0);
#pragma unroll
  for (int tt = 0; tt < 4; ++tt) d[tt] = mfma32(wf[tt], ob, (f4){0.f, 0.f, 0.f, 0.f});
  const int tt = (c16 >> 1) & 3, pr = c16 >> 3;
  const f4 e = tt == 0 ? d[0] : tt == 1 ? d[1] : tt == 2 ? d[2] : d[3];
  const float v0 = pr ? e[2] : e[0], v1 = pr ? e[3] : e[1];
  const int n = 64 * w + 16 * tt + 4 * g + 2 * pr;
  gst128(rs, WS_F + ((((mat * 64 + (n >> 3)) * 128 + pidx) * 16) + (c16 & 1) * 8 + (n & 7)) * 8, (v4u){asu(v0), tag, asu(v1), tag});
}

__device__ __forceinline__ void gather_ctx(unsigned oimg, int w, int lane, int h, int dq, rsrc_t rs, unsigned tag) {
  const int b = lane >> 5, pr = (lane >> 4) & 1, s = lane & 15;
  const unsigned off = WS_CT + (((h * 2 + (s & 1)) * 2 + b) * 128 + 32 * dq + 4 * w + 2 * pr) * 8;
  v4u v;
  _Pragma("nounroll") for (int it = 0; it < SPIN_CAP; ++it) { cfence(); v = gld128(rs, off); if (__all(tagok(v, tag))) break; __builtin_amdgcn_s_sleep(2); }
  const float o0 = asf(v.x) + dpp<XOR1>(asf(v.x)), o1 = asf(v.z) + dpp<XOR1>(asf(v.z));
  if (s == 0) sto32(oimg + b * 64 + (4 * w + 2 * pr) * 2, pk(o0, o1));
}

}

__global__ void __launch_bounds__(512, 2) fused_decode(
    const char* __restrict__ Ax, const char* __restrict__ Kc, const char* __restrict__ Vc, const char* __restrict__ Wq, const char* __restrict__ Wo,
    const char* __restrict__ Bx, const char* __restrict__ Ckv, const char* __restrict__ Bwq, const char* __restrict__ Wuk, const char* __restrict__ Wuv,
    const char* __restrict__ Bwo, unsigned short* __restrict__ out, char* __restrict__ ws) {
  const int bid = blockIdx.x, tid = threadIdx.x, lane = tid & 63, wave = __builtin_amdgcn_readfirstlane(tid >> 6);
  const int g = lane >> 4, c16 = lane & 15;
  STAMP_DECL;
  STAMP(0);
  const rsrc_t rs = __builtin_amdgcn_make_buffer_rsrc(ws, 0, WS_END, 0x00020000);
  const rsrc_t rs2 = __builtin_amdgcn_make_buffer_rsrc(ws, 0, 16u << 20, 0x00020000);
  asm volatile("" :: "s"(Ax), "s"(Kc), "s"(Vc), "s"(Wq), "s"(Wo), "s"(Bx), "s"(Ckv), "s"(Bwq), "s"(Wuk), "s"(Wuv), "s"(Bwo), "s"(out), "s"(ws));
  unsigned tag;
#define LOAD_TAG() asm volatile("s_load_dword %0, %1, 0x0" : "=s"(tag) : "s"(ws) : "memory")
#define WAIT_TAG() do { asm volatile("s_waitcnt lgkmcnt(0)" : "+s"(tag) :: "memory"); tag += 1u; } while (0)
  const int qmat = bid & 1, qkh = (bid >> 1) & 1, qcb = bid >> 2;
  const char* qW = (qmat ? Bwq : Wq) + (size_t)(256 * qkh) * 8192 + qcb * 128;
  const char* qx = (qmat ? Bx : Ax) + 512 * qkh;
  const unsigned vx = (lane >> 5) * 1024 + (lane & 31) * 16;
  unsigned long long pc;
  asm volatile("s_getpc_b64 %0" : "=s"(pc));
  const char* codep = (const char*)((pc & ~255ull) + (unsigned)(((bid >> 3) % CODE_KB) * 1024));
  const unsigned vq0 = (lane >> 3) * 8192 + (((lane & 7) ^ (((lane >> 4) & 1) << 1)) << 4);

  if (bid >= NB_CU) {
    const int iA = bid - NB_CU, pair = iA & 15, split = iA >> 4, b = pair >> 3, kvh = pair & 7;
    const int nblk = split < 8 ? 43 : 42, blk0 = split < 8 ? 43 * split : 344 + 42 * (split - 8);
    const int omat = iA < 64 ? 1 : 0, ounit = iA < 64 ? 64 + iA : iA - 64;
    if (wave == 0) {
      __builtin_amdgcn_s_setprio(3);
      constexpr int W = WIN_A, IST = (W - 1) & 3;
      static_assert(W >= 4 && W <= 32, "window");
      int pub = 0, p = 0;
#define PUBLISH(val) do { const int v_ = (val); if (v_ > pub) { pub = v_; if (lane == 0) *(volatile LAS unsigned*)A_LANDED = (unsigned)v_; } } while (0)
#define PACE(i) do { vmwait<W - 1>(); if ((i) == IST) PUBLISH((4 * p + IST + 1 - W) / 4); } while (0)
      if (lane < 32) sto32(A_MISC + lane * 4, 0u);
      dma<0>(qx, vx, A_XIMG);
      dma<0>(codep, lane * 16, A_DUMMY);
#pragma unroll
      for (int u = 0; u < 4; ++u) dma<0>(qW + (size_t)u * 65536, vq0 ^ ((u & 1) << 6), SCR + u * 1024);
      bar();
#pragma nounroll
      for (p = 1; p < 8; ++p) {
#pragma unroll
        for (int i = 0; i < 4; ++i) dma<0>(qW + (size_t)(4 * p + i) * 65536, vq0 ^ ((i & 1) << 6), SCR + (4 * p + i) * 1024);
      }
      LOAD_TAG();
      STAMP(9);
      const char* kp = Kc + ((size_t)pair * 8192 + blk0 * 16) * 256;
      const char* vp = Vc + ((size_t)pair * 8192 + blk0 * 16) * 256;
      const unsigned vk0 = g * 256 + ((c16 ^ g) << 4), vv0 = g * 256 + ((c16 ^ (g << 1)) << 4);
      dma<0>(kp, vk0, 0); dma<1024>(kp, vk0 ^ 64, 0); dma<2048>(kp, vk0 ^ 128, 0); dma<3072>(kp, vk0 ^ 192, 0);
      dma<0>(vp, vv0, 4096); dma<1024>(vp, vv0 ^ 128, 4096); dma<2048>(vp, vv0, 4096); dma<3072>(vp, vv0 ^ 128, 4096);
      kp += 4096; vp += 4096;
      vmwait<39>();
      STAMP(10);
      vmwait<8>();
      PUBLISH(8);
      STAMP(11);
      p = 10;
      int slot = 1;
      for (int j = 1; j < nblk; ++j) {
        if (j >= NSLOT) {
          const unsigned need = j - NSLOT + 1;
          if (lds_peek(A_DONE + slot * 4) < need) { vmwait<0>(); PUBLISH(p); lds_wait_ge(A_DONE + slot * 4, need); }
        }
        const unsigned sb = slot * 8192;
        PACE(0); dma<0>(kp, vk0, sb); PACE(1); dma<1024>(kp, vk0 ^ 64, sb); PACE(2); dma<2048>(kp, vk0 ^ 128, sb); PACE(3); dma<3072>(kp, vk0 ^ 192, sb);
        ++p;
        PACE(0); dma<0>(vp, vv0, sb + 4096); PACE(1); dma<1024>(vp, vv0 ^ 128, sb + 4096); PACE(2); dma<2048>(vp, vv0, sb + 4096); PACE(3); dma<3072>(vp, vv0 ^ 128, sb + 4096);
        ++p;
        kp += 4096; vp += 4096;
        slot = slot == NSLOT - 1 ? 0 : slot + 1;
      }
      lds_wait_ge(A_QRDY, 1u);
      const char* op = (omat ? Bwo : Wo) + (size_t)ounit * 32768;
#pragma unroll
      for (int k = 0; k < 32; ++k) {
        if (k < W) { vmwait<W - 1>(); if ((k & 3) == IST) PUBLISH(p + (k + 1 - W) / 4); }
        dma<0>(op + k * 1024, (lane ^ (((k & 3) | (((k >> 3) & 1) << 2)) << 1)) << 4, SCR + k * 1024);
      }
      __builtin_amdgcn_s_setprio(0);
      WAIT_TAG();
      STAMP(5);
#undef PACE
#undef PUBLISH
    } else {
      bar();
      LOAD_TAG();
      WAIT_TAG();
      if (wave <= 4) {
        lds_wait_ge(A_LANDED, 8u);
        STAMP(1);
        q_unit(A_XIMG, wave - 1, lane, qmat, qcb, qkh, rs, tag);
        lgk0();
        if (lane == 0) __hip_atomic_fetch_add((LAS unsigned*)(A_MISC + 8), 1u, __ATOMIC_RELAXED, __HIP_MEMORY_SCOPE_WORKGROUP);
        STAMP(2);
      }
      if (wave == 5) {
        lds_wait_ge(A_LANDED, (unsigned)QGO);
        v4u v[8];
        const unsigned off = WS_QG + (b * 4096 + kvh * 512 + 2 * lane) * 8;
        _Pragma("nounroll") for (int it = 0; it < SPIN_CAP; ++it) {
          cfence();
          bool ok = true;
#pragma unroll
          for (int k = 0; k < 8; ++k) { v[k] = gld128(rs, off + (k & 1) * 65536 + (k >> 1) * 1024); ok &= tagok(v[k], tag); }
          if (__all(ok)) break;
          __builtin_amdgcn_s_sleep(8);
        }
#pragma unroll
        for (int hh = 0; hh < 4; ++hh) sto32(A_QIMG + hh * 256 + lane * 4, pk(asf(v[2 * hh].x) + asf(v[2 * hh + 1].x), asf(v[2 * hh].z) + asf(v[2 * hh + 1].z)));
        lds_wait_ge(A_MISC + 8, 4u);
        lgk0();
        if (lane == 0) *(volatile LAS unsigned*)A_QRDY = 1u;
      }
      lds_wait_ge(A_QRDY, 1u);
      STAMP(3);
      v4u qf[4];
#pragma unroll
      for (int s = 0; s < 4; ++s) qf[s] = lds128(A_QIMG + (c16 & 3) * 256 + s * 64 + g * 16);
      const unsigned ko = c16 * 256 + ((g ^ c16) << 4);
      const int vr = 4 * g + (c16 >> 2);
      const unsigned vo = 4096 + vr * 256 + ((((lane & 3) >> 1) ^ ((vr & 7) << 1)) << 4) + (lane & 1) * 8;
      float m = -INFINITY, l = 0.f;
      f4 acc[8];
#pragma unroll
      for (int t = 0; t < 8; ++t) acc[t] = (f4){0.f, 0.f, 0.f, 0.f};
      int slot = wave - 1, j = wave - 1;
      for (; j < nblk; j += NCONS) {
        const unsigned sb = slot * 8192;
        lds_wait_ge(A_LANDED, 9u + 2 * j);
        v4u kf[4];
#pragma unroll
        for (int s = 0; s < 4; ++s) kf[s] = lds128(sb + (ko ^ (s << 6)));
        __builtin_amdgcn_sched_barrier(0);
        f4 s4_ = {0.f, 0.f, 0.f, 0.f};
#pragma unroll
        for (int s = 0; s < 4; ++s) s4_ = mfma32(kf[s], qf[s], s4_);
        float mx = fmaxf(fmaxf(s4_[0], s4_[1]), fmaxf(s4_[2], s4_[3]));
        mx = x32max(x16max(mx));
        const float mn = fmaxf(m, mx), al = ex2((m - mn) * SC), mc = -mn * SC;
        const float p0 = ex2(fmaf(s4_[0], SC, mc)), p1 = ex2(fmaf(s4_[1], SC, mc)), p2 = ex2(fmaf(s4_[2], SC, mc)), p3 = ex2(fmaf(s4_[3], SC, mc));
        l = fmaf(l, al, (p0 + p1) + (p2 + p3));
        m = mn;
        const v2u pb = {pk(p0, p1), pk(p2, p3)};
#pragma unroll
        for (int t = 0; t < 8; ++t) acc[t] *= al;
        lds_wait_ge(A_LANDED, 10u + 2 * j);
        v2u vf[8];
#pragma unroll
        for (int t = 0; t < 8; ++t) vf[t] = trd(sb + (vo ^ (t << 5)));
        lgk0();
        if (lane == 0) *(volatile LAS unsigned*)(A_DONE + slot * 4) = (unsigned)(j + 1);
#pragma unroll
        for (int t = 0; t < 8; ++t) acc[t] = mfma16(vf[t], pb, acc[t]);
        if (j + NCONS < nblk) { slot += NCONS; if (slot >= NSLOT) slot -= NSLOT; }
      }
      STAMP(4);
      l = x32sum(x16sum(l));
      if (c16 < 4) {
#pragma unroll
        for (int t = 0; t < 8; ++t) sto128(slot * 8192 + c16 * 512 + t * 64 + g * 16, __builtin_bit_cast(v4u, acc[t]));
        if (g == 0) sto64(A_ML + ((wave - 1) * 4 + c16) * 8, (v2u){asu(m * SC), asu(l)});
      }
    }
    bar();
    if (wave >= 4) {
      const int hh = wave - 4;
      float mw[NCONS], lw[NCONS];
      f2 ow[NCONS];
#pragma unroll
      for (int c = 0; c < NCONS; ++c) {
        const int jl = c + ((nblk - 1 - c) / NCONS) * NCONS;
        const f2 t = *(const LAS f2*)(A_ML + (c * 4 + hh) * 8);
        mw[c] = t.x; lw[c] = t.y;
        ow[c] = *(const LAS f2*)((jl % NSLOT) * 8192 + hh * 512 + lane * 8);
      }
      float M = mw[0];
#pragma unroll
      for (int c = 1; c < NCONS; ++c) M = fmaxf(M, mw[c]);
      float L = 0.f, o0 = 0.f, o1 = 0.f;
#pragma unroll
      for (int c = 0; c < NCONS; ++c) { const float wt = ex2(mw[c] - M); L = fmaf(wt, lw[c], L); o0 = fmaf(wt, ow[c].x, o0); o1 = fmaf(wt, ow[c].y, o1); }
      const int rec = (b * 32 + kvh * 4 + hh) * NSPLIT_A + split;
      gst128(rs, WS_PAO + rec * 1024 + lane * 16, (v4u){asu(o0), tag, asu(o1), tag});
      if (lane == 0) gst128(rs, WS_PAM + rec * 16, (v4u){asu(M), tag, asu(L), tag});
    }
    STAMP(6);
    const int oh = ounit >> 2, odq = ounit & 3;
    if (omat) {
      gather_ctx(A_OIMG, wave, lane, oh, odq, rs, tag);
    } else {
      const int gb = lane >> 5, pr = (lane >> 4) & 1, s = c16 < NSPLIT_A ? c16 : NSPLIT_A - 1;
      const int rec = (gb * 32 + oh) * NSPLIT_A + s;
      v4u v, ml;
      _Pragma("nounroll") for (int it = 0; it < SPIN_CAP; ++it) {
        cfence();
        v = gld128(rs, WS_PAO + rec * 1024 + (32 * odq + 4 * wave + 2 * pr) * 8);
        ml = gld128(rs, WS_PAM + rec * 16);
        if (__all(tagok(v, tag) && tagok(ml, tag))) break;
        __builtin_amdgcn_s_sleep(1);
      }
      const float M = row16max(asf(ml.x));
      const float wt = c16 < NSPLIT_A ? ex2(asf(ml.x) - M) : 0.f;
      const float L = row16sum(wt * asf(ml.z)), o0 = row16sum(wt * asf(v.x)), o1 = row16sum(wt * asf(v.z));
      const float inv = 1.f / L;
      if (c16 == 0) sto32(A_OIMG + gb * 64 + (4 * wave + 2 * pr) * 2, pk(o0 * inv, o1 * inv));
    }
    vmwait<0>();
    STAMP(7);
    bar();
    out_unit(SCR, A_OIMG, wave, lane, omat, ounit, rs, tag);
    STAMP(8);
    STAMP_OUT();
  } else {
    const int b = bid >> 5, sp = bid & 31, h = bid >> 1, ch = bid & 1, w = wave;
    const char* uk = Wuk + (size_t)(256 * ch + 32 * w) * 8192 + h * 256;
    const char* uv = Wuv + (size_t)(256 * ch + 32 * w) * 8192 + h * 256;
    const char* cp = Ckv + ((size_t)b * 8192 + 256 * sp + 8 * w) * 1024;
    const unsigned vuk = g * 8192 + ((c16 ^ g) << 4), vuv = g * 8192 + ((c16 ^ (g << 1)) << 4);
    const unsigned vc0 = (lane ^ ((w & 1) ? 3 : 0)) << 4, vc1 = (lane ^ ((w & 1) ? 1 : 2)) << 4;
#define LD_UK(u) dma<0>(uk + (size_t)(u) * 32768, vuk ^ (((u) & 3) << 6), SLOT1 + (32 * w + 4 * (u)) * 256)
#define LD_UV(u) dma<0>(uv + (size_t)(u) * 32768, vuv ^ ((((u) >> 1) & 1) << 7), SLOT0 + (32 * w + 4 * (u)) * 256)
#define LD_C(rho, u) dma<((u) & 3) * 1024>(cp + (size_t)(rho) * 65536 + ((u) >> 2) * 4096, (((u) >> 2) ? vc1 : vc0) ^ (((u) & 3) << 6), ((rho) & 1) * 65536 + w * 8192 + ((u) >> 2) * 4096)
    if (w == 7) { dma<0>(qx, vx, B_XIMG); dma<0>(codep, lane * 16, B_DUMMY); }
#pragma unroll
    for (int i = 0; i < 4; ++i) dma<0>(qW + (size_t)(4 * w + i) * 65536, vq0 ^ ((i & 1) << 6), SCR + (4 * w + i) * 1024);
    asm volatile("s_barrier" ::: "memory");
    LD_UK(0); LD_UK(1);
    LOAD_TAG();
    vmwait<2>();
    WAIT_TAG();
    bar();
    STAMP(1);
    LD_UK(2); LD_UK(3);
    if (WB >= 6) { LD_UK(4); LD_UK(5); }
    v4u qg[4];
    if (w >= 4) {
      q_unit(B_XIMG, w - 4, lane, qmat, qcb, qkh, rs, tag);
    } else if (w == 0) {
      const unsigned off = WS_QG + (4 * 4096 + h * 128 + 2 * lane) * 8;
      _Pragma("nounroll") for (int it = 0; it < SPIN_CAP; ++it) {
        cfence();
        bool ok = true;
#pragma unroll
        for (int k = 0; k < 4; ++k) { qg[k] = gld128(rs, off + (k & 1) * 65536 + (k >> 1) * 32768); ok &= tagok(qg[k], tag); }
        if (__all(ok)) break;
        __builtin_amdgcn_s_sleep(2);
      }
    }
    if (WB < 6) { vmwait<WB - 2>(); LD_UK(4); LD_UK(5); }
    vmwait<WB - 2>(); LD_UK(6); LD_UK(7);
    vmwait<WB - 2>(); LD_C(0, 0); LD_C(0, 1);
    vmwait<WB - 2>(); LD_C(0, 2); LD_C(0, 3);
    bar();
    if (w == 0) {
      sto32(B_QBI + lane * 4, pk(asf(qg[0].x) + asf(qg[1].x), asf(qg[0].z) + asf(qg[1].z)));
      sto32(B_QBI + 256 + lane * 4, pk(asf(qg[2].x) + asf(qg[3].x), asf(qg[2].z) + asf(qg[3].z)));
    }
    vmwait<(WB - 2 < 4 ? WB - 2 : 4)>();
    bar();
    STAMP(2);
    LD_C(0, 4); LD_C(0, 5);
    {
      const unsigned a0 = SLOT1 + (32 * w + c16) * 256 + ((g ^ c16) << 4);
      v4u qb[4], u0[4], u1[4];
#pragma unroll
      for (int s = 0; s < 4; ++s) { qb[s] = lds128(B_QBI + (c16 & 1) * 256 + s * 64 + g * 16); u0[s] = lds128(a0 ^ (s << 6)); u1[s] = lds128((a0 ^ (s << 6)) + 4096); }
      __builtin_amdgcn_sched_barrier(0);
      f4 d0 = {0.f, 0.f, 0.f, 0.f}, d1 = d0;
#pragma unroll
      for (int s = 0; s < 4; ++s) { d0 = mfma32(u0[s], qb[s], d0); d1 = mfma32(u1[s], qb[s], d1); }
      const int rb = (c16 >> 1) & 1;
      const f4 e = rb ? d1 : d0;
      if (c16 < 4) sto64(B_QAS + (c16 & 1) * 512 + (32 * w + 16 * rb + 4 * g) * 2, (v2u){pk(e[0], e[1]), pk(e[2], e[3])});
    }
    bar();
    if (w == 0) {
      gst128(rs, WS_QAB + ((lane >> 5) * 32 + h) * 1024 + ch * 512 + (lane & 31) * 16, lds128(B_QAS + lane * 16));
      vmwait<0>();
      if (lane == 0) gst32(rs, WS_FQ + bid * FS, tag);
    }
    STAMP(3);
    vmwait<WB - 2>(); LD_C(0, 6); LD_C(0, 7);
    vmwait<WB - 2>(); LD_C(1, 0); LD_C(1, 1);
    vmwait<WB - 2>(); LD_C(1, 2); LD_C(1, 3);
    if (w == 0) {
      _Pragma("nounroll") for (int it = 0; it < SPIN_CAP; ++it) { cfence(); if (__all(gld32(rs, WS_FQ + lane * FS) == tag)) break; __builtin_amdgcn_s_sleep(2); }
    }
    bar();
    STAMP(4);
    {
      v4u t[4];
#pragma unroll
      for (int i = 0; i < 4; ++i) t[i] = gld128(rs, WS_QAB + (b * 32 + 4 * w + i) * 1024 + lane * 16);
#pragma unroll
      for (int i = 0; i < 4; ++i) sto128(SCR + (4 * w + i) * 1024 + ((lane ^ ((4 * w + i) & 15)) << 4), t[i]);
    }
    LD_C(1, 4); LD_C(1, 5); LD_C(1, 6); LD_C(1, 7);
    bar();
    const int kb4 = w & 3, hg = w >> 2;
    v4u qf[16];
#pragma unroll
    for (int s = 0; s < 16; ++s) qf[s] = lds128(SCR + (16 * hg + c16) * 1024 + ((((4 * s) & 15) ^ g ^ c16) << 4) + (s >> 2) * 256);
    STAMP(5);
    const unsigned fk = ((c16 & 3) << 2) | ((0x78u >> (2 * (c16 >> 2))) & 3u);
    const unsigned ka = (16 * kb4 + c16) * 1024 + ((g ^ fk) << 4);
    const int h2 = lane >> 5, blk = g & 1, qq = c16 >> 2, pp = lane & 3;
    const unsigned ft0 = (qq << 2) | (h2 ? 3u : 0u), ft1 = (qq << 2) | (h2 ? 1u : 2u);
    const unsigned cbase = 8 * w + 2 * blk + (pp >> 1);
    const unsigned va00 = (8 * h2 + qq) * 1024 + ((cbase ^ ft0) << 4) + (pp & 1) * 8, va01 = (8 * h2 + 4 + qq) * 1024 + ((cbase ^ ft1) << 4) + (pp & 1) * 8;
    float m = -INFINITY, l = 0.f;
    f16v acc0, acc1;
#pragma unroll
    for (int i = 0; i < 16; ++i) { acc0[i] = 0.f; acc1[i] = 0.f; }
    vmwait<8>();
#pragma nounroll
    for (int rho = 0; rho < 4; ++rho) {
      const unsigned sb = (rho & 1) * 65536;
      bar();
      f4 s4_ = {0.f, 0.f, 0.f, 0.f};
      {
        v4u kf[16];
#pragma unroll
        for (int s = 0; s < 16; ++s) kf[s] = lds128(sb + (ka ^ ((s & 3) << 6)) + (s >> 2) * 256);
        __builtin_amdgcn_sched_barrier(0);
#pragma unroll
        for (int s = 0; s < 16; ++s) s4_ = mfma32(kf[s], qf[s], s4_);
      }
      float mx = fmaxf(fmaxf(s4_[0], s4_[1]), fmaxf(s4_[2], s4_[3]));
      mx = x32max(x16max(mx));
      if (g == 0) stof(B_MX + (kb4 * 32 + 16 * hg + c16) * 4, mx);
      bar();
      const unsigned ma = B_MX + (16 * hg + c16) * 4;
      const float mn = fmaxf(fmaxf(m, fmaxf(ldsf(ma), ldsf(ma + 128))), fmaxf(ldsf(ma + 256), ldsf(ma + 384)));
      const float al = ex2((m - mn) * SC), mc = -mn * SC;
      const float p0 = ex2(fmaf(s4_[0], SC, mc)), p1 = ex2(fmaf(s4_[1], SC, mc)), p2 = ex2(fmaf(s4_[2], SC, mc)), p3 = ex2(fmaf(s4_[3], SC, mc));
      l = fmaf(l, al, (p0 + p1) + (p2 + p3));
      m = mn;
      sto64(B_P + kb4 * 1024 + (16 * hg + c16) * 32 + g * 8, (v2u){pk(p0, p1), pk(p2, p3)});
      if (kb4 == 0 && g == 0) stof(B_AL + (16 * hg + c16) * 4, al);
      v4u c0[4], c1[4];
#pragma unroll
      for (int ks = 0; ks < 4; ++ks) {
        c0[ks] = trd2(sb + ks * 16384 + va00, sb + ks * 16384 + va01);
        c1[ks] = trd2(sb + ks * 16384 + (va00 ^ 64), sb + ks * 16384 + (va01 ^ 64));
      }
      bar();
      if (rho == 2) { LD_UV(0); LD_UV(1); LD_UV(2); LD_UV(3); LD_UV(4); LD_UV(5); }
      else if (rho != 3) { LD_C(rho + 2, 0); LD_C(rho + 2, 1); LD_C(rho + 2, 2); LD_C(rho + 2, 3); LD_C(rho + 2, 4); LD_C(rho + 2, 5); }
      const float alp = ldsf(B_AL + (lane & 31) * 4);
      v4u pf[4];
#pragma unroll
      for (int ks = 0; ks < 4; ++ks) pf[ks] = lds128(B_P + ks * 1024 + (lane & 31) * 32 + h2 * 16);
      __builtin_amdgcn_sched_barrier(0);
      acc0 *= alp; acc1 *= alp;
#pragma unroll
      for (int ks = 0; ks < 4; ++ks) { acc0 = mfmaL(c0[ks], pf[ks], acc0); acc1 = mfmaL(c1[ks], pf[ks], acc1); }
      if (rho == 0) STAMP(6); else if (rho == 1) STAMP(7); else if (rho == 2) STAMP(8); else STAMP(9);
      if (rho == 2) { LD_UV(6); LD_UV(7); } else if (rho != 3) { LD_C(rho + 2, 6); LD_C(rho + 2, 7); }
      if (rho != 3) vmwait<8>();
    }
    l = x32sum(x16sum(l));
    if (g == 0) stof(B_LS + (kb4 * 32 + 16 * hg + c16) * 4, l);
    {
      const unsigned tb = B_T + w * 4096, n32 = lane & 31;
#pragma unroll
      for (int q4 = 0; q4 < 4; ++q4) {
        sto64(tb + n32 * 128 + ((q4 ^ (n32 & 7)) << 4) + h2 * 8, (v2u){pk(acc0[4 * q4], acc0[4 * q4 + 1]), pk(acc0[4 * q4 + 2], acc0[4 * q4 + 3])});
        sto64(tb + n32 * 128 + (((4 + q4) ^ (n32 & 7)) << 4) + h2 * 8, (v2u){pk(acc1[4 * q4], acc1[4 * q4 + 1]), pk(acc1[4 * q4 + 2], acc1[4 * q4 + 3])});
      }
      lgk0();
      const int hr = lane >> 3, ck = lane & 7;
#pragma unroll
      for (int i = 0; i < 4; ++i)
        gst128(rs, WS_PB + ((b * 32 + sp) * 32 + 8 * i + hr) * 1024 + w * 128 + ck * 16, lds128(tb + (8 * i + hr) * 128 + ((ck ^ hr) << 4)));
    }
    bar();
    if (kb4 == 0 && g == 0) {
      const unsigned la = B_LS + (16 * hg + c16) * 4;
      const float L = (ldsf(la) + ldsf(la + 128)) + (ldsf(la + 256) + ldsf(la + 384));
      gst64(rs, WS_PBM + ((b * 32 + sp) * 32 + 16 * hg + c16) * 8, (v2u){asu(m * SC), asu(L)});
    }
    vmwait<0>();
    bar();
    if (w == 0 && lane == 0) gst32(rs, WS_FP + bid * FS, tag);
    STAMP(10);
    {
      const char* op = Bwo + (size_t)bid * 32768 + w * 4096;
      const unsigned fo = ((w >> 1) & 1) << 3;
      dma<0>(op, (lane ^ fo) << 4, B_OU + w * 4096); dma<1024>(op, (lane ^ (fo | 2u)) << 4, B_OU + w * 4096);
      dma<2048>(op, (lane ^ (fo | 4u)) << 4, B_OU + w * 4096); dma<3072>(op, (lane ^ (fo | 6u)) << 4, B_OU + w * 4096);
    }
    if (w == 0) {
      _Pragma("nounroll") for (int it = 0; it < SPIN_CAP; ++it) { cfence(); if (__all(gld32(rs, WS_FP + lane * FS) == tag)) break; __builtin_amdgcn_s_sleep(4); }
    }
    bar();
    STAMP(11);
    {
      const int gb = lane >> 5, s = lane & 31;
      const v2u ml = gld64(rs, WS_PBM + ((gb * 32 + s) * 32 + h) * 8);
      v4u o[4];
#pragma unroll
      for (int i = 0; i < 4; ++i) o[i] = gld128(rs, WS_PB + ((gb * 32 + 4 * w + i) * 32 + h) * 1024 + ch * 512 + s * 16);
      const float M = x16max(row16max(asf(ml.x)));
      const float wt = ex2(asf(ml.x) - M);
      const float L = x16sum(row16sum(wt * asf(ml.y)));
      const float wn = wt / L;
      float a[8];
#pragma unroll
      for (int k = 0; k < 8; ++k) a[k] = 0.f;
#pragma unroll
      for (int i = 0; i < 4; ++i) {
        const float wi = asf(__builtin_amdgcn_ds_bpermute((gb * 32 + 4 * w + i) * 4, asu(wn)));
#pragma unroll
        for (int k = 0; k < 4; ++k) { a[2 * k] = fmaf(wi, asf(o[i][k] << 16), a[2 * k]); a[2 * k + 1] = fmaf(wi, asf(o[i][k] & 0xffff0000u), a[2 * k + 1]); }
      }
      sto128(B_RED + w * 2048 + lane * 32, (v4u){asu(a[0]), asu(a[1]), asu(a[2]), asu(a[3])});
      sto128(B_RED + w * 2048 + lane * 32 + 16, (v4u){asu(a[4]), asu(a[5]), asu(a[6]), asu(a[7])});
    }
    bar();
    if (tid < 256) {
      float s0 = 0.f, s1 = 0.f;
#pragma unroll
      for (int i = 0; i < 8; ++i) { const f2 t = *(const LAS f2*)(B_RED + i * 2048 + tid * 8); s0 += t.x; s1 += t.y; }
      sto32(B_OL + tid * 4, pk(s0, s1));
    }
    bar();
    {
      const int f3 = qq | ((g & 1) << 2);
      const unsigned a0 = SLOT0 + (8 * g + qq) * 256 + (((2 * w + (pp >> 1)) ^ (f3 << 1)) << 4) + (pp & 1) * 8;
      v4u wf[8], of[8];
#pragma unroll
      for (int s = 0; s < 8; ++s) { wf[s] = trd2(a0 + s * 8192, a0 + s * 8192 + 1024); of[s] = lds128(B_OL + (c16 & 1) * 512 + s * 64 + g * 16); }
      __builtin_amdgcn_sched_barrier(0);
      f4 d = {0.f, 0.f, 0.f, 0.f};
#pragma unroll
      for (int s = 0; s < 8; ++s) d = mfma32(wf[s], of[s], d);
      const int pr = (c16 >> 1) & 1;
      const float v0 = pr ? d[2] : d[0], v1 = pr ? d[3] : d[1];
      if (c16 < 4) gst128(rs, WS_CT + (((h * 2 + ch) * 2 + (c16 & 1)) * 128 + 16 * w + 4 * g + 2 * pr) * 8, (v4u){asu(v0), tag, asu(v1), tag});
    }
    STAMP(12);
    gather_ctx(B_OIMG, w, lane, bid >> 2, bid & 3, rs, tag);
    vmwait<0>();
    bar();
    out_unit(B_OU, B_OIMG, w, lane, 1, bid, rs, tag);
    STAMP(13);
    {
      const unsigned off = WS_PAM + (bid * NSPLIT_A + (bid % NSPLIT_A)) * 16;
      _Pragma("nounroll") for (int it = 0; it < SPIN_CAP; ++it) { cfence(); if (__all(gld64(rs, off).y == tag)) break; __builtin_amdgcn_s_sleep(8); }
    }
    STAMP(14);
    v4u f[4];
    _Pragma("nounroll") for (int it = 0; it < SPIN_CAP; ++it) {
      cfence();
      bool ok = true;
#pragma unroll
      for (int k = 0; k < 4; ++k) { f[k] = gld128(rs, WS_F + ((k >> 1) * 64 + bid) * 16384 + (k & 1) * 8192 + tid * 16); ok &= tagok(f[k], tag); }
      if (__all(ok)) break;
      __builtin_amdgcn_s_sleep(1);
    }
    float r[4] = {asf(f[0].x) + asf(f[1].x), asf(f[0].z) + asf(f[1].z), asf(f[2].x) + asf(f[3].x), asf(f[2].z) + asf(f[3].z)};
#pragma unroll
    for (int k = 0; k < 4; ++k) { r[k] += dpp<ROR8>(r[k]); r[k] = x32sum(x16sum(r[k])); }
    if (lane < 8) { sto64(B_FIN + w * 128 + lane * 8, (v2u){asu(r[0]), asu(r[1])}); sto64(B_FIN + w * 128 + 64 + lane * 8, (v2u){asu(r[2]), asu(r[3])}); }
    bar();
    if (tid < 16) {
      float s0 = 0.f, s1 = 0.f;
#pragma unroll
      for (int i = 0; i < 8; ++i) { const f2 t = *(const LAS f2*)(B_FIN + i * 128 + tid * 8); s0 += t.x; s1 += t.y; }
      const int mat = tid >> 3, ob = (tid >> 2) & 1, pr = tid & 3;
      *(GAS unsigned*)((GAS char*)out + (ob * 1024 + mat * 512 + 8 * bid + 2 * pr) * 2) = pk(s0, s1);
      if (bid == 0 && tid == 0) gst32(rs, WS_E, tag);
    }
    STAMP(15);
    STAMP_OUT();
  }
}

extern "C" void kernel_launch(void* const* d_in, const int* in_sizes, int n_in, void* d_out, int out_size, void* d_ws, size_t ws_size,
                              hipStream_t stream) {
  (void)in_sizes; (void)n_in; (void)out_size; (void)ws_size;
  (void)hipFuncSetAttribute((const void*)fused_decode, hipFuncAttributeMaxDynamicSharedMemorySize, LDS_BYTES);
  fused_decode<<<256, 512, LDS_BYTES, stream>>>(
      (const char*)d_in[0], (const char*)d_in[1], (const char*)d_in[2], (const char*)d_in[3], (const char*)d_in[4], (const char*)d_in[5],
      (const char*)d_in[6], (const char*)d_in[7], (const char*)d_in[8], (const char*)d_in[9], (const char*)d_in[10], (unsigned short*)d_out,
      (char*)d_ws);
}
```

```cpp
#include <hip/hip_runtime.h>
#include <stdint.h>
#pragma clang diagnostic ignored "-Wint-to-pointer-cast"

#define GAS __attribute__((address_space(1)))
#define LAS __attribute__((address_space(3)))
typedef unsigned v4u __attribute__((ext_vector_type(4)));
typedef unsigned v2u __attribute__((ext_vector_type(2)));
typedef float f4 __attribute__((ext_vector_type(4)));
typedef float f2 __attribute__((ext_vector_type(2)));
typedef float f16v __attribute__((ext_vector_type(16)));
typedef short s4 __attribute__((ext_vector_type(4)));
typedef __bf16 b8 __attribute__((ext_vector_type(8)));
typedef __bf16 b2 __attribute__((ext_vector_type(2)));

#define W1 24
#define W2 32
#define J1 18
#define DLY2 40
#define DLYP 0
#define DLYB 20
#define NBP 53
#define WB 6
#define CODE_KB 28
#define QGO 6

namespace {

constexpr int NB_CU = 64, NT_CU = 32, NSPLIT_A = 10, NSLOT = 15, NCONS = 7;
constexpr float SC = 0.08838834764831845f * 1.4426950408889634f;
constexpr int SPIN_CAP = 1 << 18;

#define OUE 0
#define PSL 8
#define PRESL 32
#define GSL 1
#define FSL 4
constexpr unsigned FS = 128;
constexpr unsigned WS_E = 0;
constexpr unsigned WS_FQ = 4096;
constexpr unsigned WS_FP = WS_FQ + 16384;
constexpr unsigned WS_QG = 65536;
constexpr unsigned WS_QAB = WS_QG + 262144;
constexpr unsigned WS_PBM = WS_QAB + 65536;
constexpr unsigned WS_PAM = WS_PBM + 16384;
constexpr unsigned WS_PAO = WS_PAM + 131072;
constexpr unsigned WS_PB = WS_PAO + 786432;
constexpr unsigned WS_F = WS_PB + 2097152;
constexpr unsigned WS_END = WS_F + 2097152;

constexpr unsigned LDS_BYTES = 163840;
constexpr unsigned SCR = 131072;
constexpr unsigned A_MISC = NSLOT * 8192;
constexpr unsigned A_LANDED = A_MISC, A_QRDY = A_MISC + 4, A_QUSED = A_MISC + 8, A_QCNT = A_MISC + 12, A_QB = A_MISC + 16, A_DONE = A_MISC + 64;
constexpr unsigned A_QU2 = 11 * 8192;
constexpr unsigned A_QIMG = A_MISC + 256;
constexpr unsigned A_ML = A_MISC + 1280;
constexpr unsigned A_OIMG = A_MISC + 1792;
constexpr unsigned A_XIMG = A_MISC + 2048, A_DUMMY = A_MISC + 4096, A_QBI = A_MISC + 5120, A_QAS = A_MISC + 5632;
constexpr unsigned SLOT0 = 0, SLOT1 = 65536;
constexpr unsigned B_MX = SCR, B_AL = SCR + 512, B_LS = SCR + 640, B_P = SCR + 2048;
constexpr unsigned B_FIN = SCR + 12288, B_T = SLOT1, B_DUMMY = SCR + 8192;
constexpr unsigned T_QBI = SCR, T_QAS = SCR + 1024, T_OL = SCR + 4096, T_RED = 0;

__device__ __forceinline__ v4u lds128(unsigned a) { return *(const LAS v4u*)a; }
__device__ __forceinline__ void sto128(unsigned a, v4u v) { *(LAS v4u*)a = v; }
__device__ __forceinline__ void sto64(unsigned a, v2u v) { *(LAS v2u*)a = v; }
__device__ __forceinline__ void sto32(unsigned a, unsigned v) { *(LAS unsigned*)a = v; }
__device__ __forceinline__ float ldsf(unsigned a) { return *(const LAS float*)a; }
__device__ __forceinline__ void stof(unsigned a, float v) { *(LAS float*)a = v; }
__device__ __forceinline__ v2u trd(unsigned a) { return __builtin_bit_cast(v2u, __builtin_amdgcn_ds_read_tr16_b64_v4i16((LAS s4*)a)); }
__device__ __forceinline__ v4u trd2(unsigned a0, unsigned a1) { const v2u x = trd(a0), y = trd(a1); return (v4u){x.x, x.y, y.x, y.y}; }
__device__ __forceinline__ f4 mfma32(v4u a, v4u b, f4 c) {
  return __builtin_amdgcn_mfma_f32_16x16x32_bf16(__builtin_bit_cast(b8, a), __builtin_bit_cast(b8, b), c, 0, 0, 0); }
__device__ __forceinline__ f4 mfma16(v2u a, v2u b, f4 c) {
  return __builtin_amdgcn_mfma_f32_16x16x16bf16_1k(__builtin_bit_cast(s4, a), __builtin_bit_cast(s4, b), c, 0, 0, 0); }
__device__ __forceinline__ f16v mfmaL(v4u a, v4u b, f16v c) {
  return __builtin_amdgcn_mfma_f32_32x32x16_bf16(__builtin_bit_cast(b8, a), __builtin_bit_cast(b8, b), c, 0, 0, 0); }
__device__ __forceinline__ unsigned pk(float lo, float hi) { return __builtin_bit_cast(unsigned, __builtin_convertvector((f2){lo, hi}, b2)); }
__device__ __forceinline__ float asf(unsigned u) { return __builtin_bit_cast(float, u); }
__device__ __forceinline__ unsigned asu(float f) { return __builtin_bit_cast(unsigned, f); }
__device__ __forceinline__ float ex2(float x) { return __builtin_amdgcn_exp2f(x); }
template <int CTRL> __device__ __forceinline__ float dpp(float x) {
  return __builtin_bit_cast(float, __builtin_amdgcn_mov_dpp(__builtin_bit_cast(int, x), CTRL, 0xf, 0xf, true)); }
constexpr int XOR1 = 0xB1, XOR2 = 0x4E, ROR4 = 0x124, ROR8 = 0x128;
__device__ __forceinline__ float x16max(float x) { auto s = __builtin_amdgcn_permlane16_swap(asu(x), asu(x), false, false); return fmaxf(asf(s[0]), asf(s[1])); }
__device__ __forceinline__ float x32max(float x) { auto s = __builtin_amdgcn_permlane32_swap(asu(x), asu(x), false, false); return fmaxf(asf(s[0]), asf(s[1])); }
__device__ __forceinline__ float x16sum(float x) { auto s = __builtin_amdgcn_permlane16_swap(asu(x), asu(x), false, false); return asf(s[0]) + asf(s[1]); }
__device__ __forceinline__ float x32sum(float x) { auto s = __builtin_amdgcn_permlane32_swap(asu(x), asu(x), false, false); return asf(s[0]) + asf(s[1]); }
__device__ __forceinline__ float row16max(float x) { x = fmaxf(x, dpp<XOR1>(x)); x = fmaxf(x, dpp<XOR2>(x)); x = fmaxf(x, dpp<ROR4>(x)); return fmaxf(x, dpp<ROR8>(x)); }
__device__ __forceinline__ float row16sum(float x) { x += dpp<XOR1>(x); x += dpp<XOR2>(x); x += dpp<ROR4>(x); return x + dpp<ROR8>(x); }

template <int IMM> __device__ __forceinline__ void dma(const void* sbase, unsigned voff, unsigned lds) {
  asm volatile("s_mov_b32 m0, %2\n\ts_nop 0\n\tglobal_load_lds_dwordx4 %0, %1 offset:%3 nt" :: "v"(voff), "s"(sbase), "s"(lds), "n"(IMM) : "memory");
}
__device__ __forceinline__ void dma_keep(const void* sbase, unsigned voff, unsigned lds) {
  asm volatile("s_mov_b32 m0, %2\n\ts_nop 0\n\tglobal_load_lds_dwordx4 %0, %1" :: "v"(voff), "s"(sbase), "s"(lds) : "memory");
}
template <int N> __device__ __forceinline__ void vmwait() { asm volatile("s_waitcnt vmcnt(%0)" :: "n"(N) : "memory"); }
__device__ __forceinline__ void bar() { asm volatile("s_waitcnt lgkmcnt(0)\n\ts_barrier" ::: "memory"); }
__device__ __forceinline__ void lgk0() { asm volatile("s_waitcnt lgkmcnt(0)" ::: "memory"); }
__device__ __forceinline__ void cfence() { asm volatile("" ::: "memory"); }
__device__ __forceinline__ unsigned opq(unsigned x) { asm volatile("" : "+v"(x)); return x; }
__device__ __forceinline__ unsigned lds_peek(unsigned a) { return __builtin_amdgcn_readfirstlane(*(volatile LAS unsigned*)a); }
__device__ __forceinline__ void lds_wait_ge(unsigned a, unsigned want) {
  _Pragma("nounroll") for (int it = 0; it < SPIN_CAP * 8; ++it) { if (lds_peek(a) >= want) break; __builtin_amdgcn_s_sleep(1); }
  cfence();
}
typedef __amdgpu_buffer_rsrc_t rsrc_t;
constexpr int SC1 = 16;
__device__ __forceinline__ v4u gld128(rsrc_t r, unsigned off) { return __builtin_amdgcn_raw_buffer_load_b128(r, off, 0, SC1); }
__device__ __forceinline__ v2u gld64(rsrc_t r, unsigned off) { return __builtin_amdgcn_raw_buffer_load_b64(r, off, 0, SC1); }
__device__ __forceinline__ unsigned gld32(rsrc_t r, unsigned off) { return __builtin_amdgcn_raw_buffer_load_b32(r, off, 0, SC1); }
__device__ __forceinline__ void gst128(rsrc_t r, unsigned off, v4u v) { __builtin_amdgcn_raw_buffer_store_b128(v, r, off, 0, SC1); }
__device__ __forceinline__ void gst64(rsrc_t r, unsigned off, v2u v) { __builtin_amdgcn_raw_buffer_store_b64(v, r, off, 0, SC1); }
__device__ __forceinline__ void gst32(rsrc_t r, unsigned off, unsigned v) { __builtin_amdgcn_raw_buffer_store_b32(v, r, off, 0, SC1); }
__device__ __forceinline__ void flag_line(rsrc_t r, unsigned off, int lane, unsigned tag) { if (lane < 8) gst128(r, off + lane * 16, (v4u){tag, tag, tag, tag}); }
__device__ __forceinline__ bool tagok(v4u v, unsigned tag) { return v.y == tag && v.w == tag; }

#define STAMP(i) do {} while (0)
#define STAMP_DECL do {} while (0)
#define STAMP_OUT() do {} while (0)

__device__ __forceinline__ void gather_qb(unsigned img, int h, int lane, rsrc_t rs, unsigned tag) {
  v4u qg[4];
  const unsigned off = WS_QG + (h * 128 + 2 * lane) * 8;
  _Pragma("nounroll") for (int it = 0; it < SPIN_CAP; ++it) {
    cfence();
    bool ok = true;
#pragma unroll
    for (int k = 0; k < 4; ++k) { qg[k] = gld128(rs, off + (k & 1) * 65536 + (k >> 1) * 32768); ok &= tagok(qg[k], tag); }
    if (__all(ok)) break;
    __builtin_amdgcn_s_sleep(1);
  }
  sto32(img + lane * 4, pk(asf(qg[0].x) + asf(qg[1].x), asf(qg[0].z) + asf(qg[1].z)));
  sto32(img + 256 + lane * 4, pk(asf(qg[2].x) + asf(qg[3].x), asf(qg[2].z) + asf(qg[3].z)));
}
__device__ __forceinline__ void lds_inc(unsigned a) { __hip_atomic_fetch_add((LAS unsigned*)a, 1u, __ATOMIC_RELAXED, __HIP_MEMORY_SCOPE_WORKGROUP); }

template <int KS> __device__ __forceinline__ void out_unit(unsigned ou, unsigned oimg, int w, int lane, int mat, int pidx, rsrc_t rs, unsigned tag) {
  const int g = lane >> 4, c16 = lane & 15, q = c16 >> 2, p = lane & 3;
  const int f3 = q | ((g & 1) << 2);
  v4u ob[KS], wf[KS][4];
#pragma unroll
  for (int k = 0; k < KS; ++k) {
    ob[k] = lds128(oimg + (c16 & 1) * 64 * KS + k * 64 + g * 16);
#pragma unroll
    for (int tt = 0; tt < 4; ++tt) {
      const unsigned a0 = ou + (8 * g + q) * 1024 + (((2 * (4 * w + tt) + (p >> 1)) ^ (f3 << 1)) << 4) + (p & 1) * 8;
      wf[k][tt] = trd2(a0 + k * 32768, a0 + k * 32768 + 4096);
    }
  }
  __builtin_amdgcn_sched_barrier(0);
  f4 d[4];
#pragma unroll
  for (int tt = 0; tt < 4; ++tt) d[tt] = (f4){0.f, 0.f, 0.f, 0.f};
#pragma unroll
  for (int k = 0; k < KS; ++k)
#pragma unroll
    for (int tt = 0; tt < 4; ++tt) d[tt] = mfma32(wf[k][tt], ob[k], d[tt]);
  const int tt = (c16 >> 1) & 3, pr = c16 >> 3;
  const f4 e = tt == 0 ? d[0] : tt == 1 ? d[1] : tt == 2 ? d[2] : d[3];
  const float v0 = pr ? e[2] : e[0], v1 = pr ? e[3] : e[1];
  const int n = 64 * w + 16 * tt + 4 * g + 2 * pr;
  gst128(rs, WS_F + ((((mat * 64 + (n >> 3)) * 128 + pidx) * 16) + (c16 & 1) * 8 + (n & 7)) * 8, (v4u){asu(v0), tag, asu(v1), tag});
}

}

__global__ void __launch_bounds__(512, 2) fused_decode(
    const char* __restrict__ Ax, const char* __restrict__ Kc, const char* __restrict__ Vc, const char* __restrict__ Wq, const char* __restrict__ Wo,
    const char* __restrict__ Bx, const char* __restrict__ Ckv, const char* __restrict__ Bwq, const char* __restrict__ Wuk, const char* __restrict__ Wuv,
    const char* __restrict__ Bwo, unsigned short* __restrict__ out, char* __restrict__ ws) {
  const int bid = blockIdx.x, tid = threadIdx.x, lane = tid & 63, wave = __builtin_amdgcn_readfirstlane(tid >> 6);
  const int g = lane >> 4, c16 = lane & 15;
  STAMP_DECL;
  STAMP(0);
  const int iA = bid - (NB_CU + NT_CU);
  const bool isA = iA >= 0, isB = bid < NB_CU;
  const int ty = iA >> 6;
  {
    unsigned long long pc;
    asm volatile("s_getpc_b64 %0" : "=s"(pc));
    const int kb = ((bid >> 3) + 4 * wave) & 31;
    dma_keep((const char*)((pc & ~255ull) + (unsigned)((kb < CODE_KB ? kb : CODE_KB - 1) * 1024)), lane * 16, isA ? A_DUMMY : B_DUMMY);
  }
  const rsrc_t rs = __builtin_amdgcn_make_buffer_rsrc(ws, 0, WS_END, 0x00020000);
  asm volatile("" :: "s"(Ax), "s"(Kc), "s"(Vc), "s"(Wq), "s"(Wo), "s"(Bx), "s"(Ckv), "s"(Bwq), "s"(Wuk), "s"(Wuv), "s"(Bwo), "s"(out), "s"(ws));
  unsigned tag;
#define GET_TAG() do { asm volatile("s_load_dword %0, %1, 0x0\n\ts_waitcnt lgkmcnt(0)" : "=s"(tag) : "s"(ws) : "memory"); tag += 1u; } while (0)
  const char* cp = Ckv + ((size_t)(bid >> 5) * 8192 + 256 * (bid & 31) + 8 * wave) * 1024;
  const unsigned vc0 = (lane ^ ((wave & 1) ? 3 : 0)) << 4, vc1 = (lane ^ ((wave & 1) ? 1 : 2)) << 4;
#define LD_C(rho, u) dma<((u) & 3) * 1024>(cp + (size_t)(rho) * 65536 + ((u) >> 2) * 4096, (((u) >> 2) ? vc1 : vc0) ^ (((u) & 3) << 6), ((rho) & 1) * 65536 + wave * 8192 + ((u) >> 2) * 4096)
  const char* uk = Wuk + (size_t)(32 * wave) * 8192 + (bid - NB_CU) * 256;
  const unsigned vuk = g * 8192 + ((c16 ^ g) << 4);
#define LD_UK(u) dma<0>(uk + (size_t)(u) * 32768, vuk ^ (((u) & 3) << 6), wave * 8192 + (u) * 1024)
  if (isA) {
    if (wave >= 1 && wave <= 4) {
      const int w1 = wave - 1, hh = ty == 2 ? iA & 31 : (iA >> 1) & 31, kh = ty == 2 ? 1 : iA & 1;
      const char* f0 = (ty == 2 ? Wuk : ty ? Wq : Bwq) + ((256 * kh + 64 * w1) * 8192u + hh * 256);
      const unsigned l0 = (w1 < 2 ? SCR : A_QU2 - 32768) + w1 * 16384;
      const unsigned fx1 = ty == 2 ? 64u : 0u, fv = ty == 2 ? vuk : g * 8192 + ((c16 ^ (g << 1)) << 4);
      if (w1 == 0 && ty < 2) dma<0>((ty ? Ax : Bx) + 512 * kh, (lane >> 5) * 1024 + (lane & 31) * 16, A_XIMG);
#pragma unroll
      for (int u = 0; u < 16; ++u) dma<0>(f0 + (size_t)u * 32768, fv ^ ((u & 1) ? fx1 : 0u) ^ ((u & 2) << 6), l0 + u * 1024);
    }
    if (tid < 32) sto32(A_MISC + tid * 4, 0u);
    bar();
  } else if (isB) {
    __builtin_amdgcn_s_sleep(DLYB);
    LD_C(0, 0); LD_C(0, 1); LD_C(0, 2); LD_C(0, 3); LD_C(0, 4); LD_C(0, 5);
  } else {
    LD_UK(0); LD_UK(1); LD_UK(2); LD_UK(3); LD_UK(4); LD_UK(5);
  }

  if (isA) {
    const int pair = iA & 15, split = iA >> 4, b = pair >> 3, kvh = pair & 7;
    constexpr int NBQ = (512 - 8 * NBP) / 2;
    const int nblk = ty < 2 ? NBP : NBQ, blk0 = ty < 2 ? split * NBP : 8 * NBP + (split - 8) * NBQ;
    if (wave == 0) {
      __builtin_amdgcn_s_setprio(3);
      static_assert(NSPLIT_A == 10 && W1 >= 4 && W1 <= W2 && W2 <= 32 && J1 <= NBP && J1 <= NBQ, "window");
      int pub = 0, p = 0, slot = 0, j = 0;
      const char* kp = Kc + ((size_t)pair * 8192 + blk0 * 16) * 256;
      const char* vp = Vc + ((size_t)pair * 8192 + blk0 * 16) * 256;
      const unsigned vk0 = g * 256 + ((c16 ^ g) << 4), vv0 = g * 256 + ((c16 ^ (g << 1)) << 4);
#define PUBLISH(val) do { const int v_ = (val); if (v_ > pub) { pub = v_; if (lane == 0) *(volatile LAS unsigned*)A_LANDED = (unsigned)v_; } } while (0)
#define PACE(W, i) do { vmwait<W - 1>(); if ((i) == ((W - 1) & 3)) PUBLISH((4 * p + (i) + 1 - W) / 4); } while (0)
#define GATE(addr, need) do { if (lds_peek(addr) < (need)) { vmwait<0>(); PUBLISH(p); lds_wait_ge(addr, need); } } while (0)
#define BLOCK(W) do { \
        if (j >= NSLOT) GATE(A_DONE + slot * 4, (unsigned)(j - NSLOT + 1)); else if (j == 11) GATE(A_QUSED, 4u); \
        const unsigned sb = slot * 8192; \
        PACE(W, 0); dma<0>(kp, vk0, sb); PACE(W, 1); dma<1024>(kp, vk0 ^ 64, sb); PACE(W, 2); dma<2048>(kp, vk0 ^ 128, sb); PACE(W, 3); dma<3072>(kp, vk0 ^ 192, sb); \
        ++p; \
        PACE(W, 0); dma<0>(vp, vv0, sb + 4096); PACE(W, 1); dma<1024>(vp, vv0 ^ 128, sb + 4096); PACE(W, 2); dma<2048>(vp, vv0, sb + 4096); PACE(W, 3); dma<3072>(vp, vv0 ^ 128, sb + 4096); \
        ++p; kp += 4096; vp += 4096; slot = slot == NSLOT - 1 ? 0 : slot + 1; } while (0)
      STAMP(9);
      GET_TAG();
      lds_wait_ge(A_QCNT, 4u);
      STAMP(10);
#pragma nounroll
      for (; j < J1; ++j) BLOCK(W1);
      STAMP(11);
#pragma nounroll
      for (; j < nblk; ++j) BLOCK(W2);
      if (ty < 2) {
        const char* op = Wo + (size_t)iA * 32768;
#pragma unroll
        for (int k = 0; k < 32; ++k) {
          if (k < W2) { vmwait<W2 - 1>(); if ((k & 3) == ((W2 - 1) & 3)) PUBLISH(p + (k + 1 - W2) / 4); }
          dma<0>(op + k * 1024, (lane ^ (((k & 3) | (((k >> 3) & 1) << 2)) << 1)) << 4, SCR + k * 1024);
        }
      } else {
        vmwait<28>(); PUBLISH(p - 7); vmwait<24>(); PUBLISH(p - 6); vmwait<20>(); PUBLISH(p - 5); vmwait<16>(); PUBLISH(p - 4);
        vmwait<12>(); PUBLISH(p - 3); vmwait<8>(); PUBLISH(p - 2); vmwait<4>(); PUBLISH(p - 1); vmwait<0>(); PUBLISH(p);
      }
      __builtin_amdgcn_s_setprio(0);
      STAMP(5);
#undef PACE
#undef PUBLISH
#undef GATE
#undef BLOCK
    } else {
      GET_TAG();
      if (wave <= 4) {
        const int w1 = wave - 1;
        vmwait<0>();
        if (ty < 2) {
          if (lane == 0) lds_inc(A_QCNT);
          const int q = c16 >> 2, p = lane & 3, f3 = q | ((g & 1) << 2);
          const unsigned a0 = (8 * g + q) * 256 + (((4 * w1 + (p >> 1)) ^ (f3 << 1)) << 4) + (p & 1) * 8;
          const unsigned aa[2] = {opq(SCR + a0), opq(A_QU2 + a0)};
          lds_wait_ge(A_QCNT, 4u);
          STAMP(1);
          v4u w0[8], w1f[8], xf[8];
#pragma unroll
          for (int s = 0; s < 8; ++s) {
            const unsigned a = aa[s >> 2] + (s & 3) * 8192;
            w0[s] = trd2(a, a + 1024); w1f[s] = trd2((a ^ 32), (a ^ 32) + 1024);
            xf[s] = lds128(A_XIMG + (c16 & 1) * 512 + g * 16 + s * 64);
          }
          __builtin_amdgcn_sched_barrier(0);
          f4 d0 = {0.f, 0.f, 0.f, 0.f}, d1 = d0;
#pragma unroll
          for (int s = 0; s < 8; ++s) { d0 = mfma32(w0[s], xf[s], d0); d1 = mfma32(w1f[s], xf[s], d1); }
          const int pr = (c16 >> 1) & 1, nb = (c16 >> 2) & 1;
          const f4 e = nb ? d1 : d0;
          if (c16 < 8) gst128(rs, WS_QG + (((ty * 2 + (iA & 1)) * 2 + (c16 & 1)) * 4096 + ((iA >> 1) & 31) * 128 + 32 * w1 + 16 * nb + 4 * g + 2 * pr) * 8,
                              (v4u){asu(pr ? e[2] : e[0]), tag, asu(pr ? e[3] : e[1]), tag});
          lgk0();
        } else {
          STAMP(1);
          const unsigned a0 = opq((w1 < 2 ? SCR : A_QU2 - 32768) + w1 * 16384 + c16 * 256 + ((g ^ c16) << 4));
          v4u uf[16];
#pragma unroll
          for (int i = 0; i < 16; ++i) uf[i] = lds128((a0 ^ ((i & 3) << 6)) + (i >> 2) * 4096);
          lds_wait_ge(A_QB, 1u);
          v4u qb[4];
#pragma unroll
          for (int s = 0; s < 4; ++s) qb[s] = lds128(A_QBI + (c16 & 1) * 256 + s * 64 + g * 16);
          __builtin_amdgcn_sched_barrier(0);
          f4 d[4];
#pragma unroll
          for (int rb = 0; rb < 4; ++rb) d[rb] = (f4){0.f, 0.f, 0.f, 0.f};
#pragma unroll
          for (int s = 0; s < 4; ++s)
#pragma unroll
            for (int rb = 0; rb < 4; ++rb) d[rb] = mfma32(uf[4 * rb + s], qb[s], d[rb]);
          const int rb = (c16 >> 1) & 3;
          const f4 e = rb == 0 ? d[0] : rb == 1 ? d[1] : rb == 2 ? d[2] : d[3];
          if (c16 < 8) sto64(A_QAS + w1 * 256 + (c16 & 1) * 128 + (16 * rb + 4 * g) * 2, (v2u){pk(e[0], e[1]), pk(e[2], e[3])});
          lgk0();
          if (lane < 16) gst128(rs, WS_QAB + ((lane >> 3) * 32 + (iA & 31)) * 1024 + 512 + w1 * 128 + (lane & 7) * 16, lds128(A_QAS + w1 * 256 + lane * 16));
          vmwait<0>();
        }
        unsigned old = 0;
        if (lane == 0) old = __hip_atomic_fetch_add((LAS unsigned*)A_QUSED, 1u, __ATOMIC_RELAXED, __HIP_MEMORY_SCOPE_WORKGROUP);
        if (ty == 2 && __builtin_amdgcn_readfirstlane(old) == 3u) {
          flag_line(rs, WS_FQ + (32 + (iA & 31)) * FS, lane, tag);
          cfence();
          if (lane == 0) *(volatile LAS unsigned*)A_QCNT = 4u;
        }
        STAMP(2);
      }
      if (wave == 5) {
        if (ty == 2) {
          gather_qb(A_QBI, iA & 31, lane, rs, tag);
          lgk0();
          if (lane == 0) *(volatile LAS unsigned*)A_QB = 1u;
          STAMP(1);
        }
        lds_wait_ge(A_LANDED, ty == 2 ? 1u : (unsigned)QGO);
        v4u v[8];
        const unsigned off = WS_QG + 131072 + (b * 4096 + kvh * 512 + 2 * lane) * 8;
        _Pragma("nounroll") for (int it = 0; it < SPIN_CAP; ++it) {
          cfence();
          bool ok = true;
#pragma unroll
          for (int k = 0; k < 8; ++k) { v[k] = gld128(rs, off + (k & 1) * 65536 + (k >> 1) * 1024); ok &= tagok(v[k], tag); }
          if (__all(ok)) break;
          __builtin_amdgcn_s_sleep(4);
        }
#pragma unroll
        for (int hh = 0; hh < 4; ++hh) sto32(A_QIMG + hh * 256 + lane * 4, pk(asf(v[2 * hh].x) + asf(v[2 * hh + 1].x), asf(v[2 * hh].z) + asf(v[2 * hh + 1].z)));
        lgk0();
        if (lane == 0) *(volatile LAS unsigned*)A_QRDY = 1u;
      }
      lds_wait_ge(A_QRDY, 1u);
      STAMP(3);
      v4u qf[4];
#pragma unroll
      for (int s = 0; s < 4; ++s) qf[s] = lds128(A_QIMG + (c16 & 3) * 256 + s * 64 + g * 16);
      const unsigned ko = c16 * 256 + ((g ^ c16) << 4);
      const int vr = 4 * g + (c16 >> 2);
      const unsigned vo = 4096 + vr * 256 + ((((lane & 3) >> 1) ^ ((vr & 7) << 1)) << 4) + (lane & 1) * 8;
      float m = -INFINITY, l = 0.f;
      f4 acc[8];
#pragma unroll
      for (int t = 0; t < 8; ++t) acc[t] = (f4){0.f, 0.f, 0.f, 0.f};
      int slot = wave - 1, j = wave - 1;
      for (; j < nblk; j += NCONS) {
        const unsigned sb = slot * 8192;
        lds_wait_ge(A_LANDED, 1u + 2 * j);
        v4u kf[4];
#pragma unroll
        for (int s = 0; s < 4; ++s) kf[s] = lds128(sb + (ko ^ (s << 6)));
        __builtin_amdgcn_sched_barrier(0);
        f4 s4_ = {0.f, 0.f, 0.f, 0.f};
#pragma unroll
        for (int s = 0; s < 4; ++s) s4_ = mfma32(kf[s], qf[s], s4_);
        float mx = fmaxf(fmaxf(s4_[0], s4_[1]), fmaxf(s4_[2], s4_[3]));
        mx = x32max(x16max(mx));
        const float mn = fmaxf(m, mx), al = ex2((m - mn) * SC), mc = -mn * SC;
        const float p0 = ex2(fmaf(s4_[0], SC, mc)), p1 = ex2(fmaf(s4_[1], SC, mc)), p2 = ex2(fmaf(s4_[2], SC, mc)), p3 = ex2(fmaf(s4_[3], SC, mc));
        l = fmaf(l, al, (p0 + p1) + (p2 + p3));
        m = mn;
        const v2u pb = {pk(p0, p1), pk(p2, p3)};
#pragma unroll
        for (int t = 0; t < 8; ++t) acc[t] *= al;
        lds_wait_ge(A_LANDED, 2u + 2 * j);
        v2u vf[8];
#pragma unroll
        for (int t = 0; t < 8; ++t) vf[t] = trd(sb + (vo ^ (t << 5)));
        lgk0();
        if (lane == 0) *(volatile LAS unsigned*)(A_DONE + slot * 4) = (unsigned)(j + 1);
#pragma unroll
        for (int t = 0; t < 8; ++t) acc[t] = mfma16(vf[t], pb, acc[t]);
        if (j + NCONS < nblk) { slot += NCONS; if (slot >= NSLOT) slot -= NSLOT; }
      }
      STAMP(4);
      l = x32sum(x16sum(l));
      if (c16 < 4) {
#pragma unroll
        for (int t = 0; t < 8; ++t) sto128(slot * 8192 + c16 * 512 + t * 64 + g * 16, __builtin_bit_cast(v4u, acc[t]));
        if (g == 0) sto128(A_ML + (c16 * 8 + wave - 1) * 16, (v4u){asu(m * SC), asu(l), (unsigned)(slot * 8192 + c16 * 512), 0u});
      }
    }
    bar();
    if (wave >= 4) {
      const int hh = wave - 4;
      v4u ml[NCONS];
      f2 ow[NCONS];
#pragma unroll
      for (int c = 0; c < NCONS; ++c) ml[c] = lds128(A_ML + (hh * 8 + c) * 16);
#pragma unroll
      for (int c = 0; c < NCONS; ++c) ow[c] = *(const LAS f2*)(ml[c].z + lane * 8);
      float M = asf(ml[0].x);
#pragma unroll
      for (int c = 1; c < NCONS; ++c) M = fmaxf(M, asf(ml[c].x));
      float L = 0.f, o0 = 0.f, o1 = 0.f;
#pragma unroll
      for (int c = 0; c < NCONS; ++c) { const float wt = ex2(asf(ml[c].x) - M); L = fmaf(wt, asf(ml[c].y), L); o0 = fmaf(wt, ow[c].x, o0); o1 = fmaf(wt, ow[c].y, o1); }
      const int rec = (b * 32 + kvh * 4 + hh) * NSPLIT_A + split;
      gst128(rs, WS_PAO + rec * 1024 + lane * 16, (v4u){asu(o0), tag, asu(o1), tag});
      if (lane < 8) gst128(rs, WS_PAM + rec * 128 + lane * 16, (v4u){asu(M), tag, asu(L), tag});
    }
    STAMP(6);
    if (ty < 2) {
      const int oh = iA >> 2, odq = iA & 3;
      if (wave == 1) {
        const int rec = ((lane >> 5) * 32 + oh) * NSPLIT_A + (g & 1) * 5;
        v4u v[5], ml[5];
        _Pragma("nounroll") for (int it = 0; it < SPIN_CAP; ++it) {
          cfence();
          bool ok = true;
#pragma unroll
          for (int i = 0; i < 5; ++i) {
            v[i] = gld128(rs, WS_PAO + (rec + i) * 1024 + (32 * odq + 2 * c16) * 8);
            ml[i] = gld128(rs, WS_PAM + (rec + i) * 128 + odq * 16);
            ok &= tagok(v[i], tag) && tagok(ml[i], tag);
          }
          if (__all(ok)) break;
          __builtin_amdgcn_s_sleep(GSL);
        }
        float M = asf(ml[0].x);
#pragma unroll
        for (int i = 1; i < 5; ++i) M = fmaxf(M, asf(ml[i].x));
        M = x16max(M);
        float L = 0.f, o0 = 0.f, o1 = 0.f;
#pragma unroll
        for (int i = 0; i < 5; ++i) { const float wt = ex2(asf(ml[i].x) - M); L = fmaf(wt, asf(ml[i].z), L); o0 = fmaf(wt, asf(v[i].x), o0); o1 = fmaf(wt, asf(v[i].z), o1); }
        const float inv = 1.f / x16sum(L);
        o0 = x16sum(o0); o1 = x16sum(o1);
        if (!(g & 1)) sto32(A_OIMG + (lane >> 5) * 64 + c16 * 4, pk(o0 * inv, o1 * inv));
      }
      vmwait<0>();
      STAMP(7);
      bar();
      out_unit<1>(SCR, A_OIMG, wave, lane, 0, iA, rs, tag);
      STAMP(8);
    }
    STAMP_OUT();
  } else if (isB) {
    const int b = bid >> 5, sp = bid & 31, w = wave, lane_ = lane;
    const unsigned vl = lane << 4;
#define LD_S(S, rho, u) asm volatile("global_load_dwordx4 %0, %1, %2 offset:%3 nt" : "=v"(S[u]) : "v"(vl), "s"(cp + (size_t)(rho) * 65536 + ((u) >> 2) * 4096), "n"(((u) & 3) * 1024) : "memory")
#define ST_S(S, rho) do { unsigned a0_ = ((rho) & 1) * 65536 + w * 8192 + vc0, a1_ = ((rho) & 1) * 65536 + w * 8192 + vc1; asm volatile("" : "+v"(a0_), "+v"(a1_)); \
      _Pragma("unroll") for (int u_ = 0; u_ < 8; ++u_) sto128(u_ * 1024 + (((u_ >> 2) ? a1_ : a0_) ^ ((u_ & 3) << 6)), S[u_]); } while (0)
#define PB() vmwait<WB - 2>()
    v4u S2[8], S3[8];
    GET_TAG();
    PB(); LD_C(0, 6); LD_C(0, 7);
    STAMP(1);
    PB(); LD_C(1, 0); LD_C(1, 1);
    PB(); LD_C(1, 2); LD_C(1, 3);
    PB(); LD_C(1, 4); LD_C(1, 5);
    PB(); LD_C(1, 6); LD_C(1, 7);
    STAMP(2);
    PB(); LD_S(S2, 2, 0); LD_S(S2, 2, 1);
    PB(); LD_S(S2, 2, 2); LD_S(S2, 2, 3);
    PB(); LD_S(S2, 2, 4); LD_S(S2, 2, 5);
    PB(); LD_S(S2, 2, 6); LD_S(S2, 2, 7);
    STAMP(3);
    if (w == 0) {
      _Pragma("nounroll") for (int it = 0; it < SPIN_CAP; ++it) {
        cfence();
        if (__all(gld32(rs, WS_FQ + lane * FS) == tag)) break;
        __builtin_amdgcn_s_sleep(PSL);
      }
    }
    bar();
    STAMP(4);
    {
      v4u t[4];
      const char* qa = ws + WS_QAB + (b * 32 + 4 * w) * 1024;
#pragma unroll
      for (int i = 0; i < 4; ++i) asm volatile("global_load_dwordx4 %0, %1, %2 offset:%3 sc1" : "=v"(t[i]) : "v"(vl), "s"(qa), "n"(i * 1024) : "memory");
      LD_S(S3, 3, 0); LD_S(S3, 3, 1);
      asm volatile("s_waitcnt vmcnt(2)" : "+v"(t[0]), "+v"(t[1]), "+v"(t[2]), "+v"(t[3]), "+v"(S2[0]), "+v"(S2[1]), "+v"(S2[2]), "+v"(S2[3]),
                   "+v"(S2[4]), "+v"(S2[5]), "+v"(S2[6]), "+v"(S2[7]) :: "memory");
      LD_S(S3, 3, 2); LD_S(S3, 3, 3); LD_S(S3, 3, 4); LD_S(S3, 3, 5);
#pragma unroll
      for (int i = 0; i < 4; ++i) sto128(SCR + (4 * w + i) * 1024 + ((lane ^ ((4 * w + i) & 15)) << 4), t[i]);
    }
    bar();
    const int kb4 = w & 3, hg = w >> 2;
    v4u qf[16];
#pragma unroll
    for (int s = 0; s < 16; ++s) qf[s] = lds128(SCR + (16 * hg + c16) * 1024 + ((((4 * s) & 15) ^ g ^ c16) << 4) + (s >> 2) * 256);
    STAMP(5);
    const unsigned fk = ((c16 & 3) << 2) | ((0x78u >> (2 * (c16 >> 2))) & 3u);
    const unsigned ka = (16 * kb4 + c16) * 1024 + ((g ^ fk) << 4);
    const int h2 = lane >> 5, blk = g & 1, qq = c16 >> 2, pp = lane & 3;
    const unsigned ft0 = (qq << 2) | (h2 ? 3u : 0u), ft1 = (qq << 2) | (h2 ? 1u : 2u);
    const unsigned cbase = 8 * w + 2 * blk + (pp >> 1);
    const unsigned va00 = (8 * h2 + qq) * 1024 + ((cbase ^ ft0) << 4) + (pp & 1) * 8, va01 = (8 * h2 + 4 + qq) * 1024 + ((cbase ^ ft1) << 4) + (pp & 1) * 8;
    const unsigned mxa = opq(B_MX + (16 * hg + c16) * 4), pwa = opq(B_P + kb4 * 1024 + (16 * hg + c16) * 32 + g * 8);
    const unsigned pra = opq(B_P + (lane & 31) * 32 + h2 * 16), ala = opq(B_AL + (lane & 31) * 4);
    float m = -INFINITY, l = 0.f;
    f16v acc0, acc1;
#pragma unroll
    for (int i = 0; i < 16; ++i) { acc0[i] = 0.f; acc1[i] = 0.f; }
#pragma unroll
    for (int rho = 0; rho < 2; ++rho) {
      unsigned kr = ka, vr0 = va00, vr1 = va01;
      asm volatile("" : "+v"(kr), "+v"(vr0), "+v"(vr1));
      bar();
      f4 sa = {0.f, 0.f, 0.f, 0.f}, sb = sa;
      {
        v4u kf[8];
#pragma unroll
        for (int s = 0; s < 8; ++s) kf[s] = lds128((kr ^ ((s & 3) << 6)) + (s >> 2) * 256);
        __builtin_amdgcn_sched_barrier(0);
#pragma unroll
        for (int s = 0; s < 8; ++s) {
          sa = mfma32(kf[s], qf[s], sa);
          kf[s] = lds128((kr ^ ((s & 3) << 6)) + (2 + (s >> 2)) * 256);
          __builtin_amdgcn_sched_barrier(0);
        }
        kr += 65536;
#pragma unroll
        for (int s = 0; s < 8; ++s) {
          sa = mfma32(kf[s], qf[8 + s], sa);
          kf[s] = lds128((kr ^ ((s & 3) << 6)) + (s >> 2) * 256);
          __builtin_amdgcn_sched_barrier(0);
        }
#pragma unroll
        for (int s = 0; s < 8; ++s) {
          sb = mfma32(kf[s], qf[s], sb);
          kf[s] = lds128((kr ^ ((s & 3) << 6)) + (2 + (s >> 2)) * 256);
          __builtin_amdgcn_sched_barrier(0);
        }
#pragma unroll
        for (int s = 0; s < 8; ++s) sb = mfma32(kf[s], qf[8 + s], sb);
      }
      if (rho == 0) { LD_S(S3, 3, 6); LD_S(S3, 3, 7); }
      float mx = fmaxf(fmaxf(fmaxf(sa[0], sa[1]), fmaxf(sa[2], sa[3])), fmaxf(fmaxf(sb[0], sb[1]), fmaxf(sb[2], sb[3])));
      mx = x32max(x16max(mx));
      if (g == 0) stof(mxa + kb4 * 128, mx);
      v4u c0[4], c1[4];
#pragma unroll
      for (int ks = 0; ks < 4; ++ks) {
        c0[ks] = trd2(ks * 16384 + vr0, ks * 16384 + vr1);
        c1[ks] = trd2(ks * 16384 + (vr0 ^ 64), ks * 16384 + (vr1 ^ 64));
      }
      if (rho == 0) STAMP(6); else STAMP(8);
      bar();
      const float mn = fmaxf(fmaxf(m, fmaxf(ldsf(mxa), ldsf(mxa + 128))), fmaxf(ldsf(mxa + 256), ldsf(mxa + 384)));
      const float al = ex2((m - mn) * SC), mc = -mn * SC;
      float p[8];
#pragma unroll
      for (int i = 0; i < 4; ++i) { p[i] = ex2(fmaf(sa[i], SC, mc)); p[4 + i] = ex2(fmaf(sb[i], SC, mc)); }
      l = fmaf(l, al, ((p[0] + p[1]) + (p[2] + p[3])) + ((p[4] + p[5]) + (p[6] + p[7])));
      m = mn;
      sto64(pwa, (v2u){pk(p[0], p[1]), pk(p[2], p[3])});
      sto64(pwa + 4096, (v2u){pk(p[4], p[5]), pk(p[6], p[7])});
      if (kb4 == 0 && g == 0) stof(mxa + (B_AL - B_MX), al);
      bar();
      const float alp = rho ? ldsf(ala) : 0.f;
      v4u pf[4];
#pragma unroll
      for (int ks = 0; ks < 4; ++ks) pf[ks] = lds128(pra + ks * 1024);
      __builtin_amdgcn_sched_barrier(0);
      if (rho) { acc0 *= alp; acc1 *= alp; }
      vr0 += 65536; vr1 += 65536;
#pragma unroll
      for (int ks = 0; ks < 4; ++ks) {
        acc0 = mfmaL(c0[ks], pf[ks], acc0); acc1 = mfmaL(c1[ks], pf[ks], acc1);
        c0[ks] = trd2(ks * 16384 + vr0, ks * 16384 + vr1);
        c1[ks] = trd2(ks * 16384 + (vr0 ^ 64), ks * 16384 + (vr1 ^ 64));
        pf[ks] = lds128(pra + 4096 + ks * 1024);
        __builtin_amdgcn_sched_barrier(0);
      }
      bar();
      if (rho == 0) {
        ST_S(S2, 0);
        vmwait<0>();
        ST_S(S3, 1);
      }
#pragma unroll
      for (int ks = 0; ks < 4; ++ks) { acc0 = mfmaL(c0[ks], pf[ks], acc0); acc1 = mfmaL(c1[ks], pf[ks], acc1); }
      if (rho == 0) STAMP(7); else STAMP(9);
    }
    {
    const int lane = (int)opq((unsigned)lane_), g = lane >> 4, c16 = lane & 15, h2 = lane >> 5, tid = wave * 64 + lane;
    l = x32sum(x16sum(l));
    if (g == 0) stof(mxa + (B_LS - B_MX) + kb4 * 128, l);
    {
      const unsigned tb = B_T + w * 4096, n32 = lane & 31;
#pragma unroll
      for (int q4 = 0; q4 < 4; ++q4) {
        sto64(tb + n32 * 128 + ((q4 ^ (n32 & 7)) << 4) + h2 * 8, (v2u){pk(acc0[4 * q4], acc0[4 * q4 + 1]), pk(acc0[4 * q4 + 2], acc0[4 * q4 + 3])});
        sto64(tb + n32 * 128 + (((4 + q4) ^ (n32 & 7)) << 4) + h2 * 8, (v2u){pk(acc1[4 * q4], acc1[4 * q4 + 1]), pk(acc1[4 * q4 + 2], acc1[4 * q4 + 3])});
      }
      lgk0();
      const int hr = lane >> 3, ck = lane & 7;
#pragma unroll
      for (int i = 0; i < 4; ++i)
        gst128(rs, WS_PB + ((b * 32 + sp) * 32 + 8 * i + hr) * 1024 + w * 128 + ck * 16, lds128(tb + (8 * i + hr) * 128 + ((ck ^ hr) << 4)));
    }
    bar();
    if (kb4 == 0 && g == 0) {
      const unsigned la = B_LS + (16 * hg + c16) * 4;
      const float L = (ldsf(la) + ldsf(la + 128)) + (ldsf(la + 256) + ldsf(la + 384));
      gst64(rs, WS_PBM + ((b * 32 + sp) * 32 + 16 * hg + c16) * 8, (v2u){asu(m * SC), asu(L)});
    }
    vmwait<0>();
    bar();
    if (w == 0) flag_line(rs, WS_FP + bid * FS, lane, tag);
    STAMP(10);
    {
      const unsigned off = WS_PAM + (bid * NSPLIT_A + (bid % NSPLIT_A)) * 128;
      _Pragma("nounroll") for (int it = 0; it < SPIN_CAP; ++it) { cfence(); if (__all(gld64(rs, off).y == tag)) break; __builtin_amdgcn_s_sleep(PRESL); }
    }
    STAMP(14);
    v4u f[3];
    _Pragma("nounroll") for (int it = 0; it < SPIN_CAP; ++it) {
      cfence();
      bool ok = true;
#pragma unroll
      for (int k = 0; k < 3; ++k) { f[k] = gld128(rs, WS_F + ((k >> 1) * 64 + bid) * 16384 + (k < 2 ? k * 8192 + tid * 16 : (tid & 255) * 16)); ok &= tagok(f[k], tag); }
      if (__all(ok)) break;
      __builtin_amdgcn_s_sleep(FSL);
    }
    float r[4] = {asf(f[0].x) + asf(f[1].x), asf(f[0].z) + asf(f[1].z), w < 4 ? asf(f[2].x) : 0.f, w < 4 ? asf(f[2].z) : 0.f};
#pragma unroll
    for (int k = 0; k < 4; ++k) { r[k] += dpp<ROR8>(r[k]); r[k] = x32sum(x16sum(r[k])); }
    if (lane < 8) { sto64(B_FIN + w * 128 + lane * 8, (v2u){asu(r[0]), asu(r[1])}); sto64(B_FIN + w * 128 + 64 + lane * 8, (v2u){asu(r[2]), asu(r[3])}); }
    bar();
    if (tid < 16) {
      float s0 = 0.f, s1 = 0.f;
#pragma unroll
      for (int i = 0; i < 8; ++i) { const f2 t = *(const LAS f2*)(B_FIN + i * 128 + tid * 8); s0 += t.x; s1 += t.y; }
      const int mat = tid >> 3, ob = (tid >> 2) & 1, pr = tid & 3;
      *(GAS unsigned*)((GAS char*)out + (ob * 1024 + mat * 512 + 8 * bid + 2 * pr) * 2) = pk(s0, s1);
      if (bid == 0 && tid == 0) gst32(rs, WS_E, tag);
    }
    STAMP(15);
    STAMP_OUT();
    }
  } else {
    const int h = bid - NB_CU, w = wave;
    LD_UK(6); LD_UK(7);
    GET_TAG();
    if (w == 0) gather_qb(T_QBI, h, lane, rs, tag);
    vmwait<0>();
    STAMP(1);
    {
      const unsigned a0 = w * 8192 + c16 * 256 + ((g ^ c16) << 4);
      v4u uf[8];
#pragma unroll
      for (int i = 0; i < 8; ++i) uf[i] = lds128((a0 ^ ((i & 3) << 6)) + (i >> 2) * 4096);
      bar();
      STAMP(2);
      v4u qb[4];
#pragma unroll
      for (int s = 0; s < 4; ++s) qb[s] = lds128(T_QBI + (c16 & 1) * 256 + s * 64 + g * 16);
      __builtin_amdgcn_sched_barrier(0);
      f4 d0 = {0.f, 0.f, 0.f, 0.f}, d1 = d0;
#pragma unroll
      for (int s = 0; s < 4; ++s) { d0 = mfma32(uf[s], qb[s], d0); d1 = mfma32(uf[4 + s], qb[s], d1); }
      const int rb = (c16 >> 1) & 1;
      const f4 e = rb ? d1 : d0;
      if (c16 < 4) sto64(T_QAS + (c16 & 1) * 512 + (32 * w + 16 * rb + 4 * g) * 2, (v2u){pk(e[0], e[1]), pk(e[2], e[3])});
      bar();
      if (w == 0) {
        gst128(rs, WS_QAB + ((lane >> 5) * 32 + h) * 1024 + (lane & 31) * 16, lds128(T_QAS + lane * 16));
        vmwait<0>();
        flag_line(rs, WS_FQ + h * FS, lane, tag);
      }
    }
    bar();
    STAMP(3);
    v4u OU[16];
    {
      const char* uv = Wuv + (size_t)(64 * w) * 8192 + h * 256;
      const unsigned vuv = g * 8192 + ((c16 ^ (g << 1)) << 4);
#pragma unroll
      for (int u = 0; u < 16; ++u) { if (u >= 6 && !(u & 1)) PB(); dma<0>(uv + (size_t)u * 32768, vuv ^ (((u >> 1) & 1) << 7), w * 16384 + u * 1024); }
      const char* ow = Bwo + (size_t)(h * 128 + 16 * w) * 1024;
      const unsigned vo = g * 4096 + c16 * 16;
#pragma unroll
      for (int u = 0; u < 16; ++u) {
        if (!(u & 1)) PB();
        asm volatile("global_load_dwordx4 %0, %1, %2 offset:%3 nt" : "=v"(OU[u]) : "v"(vo), "s"(ow), "n"((u >> 2) * 1024 + (u & 3) * 256) : "memory");
      }
    }
    asm volatile("s_waitcnt vmcnt(0)" : "+v"(OU[0]), "+v"(OU[1]), "+v"(OU[2]), "+v"(OU[3]), "+v"(OU[4]), "+v"(OU[5]), "+v"(OU[6]), "+v"(OU[7]),
                 "+v"(OU[8]), "+v"(OU[9]), "+v"(OU[10]), "+v"(OU[11]), "+v"(OU[12]), "+v"(OU[13]), "+v"(OU[14]), "+v"(OU[15]) :: "memory");
    bar();
    STAMP(4);
    v4u wf[16];
    {
      const int q = c16 >> 2, p = lane & 3, f3 = q | ((g & 1) << 2);
      const unsigned a0 = (8 * g + q) * 256 + (((2 * w + (p >> 1)) ^ (f3 << 1)) << 4) + (p & 1) * 8;
      const unsigned aa[2] = {opq(a0), opq(a0 + 65536)};
#pragma unroll
      for (int s = 0; s < 16; ++s) wf[s] = trd2(aa[s >> 3] + (s & 7) * 8192, aa[s >> 3] + (s & 7) * 8192 + 1024);
    }
    if (w == 0) {
      _Pragma("nounroll") for (int it = 0; it < SPIN_CAP; ++it) { cfence(); if (__all(gld32(rs, WS_FP + lane * FS) == tag)) break; __builtin_amdgcn_s_sleep(PSL); }
    }
    bar();
    STAMP(5);
    {
      const int sub = lane >> 3, ck = lane & 7;
      const v2u ml = gld64(rs, WS_PBM + (lane * 32 + h) * 8);
      v4u o[8];
#pragma unroll
      for (int i = 0; i < 8; ++i) o[i] = gld128(rs, WS_PB + ((8 * i + sub) * 32 + h) * 1024 + w * 128 + ck * 16);
      const float M = x16max(row16max(asf(ml.x)));
      const float wt = ex2(asf(ml.x) - M);
      const float L = x16sum(row16sum(wt * asf(ml.y)));
      const float wn = wt / L;
      float wi[8];
#pragma unroll
      for (int i = 0; i < 8; ++i) wi[i] = asf(__builtin_amdgcn_ds_bpermute((8 * i + sub) * 4, asu(wn)));
      float a[16];
#pragma unroll
      for (int k = 0; k < 16; ++k) a[k] = 0.f;
#pragma unroll
      for (int i = 0; i < 8; ++i)
#pragma unroll
        for (int k = 0; k < 4; ++k) {
          a[(i >> 2) * 8 + 2 * k] = fmaf(wi[i], asf(o[i][k] << 16), a[(i >> 2) * 8 + 2 * k]);
          a[(i >> 2) * 8 + 2 * k + 1] = fmaf(wi[i], asf(o[i][k] & 0xffff0000u), a[(i >> 2) * 8 + 2 * k + 1]);
        }
      float r[8];
#pragma unroll
      for (int k = 0; k < 8; ++k) {
        auto sw = __builtin_amdgcn_permlane32_swap(asu(a[k]), asu(a[8 + k]), false, false);
        r[k] = x16sum(asf(sw[0]) + asf(sw[1]));
        r[k] += dpp<ROR8>(r[k]);
      }
      if ((lane & 24) == 0) sto128(T_OL + (lane >> 5) * 1024 + w * 128 + ck * 16, (v4u){pk(r[0], r[1]), pk(r[2], r[3]), pk(r[4], r[5]), pk(r[6], r[7])});
    }
    bar();
    STAMP(6);
    {
      v4u of[16];
      const unsigned oa = opq(T_OL + (c16 >> 3) * 1024 + g * 16);
#pragma unroll
      for (int s = 0; s < 16; ++s) of[s] = lds128(oa + s * 64);
      __builtin_amdgcn_sched_barrier(0);
      f4 d0 = {0.f, 0.f, 0.f, 0.f}, d1 = d0;
#pragma unroll
      for (int s = 0; s < 8; ++s) { d0 = mfma32(wf[2 * s], of[2 * s], d0); d1 = mfma32(wf[2 * s + 1], of[2 * s + 1], d1); }
      d0 += d1;
      const unsigned half = (c16 & 1) ? 0xffff0000u : 0x0000ffffu;
      const int rr = (c16 >> 1) & 3;
      f4 o4[4];
#pragma unroll
      for (int cb = 0; cb < 4; ++cb) o4[cb] = (f4){0.f, 0.f, 0.f, 0.f};
#pragma unroll
      for (int u = 0; u < 4; ++u) {
        const unsigned x = pk(d0[u], d0[u]) & half;
        const v4u bo = {rr == 0 ? x : 0u, rr == 1 ? x : 0u, rr == 2 ? x : 0u, rr == 3 ? x : 0u};
#pragma unroll
        for (int cb = 0; cb < 4; ++cb) o4[cb] = mfma32(OU[4 * u + cb], bo, o4[cb]);
      }
#pragma unroll
      for (int cb = 0; cb < 4; ++cb)
#pragma unroll
        for (int r = 0; r < 4; ++r) stof(T_RED + w * 4096 + cb * 1024 + r * 64 + g * 256 + c16 * 4, o4[cb][r]);
    }
    bar();
    {
      const int j = tid >> 3, pr = tid & 7;
      float s0 = 0.f, s1 = 0.f;
#pragma unroll
      for (int i = 0; i < 8; ++i) { const f2 t = *(const LAS f2*)(T_RED + i * 4096 + tid * 8); s0 += t.x; s1 += t.y; }
      gst128(rs, WS_F + (((64 + j) * 128 + h) * 16 + 2 * pr) * 8, (v4u){asu(s0), tag, asu(s1), tag});
    }
    STAMP(7);
    STAMP_OUT();
  }
}

extern "C" void kernel_launch(void* const* d_in, const int* in_sizes, int n_in, void* d_out, int out_size, void* d_ws, size_t ws_size,
                              hipStream_t stream) {
  (void)in_sizes; (void)n_in; (void)out_size; (void)ws_size;
  (void)hipFuncSetAttribute((const void*)fused_decode, hipFuncAttributeMaxDynamicSharedMemorySize, LDS_BYTES);
  fused_decode<<<256, 512, LDS_BYTES, stream>>>(
      (const char*)d_in[0], (const char*)d_in[1], (const char*)d_in[2], (const char*)d_in[3], (const char*)d_in[4], (const char*)d_in[5],
      (const char*)d_in[6], (const char*)d_in[7], (const char*)d_in[8], (const char*)d_in[9], (const char*)d_in[10], (unsigned short*)d_out,
      (char*)d_ws);
}
```

```cpp
#include <hip/hip_runtime.h>
#include <stdint.h>
#pragma clang diagnostic ignored "-Wint-to-pointer-cast"

#define GAS __attribute__((address_space(1)))
#define LAS __attribute__((address_space(3)))
typedef unsigned v4u __attribute__((ext_vector_type(4)));
typedef unsigned v2u __attribute__((ext_vector_type(2)));
typedef float f4 __attribute__((ext_vector_type(4)));
typedef float f2 __attribute__((ext_vector_type(2)));
typedef float f16v __attribute__((ext_vector_type(16)));
typedef short s4 __attribute__((ext_vector_type(4)));
typedef __bf16 b8 __attribute__((ext_vector_type(8)));
typedef __bf16 b2 __attribute__((ext_vector_type(2)));

#define W1 12
#define W2 24
#define J1 18
#define DLY2 40
#define DLYP 30
#define DLYB 10
#define NB0 55
#define NB1 52
#define WB 6
#define PRE2 1
#define SKIPA 0
#define SKIPB 0
#define WT 6
#define CODE_KB 24
#define QGO 4

namespace {

constexpr int NB_CU = 64, NT_CU = 32, NSPLIT_A = 10, NSLOT = 14, NCONS = 6;
constexpr float SC = 0.08838834764831845f * 1.4426950408889634f;
constexpr int SPIN_CAP = 1 << 18;

#define NS3 4
#define PSL 4
#define PSL1 8
#define PRESL 8
#define GSL 1
#define FSL 4
constexpr unsigned FS = 128;
constexpr unsigned WS_E = 0;
constexpr unsigned WS_FQ = 4096;
constexpr unsigned WS_FP = WS_FQ + 16384;
constexpr unsigned WS_QG = 65536;
constexpr unsigned WS_QAB = WS_QG + 262144;
constexpr unsigned WS_PBM = WS_QAB + 65536;
constexpr unsigned WS_PAM = WS_PBM + 32768;
constexpr unsigned WS_PAO = WS_PAM + 131072;
constexpr unsigned WS_PB = WS_PAO + 786432;
constexpr unsigned WS_F = WS_PB + 2097152;
constexpr unsigned WS_END = WS_F + 2097152;

constexpr unsigned LDS_BYTES = 163840;
constexpr unsigned SCR = 131072;
constexpr unsigned A_MISC = NSLOT * 8192;
constexpr unsigned A_LANDK = A_MISC, A_LANDV = A_MISC + 4, A_QUSED = A_MISC + 8, A_QCNT = A_MISC + 12, A_QB = A_MISC + 16, A_QRDY = A_MISC + 20, A_OUL = A_MISC + 24, A_OUL2 = A_MISC + 28;
constexpr unsigned A_DONE = A_MISC + 64;
constexpr unsigned A_QU2 = 10 * 8192;
constexpr unsigned A_QIMG = A_MISC + 256;
constexpr unsigned A_ML = A_MISC + 1280;
constexpr unsigned A_OIMG = A_MISC + 1792;
constexpr unsigned A_ST = A_MISC + 2048;
constexpr unsigned A_XIMG = A_MISC + 4096, A_DUMMY = A_MISC + 5120, A_QBI = A_MISC + 6144, A_QAS = A_MISC + 6656;
constexpr unsigned SLOT0 = 0, SLOT1 = 65536;
constexpr unsigned B_MX = SCR, B_AL = SCR + 512, B_LS = SCR + 640, B_P = SCR + 2048;
constexpr unsigned B_FM = SCR + 1280, B_FIN = SCR + 16384, B_T = SLOT1, B_DUMMY = SCR + 8192;
constexpr unsigned T_QBI = SCR, T_QAS = SCR + 1024, T_OL = SCR + 4096, T_RDY = SCR + 6400, T_WN = SCR + 6656, T_RED = 0;

__device__ __forceinline__ v4u lds128(unsigned a) { return *(const LAS v4u*)a; }
__device__ __forceinline__ void sto128(unsigned a, v4u v) { *(LAS v4u*)a = v; }
__device__ __forceinline__ void sto64(unsigned a, v2u v) { *(LAS v2u*)a = v; }
__device__ __forceinline__ void sto32(unsigned a, unsigned v) { *(LAS unsigned*)a = v; }
__device__ __forceinline__ float ldsf(unsigned a) { return *(const LAS float*)a; }
__device__ __forceinline__ void stof(unsigned a, float v) { *(LAS float*)a = v; }
__device__ __forceinline__ v2u trd(unsigned a) { return __builtin_bit_cast(v2u, __builtin_amdgcn_ds_read_tr16_b64_v4i16((LAS s4*)a)); }
__device__ __forceinline__ v4u trd2(unsigned a0, unsigned a1) { const v2u x = trd(a0), y = trd(a1); return (v4u){x.x, x.y, y.x, y.y}; }
__device__ __forceinline__ f4 mfma32(v4u a, v4u b, f4 c) {
  return __builtin_amdgcn_mfma_f32_16x16x32_bf16(__builtin_bit_cast(b8, a), __builtin_bit_cast(b8, b), c, 0, 0, 0); }
__device__ __forceinline__ f4 mfma16(v2u a, v2u b, f4 c) {
  return __builtin_amdgcn_mfma_f32_16x16x16bf16_1k(__builtin_bit_cast(s4, a), __builtin_bit_cast(s4, b), c, 0, 0, 0); }
__device__ __forceinline__ f16v mfmaL(v4u a, v4u b, f16v c) {
  return __builtin_amdgcn_mfma_f32_32x32x16_bf16(__builtin_bit_cast(b8, a), __builtin_bit_cast(b8, b), c, 0, 0, 0); }
__device__ __forceinline__ unsigned pk(float lo, float hi) { return __builtin_bit_cast(unsigned, __builtin_convertvector((f2){lo, hi}, b2)); }
__device__ __forceinline__ float asf(unsigned u) { return __builtin_bit_cast(float, u); }
__device__ __forceinline__ unsigned asu(float f) { return __builtin_bit_cast(unsigned, f); }
__device__ __forceinline__ float ex2(float x) { return __builtin_amdgcn_exp2f(x); }
template <int CTRL> __device__ __forceinline__ float dpp(float x) {
  return __builtin_bit_cast(float, __builtin_amdgcn_mov_dpp(__builtin_bit_cast(int, x), CTRL, 0xf, 0xf, true)); }
constexpr int XOR1 = 0xB1, XOR2 = 0x4E, ROR4 = 0x124, ROR8 = 0x128;
__device__ __forceinline__ float x16max(float x) { auto s = __builtin_amdgcn_permlane16_swap(asu(x), asu(x), false, false); return fmaxf(asf(s[0]), asf(s[1])); }
__device__ __forceinline__ float x32max(float x) { auto s = __builtin_amdgcn_permlane32_swap(asu(x), asu(x), false, false); return fmaxf(asf(s[0]), asf(s[1])); }
__device__ __forceinline__ float x16sum(float x) { auto s = __builtin_amdgcn_permlane16_swap(asu(x), asu(x), false, false); return asf(s[0]) + asf(s[1]); }
__device__ __forceinline__ float x32sum(float x) { auto s = __builtin_amdgcn_permlane32_swap(asu(x), asu(x), false, false); return asf(s[0]) + asf(s[1]); }
__device__ __forceinline__ float row16max(float x) { x = fmaxf(x, dpp<XOR1>(x)); x = fmaxf(x, dpp<XOR2>(x)); x = fmaxf(x, dpp<ROR4>(x)); return fmaxf(x, dpp<ROR8>(x)); }
__device__ __forceinline__ float row16sum(float x) { x += dpp<XOR1>(x); x += dpp<XOR2>(x); x += dpp<ROR4>(x); return x + dpp<ROR8>(x); }

template <int IMM> __device__ __forceinline__ void dma(const void* sbase, unsigned voff, unsigned lds) {
  asm volatile("s_mov_b32 m0, %2\n\ts_nop 0\n\tglobal_load_lds_dwordx4 %0, %1 offset:%3 nt" :: "v"(voff), "s"(sbase), "s"(lds), "n"(IMM) : "memory");
}
template <int W> __device__ __forceinline__ void dma4(const void* sbase, unsigned v0, unsigned v1, unsigned v2, unsigned v3, unsigned lds) {
  asm volatile("s_mov_b32 m0, %5\n\ts_waitcnt vmcnt(%6)\n\tglobal_load_lds_dwordx4 %0, %4 nt\n\tglobal_load_lds_dwordx4 %1, %4 offset:1024 nt\n\t"
               "s_waitcnt vmcnt(%6)\n\tglobal_load_lds_dwordx4 %2, %4 offset:2048 nt\n\tglobal_load_lds_dwordx4 %3, %4 offset:3072 nt"
               :: "v"(v0), "v"(v1), "v"(v2), "v"(v3), "s"(sbase), "s"(lds), "n"(W - 2) : "memory");
}
__device__ __forceinline__ void dma_keep(const void* sbase, unsigned voff, unsigned lds) {
  asm volatile("s_mov_b32 m0, %2\n\ts_nop 0\n\tglobal_load_lds_dwordx4 %0, %1" :: "v"(voff), "s"(sbase), "s"(lds) : "memory");
}
template <int N> __device__ __forceinline__ void vmwait() { asm volatile("s_waitcnt vmcnt(%0)" :: "n"(N) : "memory"); }
__device__ __forceinline__ void bar() { asm volatile("s_waitcnt lgkmcnt(0)\n\ts_barrier" ::: "memory"); }
__device__ __forceinline__ void lgk0() { asm volatile("s_waitcnt lgkmcnt(0)" ::: "memory"); }
__device__ __forceinline__ void cfence() { asm volatile("" ::: "memory"); }
__device__ __forceinline__ unsigned opq(unsigned x) { asm volatile("" : "+v"(x)); return x; }
__device__ __forceinline__ unsigned lds_peek(unsigned a) { return __builtin_amdgcn_readfirstlane(*(volatile LAS unsigned*)a); }
template <int SL = 1> __device__ __forceinline__ void lds_wait_ge(unsigned a, unsigned want) {
  _Pragma("nounroll") for (int it = 0; it < SPIN_CAP * 8; ++it) { if (lds_peek(a) >= want) break; if (SL) __builtin_amdgcn_s_sleep(SL); }
  cfence();
}
typedef __amdgpu_buffer_rsrc_t rsrc_t;
constexpr int SC1 = 16;
__device__ __forceinline__ v4u gld128(rsrc_t r, unsigned off) { return __builtin_amdgcn_raw_buffer_load_b128(r, off, 0, SC1); }
__device__ __forceinline__ v2u gld64(rsrc_t r, unsigned off) { return __builtin_amdgcn_raw_buffer_load_b64(r, off, 0, SC1); }
__device__ __forceinline__ unsigned gld32(rsrc_t r, unsigned off) { return __builtin_amdgcn_raw_buffer_load_b32(r, off, 0, SC1); }
__device__ __forceinline__ void gst128(rsrc_t r, unsigned off, v4u v) { __builtin_amdgcn_raw_buffer_store_b128(v, r, off, 0, SC1); }
__device__ __forceinline__ void gst64(rsrc_t r, unsigned off, v2u v) { __builtin_amdgcn_raw_buffer_store_b64(v, r, off, 0, SC1); }
__device__ __forceinline__ void gst32(rsrc_t r, unsigned off, unsigned v) { __builtin_amdgcn_raw_buffer_store_b32(v, r, off, 0, SC1); }
__device__ __forceinline__ void flag_line(rsrc_t r, unsigned off, int lane, unsigned tag) { if (lane < 8) gst128(r, off + lane * 16, (v4u){tag, tag, tag, tag}); }
__device__ __forceinline__ bool tagok(v4u v, unsigned tag) { return v.y == tag && v.w == tag; }

#define STAMP(i) do {} while (0)
#define STAMP_DECL do {} while (0)
#define STAMP_OUT() do {} while (0)

__device__ __forceinline__ void gather_qb(unsigned img, int h, int lane, rsrc_t rs, unsigned tag) {
  v4u qg[4];
  const unsigned off = WS_QG + (h * 128 + 2 * lane) * 8;
  _Pragma("nounroll") for (int it = 0; it < SPIN_CAP; ++it) {
    cfence();
    bool ok = true;
#pragma unroll
    for (int k = 0; k < 4; ++k) { qg[k] = gld128(rs, off + (k & 1) * 65536 + (k >> 1) * 32768); ok &= tagok(qg[k], tag); }
    if (__all(ok)) break;
    __builtin_amdgcn_s_sleep(1);
  }
  sto32(img + lane * 4, pk(asf(qg[0].x) + asf(qg[1].x), asf(qg[0].z) + asf(qg[1].z)));
  sto32(img + 256 + lane * 4, pk(asf(qg[2].x) + asf(qg[3].x), asf(qg[2].z) + asf(qg[3].z)));
}
__device__ __forceinline__ void lds_inc(unsigned a) { __hip_atomic_fetch_add((LAS unsigned*)a, 1u, __ATOMIC_RELAXED, __HIP_MEMORY_SCOPE_WORKGROUP); }

__device__ __forceinline__ void out_frags(unsigned ou, int w, int lane, v4u (&wf)[4]) {
  const int g = lane >> 4, q = (lane & 15) >> 2, p = lane & 3, f3 = q | ((g & 1) << 2);
#pragma unroll
  for (int tt = 0; tt < 4; ++tt) {
    const unsigned a0 = ou + (8 * g + q) * 1024 + (((2 * (4 * w + tt) + (p >> 1)) ^ (f3 << 1)) << 4) + (p & 1) * 8;
    wf[tt] = trd2(a0, a0 + 4096);
  }
}

}

__global__ void __launch_bounds__(512, 2) fused_decode(
    const char* __restrict__ Ax, const char* __restrict__ Kc, const char* __restrict__ Vc, const char* __restrict__ Wq, const char* __restrict__ Wo,
    const char* __restrict__ Bx, const char* __restrict__ Ckv, const char* __restrict__ Bwq, const char* __restrict__ Wuk, const char* __restrict__ Wuv,
    const char* __restrict__ Bwo, unsigned short* __restrict__ out, char* __restrict__ ws) {
  const int bid = blockIdx.x, tid = threadIdx.x, lane = tid & 63, wave = __builtin_amdgcn_readfirstlane(tid >> 6);
  const int g = lane >> 4, c16 = lane & 15;
  STAMP_DECL;
  STAMP(0);
  const int iA = bid - (NB_CU + NT_CU);
  const bool isA = iA >= 0, isB = bid < NB_CU;
  const int ty = iA >> 6;
  int dA = 95 - bid, dU = 223 - bid, dB = bid - 64, dQ = 159 - bid, dW = 2 - wave;
  asm("" : "+s"(dA), "+s"(dU), "+s"(dB), "+s"(dQ), "+s"(dW));
  const int mA = dA >> 31, mU = dU >> 31, mB = dB >> 31, mP = mA & ~mU, sh = mP & 1;
  const int hh = (bid >> sh) & 31;
  const unsigned row0 = (mA & ((((bid | mU) & 1) << 8) + 64 * wave - 64)) | (~mA & (32 * wave));
  const unsigned l0 = (mA & (SCR - 16384 + wave * 16384 - ((dW >> 31) & (SCR + 32768 - A_QU2)))) | (~mA & (wave * 8192));
  const unsigned karg = 0x40 - (mP & (8 + ((dQ >> 31) & 32))), more = mA;
  if ((unsigned)(wave + mA + (mB & 8)) < (unsigned)(8 + (mA & -4))) {
    const unsigned b0 = (row0 + g) * 8192 + hh * 256 + ((c16 ^ (g << sh)) << 4), x1 = ~mP & 64;
    unsigned v0 = b0, v1 = (b0 ^ x1) + 32768, v2 = (b0 ^ 128) + 65536, v3 = (b0 ^ x1 ^ 128) + 98304;
    if (DLYP && karg == 0x18) __builtin_amdgcn_s_sleep(DLYP);
    unsigned long long wp;
#define D4 "global_load_lds_dwordx4 %1, %0 nt\n\ts_add_u32 m0, m0, 0x400\n\tv_add_u32 %1, 0x20000, %1\n\t" \
           "global_load_lds_dwordx4 %2, %0 nt\n\ts_add_u32 m0, m0, 0x400\n\tv_add_u32 %2, 0x20000, %2\n\t" \
           "global_load_lds_dwordx4 %3, %0 nt\n\ts_add_u32 m0, m0, 0x400\n\tv_add_u32 %3, 0x20000, %3\n\t" \
           "global_load_lds_dwordx4 %4, %0 nt\n\ts_add_u32 m0, m0, 0x400\n\tv_add_u32 %4, 0x20000, %4\n\t"
    asm volatile("s_load_dwordx2 %0, %5, %6\n\ts_mov_b32 m0, %7\n\ts_waitcnt lgkmcnt(0)\n\t" D4 D4
                 "s_cmp_eq_u32 %8, 0\n\ts_cbranch_scc1 1f\n\t" D4 D4 "1:"
                 : "=&s"(wp), "+v"(v0), "+v"(v1), "+v"(v2), "+v"(v3)
                 : "s"(__builtin_amdgcn_kernarg_segment_ptr()), "s"(karg), "s"(l0), "s"(more) : "memory", "scc");
#undef D4
  }
  {
    unsigned long long pc;
    asm volatile("s_getpc_b64 %0" : "=s"(pc));
    const int kb = ((bid >> 3) + 4 * wave) & 31;
    if (CODE_KB) dma_keep((const char*)((pc & ~1023ull) + (unsigned)((kb < CODE_KB ? kb : CODE_KB - 1) * 1024)), lane * 16, isA ? A_DUMMY : B_DUMMY);
  }
  const rsrc_t rs = __builtin_amdgcn_make_buffer_rsrc(ws, 0, WS_END, 0x00020000);
  asm volatile("" :: "s"(Ax), "s"(Kc), "s"(Vc), "s"(Wq), "s"(Wo), "s"(Bx), "s"(Ckv), "s"(Bwq), "s"(Wuk), "s"(Wuv), "s"(Bwo), "s"(out), "s"(ws));
  unsigned tag;
#define GET_TAG() do { asm volatile("s_load_dword %0, %1, 0x0\n\ts_waitcnt lgkmcnt(0)" : "=s"(tag) : "s"(ws) : "memory"); tag += 1u; } while (0)
  const char* cp = Ckv + ((size_t)(bid >> 5) * 8192 + 256 * (bid & 31) + 8 * wave) * 1024;
  const unsigned vc0 = (lane ^ ((wave & 1) ? 3 : 0)) << 4, vc1 = (lane ^ ((wave & 1) ? 1 : 2)) << 4;
#define LD_C(rho, u) dma<((u) & 3) * 1024>(cp + (size_t)(rho) * 65536 + ((u) >> 2) * 4096, (((u) >> 2) ? vc1 : vc0) ^ (((u) & 3) << 6), ((rho) & 1) * 65536 + wave * 8192 + ((u) >> 2) * 4096)
  if (isA) {
    if (wave == 1 && ty < 2) dma<0>((ty ? Ax : Bx) + 512 * (bid & 1), (lane >> 5) * 1024 + (lane & 31) * 16, A_XIMG);
    if (tid < 32) sto32(A_MISC + tid * 4, 0u);
    bar();
  } else if (isB) {
    __builtin_amdgcn_s_sleep(DLYB);
    LD_C(0, 0); LD_C(0, 1); LD_C(0, 2); LD_C(0, 3); LD_C(0, 4); LD_C(0, 5);
  }

  if (isA) {
    const int split = iA >> 4, pair = (iA + split) & 15, b = pair >> 3, kvh = pair & 7;
    constexpr int NB2 = (512 - 4 * NB0 - 4 * NB1) / 2;
    const int nblk = ty == 0 ? NB0 : ty == 1 ? NB1 : NB2, blk0 = (ty == 0 ? 0 : ty == 1 ? 4 * NB0 : 4 * (NB0 + NB1)) + (split & 3) * nblk;
    if (wave == 0 || wave == 7) {
      __builtin_amdgcn_s_setprio(3);
      static_assert(NSPLIT_A == 10 && W1 >= 4 && W1 % 4 == 0 && W2 % 4 == 0 && W1 <= W2 && W2 <= 32 && J1 <= NB0 && J1 <= NB1 && J1 <= NB2, "window");
      const bool isv = wave == 7;
      int pub = 0, j = 0;
      unsigned sl = 0;
      const char* gp = (isv ? Vc : Kc) + ((size_t)pair * 8192 + blk0 * 16) * 256;
      const unsigned v0 = opq(g * 256 + ((c16 ^ (isv ? g << 1 : g)) << 4));
      const unsigned v1 = opq(v0 ^ (isv ? 128 : 64)), v2 = opq(v0 ^ (isv ? 0 : 128)), v3 = opq(v0 ^ (isv ? 128 : 192));
      const unsigned lo = isv ? 4096 : 0, la = opq(lane ? A_MISC + 1536 + lane * 4 : isv ? A_LANDV : A_LANDK);
      unsigned dn = 0;
#define PUBLISH(val) do { pub = max(pub, (val)); *(volatile LAS unsigned*)la = (unsigned)pub; } while (0)
#define GATE(addr, need) do { if (lds_peek(addr) < (need)) { vmwait<0>(); PUBLISH(j); lds_wait_ge(addr, need); } } while (0)
#define PART(W) do { \
        if (j >= NSLOT) { if ((unsigned)__builtin_amdgcn_readfirstlane(dn) < (unsigned)(j - NSLOT + 1)) GATE(A_DONE + sl, (unsigned)(j - NSLOT + 1)); } \
        else if (j == 10) GATE(A_QUSED, 4u); \
        dma4<W>(gp, v0, v1, v2, v3, sl * 2048 + lo); \
        ++j; gp += 4096; sl = sl == 4 * (NSLOT - 1) ? 0 : sl + 4; \
        PUBLISH(j - W / 4); dn = *(volatile LAS unsigned*)(A_DONE + sl); } while (0)
      STAMP(9);
      GET_TAG();
      lds_wait_ge(A_QCNT, 4u);
      STAMP(10);
#pragma nounroll
      while (j < J1) PART(W1);
      STAMP(11);
#pragma nounroll
      while (j < nblk) PART(W2);
      if (ty < 2) {
        const char* op = Wo + (size_t)iA * 32768 + (isv ? 16384 : 0);
        const unsigned o0 = lane << 4, o1 = o0 ^ 32, o2 = o0 ^ 64, o3 = o0 ^ 96;
#pragma unroll
        for (int k = 0; k < 4; ++k) {
          const unsigned x = ((k >> 1) & 1) << 7;
          if (4 * k + 4 <= W2) { dma4<W2>(op + k * 4096, o0 ^ x, o1 ^ x, o2 ^ x, o3 ^ x, SCR + lo * 4 + k * 4096); PUBLISH(j + k + 1 - W2 / 4); }
          else dma4<62>(op + k * 4096, o0 ^ x, o1 ^ x, o2 ^ x, o3 ^ x, SCR + lo * 4 + k * 4096);
        }
        if (W2 > 16) { vmwait<28>(); PUBLISH(j - 3); vmwait<24>(); PUBLISH(j - 2); vmwait<20>(); PUBLISH(j - 1); vmwait<16>(); PUBLISH(j); }
      } else {
        vmwait<28>(); PUBLISH(j - 7); vmwait<24>(); PUBLISH(j - 6); vmwait<20>(); PUBLISH(j - 5); vmwait<16>(); PUBLISH(j - 4);
        vmwait<12>(); PUBLISH(j - 3); vmwait<8>(); PUBLISH(j - 2); vmwait<4>(); PUBLISH(j - 1); vmwait<0>(); PUBLISH(j);
      }
      __builtin_amdgcn_s_setprio(0);
      STAMP(5);
#undef PUBLISH
#undef GATE
#undef PART
    } else {
      GET_TAG();
      vmwait<0>();
      if (wave <= 4) {
        const int w1 = wave - 1;
        if (ty < 2) {
          if (lane == 0) lds_inc(A_QCNT);
          const int q = c16 >> 2, p = lane & 3, f3 = q | ((g & 1) << 2);
          const unsigned a0 = (8 * g + q) * 256 + (((4 * w1 + (p >> 1)) ^ (f3 << 1)) << 4) + (p & 1) * 8;
          const unsigned aa[2] = {opq(SCR + a0), opq(A_QU2 + a0)};
          lds_wait_ge(A_QCNT, 4u);
          STAMP(1);
          v4u w0[8], w1f[8], xf[8];
#pragma unroll
          for (int s = 0; s < 8; ++s) {
            const unsigned a = aa[s >> 2] + (s & 3) * 8192;
            w0[s] = trd2(a, a + 1024); w1f[s] = trd2((a ^ 32), (a ^ 32) + 1024);
            xf[s] = lds128(A_XIMG + (c16 & 1) * 512 + g * 16 + s * 64);
          }
          __builtin_amdgcn_sched_barrier(0);
          f4 d0 = {0.f, 0.f, 0.f, 0.f}, d1 = d0;
#pragma unroll
          for (int s = 0; s < 8; ++s) { d0 = mfma32(w0[s], xf[s], d0); d1 = mfma32(w1f[s], xf[s], d1); }
          const int pr = (c16 >> 1) & 1, nb = (c16 >> 2) & 1;
          const f4 e = nb ? d1 : d0;
          if (c16 < 8) gst128(rs, WS_QG + (((ty * 2 + (bid & 1)) * 2 + (c16 & 1)) * 4096 + hh * 128 + 32 * w1 + 16 * nb + 4 * g + 2 * pr) * 8,
                              (v4u){asu(pr ? e[2] : e[0]), tag, asu(pr ? e[3] : e[1]), tag});
          lgk0();
        } else {
          STAMP(1);
          if (w1 == 3 && lane == 0) *(volatile LAS unsigned*)A_OUL2 = 1u;
          const unsigned a0 = opq((w1 < 2 ? SCR : A_QU2 - 32768) + w1 * 16384 + c16 * 256 + ((g ^ c16) << 4));
          v4u uf[16];
#pragma unroll
          for (int i = 0; i < 16; ++i) uf[i] = lds128((a0 ^ ((i & 3) << 6)) + (i >> 2) * 4096);
          lds_wait_ge(A_QB, 1u);
          v4u qb[4];
#pragma unroll
          for (int s = 0; s < 4; ++s) qb[s] = lds128(A_QBI + (c16 & 1) * 256 + s * 64 + g * 16);
          __builtin_amdgcn_sched_barrier(0);
          f4 d[4];
#pragma unroll
          for (int rb = 0; rb < 4; ++rb) d[rb] = (f4){0.f, 0.f, 0.f, 0.f};
#pragma unroll
          for (int s = 0; s < 4; ++s)
#pragma unroll
            for (int rb = 0; rb < 4; ++rb) d[rb] = mfma32(uf[4 * rb + s], qb[s], d[rb]);
          const int rb = (c16 >> 1) & 3;
          const f4 e = rb == 0 ? d[0] : rb == 1 ? d[1] : rb == 2 ? d[2] : d[3];
          if (c16 < 8) sto64(A_QAS + w1 * 256 + (c16 & 1) * 128 + (16 * rb + 4 * g) * 2, (v2u){pk(e[0], e[1]), pk(e[2], e[3])});
          lgk0();
          if (lane < 16) gst128(rs, WS_QAB + ((lane >> 3) * 32 + hh) * 1024 + 512 + w1 * 128 + (lane & 7) * 16, lds128(A_QAS + w1 * 256 + lane * 16));
          vmwait<0>();
        }
        unsigned old = 0;
        if (lane == 0) old = __hip_atomic_fetch_add((LAS unsigned*)A_QUSED, 1u, __ATOMIC_RELAXED, __HIP_MEMORY_SCOPE_WORKGROUP);
        if (ty == 2 && __builtin_amdgcn_readfirstlane(old) == 3u) {
          flag_line(rs, WS_FQ + (32 + hh) * FS, lane, tag);
          cfence();
          if (lane == 0) *(volatile LAS unsigned*)A_QCNT = 4u;
        }
        STAMP(2);
      }
      if (wave == 5) {
        if (ty == 2) {
          lds_wait_ge(A_OUL2, 1u);
          gather_qb(A_QBI, hh, lane, rs, tag);
          lgk0();
          if (lane == 0) *(volatile LAS unsigned*)A_QB = 1u;
          STAMP(1);
        }
        lds_wait_ge(A_LANDK, ty == 2 ? 1u : (unsigned)QGO);
        v4u v[8];
        const unsigned off = WS_QG + 131072 + (b * 4096 + kvh * 512 + 2 * lane) * 8;
        _Pragma("nounroll") for (int it = 0; it < SPIN_CAP; ++it) {
          cfence();
          bool ok = true;
#pragma unroll
          for (int k = 0; k < 8; ++k) { v[k] = gld128(rs, off + (k & 1) * 65536 + (k >> 1) * 1024); ok &= tagok(v[k], tag); }
          if (__all(ok)) break;
          __builtin_amdgcn_s_sleep(4);
        }
#pragma unroll
        for (int k = 0; k < 4; ++k) sto32(A_QIMG + k * 256 + lane * 4, pk(asf(v[2 * k].x) + asf(v[2 * k + 1].x), asf(v[2 * k].z) + asf(v[2 * k + 1].z)));
        lgk0();
        if (lane == 0) *(volatile LAS unsigned*)A_QRDY = 1u;
      }
      lds_wait_ge(A_QRDY, 1u);
      STAMP(3);
      v4u qf[4];
#pragma unroll
      for (int s = 0; s < 4; ++s) qf[s] = lds128(A_QIMG + (c16 & 3) * 256 + s * 64 + g * 16);
      const unsigned ko = c16 * 256 + ((g ^ c16) << 4);
      const int vr = 4 * g + (c16 >> 2);
      const unsigned vo = 4096 + vr * 256 + ((((lane & 3) >> 1) ^ ((vr & 7) << 1)) << 4) + (lane & 1) * 8;
      float m = -INFINITY, l = 0.f;
      f4 acc[8];
#pragma unroll
      for (int t = 0; t < 8; ++t) acc[t] = (f4){0.f, 0.f, 0.f, 0.f};
      int slot = wave - 1;
      for (int j = wave - 1; j < nblk; j += NCONS) {
        const unsigned sb = slot * 8192;
        lds_wait_ge(A_LANDK, (unsigned)(j + 1));
        v4u kf[4];
#pragma unroll
        for (int s = 0; s < 4; ++s) kf[s] = lds128(sb + (ko ^ (s << 6)));
        __builtin_amdgcn_sched_barrier(0);
        f4 s4_ = {0.f, 0.f, 0.f, 0.f};
#pragma unroll
        for (int s = 0; s < 4; ++s) s4_ = mfma32(kf[s], qf[s], s4_);
        float mx = fmaxf(fmaxf(s4_[0], s4_[1]), fmaxf(s4_[2], s4_[3]));
        mx = x32max(x16max(mx));
        const float mn = fmaxf(m, mx), al = ex2((m - mn) * SC), mc = -mn * SC;
        const float p0 = ex2(fmaf(s4_[0], SC, mc)), p1 = ex2(fmaf(s4_[1], SC, mc)), p2 = ex2(fmaf(s4_[2], SC, mc)), p3 = ex2(fmaf(s4_[3], SC, mc));
        l = fmaf(l, al, (p0 + p1) + (p2 + p3));
        m = mn;
        v2u pb = {pk(p0, p1), pk(p2, p3)};
        unsigned va[8];
#pragma unroll
        for (int t = 0; t < 8; ++t) va[t] = sb + (vo ^ (t << 5));
        if (!__all(al == 1.f)) {
#pragma unroll
          for (int t = 0; t < 8; ++t) acc[t] *= al;
        }
        asm volatile("" : "+v"(pb), "+v"(l), "+v"(acc[0]), "+v"(acc[1]), "+v"(acc[2]), "+v"(acc[3]), "+v"(acc[4]), "+v"(acc[5]), "+v"(acc[6]), "+v"(acc[7]),
                     "+v"(va[0]), "+v"(va[1]), "+v"(va[2]), "+v"(va[3]), "+v"(va[4]), "+v"(va[5]), "+v"(va[6]), "+v"(va[7]));
        lds_wait_ge<0>(A_LANDV, (unsigned)(j + 1));
        v2u vf[8];
#pragma unroll
        for (int t = 0; t < 8; ++t) vf[t] = trd(va[t]);
        lgk0();
        if (lane == 0) *(volatile LAS unsigned*)(A_DONE + slot * 4) = (unsigned)(j + 1);
#pragma unroll
        for (int t = 0; t < 8; ++t) acc[t] = mfma16(vf[t], pb, acc[t]);
        slot += NCONS; if (slot >= NSLOT) slot -= NSLOT;
      }
      STAMP(4);
      l = x32sum(x16sum(l));
      if (c16 < 4) {
#pragma unroll
        for (int t = 0; t < 8; ++t) sto128(A_ST + (wave - 1) * 2048 + c16 * 512 + ((t ^ c16) * 64) + g * 16, __builtin_bit_cast(v4u, acc[t]));
        if (g == 0) sto64(A_ML + (c16 * 8 + wave - 1) * 8, (v2u){asu(m * SC), asu(l)});
      }
    }
    bar();
    if (wave >= 3 && wave <= 6) {
      const int hq = wave - 3;
      v2u ml[NCONS];
      f2 ow[NCONS];
      const unsigned sa = opq(A_ST + hq * 512 + ((lane * 8) ^ (hq << 6))), ma = opq(A_ML + hq * 64);
#pragma unroll
      for (int c = 0; c < NCONS; ++c) { ml[c] = *(const LAS v2u*)(ma + c * 8); ow[c] = *(const LAS f2*)(sa + c * 2048); }
      float M = asf(ml[0].x);
#pragma unroll
      for (int c = 1; c < NCONS; ++c) M = fmaxf(M, asf(ml[c].x));
      float L = 0.f, o0 = 0.f, o1 = 0.f;
#pragma unroll
      for (int c = 0; c < NCONS; ++c) { const float wt = ex2(asf(ml[c].x) - M); L = fmaf(wt, asf(ml[c].y), L); o0 = fmaf(wt, ow[c].x, o0); o1 = fmaf(wt, ow[c].y, o1); }
      const int rec = (b * 32 + kvh * 4 + hq) * NSPLIT_A + split;
      gst128(rs, WS_PAO + rec * 1024 + lane * 16, (v4u){asu(o0), tag, asu(o1), tag});
      if (lane < 8) gst128(rs, WS_PAM + rec * 128 + lane * 16, (v4u){asu(M), tag, asu(L), tag});
    }
    STAMP(6);
    if (ty < 2) {
      const int oh = iA >> 2, odq = iA & 3;
      if (wave == 0 || wave == 7) { vmwait<0>(); if (lane == 0) lds_inc(A_OUL); }
      lds_wait_ge(A_OUL, 2u);
      v4u wf[4];
      out_frags(SCR, wave, lane, wf);
      const int tt = (c16 >> 1) & 3, pr = c16 >> 3, n = 64 * wave + 16 * tt + 4 * g + 2 * pr;
      unsigned fo = WS_F + ((((n >> 3) * 128 + iA) * 16) + (c16 & 1) * 8 + (n & 7)) * 8, oa = A_OIMG + (c16 & 1) * 64 + g * 16;
      asm volatile("" : "+v"(fo), "+v"(oa));
      if (wave == 1) {
        const int rec = ((lane >> 5) * 32 + oh) * NSPLIT_A + (g & 1) * 5;
        v4u v[5], ml[5];
        _Pragma("nounroll") for (int it = 0; it < SPIN_CAP; ++it) {
          cfence();
          bool ok = true;
#pragma unroll
          for (int i = 0; i < 5; ++i) {
            v[i] = gld128(rs, WS_PAO + (rec + i) * 1024 + (32 * odq + 2 * c16) * 8);
            ml[i] = gld128(rs, WS_PAM + (rec + i) * 128 + odq * 16);
            ok &= tagok(v[i], tag) && tagok(ml[i], tag);
          }
          if (__all(ok)) break;
          __builtin_amdgcn_s_sleep(GSL);
        }
        float M = asf(ml[0].x);
#pragma unroll
        for (int i = 1; i < 5; ++i) M = fmaxf(M, asf(ml[i].x));
        M = x16max(M);
        float L = 0.f, o0 = 0.f, o1 = 0.f;
#pragma unroll
        for (int i = 0; i < 5; ++i) { const float wt = ex2(asf(ml[i].x) - M); L = fmaf(wt, asf(ml[i].z), L); o0 = fmaf(wt, asf(v[i].x), o0); o1 = fmaf(wt, asf(v[i].z), o1); }
        const float inv = 1.f / x16sum(L);
        o0 = x16sum(o0); o1 = x16sum(o1);
        if (!(g & 1)) sto32(A_OIMG + (lane >> 5) * 64 + c16 * 4, pk(o0 * inv, o1 * inv));
      }
      STAMP(7);
      bar();
      const v4u ob = lds128(oa);
      f4 d[4];
#pragma unroll
      for (int t = 0; t < 4; ++t) d[t] = mfma32(wf[t], ob, (f4){0.f, 0.f, 0.f, 0.f});
      const f4 e = tt == 0 ? d[0] : tt == 1 ? d[1] : tt == 2 ? d[2] : d[3];
      gst128(rs, fo, (v4u){asu(pr ? e[2] : e[0]), tag, asu(pr ? e[3] : e[1]), tag});
      STAMP(8);
    }
    STAMP_OUT();
  } else if (isB) {
    const int b = bid >> 5, sp = bid & 31, w = wave, lane_ = lane;
    const unsigned vl = lane << 4;
#define LD_S(S, rho, u) asm volatile("global_load_dwordx4 %0, %1, %2 offset:%3 nt" : "=v"(S[u]) : "v"(vl), "s"(cp + (size_t)(rho) * 65536 + ((u) >> 2) * 4096), "n"(((u) & 3) * 1024) : "memory")
#define ST_S(S, rho) do { unsigned a0_ = ((rho) & 1) * 65536 + w * 8192 + vc0, a1_ = ((rho) & 1) * 65536 + w * 8192 + vc1; asm volatile("" : "+v"(a0_), "+v"(a1_)); \
      _Pragma("unroll") for (int u_ = 0; u_ < 8; ++u_) sto128(u_ * 1024 + (((u_ >> 2) ? a1_ : a0_) ^ ((u_ & 3) << 6)), S[u_]); } while (0)
#define PB() vmwait<WB - 2>()
    v4u S2[8], S3[8];
    GET_TAG();
    PB(); LD_C(0, 6); LD_C(0, 7);
    STAMP(1);
    PB(); LD_C(1, 0); LD_C(1, 1);
    PB(); LD_C(1, 2); LD_C(1, 3);
    PB(); LD_C(1, 4); LD_C(1, 5);
    PB(); LD_C(1, 6); LD_C(1, 7);
    STAMP(2);
    PB(); LD_S(S2, 2, 0); LD_S(S2, 2, 1);
    PB(); LD_S(S2, 2, 2); LD_S(S2, 2, 3);
    PB(); LD_S(S2, 2, 4); LD_S(S2, 2, 5);
    PB(); LD_S(S2, 2, 6); LD_S(S2, 2, 7);
#pragma unroll
    for (int u = 0; u < NS3; u += 2) { PB(); LD_S(S3, 3, u); LD_S(S3, 3, u + 1); }
    STAMP(3);
    if (w == 0) {
      _Pragma("nounroll") for (int it = 0; it < SPIN_CAP; ++it) { cfence(); if (__all(gld32(rs, WS_FQ + lane * FS) == tag)) break; __builtin_amdgcn_s_sleep(PSL); }
    }
    bar();
    STAMP(4);
    {
      v4u t[4];
      const char* qa = ws + WS_QAB + (b * 32 + 4 * w) * 1024;
#pragma unroll
      for (int i = 0; i < 4; ++i) asm volatile("global_load_dwordx4 %0, %1, %2 offset:%3 sc1" : "=v"(t[i]) : "v"(vl), "s"(qa), "n"(i * 1024) : "memory");
      constexpr int NA = NS3 < 8 ? 2 : 0, NC = 8 - NS3 - NA < 4 ? 8 - NS3 - NA : 4;
#pragma unroll
      for (int u = NS3; u < NS3 + NA; ++u) LD_S(S3, 3, u);
      asm volatile("s_waitcnt vmcnt(%12)" : "+v"(t[0]), "+v"(t[1]), "+v"(t[2]), "+v"(t[3]), "+v"(S2[0]), "+v"(S2[1]), "+v"(S2[2]), "+v"(S2[3]),
                   "+v"(S2[4]), "+v"(S2[5]), "+v"(S2[6]), "+v"(S2[7]) : "n"(NA) : "memory");
#pragma unroll
      for (int u = NS3 + NA; u < NS3 + NA + NC; ++u) LD_S(S3, 3, u);
#pragma unroll
      for (int i = 0; i < 4; ++i) sto128(SCR + (4 * w + i) * 1024 + ((lane ^ ((4 * w + i) & 15)) << 4), t[i]);
    }
    bar();
    const int kb4 = w & 3, hg = w >> 2;
    v4u qf[16];
#pragma unroll
    for (int s = 0; s < 16; ++s) qf[s] = lds128(SCR + (16 * hg + c16) * 1024 + ((((4 * s) & 15) ^ g ^ c16) << 4) + (s >> 2) * 256);
    STAMP(5);
    const unsigned fk = ((c16 & 3) << 2) | ((0x78u >> (2 * (c16 >> 2))) & 3u);
    const unsigned ka = (16 * kb4 + c16) * 1024 + ((g ^ fk) << 4);
    const int h2 = lane >> 5, blk = g & 1, qq = c16 >> 2, pp = lane & 3;
    const unsigned ft0 = (qq << 2) | (h2 ? 3u : 0u), ft1 = (qq << 2) | (h2 ? 1u : 2u);
    const unsigned cbase = 8 * w + 2 * blk + (pp >> 1);
    const unsigned va00 = (8 * h2 + qq) * 1024 + ((cbase ^ ft0) << 4) + (pp & 1) * 8, va01 = (8 * h2 + 4 + qq) * 1024 + ((cbase ^ ft1) << 4) + (pp & 1) * 8;
    const unsigned mxa = opq(B_MX + (16 * hg + c16) * 4), pwa = opq(B_P + kb4 * 1536 + (16 * hg + c16) * 48 + g * 8);
    const unsigned pra = opq(B_P + (lane & 31) * 48 + h2 * 16), ala = opq(B_AL + (lane & 31) * 4);
    float m = -INFINITY, l = 0.f;
    f16v acc0, acc1;
#pragma unroll
    for (int i = 0; i < 16; ++i) { acc0[i] = 0.f; acc1[i] = 0.f; }
#pragma unroll
    for (int rho = 0; rho < 2; ++rho) {
      unsigned kr = ka, vr0 = va00, vr1 = va01;
      asm volatile("" : "+v"(kr), "+v"(vr0), "+v"(vr1));
      bar();
      f4 sa = {0.f, 0.f, 0.f, 0.f}, sb = sa;
      {
        v4u kf[8];
#pragma unroll
        for (int s = 0; s < 8; ++s) kf[s] = lds128((kr ^ ((s & 3) << 6)) + (s >> 2) * 256);
        __builtin_amdgcn_sched_barrier(0);
#pragma unroll
        for (int s = 0; s < 8; ++s) {
          sa = mfma32(kf[s], qf[s], sa);
          kf[s] = lds128((kr ^ ((s & 3) << 6)) + (2 + (s >> 2)) * 256);
          __builtin_amdgcn_sched_barrier(0);
        }
        kr += 65536;
#pragma unroll
        for (int s = 0; s < 8; ++s) {
          sa = mfma32(kf[s], qf[8 + s], sa);
          kf[s] = lds128((kr ^ ((s & 3) << 6)) + (s >> 2) * 256);
          __builtin_amdgcn_sched_barrier(0);
        }
#pragma unroll
        for (int s = 0; s < 8; ++s) {
          sb = mfma32(kf[s], qf[s], sb);
          kf[s] = lds128((kr ^ ((s & 3) << 6)) + (2 + (s >> 2)) * 256);
          __builtin_amdgcn_sched_barrier(0);
        }
#pragma unroll
        for (int s = 0; s < 8; ++s) sb = mfma32(kf[s], qf[8 + s], sb);
      }
      if (rho == 0 && NS3 == 0) { LD_S(S3, 3, 6); LD_S(S3, 3, 7); }
      float mx = fmaxf(fmaxf(fmaxf(sa[0], sa[1]), fmaxf(sa[2], sa[3])), fmaxf(fmaxf(sb[0], sb[1]), fmaxf(sb[2], sb[3])));
      mx = x32max(x16max(mx));
      if (g == 0) stof(mxa + kb4 * 128, mx);
      v4u c0[4], c1[4];
#pragma unroll
      for (int ks = 0; ks < 4; ++ks) {
        c0[ks] = trd2(ks * 16384 + vr0, ks * 16384 + vr1);
        c1[ks] = trd2(ks * 16384 + (vr0 ^ 64), ks * 16384 + (vr1 ^ 64));
      }
      if (rho == 0) STAMP(6); else STAMP(8);
      bar();
      const float mn = fmaxf(fmaxf(m, fmaxf(ldsf(mxa), ldsf(mxa + 128))), fmaxf(ldsf(mxa + 256), ldsf(mxa + 384)));
      const float al = ex2((m - mn) * SC), mc = -mn * SC;
      float p[8];
#pragma unroll
      for (int i = 0; i < 4; ++i) { p[i] = ex2(fmaf(sa[i], SC, mc)); p[4 + i] = ex2(fmaf(sb[i], SC, mc)); }
      l = fmaf(l, al, ((p[0] + p[1]) + (p[2] + p[3])) + ((p[4] + p[5]) + (p[6] + p[7])));
      m = mn;
      sto64(pwa, (v2u){pk(p[0], p[1]), pk(p[2], p[3])});
      sto64(pwa + 6144, (v2u){pk(p[4], p[5]), pk(p[6], p[7])});
      if (kb4 == 0 && g == 0) stof(mxa + (B_AL - B_MX), al);
      if (rho == 1) { l = x32sum(x16sum(l)); if (g == 0) stof(mxa + (B_LS - B_MX) + kb4 * 128, l); }
      bar();
      if (rho == 1 && kb4 == 0 && g == 0) {
        const unsigned la = mxa + (B_LS - B_MX);
        sto64(B_FM + (16 * hg + c16) * 8, (v2u){asu(m * SC), asu((ldsf(la) + ldsf(la + 128)) + (ldsf(la + 256) + ldsf(la + 384)))});
      }
      const float alp = rho ? ldsf(ala) : 0.f;
      v4u pf[4];
#pragma unroll
      for (int ks = 0; ks < 4; ++ks) pf[ks] = lds128(pra + ks * 1536);
      __builtin_amdgcn_sched_barrier(0);
      if (rho) { acc0 *= alp; acc1 *= alp; }
      vr0 += 65536; vr1 += 65536;
#pragma unroll
      for (int ks = 0; ks < 4; ++ks) {
        acc0 = mfmaL(c0[ks], pf[ks], acc0); acc1 = mfmaL(c1[ks], pf[ks], acc1);
        c0[ks] = trd2(ks * 16384 + vr0, ks * 16384 + vr1);
        c1[ks] = trd2(ks * 16384 + (vr0 ^ 64), ks * 16384 + (vr1 ^ 64));
        pf[ks] = lds128(pra + 6144 + ks * 1536);
        __builtin_amdgcn_sched_barrier(0);
      }
      bar();
      if (rho == 0) {
        ST_S(S2, 0);
        vmwait<0>();
        ST_S(S3, 1);
      }
#pragma unroll
      for (int ks = 0; ks < 4; ++ks) { acc0 = mfmaL(c0[ks], pf[ks], acc0); acc1 = mfmaL(c1[ks], pf[ks], acc1); }
      if (rho == 0) STAMP(7); else STAMP(9);
    }
    {
    const int lane = (int)opq((unsigned)lane_), h2 = lane >> 5, tid = wave * 64 + lane;
    {
      const unsigned tb = B_T + w * 4096, n32 = lane & 31;
#pragma unroll
      for (int q4 = 0; q4 < 4; ++q4) {
        sto64(tb + n32 * 128 + ((q4 ^ (n32 & 7)) << 4) + h2 * 8, (v2u){pk(acc0[4 * q4], acc0[4 * q4 + 1]), pk(acc0[4 * q4 + 2], acc0[4 * q4 + 3])});
        sto64(tb + n32 * 128 + (((4 + q4) ^ (n32 & 7)) << 4) + h2 * 8, (v2u){pk(acc1[4 * q4], acc1[4 * q4 + 1]), pk(acc1[4 * q4 + 2], acc1[4 * q4 + 3])});
      }
      lgk0();
      const int hr = lane >> 3, ck = lane & 7;
#pragma unroll
      for (int i = 0; i < 4; ++i)
        gst128(rs, WS_PB + (((8 * i + hr) * 8 + w) * 64 + bid) * 128 + ck * 16, lds128(tb + (8 * i + hr) * 128 + ((ck ^ hr) << 4)));
    }
    vmwait<0>();
    bar();
    if (w == 0 && lane < 32) { const v2u fm = *(const LAS v2u*)(B_FM + lane * 8); gst128(rs, WS_PBM + bid * 512 + lane * 16, (v4u){fm.x, tag, fm.y, tag}); }
    STAMP(10);
    if (w == 0) {
      const unsigned off = SKIPA ? WS_F + (64 + bid) * 16384 + (bid & 31) * 128 : PRE2 ? WS_F + bid * 16384 + (2 * bid) * 128 : WS_PAM + (bid * NSPLIT_A + (bid % NSPLIT_A)) * 128;
      _Pragma("nounroll") for (int it = 0; it < SPIN_CAP; ++it) { cfence(); if (__all(gld64(rs, off).y == tag)) break; __builtin_amdgcn_s_sleep(PRESL); }
    }
    bar();
    STAMP(14);
    v4u f[3];
    _Pragma("nounroll") for (int it = 0; it < SPIN_CAP; ++it) {
      cfence();
      bool ok = true;
#pragma unroll
      for (int k = 0; k < 3; ++k) { f[k] = gld128(rs, (k < 2 ? SKIPA : SKIPB) ? 0xfffffff0u : WS_F + ((k >> 1) * 64 + bid) * 16384 + (k < 2 ? k * 8192 + tid * 16 : (tid & 255) * 16)); ok &= (k < 2 ? SKIPA : SKIPB) || tagok(f[k], tag); }
      if (__all(ok)) break;
      __builtin_amdgcn_s_sleep(FSL);
    }
    float r[4] = {asf(f[0].x) + asf(f[1].x), asf(f[0].z) + asf(f[1].z), w < 4 ? asf(f[2].x) : 0.f, w < 4 ? asf(f[2].z) : 0.f};
#pragma unroll
    for (int k = 0; k < 4; ++k) { r[k] += dpp<ROR8>(r[k]); r[k] = x32sum(x16sum(r[k])); }
    if (lane < 8) { sto64(B_FIN + w * 128 + lane * 8, (v2u){asu(r[0]), asu(r[1])}); sto64(B_FIN + w * 128 + 64 + lane * 8, (v2u){asu(r[2]), asu(r[3])}); }
    bar();
    if (tid < 16) {
      float s0 = 0.f, s1 = 0.f;
#pragma unroll
      for (int i = 0; i < 8; ++i) { const f2 t = *(const LAS f2*)(B_FIN + i * 128 + tid * 8); s0 += t.x; s1 += t.y; }
      const int mat = tid >> 3, ob = (tid >> 2) & 1, pr = tid & 3;
      *(GAS unsigned*)((GAS char*)out + (ob * 1024 + mat * 512 + 8 * bid + 2 * pr) * 2) = pk(s0, s1);
      if (!SKIPA && !SKIPB && bid == 0 && tid == 0) gst32(rs, WS_E, tag);
    }
    STAMP(15);
    STAMP_OUT();
    }
  } else {
    const int h = bid - NB_CU, w = wave;
    GET_TAG();
    if (w == 0) gather_qb(T_QBI, h, lane, rs, tag);
    vmwait<0>();
    STAMP(1);
    {
      const unsigned a0 = w * 8192 + c16 * 256 + ((g ^ c16) << 4);
      v4u uf[8];
#pragma unroll
      for (int i = 0; i < 8; ++i) uf[i] = lds128((a0 ^ ((i & 3) << 6)) + (i >> 2) * 4096);
      bar();
      STAMP(2);
      v4u qb[4];
#pragma unroll
      for (int s = 0; s < 4; ++s) qb[s] = lds128(T_QBI + (c16 & 1) * 256 + s * 64 + g * 16);
      __builtin_amdgcn_sched_barrier(0);
      f4 d0 = {0.f, 0.f, 0.f, 0.f}, d1 = d0;
#pragma unroll
      for (int s = 0; s < 4; ++s) { d0 = mfma32(uf[s], qb[s], d0); d1 = mfma32(uf[4 + s], qb[s], d1); }
      const int rb = (c16 >> 1) & 1;
      const f4 e = rb ? d1 : d0;
      if (c16 < 4) sto64(T_QAS + (c16 & 1) * 512 + (32 * w + 16 * rb + 4 * g) * 2, (v2u){pk(e[0], e[1]), pk(e[2], e[3])});
      bar();
      if (w == 0) {
        gst128(rs, WS_QAB + ((lane >> 5) * 32 + h) * 1024 + (lane & 31) * 16, lds128(T_QAS + lane * 16));
        vmwait<0>();
        flag_line(rs, WS_FQ + h * FS, lane, tag);
      }
    }
    bar();
    STAMP(3);
    v4u OU[16];
    {
      const char* uv = Wuv + (size_t)(64 * w) * 8192 + h * 256;
      const unsigned vuv = g * 8192 + ((c16 ^ (g << 1)) << 4);
#pragma unroll
      for (int u = 0; u < 16; ++u) { if (u >= WT && !(u & 1)) vmwait<WT - 2>(); dma<0>(uv + (size_t)u * 32768, vuv ^ (((u >> 1) & 1) << 7), w * 16384 + u * 1024); }
      const char* ow = Bwo + (size_t)(h * 128 + 16 * w) * 1024;
      const unsigned vo = g * 4096 + c16 * 16;
#pragma unroll
      for (int u = 0; u < 16; ++u) {
        if (!(u & 1)) vmwait<WT - 2>();
        asm volatile("global_load_dwordx4 %0, %1, %2 offset:%3 nt" : "=v"(OU[u]) : "v"(vo), "s"(ow), "n"((u >> 2) * 1024 + (u & 3) * 256) : "memory");
      }
    }
    asm volatile("s_waitcnt vmcnt(0)" : "+v"(OU[0]), "+v"(OU[1]), "+v"(OU[2]), "+v"(OU[3]), "+v"(OU[4]), "+v"(OU[5]), "+v"(OU[6]), "+v"(OU[7]),
                 "+v"(OU[8]), "+v"(OU[9]), "+v"(OU[10]), "+v"(OU[11]), "+v"(OU[12]), "+v"(OU[13]), "+v"(OU[14]), "+v"(OU[15]) :: "memory");
    if (tid == 0) sto32(T_RDY, 0u);
    bar();
    STAMP(4);
    v4u wf[16];
    {
      const int q = c16 >> 2, p = lane & 3, f3 = q | ((g & 1) << 2);
      const unsigned a0 = (8 * g + q) * 256 + (((2 * w + (p >> 1)) ^ (f3 << 1)) << 4) + (p & 1) * 8;
      const unsigned aa[2] = {opq(a0), opq(a0 + 65536)};
#pragma unroll
      for (int s = 0; s < 16; ++s) wf[s] = trd2(aa[s >> 3] + (s & 7) * 8192, aa[s >> 3] + (s & 7) * 8192 + 1024);
    }
    STAMP(5);
    {
      const int sub = lane >> 3, ck = lane & 7;
      v4u o[8];
#pragma unroll
      for (int i = 0; i < 8; ++i) { o[i] = (v4u){0u, 0u, 0u, 0u}; asm volatile("" : "+v"(o[i])); }
      const char* pb = ws + WS_PB + (h * 8 + w) * 8192;
      const unsigned vl = lane << 4;
      unsigned done = 0;
      v4u ml = {0u, ~tag, 0u, ~tag};
      if (w == 0) {
        _Pragma("nounroll") for (int it = 0; it < SPIN_CAP; ++it) { cfence(); if (__all(gld32(rs, WS_PBM + 2 * h * 512 + h * 16 + 4) == tag)) break; __builtin_amdgcn_s_sleep(PSL1); }
      }
      _Pragma("nounroll") for (int it = 0; it < SPIN_CAP * 8 && done != 255u; ++it) {
        unsigned rdy;
        if (w == 0) {
          cfence();
          const v4u t = gld128(rs, tagok(ml, tag) ? 0xfffffff0u : WS_PBM + lane * 512 + h * 16);
          if (!tagok(ml, tag)) ml = t;
          unsigned long long m = __ballot(tagok(ml, tag));
          m &= m >> 4; m &= m >> 2; m &= m >> 1; m &= 0x0101010101010101ull;
          rdy = (unsigned)((m * 0x0102040810204080ull) >> 56);
          if (lane == 0) *(volatile LAS unsigned*)T_RDY = rdy;
        } else {
          rdy = lds_peek(T_RDY) & 255u;
          if (rdy == done) { __builtin_amdgcn_s_sleep(1); continue; }
        }
        const unsigned add = rdy & ~done;
        done = rdy;
#pragma unroll
        for (int i = 0; i < 8; ++i)
          if (add & (1u << i)) asm volatile("global_load_dwordx4 %0, %1, %2 offset:%3 sc1" : "+v"(o[i]) : "v"(vl), "s"(pb + (i >> 2) * 4096), "n"((i & 3) * 1024) : "memory");
      }
      if (w == 0) {
        const float M = x16max(row16max(asf(ml.x)));
        const float wt = ex2(asf(ml.x) - M);
        stof(T_WN + lane * 4, wt / x16sum(row16sum(wt * asf(ml.z))));
        lgk0();
        if (lane == 0) *(volatile LAS unsigned*)T_RDY = 511u;
      } else {
        lds_wait_ge<0>(T_RDY, 511u);
      }
      float wi[8];
#pragma unroll
      for (int i = 0; i < 8; ++i) wi[i] = ldsf(T_WN + (8 * i + sub) * 4);
      asm volatile("s_waitcnt vmcnt(0)" : "+v"(o[0]), "+v"(o[1]), "+v"(o[2]), "+v"(o[3]), "+v"(o[4]), "+v"(o[5]), "+v"(o[6]), "+v"(o[7]) :: "memory");
      STAMP(8);
      float a[16];
#pragma unroll
      for (int k = 0; k < 16; ++k) a[k] = 0.f;
#pragma unroll
      for (int i = 0; i < 8; ++i)
#pragma unroll
        for (int k = 0; k < 4; ++k) {
          a[(i >> 2) * 8 + 2 * k] = fmaf(wi[i], asf(o[i][k] << 16), a[(i >> 2) * 8 + 2 * k]);
          a[(i >> 2) * 8 + 2 * k + 1] = fmaf(wi[i], asf(o[i][k] & 0xffff0000u), a[(i >> 2) * 8 + 2 * k + 1]);
        }
      float r[8];
#pragma unroll
      for (int k = 0; k < 8; ++k) {
        auto sw = __builtin_amdgcn_permlane32_swap(asu(a[k]), asu(a[8 + k]), false, false);
        r[k] = x16sum(asf(sw[0]) + asf(sw[1]));
        r[k] += dpp<ROR8>(r[k]);
      }
      if ((lane & 24) == 0) sto128(T_OL + (lane >> 5) * 1024 + w * 128 + ck * 16, (v4u){pk(r[0], r[1]), pk(r[2], r[3]), pk(r[4], r[5]), pk(r[6], r[7])});
    }
    bar();
    STAMP(6);
    {
      v4u of[16];
      const unsigned oa = opq(T_OL + (c16 >> 3) * 1024 + g * 16);
#pragma unroll
      for (int s = 0; s < 16; ++s) of[s] = lds128(oa + s * 64);
      __builtin_amdgcn_sched_barrier(0);
      f4 d0 = {0.f, 0.f, 0.f, 0.f}, d1 = d0;
#pragma unroll
      for (int s = 0; s < 8; ++s) { d0 = mfma32(wf[2 * s], of[2 * s], d0); d1 = mfma32(wf[2 * s + 1], of[2 * s + 1], d1); }
      d0 += d1;
      const unsigned half = (c16 & 1) ? 0xffff0000u : 0x0000ffffu;
      const int rr = (c16 >> 1) & 3;
      f4 o4[4];
#pragma unroll
      for (int cb = 0; cb < 4; ++cb) o4[cb] = (f4){0.f, 0.f, 0.f, 0.f};
#pragma unroll
      for (int u = 0; u < 4; ++u) {
        const unsigned x = pk(d0[u], d0[u]) & half;
        const v4u bo = {rr == 0 ? x : 0u, rr == 1 ? x : 0u, rr == 2 ? x : 0u, rr == 3 ? x : 0u};
#pragma unroll
        for (int cb = 0; cb < 4; ++cb) o4[cb] = mfma32(OU[4 * u + cb], bo, o4[cb]);
      }
#pragma unroll
      for (int cb = 0; cb < 4; ++cb)
#pragma unroll
        for (int r = 0; r < 4; ++r) stof(T_RED + w * 4096 + cb * 1024 + r * 64 + g * 256 + c16 * 4, o4[cb][r]);
    }
    bar();
    {
      const int j = tid >> 3, pr = tid & 7;
      float s0 = 0.f, s1 = 0.f;
#pragma unroll
      for (int i = 0; i < 8; ++i) { const f2 t = *(const LAS f2*)(T_RED + i * 4096 + tid * 8); s0 += t.x; s1 += t.y; }
      gst128(rs, WS_F + (((64 + j) * 128 + h) * 16 + 2 * pr) * 8, (v4u){asu(s0), tag, asu(s1), tag});
    }
    STAMP(7);
    STAMP_OUT();
  }
}

extern "C" void kernel_launch(void* const* d_in, const int* in_sizes, int n_in, void* d_out, int out_size, void* d_ws, size_t ws_size,
                              hipStream_t stream) {
  (void)in_sizes; (void)n_in; (void)out_size; (void)ws_size;
  (void)hipFuncSetAttribute((const void*)fused_decode, hipFuncAttributeMaxDynamicSharedMemorySize, LDS_BYTES);
  fused_decode<<<256, 512, LDS_BYTES, stream>>>(
      (const char*)d_in[0], (const char*)d_in[1], (const char*)d_in[2], (const char*)d_in[3], (const char*)d_in[4], (const char*)d_in[5],
      (const char*)d_in[6], (const char*)d_in[7], (const char*)d_in[8], (const char*)d_in[9], (const char*)d_in[10], (unsigned short*)d_out,
      (char*)d_ws);
}
```

```cpp
#include <hip/hip_runtime.h>
#include <stdint.h>
#pragma clang diagnostic ignored "-Wint-to-pointer-cast"

#define GAS __attribute__((address_space(1)))
#define LAS __attribute__((address_space(3)))
typedef unsigned v4u __attribute__((ext_vector_type(4)));
typedef unsigned v2u __attribute__((ext_vector_type(2)));
typedef float f4 __attribute__((ext_vector_type(4)));
typedef float f2 __attribute__((ext_vector_type(2)));
typedef float f16v __attribute__((ext_vector_type(16)));
typedef short s4 __attribute__((ext_vector_type(4)));
typedef __bf16 b8 __attribute__((ext_vector_type(8)));
typedef __bf16 b2 __attribute__((ext_vector_type(2)));

#define W1 12
#define W2 24
#define J1 18
#define DLY2 40
#define DLYP 30
#define DLYB 10
#define NB0 55
#define NB1 52
#define WB 6
#define PRE2 1
#define SKIPA 0
#define SKIPB 0
#define WT 6
#define CODE_KB 24
#define QGO 4

namespace {

constexpr int NB_CU = 64, NT_CU = 32, NSPLIT_A = 10, NSLOT = 18, NLOW = 14, NCONS = 6;
constexpr float SC = 0.08838834764831845f * 1.4426950408889634f;
constexpr int SPIN_CAP = 1 << 18;

#define NS3 4
#define PSL 4
#define PSL1 8
#define PRESL 8
#define GSL 1
#define FSL 4
constexpr unsigned FS = 128;
constexpr unsigned WS_E = 0;
constexpr unsigned WS_FQ = 4096;
constexpr unsigned WS_FP = WS_FQ + 16384;
constexpr unsigned WS_QG = 65536;
constexpr unsigned WS_QAB = WS_QG + 262144;
constexpr unsigned WS_PBM = WS_QAB + 65536;
constexpr unsigned WS_PAM = WS_PBM + 32768;
constexpr unsigned WS_PAO = WS_PAM + 131072;
constexpr unsigned WS_PB = WS_PAO + 786432;
constexpr unsigned WS_F = WS_PB + 2097152;
constexpr unsigned WS_END = WS_F + 2097152;

constexpr unsigned LDS_BYTES = 163840;
constexpr unsigned SCR = 131072;
constexpr unsigned A_MISC = NLOW * 8192;
constexpr unsigned A_LANDK = A_MISC, A_LANDV = A_MISC + 4, A_QUSED = A_MISC + 8, A_QCNT = A_MISC + 12, A_QB = A_MISC + 16, A_QRDY = A_MISC + 20, A_OUL = A_MISC + 24, A_OUL2 = A_MISC + 28;
constexpr unsigned A_DONE = A_MISC + 64;
constexpr unsigned A_QU2 = 10 * 8192;
constexpr unsigned A_QIMG = A_MISC + 256;
constexpr unsigned A_ML = A_MISC + 1280;
constexpr unsigned A_OIMG = A_MISC + 1792;
constexpr unsigned A_ST = A_MISC + 2048;
constexpr unsigned A_XIMG = A_MISC + 4096, A_DUMMY = A_MISC + 5120, A_QBI = A_MISC + 6144, A_QAS = A_MISC + 6656;
constexpr unsigned SLOT0 = 0, SLOT1 = 65536;
constexpr unsigned B_MX = SCR, B_AL = SCR + 512, B_LS = SCR + 640, B_P = SCR + 2048;
constexpr unsigned B_FM = SCR + 1280, B_FIN = SCR + 16384, B_T = SLOT1, B_DUMMY = SCR + 8192;
constexpr unsigned T_QBI = SCR, T_QAS = SCR + 1024, T_OL = SCR + 4096, T_RDY = SCR + 6400, T_WN = SCR + 6656, T_RED = 0;

__device__ __forceinline__ unsigned slot_addr(unsigned s) { return s * 8192 + (s >= NLOW ? 16384u : 0u); }
__device__ __forceinline__ v4u lds128(unsigned a) { return *(const LAS v4u*)a; }
__device__ __forceinline__ void sto128(unsigned a, v4u v) { *(LAS v4u*)a = v; }
__device__ __forceinline__ void sto64(unsigned a, v2u v) { *(LAS v2u*)a = v; }
__device__ __forceinline__ void sto32(unsigned a, unsigned v) { *(LAS unsigned*)a = v; }
__device__ __forceinline__ float ldsf(unsigned a) { return *(const LAS float*)a; }
__device__ __forceinline__ void stof(unsigned a, float v) { *(LAS float*)a = v; }
__device__ __forceinline__ v2u trd(unsigned a) { return __builtin_bit_cast(v2u, __builtin_amdgcn_ds_read_tr16_b64_v4i16((LAS s4*)a)); }
__device__ __forceinline__ v4u trd2(unsigned a0, unsigned a1) { const v2u x = trd(a0), y = trd(a1); return (v4u){x.x, x.y, y.x, y.y}; }
__device__ __forceinline__ f4 mfma32(v4u a, v4u b, f4 c) {
  return __builtin_amdgcn_mfma_f32_16x16x32_bf16(__builtin_bit_cast(b8, a), __builtin_bit_cast(b8, b), c, 0, 0, 0); }
__device__ __forceinline__ f4 mfma16(v2u a, v2u b, f4 c) {
  return __builtin_amdgcn_mfma_f32_16x16x16bf16_1k(__builtin_bit_cast(s4, a), __builtin_bit_cast(s4, b), c, 0, 0, 0); }
__device__ __forceinline__ f16v mfmaL(v4u a, v4u b, f16v c) {
  return __builtin_amdgcn_mfma_f32_32x32x16_bf16(__builtin_bit_cast(b8, a), __builtin_bit_cast(b8, b), c, 0, 0, 0); }
__device__ __forceinline__ unsigned pk(float lo, float hi) { return __builtin_bit_cast(unsigned, __builtin_convertvector((f2){lo, hi}, b2)); }
__device__ __forceinline__ float asf(unsigned u) { return __builtin_bit_cast(float, u); }
__device__ __forceinline__ unsigned asu(float f) { return __builtin_bit_cast(unsigned, f); }
__device__ __forceinline__ float ex2(float x) { return __builtin_amdgcn_exp2f(x); }
template <int CTRL> __device__ __forceinline__ float dpp(float x) {
  return __builtin_bit_cast(float, __builtin_amdgcn_mov_dpp(__builtin_bit_cast(int, x), CTRL, 0xf, 0xf, true)); }
constexpr int XOR1 = 0xB1, XOR2 = 0x4E, ROR4 = 0x124, ROR8 = 0x128;
__device__ __forceinline__ float x16max(float x) { auto s = __builtin_amdgcn_permlane16_swap(asu(x), asu(x), false, false); return fmaxf(asf(s[0]), asf(s[1])); }
__device__ __forceinline__ float x32max(float x) { auto s = __builtin_amdgcn_permlane32_swap(asu(x), asu(x), false, false); return fmaxf(asf(s[0]), asf(s[1])); }
__device__ __forceinline__ float x16sum(float x) { auto s = __builtin_amdgcn_permlane16_swap(asu(x), asu(x), false, false); return asf(s[0]) + asf(s[1]); }
__device__ __forceinline__ float x32sum(float x) { auto s = __builtin_amdgcn_permlane32_swap(asu(x), asu(x), false, false); return asf(s[0]) + asf(s[1]); }
__device__ __forceinline__ float row16max(float x) { x = fmaxf(x, dpp<XOR1>(x)); x = fmaxf(x, dpp<XOR2>(x)); x = fmaxf(x, dpp<ROR4>(x)); return fmaxf(x, dpp<ROR8>(x)); }
__device__ __forceinline__ float row16sum(float x) { x += dpp<XOR1>(x); x += dpp<XOR2>(x); x += dpp<ROR4>(x); return x + dpp<ROR8>(x); }

template <int IMM> __device__ __forceinline__ void dma(const void* sbase, unsigned voff, unsigned lds) {
  asm volatile("s_mov_b32 m0, %2\n\ts_nop 0\n\tglobal_load_lds_dwordx4 %0, %1 offset:%3 nt" :: "v"(voff), "s"(sbase), "s"(lds), "n"(IMM) : "memory");
}
template <int W> __device__ __forceinline__ void dma4(const void* sbase, unsigned v0, unsigned v1, unsigned v2, unsigned v3, unsigned lds) {
  asm volatile("s_mov_b32 m0, %5\n\ts_waitcnt vmcnt(%6)\n\tglobal_load_lds_dwordx4 %0, %4 nt\n\tglobal_load_lds_dwordx4 %1, %4 offset:1024 nt\n\t"
               "s_waitcnt vmcnt(%6)\n\tglobal_load_lds_dwordx4 %2, %4 offset:2048 nt\n\tglobal_load_lds_dwordx4 %3, %4 offset:3072 nt"
               :: "v"(v0), "v"(v1), "v"(v2), "v"(v3), "s"(sbase), "s"(lds), "n"(W - 2) : "memory");
}
__device__ __forceinline__ void dma_keep(const void* sbase, unsigned voff, unsigned lds) {
  asm volatile("s_mov_b32 m0, %2\n\ts_nop 0\n\tglobal_load_lds_dwordx4 %0, %1" :: "v"(voff), "s"(sbase), "s"(lds) : "memory");
}
template <int N> __device__ __forceinline__ void vmwait() { asm volatile("s_waitcnt vmcnt(%0)" :: "n"(N) : "memory"); }
__device__ __forceinline__ void bar() { asm volatile("s_waitcnt lgkmcnt(0)\n\ts_barrier" ::: "memory"); }
__device__ __forceinline__ void lgk0() { asm volatile("s_waitcnt lgkmcnt(0)" ::: "memory"); }
__device__ __forceinline__ void cfence() { asm volatile("" ::: "memory"); }
__device__ __forceinline__ unsigned opq(unsigned x) { asm volatile("" : "+v"(x)); return x; }
__device__ __forceinline__ unsigned lds_peek(unsigned a) { return __builtin_amdgcn_readfirstlane(*(volatile LAS unsigned*)a); }
template <int SL = 1> __device__ __forceinline__ void lds_wait_ge(unsigned a, unsigned want) {
  _Pragma("nounroll") for (int it = 0; it < SPIN_CAP * 8; ++it) { if (lds_peek(a) >= want) break; if (SL) __builtin_amdgcn_s_sleep(SL); }
  cfence();
}
typedef __amdgpu_buffer_rsrc_t rsrc_t;
constexpr int SC1 = 16;
__device__ __forceinline__ v4u gld128(rsrc_t r, unsigned off) { return __builtin_amdgcn_raw_buffer_load_b128(r, off, 0, SC1); }
__device__ __forceinline__ v2u gld64(rsrc_t r, unsigned off) { return __builtin_amdgcn_raw_buffer_load_b64(r, off, 0, SC1); }
__device__ __forceinline__ unsigned gld32(rsrc_t r, unsigned off) { return __builtin_amdgcn_raw_buffer_load_b32(r, off, 0, SC1); }
__device__ __forceinline__ void gst128(rsrc_t r, unsigned off, v4u v) { __builtin_amdgcn_raw_buffer_store_b128(v, r, off, 0, SC1); }
__device__ __forceinline__ void gst64(rsrc_t r, unsigned off, v2u v) { __builtin_amdgcn_raw_buffer_store_b64(v, r, off, 0, SC1); }
__device__ __forceinline__ void gst32(rsrc_t r, unsigned off, unsigned v) { __builtin_amdgcn_raw_buffer_store_b32(v, r, off, 0, SC1); }
__device__ __forceinline__ void flag_line(rsrc_t r, unsigned off, int lane, unsigned tag) { if (lane < 8) gst128(r, off + lane * 16, (v4u){tag, tag, tag, tag}); }
__device__ __forceinline__ bool tagok(v4u v, unsigned tag) { return v.y == tag && v.w == tag; }

#define STAMP(i) do {} while (0)
#define STAMP_DECL do {} while (0)
#define STAMP_OUT() do {} while (0)

__device__ __forceinline__ void gather_qb(unsigned img, int h, int lane, rsrc_t rs, unsigned tag) {
  v4u qg[4];
  const unsigned off = WS_QG + (h * 128 + 2 * lane) * 8;
  _Pragma("nounroll") for (int it = 0; it < SPIN_CAP; ++it) {
    cfence();
    bool ok = true;
#pragma unroll
    for (int k = 0; k < 4; ++k) { qg[k] = gld128(rs, off + (k & 1) * 65536 + (k >> 1) * 32768); ok &= tagok(qg[k], tag); }
    if (__all(ok)) break;
    __builtin_amdgcn_s_sleep(1);
  }
  sto32(img + lane * 4, pk(asf(qg[0].x) + asf(qg[1].x), asf(qg[0].z) + asf(qg[1].z)));
  sto32(img + 256 + lane * 4, pk(asf(qg[2].x) + asf(qg[3].x), asf(qg[2].z) + asf(qg[3].z)));
}
__device__ __forceinline__ void lds_inc(unsigned a) { __hip_atomic_fetch_add((LAS unsigned*)a, 1u, __ATOMIC_RELAXED, __HIP_MEMORY_SCOPE_WORKGROUP); }

__device__ __forceinline__ void out_frags(unsigned gb, int w, int lane, v4u (&wf)[4]) {
  const int g = lane >> 4, q = (lane & 15) >> 2, p = lane & 3, f3 = q | ((g & 1) << 2);
#pragma unroll
  for (int tt = 0; tt < 4; ++tt) {
    const unsigned a0 = gb + q * 1024 + (((2 * (4 * w + tt) + (p >> 1)) ^ (f3 << 1)) << 4) + (p & 1) * 8;
    wf[tt] = trd2(a0, a0 + 4096);
  }
}

}

__global__ void __launch_bounds__(512, 2) fused_decode(
    const char* __restrict__ Ax, const char* __restrict__ Kc, const char* __restrict__ Vc, const char* __restrict__ Wq, const char* __restrict__ Wo,
    const char* __restrict__ Bx, const char* __restrict__ Ckv, const char* __restrict__ Bwq, const char* __restrict__ Wuk, const char* __restrict__ Wuv,
    const char* __restrict__ Bwo, unsigned short* __restrict__ out, char* __restrict__ ws) {
  const int bid = blockIdx.x, tid = threadIdx.x, lane = tid & 63, wave = __builtin_amdgcn_readfirstlane(tid >> 6);
  const int g = lane >> 4, c16 = lane & 15;
  STAMP_DECL;
  STAMP(0);
  const int iA = bid - (NB_CU + NT_CU);
  const bool isA = iA >= 0, isB = bid < NB_CU;
  const int ty = iA >> 6;
  int dA = 95 - bid, dU = 223 - bid, dB = bid - 64, dQ = 159 - bid, dW = 2 - wave;
  asm("" : "+s"(dA), "+s"(dU), "+s"(dB), "+s"(dQ), "+s"(dW));
  const int mA = dA >> 31, mU = dU >> 31, mB = dB >> 31, mP = mA & ~mU, sh = mP & 1;
  const int hh = (bid >> sh) & 31;
  const unsigned row0 = (mA & ((((bid | mU) & 1) << 8) + 64 * wave - 64)) | (~mA & (32 * wave));
  const unsigned l0 = (mA & (SCR - 16384 + wave * 16384 - ((dW >> 31) & (SCR + 32768 - A_QU2)))) | (~mA & (wave * 8192));
  const unsigned karg = 0x40 - (mP & (8 + ((dQ >> 31) & 32))), more = mA;
  if ((unsigned)(wave + mA + (mB & 8)) < (unsigned)(8 + (mA & -4))) {
    const unsigned b0 = (row0 + g) * 8192 + hh * 256 + ((c16 ^ (g << sh)) << 4), x1 = ~mP & 64;
    unsigned v0 = b0, v1 = (b0 ^ x1) + 32768, v2 = (b0 ^ 128) + 65536, v3 = (b0 ^ x1 ^ 128) + 98304;
    if (DLYP && karg == 0x18) __builtin_amdgcn_s_sleep(DLYP);
    unsigned long long wp;
#define D4 "global_load_lds_dwordx4 %1, %0 nt\n\ts_add_u32 m0, m0, 0x400\n\tv_add_u32 %1, 0x20000, %1\n\t" \
           "global_load_lds_dwordx4 %2, %0 nt\n\ts_add_u32 m0, m0, 0x400\n\tv_add_u32 %2, 0x20000, %2\n\t" \
           "global_load_lds_dwordx4 %3, %0 nt\n\ts_add_u32 m0, m0, 0x400\n\tv_add_u32 %3, 0x20000, %3\n\t" \
           "global_load_lds_dwordx4 %4, %0 nt\n\ts_add_u32 m0, m0, 0x400\n\tv_add_u32 %4, 0x20000, %4\n\t"
    asm volatile("s_load_dwordx2 %0, %5, %6\n\ts_mov_b32 m0, %7\n\ts_waitcnt lgkmcnt(0)\n\t" D4 D4
                 "s_cmp_eq_u32 %8, 0\n\ts_cbranch_scc1 1f\n\t" D4 D4 "1:"
                 : "=&s"(wp), "+v"(v0), "+v"(v1), "+v"(v2), "+v"(v3)
                 : "s"(__builtin_amdgcn_kernarg_segment_ptr()), "s"(karg), "s"(l0), "s"(more) : "memory", "scc");
#undef D4
  }
  {
    unsigned long long pc;
    asm volatile("s_getpc_b64 %0" : "=s"(pc));
    const int kb = ((bid >> 3) + 4 * wave) & 31;
    if (CODE_KB) dma_keep((const char*)((pc & ~1023ull) + (unsigned)((kb < CODE_KB ? kb : CODE_KB - 1) * 1024)), lane * 16, isA ? A_DUMMY : B_DUMMY);
  }
  const rsrc_t rs = __builtin_amdgcn_make_buffer_rsrc(ws, 0, WS_END, 0x00020000);
  asm volatile("" :: "s"(Ax), "s"(Kc), "s"(Vc), "s"(Wq), "s"(Wo), "s"(Bx), "s"(Ckv), "s"(Bwq), "s"(Wuk), "s"(Wuv), "s"(Bwo), "s"(out), "s"(ws));
  unsigned tag;
#define GET_TAG() do { asm volatile("s_load_dword %0, %1, 0x0\n\ts_waitcnt lgkmcnt(0)" : "=s"(tag) : "s"(ws) : "memory"); tag += 1u; } while (0)
  const char* cp = Ckv + ((size_t)(bid >> 5) * 8192 + 256 * (bid & 31) + 8 * wave) * 1024;
  const unsigned vc0 = (lane ^ ((wave & 1) ? 3 : 0)) << 4, vc1 = (lane ^ ((wave & 1) ? 1 : 2)) << 4;
#define LD_C(rho, u) dma<((u) & 3) * 1024>(cp + (size_t)(rho) * 65536 + ((u) >> 2) * 4096, (((u) >> 2) ? vc1 : vc0) ^ (((u) & 3) << 6), ((rho) & 1) * 65536 + wave * 8192 + ((u) >> 2) * 4096)
  if (isA) {
    if (wave == 1 && ty < 2) dma<0>((ty ? Ax : Bx) + 512 * (bid & 1), (lane >> 5) * 1024 + (lane & 31) * 16, A_XIMG);
    if (tid < 64) sto32(A_MISC + tid * 4, 0u);
    bar();
  } else if (isB) {
    __builtin_amdgcn_s_sleep(DLYB);
    LD_C(0, 0); LD_C(0, 1); LD_C(0, 2); LD_C(0, 3); LD_C(0, 4); LD_C(0, 5);
  }

  if (isA) {
    const int split = iA >> 4, pair = (iA + split) & 15, b = pair >> 3, kvh = pair & 7;
    constexpr int NB2 = (512 - 4 * NB0 - 4 * NB1) / 2;
    const int nblk = ty == 0 ? NB0 : ty == 1 ? NB1 : NB2, blk0 = (ty == 0 ? 0 : ty == 1 ? 4 * NB0 : 4 * (NB0 + NB1)) + (split & 3) * nblk;
    if (wave == 0 || wave == 7) {
      __builtin_amdgcn_s_setprio(3);
      static_assert(NSPLIT_A == 10 && W1 >= 4 && W1 % 4 == 0 && W2 % 4 == 0 && W1 <= W2 && W2 <= 32 && J1 <= NB0 && J1 <= NB1 && J1 <= NB2, "window");
      const bool isv = wave == 7;
      int pub = 0, j = 0;
      unsigned sl = 0;
      const char* gp = (isv ? Vc : Kc) + ((size_t)pair * 8192 + blk0 * 16) * 256;
      const unsigned v0 = opq(g * 256 + ((c16 ^ (isv ? g << 1 : g)) << 4));
      const unsigned v1 = opq(v0 ^ (isv ? 128 : 64)), v2 = opq(v0 ^ (isv ? 0 : 128)), v3 = opq(v0 ^ (isv ? 128 : 192));
      const unsigned lo = isv ? 4096 : 0, la = opq(lane ? A_MISC + 1536 + lane * 4 : isv ? A_LANDV : A_LANDK);
      unsigned dn = 0;
#define PUBLISH(val) do { pub = max(pub, (val)); *(volatile LAS unsigned*)la = (unsigned)pub; } while (0)
#define GATE(addr, need) do { if (lds_peek(addr) < (need)) { vmwait<0>(); PUBLISH(j); lds_wait_ge(addr, need); } } while (0)
#define PART(W) do { \
        if (j >= NSLOT) { if ((unsigned)__builtin_amdgcn_readfirstlane(dn) < (unsigned)(j - NSLOT + 1)) GATE(A_DONE + sl, (unsigned)(j - NSLOT + 1)); } \
        else if (j == 10) GATE(A_QUSED, 4u); \
        dma4<W>(gp, v0, v1, v2, v3, slot_addr(sl >> 2) + lo); \
        ++j; gp += 4096; sl = sl == 4 * (NSLOT - 1) ? 0 : sl + 4; \
        PUBLISH(j - W / 4); dn = *(volatile LAS unsigned*)(A_DONE + sl); } while (0)
      STAMP(9);
      GET_TAG();
      lds_wait_ge(A_QCNT, 4u);
      STAMP(10);
#pragma nounroll
      while (j < J1) PART(W1);
      STAMP(11);
#pragma nounroll
      while (j < nblk) PART(W2);
      if (ty < 2) {
        const char* op = Wo + (size_t)iA * 32768 + lo;
        const unsigned o0 = lane << 4, o1 = o0 ^ 32, o2 = o0 ^ 64, o3 = o0 ^ 96;
#pragma unroll
        for (int k = 0; k < 4; ++k) {
          const unsigned x = (k & 1) << 7, need = j + k - NSLOT + 1;
          if (lds_peek(A_DONE + sl) < need) { vmwait<0>(); PUBLISH(j); lds_wait_ge(A_DONE + sl, need); }
          if (4 * k + 4 <= W2) { dma4<W2>(op + k * 8192, o0 ^ x, o1 ^ x, o2 ^ x, o3 ^ x, slot_addr(sl >> 2) + lo); PUBLISH(j + k + 1 - W2 / 4); }
          else dma4<62>(op + k * 8192, o0 ^ x, o1 ^ x, o2 ^ x, o3 ^ x, slot_addr(sl >> 2) + lo);
          sl = sl == 4 * (NSLOT - 1) ? 0 : sl + 4;
        }
        if (W2 > 16) { vmwait<28>(); PUBLISH(j - 3); vmwait<24>(); PUBLISH(j - 2); vmwait<20>(); PUBLISH(j - 1); vmwait<16>(); PUBLISH(j); }
      } else {
        vmwait<28>(); PUBLISH(j - 7); vmwait<24>(); PUBLISH(j - 6); vmwait<20>(); PUBLISH(j - 5); vmwait<16>(); PUBLISH(j - 4);
        vmwait<12>(); PUBLISH(j - 3); vmwait<8>(); PUBLISH(j - 2); vmwait<4>(); PUBLISH(j - 1); vmwait<0>(); PUBLISH(j);
      }
      __builtin_amdgcn_s_setprio(0);
      STAMP(5);
#undef PUBLISH
#undef GATE
#undef PART
    } else {
      GET_TAG();
      vmwait<0>();
      if (wave <= 4) {
        const int w1 = wave - 1;
        if (ty < 2) {
          if (lane == 0) lds_inc(A_QCNT);
          const int q = c16 >> 2, p = lane & 3, f3 = q | ((g & 1) << 2);
          const unsigned a0 = (8 * g + q) * 256 + (((4 * w1 + (p >> 1)) ^ (f3 << 1)) << 4) + (p & 1) * 8;
          const unsigned aa[2] = {opq(SCR + a0), opq(A_QU2 + a0)};
          lds_wait_ge(A_QCNT, 4u);
          STAMP(1);
          v4u w0[8], w1f[8], xf[8];
#pragma unroll
          for (int s = 0; s < 8; ++s) {
            const unsigned a = aa[s >> 2] + (s & 3) * 8192;
            w0[s] = trd2(a, a + 1024); w1f[s] = trd2((a ^ 32), (a ^ 32) + 1024);
            xf[s] = lds128(A_XIMG + (c16 & 1) * 512 + g * 16 + s * 64);
          }
          __builtin_amdgcn_sched_barrier(0);
          f4 d0 = {0.f, 0.f, 0.f, 0.f}, d1 = d0;
#pragma unroll
          for (int s = 0; s < 8; ++s) { d0 = mfma32(w0[s], xf[s], d0); d1 = mfma32(w1f[s], xf[s], d1); }
          const int pr = (c16 >> 1) & 1, nb = (c16 >> 2) & 1;
          const f4 e = nb ? d1 : d0;
          if (c16 < 8) gst128(rs, WS_QG + (((ty * 2 + (bid & 1)) * 2 + (c16 & 1)) * 4096 + hh * 128 + 32 * w1 + 16 * nb + 4 * g + 2 * pr) * 8,
                              (v4u){asu(pr ? e[2] : e[0]), tag, asu(pr ? e[3] : e[1]), tag});
          lgk0();
        } else {
          STAMP(1);
          if (w1 == 3 && lane == 0) *(volatile LAS unsigned*)A_OUL2 = 1u;
          const unsigned a0 = opq((w1 < 2 ? SCR : A_QU2 - 32768) + w1 * 16384 + c16 * 256 + ((g ^ c16) << 4));
          v4u uf[16];
#pragma unroll
          for (int i = 0; i < 16; ++i) uf[i] = lds128((a0 ^ ((i & 3) << 6)) + (i >> 2) * 4096);
          lds_wait_ge(A_QB, 1u);
          v4u qb[4];
#pragma unroll
          for (int s = 0; s < 4; ++s) qb[s] = lds128(A_QBI + (c16 & 1) * 256 + s * 64 + g * 16);
          __builtin_amdgcn_sched_barrier(0);
          f4 d[4];
#pragma unroll
          for (int rb = 0; rb < 4; ++rb) d[rb] = (f4){0.f, 0.f, 0.f, 0.f};
#pragma unroll
          for (int s = 0; s < 4; ++s)
#pragma unroll
            for (int rb = 0; rb < 4; ++rb) d[rb] = mfma32(uf[4 * rb + s], qb[s], d[rb]);
          const int rb = (c16 >> 1) & 3;
          const f4 e = rb == 0 ? d[0] : rb == 1 ? d[1] : rb == 2 ? d[2] : d[3];
          if (c16 < 8) sto64(A_QAS + w1 * 256 + (c16 & 1) * 128 + (16 * rb + 4 * g) * 2, (v2u){pk(e[0], e[1]), pk(e[2], e[3])});
          lgk0();
          if (lane < 16) gst128(rs, WS_QAB + ((lane >> 3) * 32 + hh) * 1024 + 512 + w1 * 128 + (lane & 7) * 16, lds128(A_QAS + w1 * 256 + lane * 16));
          vmwait<0>();
        }
        unsigned old = 0;
        if (lane == 0) old = __hip_atomic_fetch_add((LAS unsigned*)A_QUSED, 1u, __ATOMIC_RELAXED, __HIP_MEMORY_SCOPE_WORKGROUP);
        if (ty == 2 && __builtin_amdgcn_readfirstlane(old) == 3u) {
          flag_line(rs, WS_FQ + (32 + hh) * FS, lane, tag);
          cfence();
          if (lane == 0) *(volatile LAS unsigned*)A_QCNT = 4u;
        }
        STAMP(2);
      }
      if (wave == 5) {
        if (ty == 2) {
          lds_wait_ge(A_OUL2, 1u);
          gather_qb(A_QBI, hh, lane, rs, tag);
          lgk0();
          if (lane == 0) *(volatile LAS unsigned*)A_QB = 1u;
          STAMP(1);
        }
        lds_wait_ge(A_LANDK, ty == 2 ? 1u : (unsigned)QGO);
        v4u v[8];
        const unsigned off = WS_QG + 131072 + (b * 4096 + kvh * 512 + 2 * lane) * 8;
        _Pragma("nounroll") for (int it = 0; it < SPIN_CAP; ++it) {
          cfence();
          bool ok = true;
#pragma unroll
          for (int k = 0; k < 8; ++k) { v[k] = gld128(rs, off + (k & 1) * 65536 + (k >> 1) * 1024); ok &= tagok(v[k], tag); }
          if (__all(ok)) break;
          __builtin_amdgcn_s_sleep(4);
        }
#pragma unroll
        for (int k = 0; k < 4; ++k) sto32(A_QIMG + k * 256 + lane * 4, pk(asf(v[2 * k].x) + asf(v[2 * k + 1].x), asf(v[2 * k].z) + asf(v[2 * k + 1].z)));
        lgk0();
        if (lane == 0) *(volatile LAS unsigned*)A_QRDY = 1u;
      }
      lds_wait_ge(A_QRDY, 1u);
      STAMP(3);
      v4u qf[4];
#pragma unroll
      for (int s = 0; s < 4; ++s) qf[s] = lds128(A_QIMG + (c16 & 3) * 256 + s * 64 + g * 16);
      const unsigned ko = c16 * 256 + ((g ^ c16) << 4);
      const int vr = 4 * g + (c16 >> 2);
      const unsigned vo = 4096 + vr * 256 + ((((lane & 3) >> 1) ^ ((vr & 7) << 1)) << 4) + (lane & 1) * 8;
      float m = -INFINITY, l = 0.f;
      f4 acc[8];
#pragma unroll
      for (int t = 0; t < 8; ++t) acc[t] = (f4){0.f, 0.f, 0.f, 0.f};
      int slot = wave - 1;
      for (int j = wave - 1; j < nblk; j += NCONS) {
        const unsigned sb = slot_addr(slot);
        lds_wait_ge(A_LANDK, (unsigned)(j + 1));
        v4u kf[4];
#pragma unroll
        for (int s = 0; s < 4; ++s) kf[s] = lds128(sb + (ko ^ (s << 6)));
        __builtin_amdgcn_sched_barrier(0);
        f4 s4_ = {0.f, 0.f, 0.f, 0.f};
#pragma unroll
        for (int s = 0; s < 4; ++s) s4_ = mfma32(kf[s], qf[s], s4_);
        float mx = fmaxf(fmaxf(s4_[0], s4_[1]), fmaxf(s4_[2], s4_[3]));
        mx = x32max(x16max(mx));
        const float mn = fmaxf(m, mx), al = ex2((m - mn) * SC), mc = -mn * SC;
        const float p0 = ex2(fmaf(s4_[0], SC, mc)), p1 = ex2(fmaf(s4_[1], SC, mc)), p2 = ex2(fmaf(s4_[2], SC, mc)), p3 = ex2(fmaf(s4_[3], SC, mc));
        l = fmaf(l, al, (p0 + p1) + (p2 + p3));
        m = mn;
        v2u pb = {pk(p0, p1), pk(p2, p3)};
        unsigned va[8];
#pragma unroll
        for (int t = 0; t < 8; ++t) va[t] = sb + (vo ^ (t << 5));
        if (!__all(al == 1.f)) {
#pragma unroll
          for (int t = 0; t < 8; ++t) acc[t] *= al;
        }
        asm volatile("" : "+v"(pb), "+v"(l), "+v"(acc[0]), "+v"(acc[1]), "+v"(acc[2]), "+v"(acc[3]), "+v"(acc[4]), "+v"(acc[5]), "+v"(acc[6]), "+v"(acc[7]),
                     "+v"(va[0]), "+v"(va[1]), "+v"(va[2]), "+v"(va[3]), "+v"(va[4]), "+v"(va[5]), "+v"(va[6]), "+v"(va[7]));
        lds_wait_ge<0>(A_LANDV, (unsigned)(j + 1));
        v2u vf[8];
#pragma unroll
        for (int t = 0; t < 8; ++t) vf[t] = trd(va[t]);
        lgk0();
        if (lane == 0) *(volatile LAS unsigned*)(A_DONE + slot * 4) = (unsigned)(j + 1);
#pragma unroll
        for (int t = 0; t < 8; ++t) acc[t] = mfma16(vf[t], pb, acc[t]);
        slot += NCONS; if (slot >= NSLOT) slot -= NSLOT;
      }
      STAMP(4);
      l = x32sum(x16sum(l));
      if (c16 < 4) {
#pragma unroll
        for (int t = 0; t < 8; ++t) sto128(A_ST + (wave - 1) * 2048 + c16 * 512 + ((t ^ c16) * 64) + g * 16, __builtin_bit_cast(v4u, acc[t]));
        if (g == 0) sto64(A_ML + (c16 * 8 + wave - 1) * 8, (v2u){asu(m * SC), asu(l)});
      }
    }
    bar();
    if (wave >= 3 && wave <= 6) {
      const int hq = wave - 3;
      v2u ml[NCONS];
      f2 ow[NCONS];
      const unsigned sa = opq(A_ST + hq * 512 + ((lane * 8) ^ (hq << 6))), ma = opq(A_ML + hq * 64);
#pragma unroll
      for (int c = 0; c < NCONS; ++c) { ml[c] = *(const LAS v2u*)(ma + c * 8); ow[c] = *(const LAS f2*)(sa + c * 2048); }
      float M = asf(ml[0].x);
#pragma unroll
      for (int c = 1; c < NCONS; ++c) M = fmaxf(M, asf(ml[c].x));
      float L = 0.f, o0 = 0.f, o1 = 0.f;
#pragma unroll
      for (int c = 0; c < NCONS; ++c) { const float wt = ex2(asf(ml[c].x) - M); L = fmaf(wt, asf(ml[c].y), L); o0 = fmaf(wt, ow[c].x, o0); o1 = fmaf(wt, ow[c].y, o1); }
      const int rec = (b * 32 + kvh * 4 + hq) * NSPLIT_A + split;
      gst128(rs, WS_PAO + rec * 1024 + lane * 16, (v4u){asu(o0), tag, asu(o1), tag});
      if (lane < 8) gst128(rs, WS_PAM + rec * 128 + lane * 16, (v4u){asu(M), tag, asu(L), tag});
    }
    STAMP(6);
    if (ty < 2) {
      const int oh = iA >> 2, odq = iA & 3;
      if (wave == 0 || wave == 7) { vmwait<0>(); if (lane == 0) lds_inc(A_OUL); }
      lds_wait_ge(A_OUL, 2u);
      v4u wf[4];
      out_frags(slot_addr((nblk + g) % NSLOT), wave, lane, wf);
      const int tt = (c16 >> 1) & 3, pr = c16 >> 3, n = 64 * wave + 16 * tt + 4 * g + 2 * pr;
      unsigned fo = WS_F + ((((n >> 3) * 128 + iA) * 16) + (c16 & 1) * 8 + (n & 7)) * 8, oa = A_OIMG + (c16 & 1) * 64 + g * 16;
      asm volatile("" : "+v"(fo), "+v"(oa));
      if (wave == 1) {
        const int rec = ((lane >> 5) * 32 + oh) * NSPLIT_A + (g & 1) * 5;
        v4u v[5], ml[5];
        _Pragma("nounroll") for (int it = 0; it < SPIN_CAP; ++it) {
          cfence();
          bool ok = true;
#pragma unroll
          for (int i = 0; i < 5; ++i) {
            v[i] = gld128(rs, WS_PAO + (rec + i) * 1024 + (32 * odq + 2 * c16) * 8);
            ml[i] = gld128(rs, WS_PAM + (rec + i) * 128 + odq * 16);
            ok &= tagok(v[i], tag) && tagok(ml[i], tag);
          }
          if (__all(ok)) break;
          __builtin_amdgcn_s_sleep(GSL);
        }
        float M = asf(ml[0].x);
#pragma unroll
        for (int i = 1; i < 5; ++i) M = fmaxf(M, asf(ml[i].x));
        M = x16max(M);
        float L = 0.f, o0 = 0.f, o1 = 0.f;
#pragma unroll
        for (int i = 0; i < 5; ++i) { const float wt = ex2(asf(ml[i].x) - M); L = fmaf(wt, asf(ml[i].z), L); o0 = fmaf(wt, asf(v[i].x), o0); o1 = fmaf(wt, asf(v[i].z), o1); }
        const float inv = 1.f / x16sum(L);
        o0 = x16sum(o0); o1 = x16sum(o1);
        if (!(g & 1)) sto32(A_OIMG + (lane >> 5) * 64 + c16 * 4, pk(o0 * inv, o1 * inv));
      }
      STAMP(7);
      bar();
      const v4u ob = lds128(oa);
      f4 d[4];
#pragma unroll
      for (int t = 0; t < 4; ++t) d[t] = mfma32(wf[t], ob, (f4){0.f, 0.f, 0.f, 0.f});
      const f4 e = tt == 0 ? d[0] : tt == 1 ? d[1] : tt == 2 ? d[2] : d[3];
      gst128(rs, fo, (v4u){asu(pr ? e[2] : e[0]), tag, asu(pr ? e[3] : e[1]), tag});
      STAMP(8);
    }
    STAMP_OUT();
  } else if (isB) {
    const int b = bid >> 5, sp = bid & 31, w = wave, lane_ = lane;
    const unsigned vl = lane << 4;
#define LD_S(S, rho, u) asm volatile("global_load_dwordx4 %0, %1, %2 offset:%3 nt" : "=v"(S[u]) : "v"(vl), "s"(cp + (size_t)(rho) * 65536 + ((u) >> 2) * 4096), "n"(((u) & 3) * 1024) : "memory")
#define ST_S(S, rho) do { unsigned a0_ = ((rho) & 1) * 65536 + w * 8192 + vc0, a1_ = ((rho) & 1) * 65536 + w * 8192 + vc1; asm volatile("" : "+v"(a0_), "+v"(a1_)); \
      _Pragma("unroll") for (int u_ = 0; u_ < 8; ++u_) sto128(u_ * 1024 + (((u_ >> 2) ? a1_ : a0_) ^ ((u_ & 3) << 6)), S[u_]); } while (0)
#define PB() vmwait<WB - 2>()
    v4u S2[8], S3[8];
    GET_TAG();
    PB(); LD_C(0, 6); LD_C(0, 7);
    STAMP(1);
    PB(); LD_C(1, 0); LD_C(1, 1);
    PB(); LD_C(1, 2); LD_C(1, 3);
    PB(); LD_C(1, 4); LD_C(1, 5);
    PB(); LD_C(1, 6); LD_C(1, 7);
    STAMP(2);
    PB(); LD_S(S2, 2, 0); LD_S(S2, 2, 1);
    PB(); LD_S(S2, 2, 2); LD_S(S2, 2, 3);
    PB(); LD_S(S2, 2, 4); LD_S(S2, 2, 5);
    PB(); LD_S(S2, 2, 6); LD_S(S2, 2, 7);
#pragma unroll
    for (int u = 0; u < NS3; u += 2) { PB(); LD_S(S3, 3, u); LD_S(S3, 3, u + 1); }
    STAMP(3);
    if (w == 0) {
      _Pragma("nounroll") for (int it = 0; it < SPIN_CAP; ++it) { cfence(); if (__all(gld32(rs, WS_FQ + lane * FS) == tag)) break; __builtin_amdgcn_s_sleep(PSL); }
    }
    bar();
    STAMP(4);
    {
      v4u t[4];
      const char* qa = ws + WS_QAB + (b * 32 + 4 * w) * 1024;
#pragma unroll
      for (int i = 0; i < 4; ++i) asm volatile("global_load_dwordx4 %0, %1, %2 offset:%3 sc1" : "=v"(t[i]) : "v"(vl), "s"(qa), "n"(i * 1024) : "memory");
      constexpr int NA = NS3 < 8 ? 2 : 0, NC = 8 - NS3 - NA < 4 ? 8 - NS3 - NA : 4;
#pragma unroll
      for (int u = NS3; u < NS3 + NA; ++u) LD_S(S3, 3, u);
      asm volatile("s_waitcnt vmcnt(%12)" : "+v"(t[0]), "+v"(t[1]), "+v"(t[2]), "+v"(t[3]), "+v"(S2[0]), "+v"(S2[1]), "+v"(S2[2]), "+v"(S2[3]),
                   "+v"(S2[4]), "+v"(S2[5]), "+v"(S2[6]), "+v"(S2[7]) : "n"(NA) : "memory");
#pragma unroll
      for (int u = NS3 + NA; u < NS3 + NA + NC; ++u) LD_S(S3, 3, u);
#pragma unroll
      for (int i = 0; i < 4; ++i) sto128(SCR + (4 * w + i) * 1024 + ((lane ^ ((4 * w + i) & 15)) << 4), t[i]);
    }
    bar();
    const int kb4 = w & 3, hg = w >> 2;
    v4u qf[16];
#pragma unroll
    for (int s = 0; s < 16; ++s) qf[s] = lds128(SCR + (16 * hg + c16) * 1024 + ((((4 * s) & 15) ^ g ^ c16) << 4) + (s >> 2) * 256);
    STAMP(5);
    const unsigned fk = ((c16 & 3) << 2) | ((0x78u >> (2 * (c16 >> 2))) & 3u);
    const unsigned ka = (16 * kb4 + c16) * 1024 + ((g ^ fk) << 4);
    const int h2 = lane >> 5, blk = g & 1, qq = c16 >> 2, pp = lane & 3;
    const unsigned ft0 = (qq << 2) | (h2 ? 3u : 0u), ft1 = (qq << 2) | (h2 ? 1u : 2u);
    const unsigned cbase = 8 * w + 2 * blk + (pp >> 1);
    const unsigned va00 = (8 * h2 + qq) * 1024 + ((cbase ^ ft0) << 4) + (pp & 1) * 8, va01 = (8 * h2 + 4 + qq) * 1024 + ((cbase ^ ft1) << 4) + (pp & 1) * 8;
    const unsigned mxa = opq(B_MX + (16 * hg + c16) * 4), pwa = opq(B_P + kb4 * 1536 + (16 * hg + c16) * 48 + g * 8);
    const unsigned pra = opq(B_P + (lane & 31) * 48 + h2 * 16), ala = opq(B_AL + (lane & 31) * 4);
    float m = -INFINITY, l = 0.f;
    f16v acc0, acc1;
#pragma unroll
    for (int i = 0; i < 16; ++i) { acc0[i] = 0.f; acc1[i] = 0.f; }
#pragma unroll
    for (int rho = 0; rho < 2; ++rho) {
      unsigned kr = ka, vr0 = va00, vr1 = va01;
      asm volatile("" : "+v"(kr), "+v"(vr0), "+v"(vr1));
      bar();
      f4 sa = {0.f, 0.f, 0.f, 0.f}, sb = sa;
      {
        v4u kf[8];
#pragma unroll
        for (int s = 0; s < 8; ++s) kf[s] = lds128((kr ^ ((s & 3) << 6)) + (s >> 2) * 256);
        __builtin_amdgcn_sched_barrier(0);
#pragma unroll
        for (int s = 0; s < 8; ++s) {
          sa = mfma32(kf[s], qf[s], sa);
          kf[s] = lds128((kr ^ ((s & 3) << 6)) + (2 + (s >> 2)) * 256);
          __builtin_amdgcn_sched_barrier(0);
        }
        kr += 65536;
#pragma unroll
        for (int s = 0; s < 8; ++s) {
          sa = mfma32(kf[s], qf[8 + s], sa);
          kf[s] = lds128((kr ^ ((s & 3) << 6)) + (s >> 2) * 256);
          __builtin_amdgcn_sched_barrier(0);
        }
#pragma unroll
        for (int s = 0; s < 8; ++s) {
          sb = mfma32(kf[s], qf[s], sb);
          kf[s] = lds128((kr ^ ((s & 3) << 6)) + (2 + (s >> 2)) * 256);
          __builtin_amdgcn_sched_barrier(0);
        }
#pragma unroll
        for (int s = 0; s < 8; ++s) sb = mfma32(kf[s], qf[8 + s], sb);
      }
      if (rho == 0 && NS3 == 0) { LD_S(S3, 3, 6); LD_S(S3, 3, 7); }
      float mx = fmaxf(fmaxf(fmaxf(sa[0], sa[1]), fmaxf(sa[2], sa[3])), fmaxf(fmaxf(sb[0], sb[1]), fmaxf(sb[2], sb[3])));
      mx = x32max(x16max(mx));
      if (g == 0) stof(mxa + kb4 * 128, mx);
      v4u c0[4], c1[4];
#pragma unroll
      for (int ks = 0; ks < 4; ++ks) {
        c0[ks] = trd2(ks * 16384 + vr0, ks * 16384 + vr1);
        c1[ks] = trd2(ks * 16384 + (vr0 ^ 64), ks * 16384 + (vr1 ^ 64));
      }
      if (rho == 0) STAMP(6); else STAMP(8);
      bar();
      const float mn = fmaxf(fmaxf(m, fmaxf(ldsf(mxa), ldsf(mxa + 128))), fmaxf(ldsf(mxa + 256), ldsf(mxa + 384)));
      const float al = ex2((m - mn) * SC), mc = -mn * SC;
      float p[8];
#pragma unroll
      for (int i = 0; i < 4; ++i) { p[i] = ex2(fmaf(sa[i], SC, mc)); p[4 + i] = ex2(fmaf(sb[i], SC, mc)); }
      l = fmaf(l, al, ((p[0] + p[1]) + (p[2] + p[3])) + ((p[4] + p[5]) + (p[6] + p[7])));
      m = mn;
      sto64(pwa, (v2u){pk(p[0], p[1]), pk(p[2], p[3])});
      sto64(pwa + 6144, (v2u){pk(p[4], p[5]), pk(p[6], p[7])});
      if (kb4 == 0 && g == 0) stof(mxa + (B_AL - B_MX), al);
      if (rho == 1) { l = x32sum(x16sum(l)); if (g == 0) stof(mxa + (B_LS - B_MX) + kb4 * 128, l); }
      bar();
      if (rho == 1 && kb4 == 0 && g == 0) {
        const unsigned la = mxa + (B_LS - B_MX);
        sto64(B_FM + (16 * hg + c16) * 8, (v2u){asu(m * SC), asu((ldsf(la) + ldsf(la + 128)) + (ldsf(la + 256) + ldsf(la + 384)))});
      }
      const float alp = rho ? ldsf(ala) : 0.f;
      v4u pf[4];
#pragma unroll
      for (int ks = 0; ks < 4; ++ks) pf[ks] = lds128(pra + ks * 1536);
      __builtin_amdgcn_sched_barrier(0);
      if (rho) { acc0 *= alp; acc1 *= alp; }
      vr0 += 65536; vr1 += 65536;
#pragma unroll
      for (int ks = 0; ks < 4; ++ks) {
        acc0 = mfmaL(c0[ks], pf[ks], acc0); acc1 = mfmaL(c1[ks], pf[ks], acc1);
        c0[ks] = trd2(ks * 16384 + vr0, ks * 16384 + vr1);
        c1[ks] = trd2(ks * 16384 + (vr0 ^ 64), ks * 16384 + (vr1 ^ 64));
        pf[ks] = lds128(pra + 6144 + ks * 1536);
        __builtin_amdgcn_sched_barrier(0);
      }
      bar();
      if (rho == 0) {
        ST_S(S2, 0);
        vmwait<0>();
        ST_S(S3, 1);
      }
#pragma unroll
      for (int ks = 0; ks < 4; ++ks) { acc0 = mfmaL(c0[ks], pf[ks], acc0); acc1 = mfmaL(c1[ks], pf[ks], acc1); }
      if (rho == 0) STAMP(7); else STAMP(9);
    }
    {
    const int lane = (int)opq((unsigned)lane_), h2 = lane >> 5, tid = wave * 64 + lane;
    {
      const unsigned tb = B_T + w * 4096, n32 = lane & 31;
#pragma unroll
      for (int q4 = 0; q4 < 4; ++q4) {
        sto64(tb + n32 * 128 + ((q4 ^ (n32 & 7)) << 4) + h2 * 8, (v2u){pk(acc0[4 * q4], acc0[4 * q4 + 1]), pk(acc0[4 * q4 + 2], acc0[4 * q4 + 3])});
        sto64(tb + n32 * 128 + (((4 + q4) ^ (n32 & 7)) << 4) + h2 * 8, (v2u){pk(acc1[4 * q4], acc1[4 * q4 + 1]), pk(acc1[4 * q4 + 2], acc1[4 * q4 + 3])});
      }
      lgk0();
      const int hr = lane >> 3, ck = lane & 7;
#pragma unroll
      for (int i = 0; i < 4; ++i)
        gst128(rs, WS_PB + (((8 * i + hr) * 8 + w) * 64 + bid) * 128 + ck * 16, lds128(tb + (8 * i + hr) * 128 + ((ck ^ hr) << 4)));
    }
    vmwait<0>();
    bar();
    if (w == 0 && lane < 32) { const v2u fm = *(const LAS v2u*)(B_FM + lane * 8); gst128(rs, WS_PBM + bid * 512 + lane * 16, (v4u){fm.x, tag, fm.y, tag}); }
    STAMP(10);
    if (w == 0) {
      const unsigned off = SKIPA ? WS_F + (64 + bid) * 16384 + (bid & 31) * 128 : PRE2 ? WS_F + bid * 16384 + (2 * bid) * 128 : WS_PAM + (bid * NSPLIT_A + (bid % NSPLIT_A)) * 128;
      _Pragma("nounroll") for (int it = 0; it < SPIN_CAP; ++it) { cfence(); if (__all(gld64(rs, off).y == tag)) break; __builtin_amdgcn_s_sleep(PRESL); }
    }
    bar();
    STAMP(14);
    v4u f[3];
    _Pragma("nounroll") for (int it = 0; it < SPIN_CAP; ++it) {
      cfence();
      bool ok = true;
#pragma unroll
      for (int k = 0; k < 3; ++k) { f[k] = gld128(rs, (k < 2 ? SKIPA : SKIPB) ? 0xfffffff0u : WS_F + ((k >> 1) * 64 + bid) * 16384 + (k < 2 ? k * 8192 + tid * 16 : (tid & 255) * 16)); ok &= (k < 2 ? SKIPA : SKIPB) || tagok(f[k], tag); }
      if (__all(ok)) break;
      __builtin_amdgcn_s_sleep(FSL);
    }
    float r[4] = {asf(f[0].x) + asf(f[1].x), asf(f[0].z) + asf(f[1].z), w < 4 ? asf(f[2].x) : 0.f, w < 4 ? asf(f[2].z) : 0.f};
#pragma unroll
    for (int k = 0; k < 4; ++k) { r[k] += dpp<ROR8>(r[k]); r[k] = x32sum(x16sum(r[k])); }
    if (lane < 8) { sto64(B_FIN + w * 128 + lane * 8, (v2u){asu(r[0]), asu(r[1])}); sto64(B_FIN + w * 128 + 64 + lane * 8, (v2u){asu(r[2]), asu(r[3])}); }
    bar();
    if (tid < 16) {
      float s0 = 0.f, s1 = 0.f;
#pragma unroll
      for (int i = 0; i < 8; ++i) { const f2 t = *(const LAS f2*)(B_FIN + i * 128 + tid * 8); s0 += t.x; s1 += t.y; }
      const int mat = tid >> 3, ob = (tid >> 2) & 1, pr = tid & 3;
      *(GAS unsigned*)((GAS char*)out + (ob * 1024 + mat * 512 + 8 * bid + 2 * pr) * 2) = pk(s0, s1);
      if (!SKIPA && !SKIPB && bid == 0 && tid == 0) gst32(rs, WS_E, tag);
    }
    STAMP(15);
    STAMP_OUT();
    }
  } else {
    const int h = bid - NB_CU, w = wave;
    GET_TAG();
    if (w == 0) gather_qb(T_QBI, h, lane, rs, tag);
    vmwait<0>();
    STAMP(1);
    {
      const unsigned a0 = w * 8192 + c16 * 256 + ((g ^ c16) << 4);
      v4u uf[8];
#pragma unroll
      for (int i = 0; i < 8; ++i) uf[i] = lds128((a0 ^ ((i & 3) << 6)) + (i >> 2) * 4096);
      bar();
      STAMP(2);
      v4u qb[4];
#pragma unroll
      for (int s = 0; s < 4; ++s) qb[s] = lds128(T_QBI + (c16 & 1) * 256 + s * 64 + g * 16);
      __builtin_amdgcn_sched_barrier(0);
      f4 d0 = {0.f, 0.f, 0.f, 0.f}, d1 = d0;
#pragma unroll
      for (int s = 0; s < 4; ++s) { d0 = mfma32(uf[s], qb[s], d0); d1 = mfma32(uf[4 + s], qb[s], d1); }
      const int rb = (c16 >> 1) & 1;
      const f4 e = rb ? d1 : d0;
      if (c16 < 4) sto64(T_QAS + (c16 & 1) * 512 + (32 * w + 16 * rb + 4 * g) * 2, (v2u){pk(e[0], e[1]), pk(e[2], e[3])});
      bar();
      if (w == 0) {
        gst128(rs, WS_QAB + ((lane >> 5) * 32 + h) * 1024 + (lane & 31) * 16, lds128(T_QAS + lane * 16));
        vmwait<0>();
        flag_line(rs, WS_FQ + h * FS, lane, tag);
      }
    }
    bar();
    STAMP(3);
    v4u OU[16];
    {
      const char* uv = Wuv + (size_t)(64 * w) * 8192 + h * 256;
      const unsigned vuv = g * 8192 + ((c16 ^ (g << 1)) << 4);
#pragma unroll
      for (int u = 0; u < 16; ++u) { if (u >= WT && !(u & 1)) vmwait<WT - 2>(); dma<0>(uv + (size_t)u * 32768, vuv ^ (((u >> 1) & 1) << 7), w * 16384 + u * 1024); }
      const char* ow = Bwo + (size_t)(h * 128 + 16 * w) * 1024;
      const unsigned vo = g * 4096 + c16 * 16;
#pragma unroll
      for (int u = 0; u < 16; ++u) {
        if (!(u & 1)) vmwait<WT - 2>();
        asm volatile("global_load_dwordx4 %0, %1, %2 offset:%3 nt" : "=v"(OU[u]) : "v"(vo), "s"(ow), "n"((u >> 2) * 1024 + (u & 3) * 256) : "memory");
      }
    }
    asm volatile("s_waitcnt vmcnt(0)" : "+v"(OU[0]), "+v"(OU[1]), "+v"(OU[2]), "+v"(OU[3]), "+v"(OU[4]), "+v"(OU[5]), "+v"(OU[6]), "+v"(OU[7]),
                 "+v"(OU[8]), "+v"(OU[9]), "+v"(OU[10]), "+v"(OU[11]), "+v"(OU[12]), "+v"(OU[13]), "+v"(OU[14]), "+v"(OU[15]) :: "memory");
    if (tid == 0) sto32(T_RDY, 0u);
    bar();
    STAMP(4);
    v4u wf[16];
    {
      const int q = c16 >> 2, p = lane & 3, f3 = q | ((g & 1) << 2);
      const unsigned a0 = (8 * g + q) * 256 + (((2 * w + (p >> 1)) ^ (f3 << 1)) << 4) + (p & 1) * 8;
      const unsigned aa[2] = {opq(a0), opq(a0 + 65536)};
#pragma unroll
      for (int s = 0; s < 16; ++s) wf[s] = trd2(aa[s >> 3] + (s & 7) * 8192, aa[s >> 3] + (s & 7) * 8192 + 1024);
    }
    STAMP(5);
    {
      const int sub = lane >> 3, ck = lane & 7;
      v4u o[8];
#pragma unroll
      for (int i = 0; i < 8; ++i) { o[i] = (v4u){0u, 0u, 0u, 0u}; asm volatile("" : "+v"(o[i])); }
      const char* pb = ws + WS_PB + (h * 8 + w) * 8192;
      const unsigned vl = lane << 4;
      unsigned done = 0;
      v4u ml = {0u, ~tag, 0u, ~tag};
      if (w == 0) {
        _Pragma("nounroll") for (int it = 0; it < SPIN_CAP; ++it) { cfence(); if (__all(gld32(rs, WS_PBM + 2 * h * 512 + h * 16 + 4) == tag)) break; __builtin_amdgcn_s_sleep(PSL1); }
      }
      _Pragma("nounroll") for (int it = 0; it < SPIN_CAP * 8 && done != 255u; ++it) {
        unsigned rdy;
        if (w == 0) {
          cfence();
          const v4u t = gld128(rs, tagok(ml, tag) ? 0xfffffff0u : WS_PBM + lane * 512 + h * 16);
          if (!tagok(ml, tag)) ml = t;
          unsigned long long m = __ballot(tagok(ml, tag));
          m &= m >> 4; m &= m >> 2; m &= m >> 1; m &= 0x0101010101010101ull;
          rdy = (unsigned)((m * 0x0102040810204080ull) >> 56);
          if (lane == 0) *(volatile LAS unsigned*)T_RDY = rdy;
        } else {
          rdy = lds_peek(T_RDY) & 255u;
          if (rdy == done) { __builtin_amdgcn_s_sleep(1); continue; }
        }
        const unsigned add = rdy & ~done;
        done = rdy;
#pragma unroll
        for (int i = 0; i < 8; ++i)
          if (add & (1u << i)) asm volatile("global_load_dwordx4 %0, %1, %2 offset:%3 sc1" : "+v"(o[i]) : "v"(vl), "s"(pb + (i >> 2) * 4096), "n"((i & 3) * 1024) : "memory");
      }
      if (w == 0) {
        const float M = x16max(row16max(asf(ml.x)));
        const float wt = ex2(asf(ml.x) - M);
        stof(T_WN + lane * 4, wt / x16sum(row16sum(wt * asf(ml.z))));
        lgk0();
        if (lane == 0) *(volatile LAS unsigned*)T_RDY = 511u;
      } else {
        lds_wait_ge<0>(T_RDY, 511u);
      }
      float wi[8];
#pragma unroll
      for (int i = 0; i < 8; ++i) wi[i] = ldsf(T_WN + (8 * i + sub) * 4);
      asm volatile("s_waitcnt vmcnt(0)" : "+v"(o[0]), "+v"(o[1]), "+v"(o[2]), "+v"(o[3]), "+v"(o[4]), "+v"(o[5]), "+v"(o[6]), "+v"(o[7]) :: "memory");
      STAMP(8);
      float a[16];
#pragma unroll
      for (int k = 0; k < 16; ++k) a[k] = 0.f;
#pragma unroll
      for (int i = 0; i < 8; ++i)
#pragma unroll
        for (int k = 0; k < 4; ++k) {
          a[(i >> 2) * 8 + 2 * k] = fmaf(wi[i], asf(o[i][k] << 16), a[(i >> 2) * 8 + 2 * k]);
          a[(i >> 2) * 8 + 2 * k + 1] = fmaf(wi[i], asf(o[i][k] & 0xffff0000u), a[(i >> 2) * 8 + 2 * k + 1]);
        }
      float r[8];
#pragma unroll
      for (int k = 0; k < 8; ++k) {
        auto sw = __builtin_amdgcn_permlane32_swap(asu(a[k]), asu(a[8 + k]), false, false);
        r[k] = x16sum(asf(sw[0]) + asf(sw[1]));
        r[k] += dpp<ROR8>(r[k]);
      }
      if ((lane & 24) == 0) sto128(T_OL + (lane >> 5) * 1024 + w * 128 + ck * 16, (v4u){pk(r[0], r[1]), pk(r[2], r[3]), pk(r[4], r[5]), pk(r[6], r[7])});
    }
    bar();
    STAMP(6);
    {
      v4u of[16];
      const unsigned oa = opq(T_OL + (c16 >> 3) * 1024 + g * 16);
#pragma unroll
      for (int s = 0; s < 16; ++s) of[s] = lds128(oa + s * 64);
      __builtin_amdgcn_sched_barrier(0);
      f4 d0 = {0.f, 0.f, 0.f, 0.f}, d1 = d0;
#pragma unroll
      for (int s = 0; s < 8; ++s) { d0 = mfma32(wf[2 * s], of[2 * s], d0); d1 = mfma32(wf[2 * s + 1], of[2 * s + 1], d1); }
      d0 += d1;
      const unsigned half = (c16 & 1) ? 0xffff0000u : 0x0000ffffu;
      const int rr = (c16 >> 1) & 3;
      f4 o4[4];
#pragma unroll
      for (int cb = 0; cb < 4; ++cb) o4[cb] = (f4){0.f, 0.f, 0.f, 0.f};
#pragma unroll
      for (int u = 0; u < 4; ++u) {
        const unsigned x = pk(d0[u], d0[u]) & half;
        const v4u bo = {rr == 0 ? x : 0u, rr == 1 ? x : 0u, rr == 2 ? x : 0u, rr == 3 ? x : 0u};
#pragma unroll
        for (int cb = 0; cb < 4; ++cb) o4[cb] = mfma32(OU[4 * u + cb], bo, o4[cb]);
      }
#pragma unroll
      for (int cb = 0; cb < 4; ++cb)
#pragma unroll
        for (int r = 0; r < 4; ++r) stof(T_RED + w * 4096 + cb * 1024 + r * 64 + g * 256 + c16 * 4, o4[cb][r]);
    }
    bar();
    {
      const int j = tid >> 3, pr = tid & 7;
      float s0 = 0.f, s1 = 0.f;
#pragma unroll
      for (int i = 0; i < 8; ++i) { const f2 t = *(const LAS f2*)(T_RED + i * 4096 + tid * 8); s0 += t.x; s1 += t.y; }
      gst128(rs, WS_F + (((64 + j) * 128 + h) * 16 + 2 * pr) * 8, (v4u){asu(s0), tag, asu(s1), tag});
    }
    STAMP(7);
    STAMP_OUT();
  }
}

extern "C" void kernel_launch(void* const* d_in, const int* in_sizes, int n_in, void* d_out, int out_size, void* d_ws, size_t ws_size,
                              hipStream_t stream) {
  (void)in_sizes; (void)n_in; (void)out_size; (void)ws_size;
  (void)hipFuncSetAttribute((const void*)fused_decode, hipFuncAttributeMaxDynamicSharedMemorySize, LDS_BYTES);
  fused_decode<<<256, 512, LDS_BYTES, stream>>>(
      (const char*)d_in[0], (const char*)d_in[1], (const char*)d_in[2], (const char*)d_in[3], (const char*)d_in[4], (const char*)d_in[5],
      (const char*)d_in[6], (const char*)d_in[7], (const char*)d_in[8], (const char*)d_in[9], (const char*)d_in[10], (unsigned short*)d_out,
      (char*)d_ws);
}
```

```cpp
#define DBG_TS
#define PROBE 0
#define PROLE 0
#define PSTAT 1
#define PZERO 0
#include <hip/hip_runtime.h>
#include <stdint.h>
#pragma clang diagnostic ignored "-Wint-to-pointer-cast"

#define GAS __attribute__((address_space(1)))
#define LAS __attribute__((address_space(3)))
typedef unsigned v4u __attribute__((ext_vector_type(4)));
typedef unsigned v2u __attribute__((ext_vector_type(2)));
typedef float f4 __attribute__((ext_vector_type(4)));
typedef float f2 __attribute__((ext_vector_type(2)));
typedef float f16v __attribute__((ext_vector_type(16)));
typedef short s4 __attribute__((ext_vector_type(4)));
typedef __bf16 b8 __attribute__((ext_vector_type(8)));
typedef __bf16 b2 __attribute__((ext_vector_type(2)));

#define W1 12
#define W2 24
#define J1 18
#define DLY2 40
#define DLYP 30
#define DLYB 10
#define NB0 55
#define NB1 52
#define WB 6
#define PRE2 1
#define SKIPA 0
#define SKIPB 0
#define WT 6
#define CODE_KB 24
#define QGO 4

namespace {

constexpr int NB_CU = 64, NT_CU = 32, NSPLIT_A = 10, NSLOT = 14, NCONS = 6;
constexpr float SC = 0.08838834764831845f * 1.4426950408889634f;
constexpr int SPIN_CAP = 1 << 18;

#define NS3 4
#define PSL 4
#define PSL1 8
#define PRESL 8
#define GSL 1
#define FSL 4
constexpr unsigned FS = 128;
constexpr unsigned WS_E = 0;
constexpr unsigned WS_FQ = 4096;
constexpr unsigned WS_FP = WS_FQ + 16384;
constexpr unsigned WS_QG = 65536;
constexpr unsigned WS_QAB = WS_QG + 262144;
constexpr unsigned WS_PBM = WS_QAB + 65536;
constexpr unsigned WS_PAM = WS_PBM + 32768;
constexpr unsigned WS_PAO = WS_PAM + 131072;
constexpr unsigned WS_PB = WS_PAO + 786432;
constexpr unsigned WS_F = WS_PB + 2097152;
constexpr unsigned WS_END = WS_F + 2097152;
constexpr unsigned WS_TS = 8u << 20;

constexpr unsigned LDS_BYTES = 163840;
constexpr unsigned SCR = 131072;
constexpr unsigned A_MISC = NSLOT * 8192;
constexpr unsigned A_LANDK = A_MISC, A_LANDV = A_MISC + 4, A_QUSED = A_MISC + 8, A_QCNT = A_MISC + 12, A_QB = A_MISC + 16, A_QRDY = A_MISC + 20, A_OUL = A_MISC + 24, A_OUL2 = A_MISC + 28;
constexpr unsigned A_DONE = A_MISC + 64;
constexpr unsigned A_QU2 = 10 * 8192;
constexpr unsigned A_QIMG = A_MISC + 256;
constexpr unsigned A_ML = A_MISC + 1280;
constexpr unsigned A_OIMG = A_MISC + 1792;
constexpr unsigned A_ST = A_MISC + 2048;
constexpr unsigned A_XIMG = A_MISC + 4096, A_DUMMY = A_MISC + 5120, A_QBI = A_MISC + 6144, A_QAS = A_MISC + 6656;
constexpr unsigned SLOT0 = 0, SLOT1 = 65536;
constexpr unsigned B_MX = SCR, B_AL = SCR + 512, B_LS = SCR + 640, B_P = SCR + 2048;
constexpr unsigned B_FM = SCR + 1280, B_FIN = SCR + 16384, B_T = SLOT1, B_DUMMY = SCR + 8192;
constexpr unsigned T_QBI = SCR, T_QAS = SCR + 1024, T_OL = SCR + 4096, T_RDY = SCR + 6400, T_WN = SCR + 6656, T_RED = 0;

__device__ __forceinline__ v4u lds128(unsigned a) { return *(const LAS v4u*)a; }
__device__ __forceinline__ void sto128(unsigned a, v4u v) { *(LAS v4u*)a = v; }
__device__ __forceinline__ void sto64(unsigned a, v2u v) { *(LAS v2u*)a = v; }
__device__ __forceinline__ void sto32(unsigned a, unsigned v) { *(LAS unsigned*)a = v; }
__device__ __forceinline__ float ldsf(unsigned a) { return *(const LAS float*)a; }
__device__ __forceinline__ void stof(unsigned a, float v) { *(LAS float*)a = v; }
__device__ __forceinline__ v2u trd(unsigned a) { return __builtin_bit_cast(v2u, __builtin_amdgcn_ds_read_tr16_b64_v4i16((LAS s4*)a)); }
__device__ __forceinline__ v4u trd2(unsigned a0, unsigned a1) { const v2u x = trd(a0), y = trd(a1); return (v4u){x.x, x.y, y.x, y.y}; }
__device__ __forceinline__ f4 mfma32(v4u a, v4u b, f4 c) {
  return __builtin_amdgcn_mfma_f32_16x16x32_bf16(__builtin_bit_cast(b8, a), __builtin_bit_cast(b8, b), c, 0, 0, 0); }
__device__ __forceinline__ f4 mfma16(v2u a, v2u b, f4 c) {
  return __builtin_amdgcn_mfma_f32_16x16x16bf16_1k(__builtin_bit_cast(s4, a), __builtin_bit_cast(s4, b), c, 0, 0, 0); }
__device__ __forceinline__ f16v mfmaL(v4u a, v4u b, f16v c) {
  return __builtin_amdgcn_mfma_f32_32x32x16_bf16(__builtin_bit_cast(b8, a), __builtin_bit_cast(b8, b), c, 0, 0, 0); }
__device__ __forceinline__ unsigned pk(float lo, float hi) { return __builtin_bit_cast(unsigned, __builtin_convertvector((f2){lo, hi}, b2)); }
__device__ __forceinline__ float asf(unsigned u) { return __builtin_bit_cast(float, u); }
__device__ __forceinline__ unsigned asu(float f) { return __builtin_bit_cast(unsigned, f); }
__device__ __forceinline__ float ex2(float x) { return __builtin_amdgcn_exp2f(x); }
template <int CTRL> __device__ __forceinline__ float dpp(float x) {
  return __builtin_bit_cast(float, __builtin_amdgcn_mov_dpp(__builtin_bit_cast(int, x), CTRL, 0xf, 0xf, true)); }
constexpr int XOR1 = 0xB1, XOR2 = 0x4E, ROR4 = 0x124, ROR8 = 0x128;
__device__ __forceinline__ float x16max(float x) { auto s = __builtin_amdgcn_permlane16_swap(asu(x), asu(x), false, false); return fmaxf(asf(s[0]), asf(s[1])); }
__device__ __forceinline__ float x32max(float x) { auto s = __builtin_amdgcn_permlane32_swap(asu(x), asu(x), false, false); return fmaxf(asf(s[0]), asf(s[1])); }
__device__ __forceinline__ float x16sum(float x) { auto s = __builtin_amdgcn_permlane16_swap(asu(x), asu(x), false, false); return asf(s[0]) + asf(s[1]); }
__device__ __forceinline__ float x32sum(float x) { auto s = __builtin_amdgcn_permlane32_swap(asu(x), asu(x), false, false); return asf(s[0]) + asf(s[1]); }
__device__ __forceinline__ float row16max(float x) { x = fmaxf(x, dpp<XOR1>(x)); x = fmaxf(x, dpp<XOR2>(x)); x = fmaxf(x, dpp<ROR4>(x)); return fmaxf(x, dpp<ROR8>(x)); }
__device__ __forceinline__ float row16sum(float x) { x += dpp<XOR1>(x); x += dpp<XOR2>(x); x += dpp<ROR4>(x); return x + dpp<ROR8>(x); }

template <int IMM> __device__ __forceinline__ void dma(const void* sbase, unsigned voff, unsigned lds) {
  asm volatile("s_mov_b32 m0, %2\n\ts_nop 0\n\tglobal_load_lds_dwordx4 %0, %1 offset:%3 nt" :: "v"(voff), "s"(sbase), "s"(lds), "n"(IMM) : "memory");
}
template <int W> __device__ __forceinline__ void dma4(const void* sbase, unsigned v0, unsigned v1, unsigned v2, unsigned v3, unsigned lds) {
  asm volatile("s_mov_b32 m0, %5\n\ts_waitcnt vmcnt(%6)\n\tglobal_load_lds_dwordx4 %0, %4 nt\n\tglobal_load_lds_dwordx4 %1, %4 offset:1024 nt\n\t"
               "s_waitcnt vmcnt(%6)\n\tglobal_load_lds_dwordx4 %2, %4 offset:2048 nt\n\tglobal_load_lds_dwordx4 %3, %4 offset:3072 nt"
               :: "v"(v0), "v"(v1), "v"(v2), "v"(v3), "s"(sbase), "s"(lds), "n"(W - 2) : "memory");
}
__device__ __forceinline__ void dma_keep(const void* sbase, unsigned voff, unsigned lds) {
  asm volatile("s_mov_b32 m0, %2\n\ts_nop 0\n\tglobal_load_lds_dwordx4 %0, %1" :: "v"(voff), "s"(sbase), "s"(lds) : "memory");
}
template <int N> __device__ __forceinline__ void vmwait() { asm volatile("s_waitcnt vmcnt(%0)" :: "n"(N) : "memory"); }
__device__ __forceinline__ void bar() { asm volatile("s_waitcnt lgkmcnt(0)\n\ts_barrier" ::: "memory"); }
__device__ __forceinline__ void lgk0() { asm volatile("s_waitcnt lgkmcnt(0)" ::: "memory"); }
__device__ __forceinline__ void cfence() { asm volatile("" ::: "memory"); }
__device__ __forceinline__ unsigned opq(unsigned x) { asm volatile("" : "+v"(x)); return x; }
__device__ __forceinline__ unsigned lds_peek(unsigned a) { return __builtin_amdgcn_readfirstlane(*(volatile LAS unsigned*)a); }
template <int SL = 1> __device__ __forceinline__ void lds_wait_ge(unsigned a, unsigned want) {
  _Pragma("nounroll") for (int it = 0; it < SPIN_CAP * 8; ++it) { if (lds_peek(a) >= want) break; if (SL) __builtin_amdgcn_s_sleep(SL); }
  cfence();
}
typedef __amdgpu_buffer_rsrc_t rsrc_t;
constexpr int SC1 = 16;
__device__ __forceinline__ v4u gld128(rsrc_t r, unsigned off) { return __builtin_amdgcn_raw_buffer_load_b128(r, off, 0, SC1); }
__device__ __forceinline__ v2u gld64(rsrc_t r, unsigned off) { return __builtin_amdgcn_raw_buffer_load_b64(r, off, 0, SC1); }
__device__ __forceinline__ unsigned gld32(rsrc_t r, unsigned off) { return __builtin_amdgcn_raw_buffer_load_b32(r, off, 0, SC1); }
__device__ __forceinline__ void gst128(rsrc_t r, unsigned off, v4u v) { __builtin_amdgcn_raw_buffer_store_b128(v, r, off, 0, SC1); }
__device__ __forceinline__ void gst64(rsrc_t r, unsigned off, v2u v) { __builtin_amdgcn_raw_buffer_store_b64(v, r, off, 0, SC1); }
__device__ __forceinline__ void gst32(rsrc_t r, unsigned off, unsigned v) { __builtin_amdgcn_raw_buffer_store_b32(v, r, off, 0, SC1); }
__device__ __forceinline__ void flag_line(rsrc_t r, unsigned off, int lane, unsigned tag) { if (lane < 8) gst128(r, off + lane * 16, (v4u){tag, tag, tag, tag}); }
__device__ __forceinline__ bool tagok(v4u v, unsigned tag) { return v.y == tag && v.w == tag; }

#define STAMP(i) do { unsigned long long t_; asm volatile("s_memrealtime %0\n\ts_waitcnt lgkmcnt(0)" : "=s"(t_) :: "memory"); tsv[i] = (unsigned)t_; } while (0)
#define STAMP_DECL unsigned tsv[16] = {0,0,0,0,0,0,0,0,0,0,0,0,0,0,0,0}
#define STAMP_OUT() do { if (lane == 0) gst128(rs2, WS_TS + (blockIdx.x * 8 + wave) * 16, (v4u){tsv[0], tag, tsv[PROBE], tag}); \
  if (blockIdx.x == 0 && wave == 0) { unsigned mn = 0xffffffffu; float mx = 0.f, sm = 0.f, cnt = 0.f; unsigned ref = tsv[0] - 1000u; \
    for (int i_ = 0; i_ < 32; ++i_) { v4u v_; for (int it = 0; it < SPIN_CAP; ++it) { cfence(); v_ = gld128(rs2, WS_TS + (i_ * 64 + lane) * 16); if (__all(tagok(v_, tag))) break; } \
      mn = min(mn, v_.x - ref); const bool role_ = PROLE == 1 ? (i_ < 8) : PROLE == 2 ? (i_ >= 8 && i_ < 12) : (i_ >= 12); if (v_.z != 0u && role_) { const float d_ = (float)(v_.z - ref); mx = fmaxf(mx, d_); sm += d_; cnt += 1.f; } } \
    float mnf = -(float)mn; mnf = -x32max(x16max(row16max(mnf))); mx = x32max(x16max(row16max(mx))); sm = x32sum(x16sum(row16sum(sm))); cnt = x32sum(x16sum(row16sum(cnt))); \
    const float X = PZERO ? 0.f : (PSTAT ? sm / cnt : mx) - mnf; const unsigned until = ref + (unsigned)(mnf + 6000.f + 8.f * X); \
    for (int it = 0; it < 100000000; ++it) { unsigned long long t_; asm volatile("s_memrealtime %0\n\ts_waitcnt lgkmcnt(0)" : "=s"(t_) :: "memory"); if ((int)((unsigned)t_ - __builtin_amdgcn_readfirstlane(until)) >= 0) break; } } } while (0)

__device__ __forceinline__ void gather_qb(unsigned img, int h, int lane, rsrc_t rs, unsigned tag) {
  v4u qg[4];
  const unsigned off = WS_QG + (h * 128 + 2 * lane) * 8;
  _Pragma("nounroll") for (int it = 0; it < SPIN_CAP; ++it) {
    cfence();
    bool ok = true;
#pragma unroll
    for (int k = 0; k < 4; ++k) { qg[k] = gld128(rs, off + (k & 1) * 65536 + (k >> 1) * 32768); ok &= tagok(qg[k], tag); }
    if (__all(ok)) break;
    __builtin_amdgcn_s_sleep(1);
  }
  sto32(img + lane * 4, pk(asf(qg[0].x) + asf(qg[1].x), asf(qg[0].z) + asf(qg[1].z)));
  sto32(img + 256 + lane * 4, pk(asf(qg[2].x) + asf(qg[3].x), asf(qg[2].z) + asf(qg[3].z)));
}
__device__ __forceinline__ void lds_inc(unsigned a) { __hip_atomic_fetch_add((LAS unsigned*)a, 1u, __ATOMIC_RELAXED, __HIP_MEMORY_SCOPE_WORKGROUP); }

__device__ __forceinline__ void out_frags(unsigned ou, int w, int lane, v4u (&wf)[4]) {
  const int g = lane >> 4, q = (lane & 15) >> 2, p = lane & 3, f3 = q | ((g & 1) << 2);
#pragma unroll
  for (int tt = 0; tt < 4; ++tt) {
    const unsigned a0 = ou + (8 * g + q) * 1024 + (((2 * (4 * w + tt) + (p >> 1)) ^ (f3 << 1)) << 4) + (p & 1) * 8;
    wf[tt] = trd2(a0, a0 + 4096);
  }
}

}

__global__ void __launch_bounds__(512, 2) fused_decode(
    const char* __restrict__ Ax, const char* __restrict__ Kc, const char* __restrict__ Vc, const char* __restrict__ Wq, const char* __restrict__ Wo,
    const char* __restrict__ Bx, const char* __restrict__ Ckv, const char* __restrict__ Bwq, const char* __restrict__ Wuk, const char* __restrict__ Wuv,
    const char* __restrict__ Bwo, unsigned short* __restrict__ out, char* __restrict__ ws) {
  const int bid = blockIdx.x, tid = threadIdx.x, lane = tid & 63, wave = __builtin_amdgcn_readfirstlane(tid >> 6);
  const int g = lane >> 4, c16 = lane & 15;
  STAMP_DECL;
  STAMP(0);
  const int iA = bid - (NB_CU + NT_CU);
  const bool isA = iA >= 0, isB = bid < NB_CU;
  const int ty = iA >> 6;
  int dA = 95 - bid, dU = 223 - bid, dB = bid - 64, dQ = 159 - bid, dW = 2 - wave;
  asm("" : "+s"(dA), "+s"(dU), "+s"(dB), "+s"(dQ), "+s"(dW));
  const int mA = dA >> 31, mU = dU >> 31, mB = dB >> 31, mP = mA & ~mU, sh = mP & 1;
  const int hh = (bid >> sh) & 31;
  const unsigned row0 = (mA & ((((bid | mU) & 1) << 8) + 64 * wave - 64)) | (~mA & (32 * wave));
  const unsigned l0 = (mA & (SCR - 16384 + wave * 16384 - ((dW >> 31) & (SCR + 32768 - A_QU2)))) | (~mA & (wave * 8192));
  const unsigned karg = 0x40 - (mP & (8 + ((dQ >> 31) & 32))), more = mA;
  if ((unsigned)(wave + mA + (mB & 8)) < (unsigned)(8 + (mA & -4))) {
    const unsigned b0 = (row0 + g) * 8192 + hh * 256 + ((c16 ^ (g << sh)) << 4), x1 = ~mP & 64;
    unsigned v0 = b0, v1 = (b0 ^ x1) + 32768, v2 = (b0 ^ 128) + 65536, v3 = (b0 ^ x1 ^ 128) + 98304;
    if (DLYP && karg == 0x18) __builtin_amdgcn_s_sleep(DLYP);
    unsigned long long wp;
#define D4 "global_load_lds_dwordx4 %1, %0 nt\n\ts_add_u32 m0, m0, 0x400\n\tv_add_u32 %1, 0x20000, %1\n\t" \
           "global_load_lds_dwordx4 %2, %0 nt\n\ts_add_u32 m0, m0, 0x400\n\tv_add_u32 %2, 0x20000, %2\n\t" \
           "global_load_lds_dwordx4 %3, %0 nt\n\ts_add_u32 m0, m0, 0x400\n\tv_add_u32 %3, 0x20000, %3\n\t" \
           "global_load_lds_dwordx4 %4, %0 nt\n\ts_add_u32 m0, m0, 0x400\n\tv_add_u32 %4, 0x20000, %4\n\t"
    asm volatile("s_load_dwordx2 %0, %5, %6\n\ts_mov_b32 m0, %7\n\ts_waitcnt lgkmcnt(0)\n\t" D4 D4
                 "s_cmp_eq_u32 %8, 0\n\ts_cbranch_scc1 1f\n\t" D4 D4 "1:"
                 : "=&s"(wp), "+v"(v0), "+v"(v1), "+v"(v2), "+v"(v3)
                 : "s"(__builtin_amdgcn_kernarg_segment_ptr()), "s"(karg), "s"(l0), "s"(more) : "memory", "scc");
#undef D4
  }
  {
    unsigned long long pc;
    asm volatile("s_getpc_b64 %0" : "=s"(pc));
    const int kb = ((bid >> 3) + 4 * wave) & 31;
    if (CODE_KB) dma_keep((const char*)((pc & ~1023ull) + (unsigned)((kb < CODE_KB ? kb : CODE_KB - 1) * 1024)), lane * 16, isA ? A_DUMMY : B_DUMMY);
  }
  const rsrc_t rs = __builtin_amdgcn_make_buffer_rsrc(ws, 0, WS_END, 0x00020000);
  const rsrc_t rs2 = __builtin_amdgcn_make_buffer_rsrc(ws, 0, 16u << 20, 0x00020000);
  asm volatile("" :: "s"(Ax), "s"(Kc), "s"(Vc), "s"(Wq), "s"(Wo), "s"(Bx), "s"(Ckv), "s"(Bwq), "s"(Wuk), "s"(Wuv), "s"(Bwo), "s"(out), "s"(ws));
  unsigned tag;
#define GET_TAG() do { asm volatile("s_load_dword %0, %1, 0x0\n\ts_waitcnt lgkmcnt(0)" : "=s"(tag) : "s"(ws) : "memory"); tag += 1u; } while (0)
  const char* cp = Ckv + ((size_t)(bid >> 5) * 8192 + 256 * (bid & 31) + 8 * wave) * 1024;
  const unsigned vc0 = (lane ^ ((wave & 1) ? 3 : 0)) << 4, vc1 = (lane ^ ((wave & 1) ? 1 : 2)) << 4;
#define LD_C(rho, u) dma<((u) & 3) * 1024>(cp + (size_t)(rho) * 65536 + ((u) >> 2) * 4096, (((u) >> 2) ? vc1 : vc0) ^ (((u) & 3) << 6), ((rho) & 1) * 65536 + wave * 8192 + ((u) >> 2) * 4096)
  if (isA) {
    if (wave == 1 && ty < 2) dma<0>((ty ? Ax : Bx) + 512 * (bid & 1), (lane >> 5) * 1024 + (lane & 31) * 16, A_XIMG);
    if (tid < 32) sto32(A_MISC + tid * 4, 0u);
    bar();
  } else if (isB) {
    __builtin_amdgcn_s_sleep(DLYB);
    LD_C(0, 0); LD_C(0, 1); LD_C(0, 2); LD_C(0, 3); LD_C(0, 4); LD_C(0, 5);
  }

  if (isA) {
    const int split = iA >> 4, pair = (iA + split) & 15, b = pair >> 3, kvh = pair & 7;
    constexpr int NB2 = (512 - 4 * NB0 - 4 * NB1) / 2;
    const int nblk = ty == 0 ? NB0 : ty == 1 ? NB1 : NB2, blk0 = (ty == 0 ? 0 : ty == 1 ? 4 * NB0 : 4 * (NB0 + NB1)) + (split & 3) * nblk;
    if (wave == 0 || wave == 7) {
      __builtin_amdgcn_s_setprio(3);
      static_assert(NSPLIT_A == 10 && W1 >= 4 && W1 % 4 == 0 && W2 % 4 == 0 && W1 <= W2 && W2 <= 32 && J1 <= NB0 && J1 <= NB1 && J1 <= NB2, "window");
      const bool isv = wave == 7;
      int pub = 0, j = 0;
      unsigned sl = 0;
      const char* gp = (isv ? Vc : Kc) + ((size_t)pair * 8192 + blk0 * 16) * 256;
      const unsigned v0 = opq(g * 256 + ((c16 ^ (isv ? g << 1 : g)) << 4));
      const unsigned v1 = opq(v0 ^ (isv ? 128 : 64)), v2 = opq(v0 ^ (isv ? 0 : 128)), v3 = opq(v0 ^ (isv ? 128 : 192));
      const unsigned lo = isv ? 4096 : 0, la = opq(lane ? A_MISC + 1536 + lane * 4 : isv ? A_LANDV : A_LANDK);
      unsigned dn = 0;
#define PUBLISH(val) do { pub = max(pub, (val)); *(volatile LAS unsigned*)la = (unsigned)pub; } while (0)
#define GATE(addr, need) do { if (lds_peek(addr) < (need)) { vmwait<0>(); PUBLISH(j); lds_wait_ge(addr, need); } } while (0)
#define PART(W) do { \
        if (j >= NSLOT) { if ((unsigned)__builtin_amdgcn_readfirstlane(dn) < (unsigned)(j - NSLOT + 1)) GATE(A_DONE + sl, (unsigned)(j - NSLOT + 1)); } \
        else if (j == 10) GATE(A_QUSED, 4u); \
        dma4<W>(gp, v0, v1, v2, v3, sl * 2048 + lo); \
        ++j; gp += 4096; sl = sl == 4 * (NSLOT - 1) ? 0 : sl + 4; \
        PUBLISH(j - W / 4); dn = *(volatile LAS unsigned*)(A_DONE + sl); } while (0)
      STAMP(9);
      GET_TAG();
      lds_wait_ge(A_QCNT, 4u);
      STAMP(10);
#pragma nounroll
      while (j < J1) PART(W1);
      STAMP(11);
#pragma nounroll
      while (j < nblk) PART(W2);
      if (ty < 2) {
        const char* op = Wo + (size_t)iA * 32768 + (isv ? 16384 : 0);
        const unsigned o0 = lane << 4, o1 = o0 ^ 32, o2 = o0 ^ 64, o3 = o0 ^ 96;
#pragma unroll
        for (int k = 0; k < 4; ++k) {
          const unsigned x = ((k >> 1) & 1) << 7;
          if (4 * k + 4 <= W2) { dma4<W2>(op + k * 4096, o0 ^ x, o1 ^ x, o2 ^ x, o3 ^ x, SCR + lo * 4 + k * 4096); PUBLISH(j + k + 1 - W2 / 4); }
          else dma4<62>(op + k * 4096, o0 ^ x, o1 ^ x, o2 ^ x, o3 ^ x, SCR + lo * 4 + k * 4096);
        }
        if (W2 > 16) { vmwait<28>(); PUBLISH(j - 3); vmwait<24>(); PUBLISH(j - 2); vmwait<20>(); PUBLISH(j - 1); vmwait<16>(); PUBLISH(j); }
      } else {
        vmwait<28>(); PUBLISH(j - 7); vmwait<24>(); PUBLISH(j - 6); vmwait<20>(); PUBLISH(j - 5); vmwait<16>(); PUBLISH(j - 4);
        vmwait<12>(); PUBLISH(j - 3); vmwait<8>(); PUBLISH(j - 2); vmwait<4>(); PUBLISH(j - 1); vmwait<0>(); PUBLISH(j);
      }
      __builtin_amdgcn_s_setprio(0);
      STAMP(5);
#undef PUBLISH
#undef GATE
#undef PART
    } else {
      GET_TAG();
      vmwait<0>();
      if (wave <= 4) {
        const int w1 = wave - 1;
        if (ty < 2) {
          if (lane == 0) lds_inc(A_QCNT);
          const int q = c16 >> 2, p = lane & 3, f3 = q | ((g & 1) << 2);
          const unsigned a0 = (8 * g + q) * 256 + (((4 * w1 + (p >> 1)) ^ (f3 << 1)) << 4) + (p & 1) * 8;
          const unsigned aa[2] = {opq(SCR + a0), opq(A_QU2 + a0)};
          lds_wait_ge(A_QCNT, 4u);
          STAMP(1);
          v4u w0[8], w1f[8], xf[8];
#pragma unroll
          for (int s = 0; s < 8; ++s) {
            const unsigned a = aa[s >> 2] + (s & 3) * 8192;
            w0[s] = trd2(a, a + 1024); w1f[s] = trd2((a ^ 32), (a ^ 32) + 1024);
            xf[s] = lds128(A_XIMG + (c16 & 1) * 512 + g * 16 + s * 64);
          }
          __builtin_amdgcn_sched_barrier(0);
          f4 d0 = {0.f, 0.f, 0.f, 0.f}, d1 = d0;
#pragma unroll
          for (int s = 0; s < 8; ++s) { d0 = mfma32(w0[s], xf[s], d0); d1 = mfma32(w1f[s], xf[s], d1); }
          const int pr = (c16 >> 1) & 1, nb = (c16 >> 2) & 1;
          const f4 e = nb ? d1 : d0;
          if (c16 < 8) gst128(rs, WS_QG + (((ty * 2 + (bid & 1)) * 2 + (c16 & 1)) * 4096 + hh * 128 + 32 * w1 + 16 * nb + 4 * g + 2 * pr) * 8,
                              (v4u){asu(pr ? e[2] : e[0]), tag, asu(pr ? e[3] : e[1]), tag});
          lgk0();
        } else {
          STAMP(1);
          if (w1 == 3 && lane == 0) *(volatile LAS unsigned*)A_OUL2 = 1u;
          const unsigned a0 = opq((w1 < 2 ? SCR : A_QU2 - 32768) + w1 * 16384 + c16 * 256 + ((g ^ c16) << 4));
          v4u uf[16];
#pragma unroll
          for (int i = 0; i < 16; ++i) uf[i] = lds128((a0 ^ ((i & 3) << 6)) + (i >> 2) * 4096);
          lds_wait_ge(A_QB, 1u);
          v4u qb[4];
#pragma unroll
          for (int s = 0; s < 4; ++s) qb[s] = lds128(A_QBI + (c16 & 1) * 256 + s * 64 + g * 16);
          __builtin_amdgcn_sched_barrier(0);
          f4 d[4];
#pragma unroll
          for (int rb = 0; rb < 4; ++rb) d[rb] = (f4){0.f, 0.f, 0.f, 0.f};
#pragma unroll
          for (int s = 0; s < 4; ++s)
#pragma unroll
            for (int rb = 0; rb < 4; ++rb) d[rb] = mfma32(uf[4 * rb + s], qb[s], d[rb]);
          const int rb = (c16 >> 1) & 3;
          const f4 e = rb == 0 ? d[0] : rb == 1 ? d[1] : rb == 2 ? d[2] : d[3];
          if (c16 < 8) sto64(A_QAS + w1 * 256 + (c16 & 1) * 128 + (16 * rb + 4 * g) * 2, (v2u){pk(e[0], e[1]), pk(e[2], e[3])});
          lgk0();
          if (lane < 16) gst128(rs, WS_QAB + ((lane >> 3) * 32 + hh) * 1024 + 512 + w1 * 128 + (lane & 7) * 16, lds128(A_QAS + w1 * 256 + lane * 16));
          vmwait<0>();
        }
        unsigned old = 0;
        if (lane == 0) old = __hip_atomic_fetch_add((LAS unsigned*)A_QUSED, 1u, __ATOMIC_RELAXED, __HIP_MEMORY_SCOPE_WORKGROUP);
        if (ty == 2 && __builtin_amdgcn_readfirstlane(old) == 3u) {
          flag_line(rs, WS_FQ + (32 + hh) * FS, lane, tag);
          cfence();
          if (lane == 0) *(volatile LAS unsigned*)A_QCNT = 4u;
        }
        STAMP(2);
      }
      if (wave == 5) {
        if (ty == 2) {
          lds_wait_ge(A_OUL2, 1u);
          gather_qb(A_QBI, hh, lane, rs, tag);
          lgk0();
          if (lane == 0) *(volatile LAS unsigned*)A_QB = 1u;
          STAMP(1);
        }
        lds_wait_ge(A_LANDK, ty == 2 ? 1u : (unsigned)QGO);
        v4u v[8];
        const unsigned off = WS_QG + 131072 + (b * 4096 + kvh * 512 + 2 * lane) * 8;
        _Pragma("nounroll") for (int it = 0; it < SPIN_CAP; ++it) {
          cfence();
          bool ok = true;
#pragma unroll
          for (int k = 0; k < 8; ++k) { v[k] = gld128(rs, off + (k & 1) * 65536 + (k >> 1) * 1024); ok &= tagok(v[k], tag); }
          if (__all(ok)) break;
          __builtin_amdgcn_s_sleep(4);
        }
#pragma unroll
        for (int k = 0; k < 4; ++k) sto32(A_QIMG + k * 256 + lane * 4, pk(asf(v[2 * k].x) + asf(v[2 * k + 1].x), asf(v[2 * k].z) + asf(v[2 * k + 1].z)));
        lgk0();
        if (lane == 0) *(volatile LAS unsigned*)A_QRDY = 1u;
      }
      lds_wait_ge(A_QRDY, 1u);
      STAMP(3);
      v4u qf[4];
#pragma unroll
      for (int s = 0; s < 4; ++s) qf[s] = lds128(A_QIMG + (c16 & 3) * 256 + s * 64 + g * 16);
      const unsigned ko = c16 * 256 + ((g ^ c16) << 4);
      const int vr = 4 * g + (c16 >> 2);
      const unsigned vo = 4096 + vr * 256 + ((((lane & 3) >> 1) ^ ((vr & 7) << 1)) << 4) + (lane & 1) * 8;
      float m = -INFINITY, l = 0.f;
      f4 acc[8];
#pragma unroll
      for (int t = 0; t < 8; ++t) acc[t] = (f4){0.f, 0.f, 0.f, 0.f};
      int slot = wave - 1;
      for (int j = wave - 1; j < nblk; j += NCONS) {
        const unsigned sb = slot * 8192;
        lds_wait_ge(A_LANDK, (unsigned)(j + 1));
        v4u kf[4];
#pragma unroll
        for (int s = 0; s < 4; ++s) kf[s] = lds128(sb + (ko ^ (s << 6)));
        __builtin_amdgcn_sched_barrier(0);
        f4 s4_ = {0.f, 0.f, 0.f, 0.f};
#pragma unroll
        for (int s = 0; s < 4; ++s) s4_ = mfma32(kf[s], qf[s], s4_);
        float mx = fmaxf(fmaxf(s4_[0], s4_[1]), fmaxf(s4_[2], s4_[3]));
        mx = x32max(x16max(mx));
        const float mn = fmaxf(m, mx), al = ex2((m - mn) * SC), mc = -mn * SC;
        const float p0 = ex2(fmaf(s4_[0], SC, mc)), p1 = ex2(fmaf(s4_[1], SC, mc)), p2 = ex2(fmaf(s4_[2], SC, mc)), p3 = ex2(fmaf(s4_[3], SC, mc));
        l = fmaf(l, al, (p0 + p1) + (p2 + p3));
        m = mn;
        v2u pb = {pk(p0, p1), pk(p2, p3)};
        unsigned va[8];
#pragma unroll
        for (int t = 0; t < 8; ++t) va[t] = sb + (vo ^ (t << 5));
        if (!__all(al == 1.f)) {
#pragma unroll
          for (int t = 0; t < 8; ++t) acc[t] *= al;
        }
        asm volatile("" : "+v"(pb), "+v"(l), "+v"(acc[0]), "+v"(acc[1]), "+v"(acc[2]), "+v"(acc[3]), "+v"(acc[4]), "+v"(acc[5]), "+v"(acc[6]), "+v"(acc[7]),
                     "+v"(va[0]), "+v"(va[1]), "+v"(va[2]), "+v"(va[3]), "+v"(va[4]), "+v"(va[5]), "+v"(va[6]), "+v"(va[7]));
        lds_wait_ge<0>(A_LANDV, (unsigned)(j + 1));
        v2u vf[8];
#pragma unroll
        for (int t = 0; t < 8; ++t) vf[t] = trd(va[t]);
        lgk0();
        if (lane == 0) *(volatile LAS unsigned*)(A_DONE + slot * 4) = (unsigned)(j + 1);
#pragma unroll
        for (int t = 0; t < 8; ++t) acc[t] = mfma16(vf[t], pb, acc[t]);
        slot += NCONS; if (slot >= NSLOT) slot -= NSLOT;
      }
      STAMP(4);
      l = x32sum(x16sum(l));
      if (c16 < 4) {
#pragma unroll
        for (int t = 0; t < 8; ++t) sto128(A_ST + (wave - 1) * 2048 + c16 * 512 + ((t ^ c16) * 64) + g * 16, __builtin_bit_cast(v4u, acc[t]));
        if (g == 0) sto64(A_ML + (c16 * 8 + wave - 1) * 8, (v2u){asu(m * SC), asu(l)});
      }
    }
    bar();
    if (wave >= 3 && wave <= 6) {
      const int hq = wave - 3;
      v2u ml[NCONS];
      f2 ow[NCONS];
      const unsigned sa = opq(A_ST + hq * 512 + ((lane * 8) ^ (hq << 6))), ma = opq(A_ML + hq * 64);
#pragma unroll
      for (int c = 0; c < NCONS; ++c) { ml[c] = *(const LAS v2u*)(ma + c * 8); ow[c] = *(const LAS f2*)(sa + c * 2048); }
      float M = asf(ml[0].x);
#pragma unroll
      for (int c = 1; c < NCONS; ++c) M = fmaxf(M, asf(ml[c].x));
      float L = 0.f, o0 = 0.f, o1 = 0.f;
#pragma unroll
      for (int c = 0; c < NCONS; ++c) { const float wt = ex2(asf(ml[c].x) - M); L = fmaf(wt, asf(ml[c].y), L); o0 = fmaf(wt, ow[c].x, o0); o1 = fmaf(wt, ow[c].y, o1); }
      const int rec = (b * 32 + kvh * 4 + hq) * NSPLIT_A + split;
      gst128(rs, WS_PAO + rec * 1024 + lane * 16, (v4u){asu(o0), tag, asu(o1), tag});
      if (lane < 8) gst128(rs, WS_PAM + rec * 128 + lane * 16, (v4u){asu(M), tag, asu(L), tag});
    }
    STAMP(6);
    if (ty < 2) {
      const int oh = iA >> 2, odq = iA & 3;
      if (wave == 0 || wave == 7) { vmwait<0>(); if (lane == 0) lds_inc(A_OUL); }
      lds_wait_ge(A_OUL, 2u);
      v4u wf[4];
      out_frags(SCR, wave, lane, wf);
      const int tt = (c16 >> 1) & 3, pr = c16 >> 3, n = 64 * wave + 16 * tt + 4 * g + 2 * pr;
      unsigned fo = WS_F + ((((n >> 3) * 128 + iA) * 16) + (c16 & 1) * 8 + (n & 7)) * 8, oa = A_OIMG + (c16 & 1) * 64 + g * 16;
      asm volatile("" : "+v"(fo), "+v"(oa));
      if (wave == 1) {
        const int rec = ((lane >> 5) * 32 + oh) * NSPLIT_A + (g & 1) * 5;
        v4u v[5], ml[5];
        _Pragma("nounroll") for (int it = 0; it < SPIN_CAP; ++it) {
          cfence();
          bool ok = true;
#pragma unroll
          for (int i = 0; i < 5; ++i) {
            v[i] = gld128(rs, WS_PAO + (rec + i) * 1024 + (32 * odq + 2 * c16) * 8);
            ml[i] = gld128(rs, WS_PAM + (rec + i) * 128 + odq * 16);
            ok &= tagok(v[i], tag) && tagok(ml[i], tag);
          }
          if (__all(ok)) break;
          __builtin_amdgcn_s_sleep(GSL);
        }
        float M = asf(ml[0].x);
#pragma unroll
        for (int i = 1; i < 5; ++i) M = fmaxf(M, asf(ml[i].x));
        M = x16max(M);
        float L = 0.f, o0 = 0.f, o1 = 0.f;
#pragma unroll
        for (int i = 0; i < 5; ++i) { const float wt = ex2(asf(ml[i].x) - M); L = fmaf(wt, asf(ml[i].z), L); o0 = fmaf(wt, asf(v[i].x), o0); o1 = fmaf(wt, asf(v[i].z), o1); }
        const float inv = 1.f / x16sum(L);
        o0 = x16sum(o0); o1 = x16sum(o1);
        if (!(g & 1)) sto32(A_OIMG + (lane >> 5) * 64 + c16 * 4, pk(o0 * inv, o1 * inv));
      }
      STAMP(7);
      bar();
      const v4u ob = lds128(oa);
      f4 d[4];
#pragma unroll
      for (int t = 0; t < 4; ++t) d[t] = mfma32(wf[t], ob, (f4){0.f, 0.f, 0.f, 0.f});
      const f4 e = tt == 0 ? d[0] : tt == 1 ? d[1] : tt == 2 ? d[2] : d[3];
      gst128(rs, fo, (v4u){asu(pr ? e[2] : e[0]), tag, asu(pr ? e[3] : e[1]), tag});
      STAMP(8);
    }
    STAMP_OUT();
  } else if (isB) {
    const int b = bid >> 5, sp = bid & 31, w = wave, lane_ = lane;
    const unsigned vl = lane << 4;
#define LD_S(S, rho, u) asm volatile("global_load_dwordx4 %0, %1, %2 offset:%3 nt" : "=v"(S[u]) : "v"(vl), "s"(cp + (size_t)(rho) * 65536 + ((u) >> 2) * 4096), "n"(((u) & 3) * 1024) : "memory")
#define ST_S(S, rho) do { unsigned a0_ = ((rho) & 1) * 65536 + w * 8192 + vc0, a1_ = ((rho) & 1) * 65536 + w * 8192 + vc1; asm volatile("" : "+v"(a0_), "+v"(a1_)); \
      _Pragma("unroll") for (int u_ = 0; u_ < 8; ++u_) sto128(u_ * 1024 + (((u_ >> 2) ? a1_ : a0_) ^ ((u_ & 3) << 6)), S[u_]); } while (0)
#define PB() vmwait<WB - 2>()
    v4u S2[8], S3[8];
    GET_TAG();
    PB(); LD_C(0, 6); LD_C(0, 7);
    STAMP(1);
    PB(); LD_C(1, 0); LD_C(1, 1);
    PB(); LD_C(1, 2); LD_C(1, 3);
    PB(); LD_C(1, 4); LD_C(1, 5);
    PB(); LD_C(1, 6); LD_C(1, 7);
    STAMP(2);
    PB(); LD_S(S2, 2, 0); LD_S(S2, 2, 1);
    PB(); LD_S(S2, 2, 2); LD_S(S2, 2, 3);
    PB(); LD_S(S2, 2, 4); LD_S(S2, 2, 5);
    PB(); LD_S(S2, 2, 6); LD_S(S2, 2, 7);
#pragma unroll
    for (int u = 0; u < NS3; u += 2) { PB(); LD_S(S3, 3, u); LD_S(S3, 3, u + 1); }
    STAMP(3);
    if (w == 0) {
      _Pragma("nounroll") for (int it = 0; it < SPIN_CAP; ++it) { cfence(); if (__all(gld32(rs, WS_FQ + lane * FS) == tag)) break; __builtin_amdgcn_s_sleep(PSL); }
    }
    bar();
    STAMP(4);
    {
      v4u t[4];
      const char* qa = ws + WS_QAB + (b * 32 + 4 * w) * 1024;
#pragma unroll
      for (int i = 0; i < 4; ++i) asm volatile("global_load_dwordx4 %0, %1, %2 offset:%3 sc1" : "=v"(t[i]) : "v"(vl), "s"(qa), "n"(i * 1024) : "memory");
      constexpr int NA = NS3 < 8 ? 2 : 0, NC = 8 - NS3 - NA < 4 ? 8 - NS3 - NA : 4;
#pragma unroll
      for (int u = NS3; u < NS3 + NA; ++u) LD_S(S3, 3, u);
      asm volatile("s_waitcnt vmcnt(%12)" : "+v"(t[0]), "+v"(t[1]), "+v"(t[2]), "+v"(t[3]), "+v"(S2[0]), "+v"(S2[1]), "+v"(S2[2]), "+v"(S2[3]),
                   "+v"(S2[4]), "+v"(S2[5]), "+v"(S2[6]), "+v"(S2[7]) : "n"(NA) : "memory");
#pragma unroll
      for (int u = NS3 + NA; u < NS3 + NA + NC; ++u) LD_S(S3, 3, u);
#pragma unroll
      for (int i = 0; i < 4; ++i) sto128(SCR + (4 * w + i) * 1024 + ((lane ^ ((4 * w + i) & 15)) << 4), t[i]);
    }
    bar();
    const int kb4 = w & 3, hg = w >> 2;
    v4u qf[16];
#pragma unroll
    for (int s = 0; s < 16; ++s) qf[s] = lds128(SCR + (16 * hg + c16) * 1024 + ((((4 * s) & 15) ^ g ^ c16) << 4) + (s >> 2) * 256);
    STAMP(5);
    const unsigned fk = ((c16 & 3) << 2) | ((0x78u >> (2 * (c16 >> 2))) & 3u);
    const unsigned ka = (16 * kb4 + c16) * 1024 + ((g ^ fk) << 4);
    const int h2 = lane >> 5, blk = g & 1, qq = c16 >> 2, pp = lane & 3;
    const unsigned ft0 = (qq << 2) | (h2 ? 3u : 0u), ft1 = (qq << 2) | (h2 ? 1u : 2u);
    const unsigned cbase = 8 * w + 2 * blk + (pp >> 1);
    const unsigned va00 = (8 * h2 + qq) * 1024 + ((cbase ^ ft0) << 4) + (pp & 1) * 8, va01 = (8 * h2 + 4 + qq) * 1024 + ((cbase ^ ft1) << 4) + (pp & 1) * 8;
    const unsigned mxa = opq(B_MX + (16 * hg + c16) * 4), pwa = opq(B_P + kb4 * 1536 + (16 * hg + c16) * 48 + g * 8);
    const unsigned pra = opq(B_P + (lane & 31) * 48 + h2 * 16), ala = opq(B_AL + (lane & 31) * 4);
    float m = -INFINITY, l = 0.f;
    f16v acc0, acc1;
#pragma unroll
    for (int i = 0; i < 16; ++i) { acc0[i] = 0.f; acc1[i] = 0.f; }
#pragma unroll
    for (int rho = 0; rho < 2; ++rho) {
      unsigned kr = ka, vr0 = va00, vr1 = va01;
      asm volatile("" : "+v"(kr), "+v"(vr0), "+v"(vr1));
      bar();
      f4 sa = {0.f, 0.f, 0.f, 0.f}, sb = sa;
      {
        v4u kf[8];
#pragma unroll
        for (int s = 0; s < 8; ++s) kf[s] = lds128((kr ^ ((s & 3) << 6)) + (s >> 2) * 256);
        __builtin_amdgcn_sched_barrier(0);
#pragma unroll
        for (int s = 0; s < 8; ++s) {
          sa = mfma32(kf[s], qf[s], sa);
          kf[s] = lds128((kr ^ ((s & 3) << 6)) + (2 + (s >> 2)) * 256);
          __builtin_amdgcn_sched_barrier(0);
        }
        kr += 65536;
#pragma unroll
        for (int s = 0; s < 8; ++s) {
          sa = mfma32(kf[s], qf[8 + s], sa);
          kf[s] = lds128((kr ^ ((s & 3) << 6)) + (s >> 2) * 256);
          __builtin_amdgcn_sched_barrier(0);
        }
#pragma unroll
        for (int s = 0; s < 8; ++s) {
          sb = mfma32(kf[s], qf[s], sb);
          kf[s] = lds128((kr ^ ((s & 3) << 6)) + (2 + (s >> 2)) * 256);
          __builtin_amdgcn_sched_barrier(0);
        }
#pragma unroll
        for (int s = 0; s < 8; ++s) sb = mfma32(kf[s], qf[8 + s], sb);
      }
      if (rho == 0 && NS3 == 0) { LD_S(S3, 3, 6); LD_S(S3, 3, 7); }
      float mx = fmaxf(fmaxf(fmaxf(sa[0], sa[1]), fmaxf(sa[2], sa[3])), fmaxf(fmaxf(sb[0], sb[1]), fmaxf(sb[2], sb[3])));
      mx = x32max(x16max(mx));
      if (g == 0) stof(mxa + kb4 * 128, mx);
      v4u c0[4], c1[4];
#pragma unroll
      for (int ks = 0; ks < 4; ++ks) {
        c0[ks] = trd2(ks * 16384 + vr0, ks * 16384 + vr1);
        c1[ks] = trd2(ks * 16384 + (vr0 ^ 64), ks * 16384 + (vr1 ^ 64));
      }
      if (rho == 0) STAMP(6); else STAMP(8);
      bar();
      const float mn = fmaxf(fmaxf(m, fmaxf(ldsf(mxa), ldsf(mxa + 128))), fmaxf(ldsf(mxa + 256), ldsf(mxa + 384)));
      const float al = ex2((m - mn) * SC), mc = -mn * SC;
      float p[8];
#pragma unroll
      for (int i = 0; i < 4; ++i) { p[i] = ex2(fmaf(sa[i], SC, mc)); p[4 + i] = ex2(fmaf(sb[i], SC, mc)); }
      l = fmaf(l, al, ((p[0] + p[1]) + (p[2] + p[3])) + ((p[4] + p[5]) + (p[6] + p[7])));
      m = mn;
      sto64(pwa, (v2u){pk(p[0], p[1]), pk(p[2], p[3])});
      sto64(pwa + 6144, (v2u){pk(p[4], p[5]), pk(p[6], p[7])});
      if (kb4 == 0 && g == 0) stof(mxa + (B_AL - B_MX), al);
      if (rho == 1) { l = x32sum(x16sum(l)); if (g == 0) stof(mxa + (B_LS - B_MX) + kb4 * 128, l); }
      bar();
      if (rho == 1 && kb4 == 0 && g == 0) {
        const unsigned la = mxa + (B_LS - B_MX);
        sto64(B_FM + (16 * hg + c16) * 8, (v2u){asu(m * SC), asu((ldsf(la) + ldsf(la + 128)) + (ldsf(la + 256) + ldsf(la + 384)))});
      }
      const float alp = rho ? ldsf(ala) : 0.f;
      v4u pf[4];
#pragma unroll
      for (int ks = 0; ks < 4; ++ks) pf[ks] = lds128(pra + ks * 1536);
      __builtin_amdgcn_sched_barrier(0);
      if (rho) { acc0 *= alp; acc1 *= alp; }
      vr0 += 65536; vr1 += 65536;
#pragma unroll
      for (int ks = 0; ks < 4; ++ks) {
        acc0 = mfmaL(c0[ks], pf[ks], acc0); acc1 = mfmaL(c1[ks], pf[ks], acc1);
        c0[ks] = trd2(ks * 16384 + vr0, ks * 16384 + vr1);
        c1[ks] = trd2(ks * 16384 + (vr0 ^ 64), ks * 16384 + (vr1 ^ 64));
        pf[ks] = lds128(pra + 6144 + ks * 1536);
        __builtin_amdgcn_sched_barrier(0);
      }
      bar();
      if (rho == 0) {
        ST_S(S2, 0);
        vmwait<0>();
        ST_S(S3, 1);
      }
#pragma unroll
      for (int ks = 0; ks < 4; ++ks) { acc0 = mfmaL(c0[ks], pf[ks], acc0); acc1 = mfmaL(c1[ks], pf[ks], acc1); }
      if (rho == 0) STAMP(7); else STAMP(9);
    }
    {
    const int lane = (int)opq((unsigned)lane_), h2 = lane >> 5, tid = wave * 64 + lane;
    {
      const unsigned tb = B_T + w * 4096, n32 = lane & 31;
#pragma unroll
      for (int q4 = 0; q4 < 4; ++q4) {
        sto64(tb + n32 * 128 + ((q4 ^ (n32 & 7)) << 4) + h2 * 8, (v2u){pk(acc0[4 * q4], acc0[4 * q4 + 1]), pk(acc0[4 * q4 + 2], acc0[4 * q4 + 3])});
        sto64(tb + n32 * 128 + (((4 + q4) ^ (n32 & 7)) << 4) + h2 * 8, (v2u){pk(acc1[4 * q4], acc1[4 * q4 + 1]), pk(acc1[4 * q4 + 2], acc1[4 * q4 + 3])});
      }
      lgk0();
      const int hr = lane >> 3, ck = lane & 7;
#pragma unroll
      for (int i = 0; i < 4; ++i)
        gst128(rs, WS_PB + (((8 * i + hr) * 8 + w) * 64 + bid) * 128 + ck * 16, lds128(tb + (8 * i + hr) * 128 + ((ck ^ hr) << 4)));
    }
    vmwait<0>();
    bar();
    if (w == 0 && lane < 32) { const v2u fm = *(const LAS v2u*)(B_FM + lane * 8); gst128(rs, WS_PBM + bid * 512 + lane * 16, (v4u){fm.x, tag, fm.y, tag}); }
    STAMP(10);
    if (w == 0) {
      const unsigned off = SKIPA ? WS_F + (64 + bid) * 16384 + (bid & 31) * 128 : PRE2 ? WS_F + bid * 16384 + (2 * bid) * 128 : WS_PAM + (bid * NSPLIT_A + (bid % NSPLIT_A)) * 128;
      _Pragma("nounroll") for (int it = 0; it < SPIN_CAP; ++it) { cfence(); if (__all(gld64(rs, off).y == tag)) break; __builtin_amdgcn_s_sleep(PRESL); }
    }
    bar();
    STAMP(14);
    v4u f[3];
    _Pragma("nounroll") for (int it = 0; it < SPIN_CAP; ++it) {
      cfence();
      bool ok = true;
#pragma unroll
      for (int k = 0; k < 3; ++k) { f[k] = gld128(rs, (k < 2 ? SKIPA : SKIPB) ? 0xfffffff0u : WS_F + ((k >> 1) * 64 + bid) * 16384 + (k < 2 ? k * 8192 + tid * 16 : (tid & 255) * 16)); ok &= (k < 2 ? SKIPA : SKIPB) || tagok(f[k], tag); }
      if (__all(ok)) break;
      __builtin_amdgcn_s_sleep(FSL);
    }
    float r[4] = {asf(f[0].x) + asf(f[1].x), asf(f[0].z) + asf(f[1].z), w < 4 ? asf(f[2].x) : 0.f, w < 4 ? asf(f[2].z) : 0.f};
#pragma unroll
    for (int k = 0; k < 4; ++k) { r[k] += dpp<ROR8>(r[k]); r[k] = x32sum(x16sum(r[k])); }
    if (lane < 8) { sto64(B_FIN + w * 128 + lane * 8, (v2u){asu(r[0]), asu(r[1])}); sto64(B_FIN + w * 128 + 64 + lane * 8, (v2u){asu(r[2]), asu(r[3])}); }
    bar();
    if (tid < 16) {
      float s0 = 0.f, s1 = 0.f;
#pragma unroll
      for (int i = 0; i < 8; ++i) { const f2 t = *(const LAS f2*)(B_FIN + i * 128 + tid * 8); s0 += t.x; s1 += t.y; }
      const int mat = tid >> 3, ob = (tid >> 2) & 1, pr = tid & 3;
      *(GAS unsigned*)((GAS char*)out + (ob * 1024 + mat * 512 + 8 * bid + 2 * pr) * 2) = pk(s0, s1);
      if (!SKIPA && !SKIPB && bid == 0 && tid == 0) gst32(rs, WS_E, tag);
    }
    STAMP(15);
    STAMP_OUT();
    }
  } else {
    const int h = bid - NB_CU, w = wave;
    GET_TAG();
    if (w == 0) gather_qb(T_QBI, h, lane, rs, tag);
    vmwait<0>();
    STAMP(1);
    {
      const unsigned a0 = w * 8192 + c16 * 256 + ((g ^ c16) << 4);
      v4u uf[8];
#pragma unroll
      for (int i = 0; i < 8; ++i) uf[i] = lds128((a0 ^ ((i & 3) << 6)) + (i >> 2) * 4096);
      bar();
      STAMP(2);
      v4u qb[4];
#pragma unroll
      for (int s = 0; s < 4; ++s) qb[s] = lds128(T_QBI + (c16 & 1) * 256 + s * 64 + g * 16);
      __builtin_amdgcn_sched_barrier(0);
      f4 d0 = {0.f, 0.f, 0.f, 0.f}, d1 = d0;
#pragma unroll
      for (int s = 0; s < 4; ++s) { d0 = mfma32(uf[s], qb[s], d0); d1 = mfma32(uf[4 + s], qb[s], d1); }
      const int rb = (c16 >> 1) & 1;
      const f4 e = rb ? d1 : d0;
      if (c16 < 4) sto64(T_QAS + (c16 & 1) * 512 + (32 * w + 16 * rb + 4 * g) * 2, (v2u){pk(e[0], e[1]), pk(e[2], e[3])});
      bar();
      if (w == 0) {
        gst128(rs, WS_QAB + ((lane >> 5) * 32 + h) * 1024 + (lane & 31) * 16, lds128(T_QAS + lane * 16));
        vmwait<0>();
        flag_line(rs, WS_FQ + h * FS, lane, tag);
      }
    }
    bar();
    STAMP(3);
    v4u OU[16];
    {
      const char* uv = Wuv + (size_t)(64 * w) * 8192 + h * 256;
      const unsigned vuv = g * 8192 + ((c16 ^ (g << 1)) << 4);
#pragma unroll
      for (int u = 0; u < 16; ++u) { if (u >= WT && !(u & 1)) vmwait<WT - 2>(); dma<0>(uv + (size_t)u * 32768, vuv ^ (((u >> 1) & 1) << 7), w * 16384 + u * 1024); }
      const char* ow = Bwo + (size_t)(h * 128 + 16 * w) * 1024;
      const unsigned vo = g * 4096 + c16 * 16;
#pragma unroll
      for (int u = 0; u < 16; ++u) {
        if (!(u & 1)) vmwait<WT - 2>();
        asm volatile("global_load_dwordx4 %0, %1, %2 offset:%3 nt" : "=v"(OU[u]) : "v"(vo), "s"(ow), "n"((u >> 2) * 1024 + (u & 3) * 256) : "memory");
      }
    }
    asm volatile("s_waitcnt vmcnt(0)" : "+v"(OU[0]), "+v"(OU[1]), "+v"(OU[2]), "+v"(OU[3]), "+v"(OU[4]), "+v"(OU[5]), "+v"(OU[6]), "+v"(OU[7]),
                 "+v"(OU[8]), "+v"(OU[9]), "+v"(OU[10]), "+v"(OU[11]), "+v"(OU[12]), "+v"(OU[13]), "+v"(OU[14]), "+v"(OU[15]) :: "memory");
    if (tid == 0) sto32(T_RDY, 0u);
    bar();
    STAMP(4);
    v4u wf[16];
    {
      const int q = c16 >> 2, p = lane & 3, f3 = q | ((g & 1) << 2);
      const unsigned a0 = (8 * g + q) * 256 + (((2 * w + (p >> 1)) ^ (f3 << 1)) << 4) + (p & 1) * 8;
      const unsigned aa[2] = {opq(a0), opq(a0 + 65536)};
#pragma unroll
      for (int s = 0; s < 16; ++s) wf[s] = trd2(aa[s >> 3] + (s & 7) * 8192, aa[s >> 3] + (s & 7) * 8192 + 1024);
    }
    STAMP(5);
    {
      const int sub = lane >> 3, ck = lane & 7;
      v4u o[8];
#pragma unroll
      for (int i = 0; i < 8; ++i) { o[i] = (v4u){0u, 0u, 0u, 0u}; asm volatile("" : "+v"(o[i])); }
      const char* pb = ws + WS_PB + (h * 8 + w) * 8192;
      const unsigned vl = lane << 4;
      unsigned done = 0;
      v4u ml = {0u, ~tag, 0u, ~tag};
      if (w == 0) {
        _Pragma("nounroll") for (int it = 0; it < SPIN_CAP; ++it) { cfence(); if (__all(gld32(rs, WS_PBM + 2 * h * 512 + h * 16 + 4) == tag)) break; __builtin_amdgcn_s_sleep(PSL1); }
      }
      _Pragma("nounroll") for (int it = 0; it < SPIN_CAP * 8 && done != 255u; ++it) {
        unsigned rdy;
        if (w == 0) {
          cfence();
          const v4u t = gld128(rs, tagok(ml, tag) ? 0xfffffff0u : WS_PBM + lane * 512 + h * 16);
          if (!tagok(ml, tag)) ml = t;
          unsigned long long m = __ballot(tagok(ml, tag));
          m &= m >> 4; m &= m >> 2; m &= m >> 1; m &= 0x0101010101010101ull;
          rdy = (unsigned)((m * 0x0102040810204080ull) >> 56);
          if (lane == 0) *(volatile LAS unsigned*)T_RDY = rdy;
        } else {
          rdy = lds_peek(T_RDY) & 255u;
          if (rdy == done) { __builtin_amdgcn_s_sleep(1); continue; }
        }
        const unsigned add = rdy & ~done;
        done = rdy;
#pragma unroll
        for (int i = 0; i < 8; ++i)
          if (add & (1u << i)) asm volatile("global_load_dwordx4 %0, %1, %2 offset:%3 sc1" : "+v"(o[i]) : "v"(vl), "s"(pb + (i >> 2) * 4096), "n"((i & 3) * 1024) : "memory");
      }
      if (w == 0) {
        const float M = x16max(row16max(asf(ml.x)));
        const float wt = ex2(asf(ml.x) - M);
        stof(T_WN + lane * 4, wt / x16sum(row16sum(wt * asf(ml.z))));
        lgk0();
        if (lane == 0) *(volatile LAS unsigned*)T_RDY = 511u;
      } else {
        lds_wait_ge<0>(T_RDY, 511u);
      }
      float wi[8];
#pragma unroll
      for (int i = 0; i < 8; ++i) wi[i] = ldsf(T_WN + (8 * i + sub) * 4);
      asm volatile("s_waitcnt vmcnt(0)" : "+v"(o[0]), "+v"(o[1]), "+v"(o[2]), "+v"(o[3]), "+v"(o[4]), "+v"(o[5]), "+v"(o[6]), "+v"(o[7]) :: "memory");
      STAMP(8);
      float a[16];
#pragma unroll
      for (int k = 0; k < 16; ++k) a[k] = 0.f;
#pragma unroll
      for (int i = 0; i < 8; ++i)
#pragma unroll
        for (int k = 0; k < 4; ++k) {
          a[(i >> 2) * 8 + 2 * k] = fmaf(wi[i], asf(o[i][k] << 16), a[(i >> 2) * 8 + 2 * k]);
          a[(i >> 2) * 8 + 2 * k + 1] = fmaf(wi[i], asf(o[i][k] & 0xffff0000u), a[(i >> 2) * 8 + 2 * k + 1]);
        }
      float r[8];
#pragma unroll
      for (int k = 0; k < 8; ++k) {
        auto sw = __builtin_amdgcn_permlane32_swap(asu(a[k]), asu(a[8 + k]), false, false);
        r[k] = x16sum(asf(sw[0]) + asf(sw[1]));
        r[k] += dpp<ROR8>(r[k]);
      }
      if ((lane & 24) == 0) sto128(T_OL + (lane >> 5) * 1024 + w * 128 + ck * 16, (v4u){pk(r[0], r[1]), pk(r[2], r[3]), pk(r[4], r[5]), pk(r[6], r[7])});
    }
    bar();
    STAMP(6);
    {
      v4u of[16];
      const unsigned oa = opq(T_OL + (c16 >> 3) * 1024 + g * 16);
#pragma unroll
      for (int s = 0; s < 16; ++s) of[s] = lds128(oa + s * 64);
      __builtin_amdgcn_sched_barrier(0);
      f4 d0 = {0.f, 0.f, 0.f, 0.f}, d1 = d0;
#pragma unroll
      for (int s = 0; s < 8; ++s) { d0 = mfma32(wf[2 * s], of[2 * s], d0); d1 = mfma32(wf[2 * s + 1], of[2 * s + 1], d1); }
      d0 += d1;
      const unsigned half = (c16 & 1) ? 0xffff0000u : 0x0000ffffu;
      const int rr = (c16 >> 1) & 3;
      f4 o4[4];
#pragma unroll
      for (int cb = 0; cb < 4; ++cb) o4[cb] = (f4){0.f, 0.f, 0.f, 0.f};
#pragma unroll
      for (int u = 0; u < 4; ++u) {
        const unsigned x = pk(d0[u], d0[u]) & half;
        const v4u bo = {rr == 0 ? x : 0u, rr == 1 ? x : 0u, rr == 2 ? x : 0u, rr == 3 ? x : 0u};
#pragma unroll
        for (int cb = 0; cb < 4; ++cb) o4[cb] = mfma32(OU[4 * u + cb], bo, o4[cb]);
      }
#pragma unroll
      for (int cb = 0; cb < 4; ++cb)
#pragma unroll
        for (int r = 0; r < 4; ++r) stof(T_RED + w * 4096 + cb * 1024 + r * 64 + g * 256 + c16 * 4, o4[cb][r]);
    }
    bar();
    {
      const int j = tid >> 3, pr = tid & 7;
      float s0 = 0.f, s1 = 0.f;
#pragma unroll
      for (int i = 0; i < 8; ++i) { const f2 t = *(const LAS f2*)(T_RED + i * 4096 + tid * 8); s0 += t.x; s1 += t.y; }
      gst128(rs, WS_F + (((64 + j) * 128 + h) * 16 + 2 * pr) * 8, (v4u){asu(s0), tag, asu(s1), tag});
    }
    STAMP(7);
    STAMP_OUT();
  }
}

extern "C" void kernel_launch(void* const* d_in, const int* in_sizes, int n_in, void* d_out, int out_size, void* d_ws, size_t ws_size,
                              hipStream_t stream) {
  (void)in_sizes; (void)n_in; (void)out_size; (void)ws_size;
  (void)hipFuncSetAttribute((const void*)fused_decode, hipFuncAttributeMaxDynamicSharedMemorySize, LDS_BYTES);
  fused_decode<<<256, 512, LDS_BYTES, stream>>>(
      (const char*)d_in[0], (const char*)d_in[1], (const char*)d_in[2], (const char*)d_in[3], (const char*)d_in[4], (const char*)d_in[5],
      (const char*)d_in[6], (const char*)d_in[7], (const char*)d_in[8], (const char*)d_in[9], (const char*)d_in[10], (unsigned short*)d_out,
      (char*)d_ws);
}
```

```cpp
#include <hip/hip_runtime.h>
#include <stdint.h>
#pragma clang diagnostic ignored "-Wint-to-pointer-cast"

#define GAS __attribute__((address_space(1)))
#define LAS __attribute__((address_space(3)))
typedef unsigned v4u __attribute__((ext_vector_type(4)));
typedef unsigned v2u __attribute__((ext_vector_type(2)));
typedef float f4 __attribute__((ext_vector_type(4)));
typedef float f2 __attribute__((ext_vector_type(2)));
typedef float f16v __attribute__((ext_vector_type(16)));
typedef short s4 __attribute__((ext_vector_type(4)));
typedef __bf16 b8 __attribute__((ext_vector_type(8)));
typedef __bf16 b2 __attribute__((ext_vector_type(2)));

#define W1 12
#define W2 24
#define J1 18
#define ROT 13
#define BROT 3
#define GMASK 0
#define FMASK 0
#define VDL 0
#define TE 6
#define UE 3
#define PREN 32
#define G2 0
#define F2 0
#define EXCL 0
#define VTAIL 0
#define ODL 5
#define BPRIO 0
#define TLBW 1
#define TLBB 0
#define TLBE 0
#define TLBN 2
#define TLBS 2097152
#define DLY2 40
#define DLYP 30
#define DLYB 10
#define NB0 55
#define NB1 52
#define WB 4
#define PRE2 2
#define SKIPA 0
#define SKIPB 0
#define WT 6
#define CODE_KB 25
#define QGO 4

namespace {

constexpr int NB_CU = 64, NT_CU = 32, NSPLIT_A = 10, NSLOT = 14, NCONS = 6;
constexpr float SC = 0.08838834764831845f * 1.4426950408889634f;
constexpr int SPIN_CAP = 1 << 18;

#define NS3 6
#define PSL 4
#define PSL1 8
#define PRESL 8
#define GSL 1
#define FSL 0
constexpr unsigned FS = 128;
constexpr unsigned WS_E = 0;
constexpr unsigned WS_FQ = 4096;
constexpr unsigned WS_FP = WS_FQ + 16384;
constexpr unsigned WS_QG = 65536;
constexpr unsigned WS_QAB = WS_QG + 262144;
constexpr unsigned WS_PBM = WS_QAB + 65536;
constexpr unsigned WS_PAM = WS_PBM + 32768;
constexpr unsigned WS_PAO = WS_PAM + 131072;
constexpr unsigned WS_PB = WS_PAO + 1048576;
constexpr unsigned WS_F = WS_PB + 2097152;
constexpr unsigned WS_END = WS_F + 2097152;

constexpr unsigned LDS_BYTES = 163840;
constexpr unsigned SCR = 131072;
constexpr unsigned A_MISC = NSLOT * 8192;
constexpr unsigned A_LANDK = A_MISC, A_LANDV = A_MISC + 4, A_QUSED = A_MISC + 8, A_QCNT = A_MISC + 12, A_QB = A_MISC + 16, A_QRDY = A_MISC + 20, A_OUL = A_MISC + 24, A_OUL2 = A_MISC + 28;
constexpr unsigned A_DONE = A_MISC + 64;
constexpr unsigned A_QU2 = 10 * 8192;
constexpr unsigned A_QIMG = A_MISC + 256;
constexpr unsigned A_ML = A_MISC + 1280;
constexpr unsigned A_OIMG = A_MISC + 1792;
constexpr unsigned A_ST = A_MISC + 2048;
constexpr unsigned A_XIMG = A_MISC + 4096, A_DUMMY = A_MISC + 5120, A_QBI = A_MISC + 6144, A_QAS = A_MISC + 6656;
constexpr unsigned SLOT0 = 0, SLOT1 = 65536;
constexpr unsigned B_MX = SCR, B_AL = SCR + 512, B_LS = SCR + 640, B_P = SCR + 2048;
constexpr unsigned B_FM = SCR + 1280, B_FIN = SCR + 16384, B_T = SLOT1, B_DUMMY = SCR + 8192;
constexpr unsigned T_QBI = SCR, T_QAS = SCR + 1024, T_OL = SCR + 4096, T_RDY = SCR + 6400, T_WN = SCR + 6656, T_RED = 0;

__device__ __forceinline__ v4u lds128(unsigned a) { return *(const LAS v4u*)a; }
__device__ __forceinline__ void sto128(unsigned a, v4u v) { *(LAS v4u*)a = v; }
__device__ __forceinline__ void sto64(unsigned a, v2u v) { *(LAS v2u*)a = v; }
__device__ __forceinline__ void sto32(unsigned a, unsigned v) { *(LAS unsigned*)a = v; }
__device__ __forceinline__ float ldsf(unsigned a) { return *(const LAS float*)a; }
__device__ __forceinline__ void stof(unsigned a, float v) { *(LAS float*)a = v; }
__device__ __forceinline__ v2u trd(unsigned a) { return __builtin_bit_cast(v2u, __builtin_amdgcn_ds_read_tr16_b64_v4i16((LAS s4*)a)); }
__device__ __forceinline__ v4u trd2(unsigned a0, unsigned a1) { const v2u x = trd(a0), y = trd(a1); return (v4u){x.x, x.y, y.x, y.y}; }
__device__ __forceinline__ f4 mfma32(v4u a, v4u b, f4 c) {
  return __builtin_amdgcn_mfma_f32_16x16x32_bf16(__builtin_bit_cast(b8, a), __builtin_bit_cast(b8, b), c, 0, 0, 0); }
__device__ __forceinline__ f4 mfma16(v2u a, v2u b, f4 c) {
  return __builtin_amdgcn_mfma_f32_16x16x16bf16_1k(__builtin_bit_cast(s4, a), __builtin_bit_cast(s4, b), c, 0, 0, 0); }
__device__ __forceinline__ f16v mfmaL(v4u a, v4u b, f16v c) {
  return __builtin_amdgcn_mfma_f32_32x32x16_bf16(__builtin_bit_cast(b8, a), __builtin_bit_cast(b8, b), c, 0, 0, 0); }
__device__ __forceinline__ unsigned pk(float lo, float hi) { return __builtin_bit_cast(unsigned, __builtin_convertvector((f2){lo, hi}, b2)); }
__device__ __forceinline__ float asf(unsigned u) { return __builtin_bit_cast(float, u); }
__device__ __forceinline__ unsigned asu(float f) { return __builtin_bit_cast(unsigned, f); }
__device__ __forceinline__ float ex2(float x) { return __builtin_amdgcn_exp2f(x); }
template <int CTRL> __device__ __forceinline__ float dpp(float x) {
  return __builtin_bit_cast(float, __builtin_amdgcn_mov_dpp(__builtin_bit_cast(int, x), CTRL, 0xf, 0xf, true)); }
constexpr int XOR1 = 0xB1, XOR2 = 0x4E, ROR4 = 0x124, ROR8 = 0x128;
__device__ __forceinline__ float x16max(float x) { auto s = __builtin_amdgcn_permlane16_swap(asu(x), asu(x), false, false); return fmaxf(asf(s[0]), asf(s[1])); }
__device__ __forceinline__ float x32max(float x) { auto s = __builtin_amdgcn_permlane32_swap(asu(x), asu(x), false, false); return fmaxf(asf(s[0]), asf(s[1])); }
__device__ __forceinline__ float x16sum(float x) { auto s = __builtin_amdgcn_permlane16_swap(asu(x), asu(x), false, false); return asf(s[0]) + asf(s[1]); }
__device__ __forceinline__ float x32sum(float x) { auto s = __builtin_amdgcn_permlane32_swap(asu(x), asu(x), false, false); return asf(s[0]) + asf(s[1]); }
__device__ __forceinline__ float row16max(float x) { x = fmaxf(x, dpp<XOR1>(x)); x = fmaxf(x, dpp<XOR2>(x)); x = fmaxf(x, dpp<ROR4>(x)); return fmaxf(x, dpp<ROR8>(x)); }
__device__ __forceinline__ float row16sum(float x) { x += dpp<XOR1>(x); x += dpp<XOR2>(x); x += dpp<ROR4>(x); return x + dpp<ROR8>(x); }

template <int IMM> __device__ __forceinline__ void dma(const void* sbase, unsigned voff, unsigned lds) {
  asm volatile("s_mov_b32 m0, %2\n\ts_nop 0\n\tglobal_load_lds_dwordx4 %0, %1 offset:%3 nt" :: "v"(voff), "s"(sbase), "s"(lds), "n"(IMM) : "memory");
}
template <int W> __device__ __forceinline__ void dma4(const void* sbase, unsigned v0, unsigned v1, unsigned v2, unsigned v3, unsigned lds) {
  asm volatile("s_mov_b32 m0, %5\n\ts_waitcnt vmcnt(%6)\n\tglobal_load_lds_dwordx4 %0, %4 nt\n\tglobal_load_lds_dwordx4 %1, %4 offset:1024 nt\n\t"
               "s_waitcnt vmcnt(%6)\n\tglobal_load_lds_dwordx4 %2, %4 offset:2048 nt\n\tglobal_load_lds_dwordx4 %3, %4 offset:3072 nt"
               :: "v"(v0), "v"(v1), "v"(v2), "v"(v3), "s"(sbase), "s"(lds), "n"(W - 2) : "memory");
}
__device__ __forceinline__ void dma_keep(const void* sbase, unsigned voff, unsigned lds) {
  asm volatile("s_mov_b32 m0, %2\n\ts_nop 0\n\tglobal_load_lds_dwordx4 %0, %1" :: "v"(voff), "s"(sbase), "s"(lds) : "memory");
}
__device__ __forceinline__ void touch(const void* sbase, unsigned voff, unsigned lds) {
  asm volatile("s_mov_b32 m0, %2\n\ts_nop 0\n\tglobal_load_lds_dword %0, %1 nt" :: "v"(voff), "s"(sbase), "s"(lds) : "memory");
}
template <int N> __device__ __forceinline__ void vmwait() { asm volatile("s_waitcnt vmcnt(%0)" :: "n"(N) : "memory"); }
__device__ __forceinline__ void bar() { asm volatile("s_waitcnt lgkmcnt(0)\n\ts_barrier" ::: "memory"); }
__device__ __forceinline__ void lgk0() { asm volatile("s_waitcnt lgkmcnt(0)" ::: "memory"); }
__device__ __forceinline__ void cfence() { asm volatile("" ::: "memory"); }
__device__ __forceinline__ unsigned opq(unsigned x) { asm volatile("" : "+v"(x)); return x; }
__device__ __forceinline__ unsigned lds_peek(unsigned a) { return __builtin_amdgcn_readfirstlane(*(volatile LAS unsigned*)a); }
template <int SL = 1> __device__ __forceinline__ void lds_wait_ge(unsigned a, unsigned want) {
  _Pragma("nounroll") for (int it = 0; it < SPIN_CAP * 8; ++it) { if (lds_peek(a) >= want) break; if (SL) __builtin_amdgcn_s_sleep(SL); }
  cfence();
}
typedef __amdgpu_buffer_rsrc_t rsrc_t;
constexpr int SC1 = 16;
__device__ __forceinline__ v4u gld128(rsrc_t r, unsigned off) { return __builtin_amdgcn_raw_buffer_load_b128(r, off, 0, SC1); }
__device__ __forceinline__ v2u gld64(rsrc_t r, unsigned off) { return __builtin_amdgcn_raw_buffer_load_b64(r, off, 0, SC1); }
__device__ __forceinline__ unsigned gld32(rsrc_t r, unsigned off) { return __builtin_amdgcn_raw_buffer_load_b32(r, off, 0, SC1); }
__device__ __forceinline__ void gst128(rsrc_t r, unsigned off, v4u v) { __builtin_amdgcn_raw_buffer_store_b128(v, r, off, 0, SC1); }
__device__ __forceinline__ void gst64(rsrc_t r, unsigned off, v2u v) { __builtin_amdgcn_raw_buffer_store_b64(v, r, off, 0, SC1); }
__device__ __forceinline__ void gst32(rsrc_t r, unsigned off, unsigned v) { __builtin_amdgcn_raw_buffer_store_b32(v, r, off, 0, SC1); }
__device__ __forceinline__ void flag_line(rsrc_t r, unsigned off, int lane, unsigned tag) { if (lane < 8) gst128(r, off + lane * 16, (v4u){tag, tag, tag, tag}); }
__device__ __forceinline__ bool tagok(v4u v, unsigned tag) { return v.y == tag && v.w == tag; }
template <int N, int GAP, class F> __device__ __forceinline__ void sweep2(v4u (&v)[N], rsrc_t rs, unsigned tag, F off) {
  v4u a[N], b[N];
#pragma unroll
  for (int k = 0; k < N; ++k) a[k] = gld128(rs, off(k));
  __builtin_amdgcn_s_sleep(GAP);
  _Pragma("nounroll") for (int it = 0; it < SPIN_CAP; ++it) {
    cfence();
#pragma unroll
    for (int k = 0; k < N; ++k) b[k] = gld128(rs, off(k));
    cfence();
    bool ok = true;
#pragma unroll
    for (int k = 0; k < N; ++k) { ok &= tagok(a[k], tag); v[k] = a[k]; }
    if (__all(ok)) break;
#pragma unroll
    for (int k = 0; k < N; ++k) a[k] = gld128(rs, off(k));
    cfence();
    ok = true;
#pragma unroll
    for (int k = 0; k < N; ++k) { ok &= tagok(b[k], tag); v[k] = b[k]; }
    if (__all(ok)) break;
  }
}
template <int N, int SL, bool MASK, class F> __device__ __forceinline__ void sweep(v4u (&v)[N], rsrc_t rs, unsigned tag, F off) {
  if (MASK) {
#pragma unroll
    for (int k = 0; k < N; ++k) v[k] = (v4u){0u, ~tag, 0u, ~tag};
  }
  _Pragma("nounroll") for (int it = 0; it < SPIN_CAP; ++it) {
    cfence();
    v4u t[N];
#pragma unroll
    for (int k = 0; k < N; ++k) t[k] = gld128(rs, MASK && tagok(v[k], tag) ? 0xfffffff0u : off(k));
    bool ok = true;
#pragma unroll
    for (int k = 0; k < N; ++k) { if (!MASK || !tagok(v[k], tag)) v[k] = t[k]; ok &= tagok(v[k], tag); }
    if (__all(ok)) break;
    if (SL) __builtin_amdgcn_s_sleep(SL);
  }
}

#define STAMP(i) do {} while (0)
#define STAMP_DECL do {} while (0)
#define STAMP_OUT() do {} while (0)

__device__ __forceinline__ void gather_qb(unsigned img, int h, int lane, rsrc_t rs, unsigned tag) {
  v4u qg[4];
  const unsigned off = WS_QG + (h * 128 + 2 * lane) * 8;
  _Pragma("nounroll") for (int it = 0; it < SPIN_CAP; ++it) {
    cfence();
    bool ok = true;
#pragma unroll
    for (int k = 0; k < 4; ++k) { qg[k] = gld128(rs, off + (k & 1) * 65536 + (k >> 1) * 32768); ok &= tagok(qg[k], tag); }
    if (__all(ok)) break;
    __builtin_amdgcn_s_sleep(1);
  }
  sto32(img + lane * 4, pk(asf(qg[0].x) + asf(qg[1].x), asf(qg[0].z) + asf(qg[1].z)));
  sto32(img + 256 + lane * 4, pk(asf(qg[2].x) + asf(qg[3].x), asf(qg[2].z) + asf(qg[3].z)));
}
__device__ __forceinline__ void lds_inc(unsigned a) { __hip_atomic_fetch_add((LAS unsigned*)a, 1u, __ATOMIC_RELAXED, __HIP_MEMORY_SCOPE_WORKGROUP); }

__device__ __forceinline__ void out_frags(unsigned ou, int w, int lane, v4u (&wf)[4]) {
  const int g = lane >> 4, q = (lane & 15) >> 2, p = lane & 3, f3 = q | ((g & 1) << 2);
#pragma unroll
  for (int tt = 0; tt < 4; ++tt) {
    const unsigned a0 = ou + (8 * g + q) * 1024 + (((2 * (4 * w + tt) + (p >> 1)) ^ (f3 << 1)) << 4) + (p & 1) * 8;
    wf[tt] = trd2(a0, a0 + 4096);
  }
}

}

#define KALIGN 4096
__global__ void __launch_bounds__(512, 2) __attribute__((aligned(KALIGN))) fused_decode(
    const char* __restrict__ Ax, const char* __restrict__ Kc, const char* __restrict__ Vc, const char* __restrict__ Wq, const char* __restrict__ Wo,
    const char* __restrict__ Bx, const char* __restrict__ Ckv, const char* __restrict__ Bwq, const char* __restrict__ Wuk, const char* __restrict__ Wuv,
    const char* __restrict__ Bwo, unsigned short* __restrict__ out, char* __restrict__ ws) {
  const int bid = blockIdx.x, tid = threadIdx.x, lane = tid & 63, wave = __builtin_amdgcn_readfirstlane(tid >> 6);
  const int g = lane >> 4, c16 = lane & 15;
  STAMP_DECL;
  STAMP(0);
  const int iA = bid - (NB_CU + NT_CU);
  const bool isA = iA >= 0, isB = bid < NB_CU;
  const int ty = iA >> 6;
  int dA = 95 - bid, dU = 223 - bid, dB = bid - 64, dQ = 159 - bid, dW = 2 - wave;
  asm("" : "+s"(dA), "+s"(dU), "+s"(dB), "+s"(dQ), "+s"(dW));
  const int mA = dA >> 31, mU = dU >> 31, mB = dB >> 31, mP = mA & ~mU, sh = mP & 1;
  const int hh = (bid >> sh) & 31;
  const unsigned row0 = (mA & ((((bid | mU) & 1) << 8) + 64 * wave - 64)) | (~mA & (32 * wave));
  const unsigned l0 = (mA & (SCR - 16384 + wave * 16384 - ((dW >> 31) & (SCR + 32768 - A_QU2)))) | (~mA & (wave * 8192));
  const unsigned karg = 0x40 - (mP & (8 + ((dQ >> 31) & 32))), more = mA;
  if ((unsigned)(wave + mA + (mB & 8)) < (unsigned)(8 + (mA & -4))) {
    const unsigned b0 = (row0 + g) * 8192 + hh * 256 + ((c16 ^ (g << sh)) << 4), x1 = ~mP & 64;
    unsigned v0 = b0, v1 = (b0 ^ x1) + 32768, v2 = (b0 ^ 128) + 65536, v3 = (b0 ^ x1 ^ 128) + 98304;
    if (DLYP && karg == 0x18) __builtin_amdgcn_s_sleep(DLYP);
    unsigned long long wp;
#define D4 "global_load_lds_dwordx4 %1, %0 nt\n\ts_add_u32 m0, m0, 0x400\n\tv_add_u32 %1, 0x20000, %1\n\t" \
           "global_load_lds_dwordx4 %2, %0 nt\n\ts_add_u32 m0, m0, 0x400\n\tv_add_u32 %2, 0x20000, %2\n\t" \
           "global_load_lds_dwordx4 %3, %0 nt\n\ts_add_u32 m0, m0, 0x400\n\tv_add_u32 %3, 0x20000, %3\n\t" \
           "global_load_lds_dwordx4 %4, %0 nt\n\ts_add_u32 m0, m0, 0x400\n\tv_add_u32 %4, 0x20000, %4\n\t"
    asm volatile("s_load_dwordx2 %0, %5, %6\n\ts_mov_b32 m0, %7\n\ts_waitcnt lgkmcnt(0)\n\t" D4 D4
                 "s_cmp_eq_u32 %8, 0\n\ts_cbranch_scc1 1f\n\t" D4 D4 "1:"
                 : "=&s"(wp), "+v"(v0), "+v"(v1), "+v"(v2), "+v"(v3)
                 : "s"(__builtin_amdgcn_kernarg_segment_ptr()), "s"(karg), "s"(l0), "s"(more) : "memory", "scc");
#undef D4
  }
  {
    unsigned long long pc;
    asm volatile("s_getpc_b64 %0" : "=s"(pc));
    const int kb = ((bid >> 3) + 4 * wave) & 31;
    if (CODE_KB) dma_keep((const char*)((pc & ~1023ull) + (unsigned)((kb < CODE_KB ? kb : CODE_KB - 1) * 1024)), lane * 16, isA ? A_DUMMY : B_DUMMY);
  }
  const rsrc_t rs = __builtin_amdgcn_make_buffer_rsrc(ws, 0, WS_END, 0x00020000);
  asm volatile("" :: "s"(Ax), "s"(Kc), "s"(Vc), "s"(Wq), "s"(Wo), "s"(Bx), "s"(Ckv), "s"(Bwq), "s"(Wuk), "s"(Wuv), "s"(Bwo), "s"(out), "s"(ws));
  unsigned tag;
#define GET_TAG() do { asm volatile("s_load_dword %0, %1, 0x0\n\ts_waitcnt lgkmcnt(0)" : "=s"(tag) : "s"(ws) : "memory"); tag += 1u; } while (0)
  const char* cp = Ckv + ((size_t)(bid >> 5) * 8192 + 256 * (bid & 31) + 8 * wave) * 1024;
  const int brot = BROT ? (bid >> BROT) : 0;
  const unsigned vc0 = (lane ^ ((wave & 1) ? 3 : 0)) << 4, vc1 = (lane ^ ((wave & 1) ? 1 : 2)) << 4;
#define LD_C(rho, u) dma<((u) & 3) * 1024>(cp + (size_t)((((rho) + brot) & 3) * 65536) + ((u) >> 2) * 4096, (((u) >> 2) ? vc1 : vc0) ^ (((u) & 3) << 6), ((rho) & 1) * 65536 + wave * 8192 + ((u) >> 2) * 4096)
  if (isA) {
    if (wave == 1 && ty < 2) dma<0>((ty ? Ax : Bx) + 512 * (bid & 1), (lane >> 5) * 1024 + (lane & 31) * 16, A_XIMG);
    if (tid < 32) sto32(A_MISC + tid * 4, 0u);
    bar();
  } else if (isB) {
    __builtin_amdgcn_s_sleep(DLYB);
    LD_C(0, 0); LD_C(0, 1); LD_C(0, 2); LD_C(0, 3); LD_C(0, 4); LD_C(0, 5);
  }

  if (isA) {
    const int split = iA >> 4, pair = (iA + split) & 15, b = pair >> 3, kvh = pair & 7;
    constexpr int NB2 = (512 - 4 * NB0 - 4 * NB1) / 2;
    const int nblk = ty == 0 ? NB0 : ty == 1 ? NB1 : NB2, blk0 = (ty == 0 ? 0 : ty == 1 ? 4 * NB0 : 4 * (NB0 + NB1)) + (split & 3) * nblk;
    if (wave == 0 || wave == 7) {
      __builtin_amdgcn_s_setprio(3);
      static_assert(NSPLIT_A == 10 && W1 >= 4 && W1 % 4 == 0 && W2 % 4 == 0 && W1 <= W2 && W2 <= 24 && J1 <= NB0 && J1 <= NB1 && J1 <= NB2, "window");
      const bool isv = wave == 7;
      int pub = 0, j = 0;
      unsigned sl = 0;
      int pos = (pair * ROT) % nblk;
      const char* gp = (isv ? Vc : Kc) + ((size_t)pair * 8192 + (blk0 + pos) * 16) * 256;
      const unsigned v0 = opq(g * 256 + ((c16 ^ (isv ? g << 1 : g)) << 4));
      const unsigned v1 = opq(v0 ^ (isv ? 128 : 64)), v2 = opq(v0 ^ (isv ? 0 : 128)), v3 = opq(v0 ^ (isv ? 128 : 192));
      const unsigned lo = isv ? 4096 : 0, la = opq(lane ? A_MISC + 1536 + lane * 4 : isv ? A_LANDV : A_LANDK);
      unsigned dn = 0;
#define PUBLISH(val) do { pub = max(pub, (val)); *(volatile LAS unsigned*)la = (unsigned)pub; } while (0)
#define GATE(addr, need) do { if (lds_peek(addr) < (need)) { vmwait<0>(); PUBLISH(j); lds_wait_ge(addr, need); } } while (0)
#define PART(W) do { \
        if (j >= NSLOT) { if ((unsigned)__builtin_amdgcn_readfirstlane(dn) < (unsigned)(j - NSLOT + 1)) GATE(A_DONE + sl, (unsigned)(j - NSLOT + 1)); } \
        else if (j == 10) GATE(A_QUSED, 4u); \
        dma4<W>(gp, v0, v1, v2, v3, sl * 2048 + lo); \
        ++j; gp += 4096; if (ROT && ++pos == nblk) gp -= nblk * 4096; sl = sl == 4 * (NSLOT - 1) ? 0 : sl + 4; \
        PUBLISH(j - W / 4); dn = *(volatile LAS unsigned*)(A_DONE + sl); } while (0)
      STAMP(9);
      GET_TAG();
      if (UE && ty == 2) {
        lds_wait_ge(A_OUL2, 1u);
#pragma nounroll
        while (j < UE) PART(W2);
      }
      lds_wait_ge(A_QCNT, 4u);
      if (VDL && isv) lds_wait_ge(A_LANDK, (unsigned)VDL);
      STAMP(10);
#pragma nounroll
      while (j < J1) PART(W1);
      STAMP(11);
#pragma nounroll
      while (j < nblk - (isv ? VTAIL : 0)) PART(W2);
      if (isv) {
#pragma nounroll
        while (j < nblk) PART(44);
      }
      if (ty < 2 && !isv) {
        const char* op = Wo + (size_t)iA * 32768;
        const unsigned o0 = lane << 4, o1 = o0 ^ 32, o2 = o0 ^ 64, o3 = o0 ^ 96;
        vmwait<20>(); PUBLISH(j - 5); vmwait<16>(); PUBLISH(j - 4); vmwait<12>(); PUBLISH(j - 3); vmwait<8>(); PUBLISH(j - 2); vmwait<4>(); PUBLISH(j - 1); vmwait<0>(); PUBLISH(j);
        if (ODL) lds_wait_ge(A_LANDV, (unsigned)(nblk - ODL));
#pragma unroll
        for (int k = 0; k < 8; ++k) {
          const unsigned x = ((k >> 1) & 1) << 7;
          dma4<62>(op + k * 4096, o0 ^ x, o1 ^ x, o2 ^ x, o3 ^ x, SCR + k * 4096);
        }
      } else {
        vmwait<40>(); PUBLISH(j - 10); vmwait<36>(); PUBLISH(j - 9); vmwait<32>(); PUBLISH(j - 8);
        vmwait<28>(); PUBLISH(j - 7); vmwait<24>(); PUBLISH(j - 6); vmwait<20>(); PUBLISH(j - 5); vmwait<16>(); PUBLISH(j - 4);
        vmwait<12>(); PUBLISH(j - 3); vmwait<8>(); PUBLISH(j - 2); vmwait<4>(); PUBLISH(j - 1); vmwait<0>(); PUBLISH(j);
      }
      __builtin_amdgcn_s_setprio(0);
      STAMP(5);
#undef PUBLISH
#undef GATE
#undef PART
    } else {
      if (wave == 6) {
        const size_t ko = ((size_t)pair * 8192 + blk0 * 16) * 256;
        if (!TLBE && lane < TLBN) { touch(Kc + ko, lane * (unsigned)(nblk * 4096 / TLBN), A_DUMMY); touch(Vc + ko, lane * (unsigned)(nblk * 4096 / TLBN), A_DUMMY); }
        if (lane < 1 && ty < 2) touch(Wo + (size_t)iA * 32768, 0, A_DUMMY);
        if (lane < WS_END / TLBS) touch(ws, lane * TLBS + (bid & 31) * 128, A_DUMMY);
      }
      GET_TAG();
      vmwait<0>();
      if (wave <= 4) {
        const int w1 = wave - 1;
        if (ty < 2) {
          if (lane == 0) lds_inc(A_QCNT);
          const int q = c16 >> 2, p = lane & 3, f3 = q | ((g & 1) << 2);
          const unsigned a0 = (8 * g + q) * 256 + (((4 * w1 + (p >> 1)) ^ (f3 << 1)) << 4) + (p & 1) * 8;
          const unsigned aa[2] = {opq(SCR + a0), opq(A_QU2 + a0)};
          lds_wait_ge(A_QCNT, 4u);
          STAMP(1);
          v4u w0[8], w1f[8], xf[8];
#pragma unroll
          for (int s = 0; s < 8; ++s) {
            const unsigned a = aa[s >> 2] + (s & 3) * 8192;
            w0[s] = trd2(a, a + 1024); w1f[s] = trd2((a ^ 32), (a ^ 32) + 1024);
            xf[s] = lds128(A_XIMG + (c16 & 1) * 512 + g * 16 + s * 64);
          }
          __builtin_amdgcn_sched_barrier(0);
          f4 d0 = {0.f, 0.f, 0.f, 0.f}, d1 = d0;
#pragma unroll
          for (int s = 0; s < 8; ++s) { d0 = mfma32(w0[s], xf[s], d0); d1 = mfma32(w1f[s], xf[s], d1); }
          const int pr = (c16 >> 1) & 1, nb = (c16 >> 2) & 1;
          const f4 e = nb ? d1 : d0;
          if (c16 < 8) gst128(rs, WS_QG + (((ty * 2 + (bid & 1)) * 2 + (c16 & 1)) * 4096 + hh * 128 + 32 * w1 + 16 * nb + 4 * g + 2 * pr) * 8,
                              (v4u){asu(pr ? e[2] : e[0]), tag, asu(pr ? e[3] : e[1]), tag});
          lgk0();
        } else {
          STAMP(1);
          if (w1 == 3 && lane == 0) *(volatile LAS unsigned*)A_OUL2 = 1u;
          const unsigned a0 = opq((w1 < 2 ? SCR : A_QU2 - 32768) + w1 * 16384 + c16 * 256 + ((g ^ c16) << 4));
          v4u uf[16];
#pragma unroll
          for (int i = 0; i < 16; ++i) uf[i] = lds128((a0 ^ ((i & 3) << 6)) + (i >> 2) * 4096);
          lds_wait_ge(A_QB, 1u);
          v4u qb[4];
#pragma unroll
          for (int s = 0; s < 4; ++s) qb[s] = lds128(A_QBI + (c16 & 1) * 256 + s * 64 + g * 16);
          __builtin_amdgcn_sched_barrier(0);
          f4 d[4];
#pragma unroll
          for (int rb = 0; rb < 4; ++rb) d[rb] = (f4){0.f, 0.f, 0.f, 0.f};
#pragma unroll
          for (int s = 0; s < 4; ++s)
#pragma unroll
            for (int rb = 0; rb < 4; ++rb) d[rb] = mfma32(uf[4 * rb + s], qb[s], d[rb]);
          const int rb = (c16 >> 1) & 3;
          const f4 e = rb == 0 ? d[0] : rb == 1 ? d[1] : rb == 2 ? d[2] : d[3];
          if (c16 < 8) sto64(A_QAS + w1 * 256 + (c16 & 1) * 128 + (16 * rb + 4 * g) * 2, (v2u){pk(e[0], e[1]), pk(e[2], e[3])});
          lgk0();
          if (lane < 16) gst128(rs, WS_QAB + ((lane >> 3) * 32 + hh) * 1024 + 512 + w1 * 128 + (lane & 7) * 16, lds128(A_QAS + w1 * 256 + lane * 16));
          vmwait<0>();
        }
        unsigned old = 0;
        if (lane == 0) old = __hip_atomic_fetch_add((LAS unsigned*)A_QUSED, 1u, __ATOMIC_RELAXED, __HIP_MEMORY_SCOPE_WORKGROUP);
        if (ty == 2 && __builtin_amdgcn_readfirstlane(old) == 3u) {
          flag_line(rs, WS_FQ + (32 + hh) * FS, lane, tag);
          cfence();
          if (lane == 0) *(volatile LAS unsigned*)A_QCNT = 4u;
        }
        STAMP(2);
      }
      if (wave == 5) {
        if (ty == 2) {
          lds_wait_ge(A_OUL2, 1u);
          gather_qb(A_QBI, hh, lane, rs, tag);
          lgk0();
          if (lane == 0) *(volatile LAS unsigned*)A_QB = 1u;
          STAMP(1);
        }
        lds_wait_ge(A_LANDK, ty == 2 ? 1u : (unsigned)QGO);
        v4u v[8];
        const unsigned off = WS_QG + 131072 + (b * 4096 + kvh * 512 + 2 * lane) * 8;
        _Pragma("nounroll") for (int it = 0; it < SPIN_CAP; ++it) {
          cfence();
          bool ok = true;
#pragma unroll
          for (int k = 0; k < 8; ++k) { v[k] = gld128(rs, off + (k & 1) * 65536 + (k >> 1) * 1024); ok &= tagok(v[k], tag); }
          if (__all(ok)) break;
          __builtin_amdgcn_s_sleep(4);
        }
#pragma unroll
        for (int k = 0; k < 4; ++k) sto32(A_QIMG + k * 256 + lane * 4, pk(asf(v[2 * k].x) + asf(v[2 * k + 1].x), asf(v[2 * k].z) + asf(v[2 * k + 1].z)));
        lgk0();
        if (lane == 0) *(volatile LAS unsigned*)A_QRDY = 1u;
      }
      lds_wait_ge(A_QRDY, 1u);
      STAMP(3);
      v4u qf[4];
#pragma unroll
      for (int s = 0; s < 4; ++s) qf[s] = lds128(A_QIMG + (c16 & 3) * 256 + s * 64 + g * 16);
      const unsigned ko = c16 * 256 + ((g ^ c16) << 4);
      const int vr = 4 * g + (c16 >> 2);
      const unsigned vo = 4096 + vr * 256 + ((((lane & 3) >> 1) ^ ((vr & 7) << 1)) << 4) + (lane & 1) * 8;
      float m = -INFINITY, l = 0.f;
      f4 acc[8];
#pragma unroll
      for (int t = 0; t < 8; ++t) acc[t] = (f4){0.f, 0.f, 0.f, 0.f};
      int slot = wave - 1;
      for (int j = wave - 1; j < nblk; j += NCONS) {
        const unsigned sb = slot * 8192;
        lds_wait_ge(A_LANDK, (unsigned)(j + 1));
        v4u kf[4];
#pragma unroll
        for (int s = 0; s < 4; ++s) kf[s] = lds128(sb + (ko ^ (s << 6)));
        __builtin_amdgcn_sched_barrier(0);
        f4 s4_ = {0.f, 0.f, 0.f, 0.f};
#pragma unroll
        for (int s = 0; s < 4; ++s) s4_ = mfma32(kf[s], qf[s], s4_);
        float mx = fmaxf(fmaxf(s4_[0], s4_[1]), fmaxf(s4_[2], s4_[3]));
        mx = x32max(x16max(mx));
        const float mn = fmaxf(m, mx), al = ex2((m - mn) * SC), mc = -mn * SC;
        const float p0 = ex2(fmaf(s4_[0], SC, mc)), p1 = ex2(fmaf(s4_[1], SC, mc)), p2 = ex2(fmaf(s4_[2], SC, mc)), p3 = ex2(fmaf(s4_[3], SC, mc));
        l = fmaf(l, al, (p0 + p1) + (p2 + p3));
        m = mn;
        v2u pb = {pk(p0, p1), pk(p2, p3)};
        unsigned va[8];
#pragma unroll
        for (int t = 0; t < 8; ++t) va[t] = sb + (vo ^ (t << 5));
        if (!__all(al == 1.f)) {
#pragma unroll
          for (int t = 0; t < 8; ++t) acc[t] *= al;
        }
        asm volatile("" : "+v"(pb), "+v"(l), "+v"(acc[0]), "+v"(acc[1]), "+v"(acc[2]), "+v"(acc[3]), "+v"(acc[4]), "+v"(acc[5]), "+v"(acc[6]), "+v"(acc[7]),
                     "+v"(va[0]), "+v"(va[1]), "+v"(va[2]), "+v"(va[3]), "+v"(va[4]), "+v"(va[5]), "+v"(va[6]), "+v"(va[7]));
        lds_wait_ge<0>(A_LANDV, (unsigned)(j + 1));
        v2u vf[8];
#pragma unroll
        for (int t = 0; t < 8; ++t) vf[t] = trd(va[t]);
        lgk0();
        if (lane == 0) *(volatile LAS unsigned*)(A_DONE + slot * 4) = (unsigned)(j + 1);
#pragma unroll
        for (int t = 0; t < 8; ++t) acc[t] = mfma16(vf[t], pb, acc[t]);
        slot += NCONS; if (slot >= NSLOT) slot -= NSLOT;
      }
      STAMP(4);
      l = x32sum(x16sum(l));
      if (c16 < 4) {
#pragma unroll
        for (int t = 0; t < 8; ++t) sto128(A_ST + (wave - 1) * 2048 + c16 * 512 + ((t ^ c16) * 64) + g * 16, __builtin_bit_cast(v4u, acc[t]));
        if (g == 0) sto64(A_ML + (c16 * 8 + wave - 1) * 8, (v2u){asu(m * SC), asu(l)});
      }
    }
    bar();
    if (wave >= 3 && wave <= 6) {
      const int hq = wave - 3;
      v2u ml[NCONS];
      f2 ow[NCONS];
      const unsigned sa = opq(A_ST + hq * 512 + ((lane * 8) ^ (hq << 6))), ma = opq(A_ML + hq * 64);
#pragma unroll
      for (int c = 0; c < NCONS; ++c) { ml[c] = *(const LAS v2u*)(ma + c * 8); ow[c] = *(const LAS f2*)(sa + c * 2048); }
      float M = asf(ml[0].x);
#pragma unroll
      for (int c = 1; c < NCONS; ++c) M = fmaxf(M, asf(ml[c].x));
      float L = 0.f, o0 = 0.f, o1 = 0.f;
#pragma unroll
      for (int c = 0; c < NCONS; ++c) { const float wt = ex2(asf(ml[c].x) - M); L = fmaf(wt, asf(ml[c].y), L); o0 = fmaf(wt, ow[c].x, o0); o1 = fmaf(wt, ow[c].y, o1); }
      const int rec = (b * 32 + kvh * 4 + hq) * NSPLIT_A + split;
      gst128(rs, WS_PAO + rec * 1024 + lane * 16, (v4u){asu(o0), tag, asu(o1), tag});
      if (lane < 8) gst128(rs, WS_PAM + rec * 128 + lane * 16, (v4u){asu(M), tag, asu(L), tag});
    }
    STAMP(6);
    if (ty < 2) {
      const int oh = iA >> 2, odq = iA & 3;
      if (wave == 0) { vmwait<0>(); if (lane == 0) lds_inc(A_OUL); }
      lds_wait_ge(A_OUL, 1u);
      v4u wf[4];
      out_frags(SCR, wave, lane, wf);
      const int tt = (c16 >> 1) & 3, pr = c16 >> 3, n = 64 * wave + 16 * tt + 4 * g + 2 * pr;
      unsigned fo = WS_F + ((((n >> 3) * 128 + iA) * 16) + (c16 & 1) * 8 + (n & 7)) * 8, oa = A_OIMG + (c16 & 1) * 64 + g * 16;
      asm volatile("" : "+v"(fo), "+v"(oa));
      if (wave == 1) {
        const int rec = ((lane >> 5) * 32 + oh) * NSPLIT_A + (g & 1) * 5;
        v4u vm[10];
        const unsigned o0_ = EXCL ? WS_PAO + rec * 1536 + odq * 384 + c16 * 16 : WS_PAO + rec * 1024 + (32 * odq + 2 * c16) * 8;
        const unsigned o1_ = EXCL ? WS_PAO + rec * 1536 + odq * 384 + 256 : WS_PAM + rec * 128 + odq * 16;
        if (G2) sweep2<10, G2>(vm, rs, tag, [&](int k) { return k < 5 ? o0_ + k * (EXCL ? 1536 : 1024) : o1_ + (k - 5) * (EXCL ? 1536 : 128); });
        else sweep<10, GSL, GMASK>(vm, rs, tag, [&](int k) { return k < 5 ? o0_ + k * (EXCL ? 1536 : 1024) : o1_ + (k - 5) * (EXCL ? 1536 : 128); });
        const v4u* v = vm; const v4u* ml = vm + 5;
        float M = asf(ml[0].x);
#pragma unroll
        for (int i = 1; i < 5; ++i) M = fmaxf(M, asf(ml[i].x));
        M = x16max(M);
        float L = 0.f, o0 = 0.f, o1 = 0.f;
#pragma unroll
        for (int i = 0; i < 5; ++i) { const float wt = ex2(asf(ml[i].x) - M); L = fmaf(wt, asf(ml[i].z), L); o0 = fmaf(wt, asf(v[i].x), o0); o1 = fmaf(wt, asf(v[i].z), o1); }
        const float inv = 1.f / x16sum(L);
        o0 = x16sum(o0); o1 = x16sum(o1);
        if (!(g & 1)) sto32(A_OIMG + (lane >> 5) * 64 + c16 * 4, pk(o0 * inv, o1 * inv));
      }
      STAMP(7);
      bar();
      const v4u ob = lds128(oa);
      f4 d[4];
#pragma unroll
      for (int t = 0; t < 4; ++t) d[t] = mfma32(wf[t], ob, (f4){0.f, 0.f, 0.f, 0.f});
      const f4 e = tt == 0 ? d[0] : tt == 1 ? d[1] : tt == 2 ? d[2] : d[3];
      gst128(rs, fo, (v4u){asu(pr ? e[2] : e[0]), tag, asu(pr ? e[3] : e[1]), tag});
      STAMP(8);
    }
    STAMP_OUT();
  } else if (isB) {
    const int b = bid >> 5, sp = bid & 31, w = wave, lane_ = lane;
    const unsigned vl = lane << 4;
#define LD_S(S, rho, u) asm volatile("global_load_dwordx4 %0, %1, %2 offset:%3 nt" : "=v"(S[u]) : "v"(vl), "s"(cp + (size_t)((((rho) + brot) & 3) * 65536) + ((u) >> 2) * 4096), "n"(((u) & 3) * 1024) : "memory")
#define ST_S(S, rho) do { unsigned a0_ = ((rho) & 1) * 65536 + w * 8192 + vc0, a1_ = ((rho) & 1) * 65536 + w * 8192 + vc1; asm volatile("" : "+v"(a0_), "+v"(a1_)); \
      _Pragma("unroll") for (int u_ = 0; u_ < 8; ++u_) sto128(u_ * 1024 + (((u_ >> 2) ? a1_ : a0_) ^ ((u_ & 3) << 6)), S[u_]); } while (0)
#define PB() vmwait<WB - 2>()
    if (BPRIO && w >= 4) __builtin_amdgcn_s_setprio(1);
    v4u S2[8], S3[8];
    if (w == 7) {
      if (TLBB && lane < TLBB) touch(Ckv + (size_t)bid * 262144, 65536 + lane * (196608 / (TLBB ? TLBB : 1)), B_DUMMY);
      if (lane < WS_END / TLBS) touch(ws, lane * TLBS + (bid & 31) * 128, B_DUMMY);
      if (lane < 1) touch(out, 0, B_DUMMY);
    }
    GET_TAG();
    PB(); LD_C(0, 6); LD_C(0, 7);
    STAMP(1);
    PB(); LD_C(1, 0); LD_C(1, 1);
    PB(); LD_C(1, 2); LD_C(1, 3);
    PB(); LD_C(1, 4); LD_C(1, 5);
    PB(); LD_C(1, 6); LD_C(1, 7);
    STAMP(2);
    PB(); LD_S(S2, 2, 0); LD_S(S2, 2, 1);
    PB(); LD_S(S2, 2, 2); LD_S(S2, 2, 3);
    PB(); LD_S(S2, 2, 4); LD_S(S2, 2, 5);
    PB(); LD_S(S2, 2, 6); LD_S(S2, 2, 7);
#pragma unroll
    for (int u = 0; u < NS3; u += 2) { PB(); LD_S(S3, 3, u); LD_S(S3, 3, u + 1); }
    STAMP(3);
    if (w == 0) {
      _Pragma("nounroll") for (int it = 0; it < SPIN_CAP; ++it) { cfence(); if (__all(gld32(rs, WS_FQ + lane * FS) == tag)) break; __builtin_amdgcn_s_sleep(PSL); }
    }
    bar();
    STAMP(4);
    {
      v4u t[4];
      const char* qa = ws + WS_QAB + (b * 32 + 4 * w) * 1024;
#pragma unroll
      for (int i = 0; i < 4; ++i) asm volatile("global_load_dwordx4 %0, %1, %2 offset:%3 sc1" : "=v"(t[i]) : "v"(vl), "s"(qa), "n"(i * 1024) : "memory");
      constexpr int NA = NS3 < 8 ? 2 : 0, NC = 8 - NS3 - NA < 4 ? 8 - NS3 - NA : 4;
#pragma unroll
      for (int u = NS3; u < NS3 + NA; ++u) LD_S(S3, 3, u);
      asm volatile("s_waitcnt vmcnt(%12)" : "+v"(t[0]), "+v"(t[1]), "+v"(t[2]), "+v"(t[3]), "+v"(S2[0]), "+v"(S2[1]), "+v"(S2[2]), "+v"(S2[3]),
                   "+v"(S2[4]), "+v"(S2[5]), "+v"(S2[6]), "+v"(S2[7]) : "n"(NA) : "memory");
#pragma unroll
      for (int u = NS3 + NA; u < NS3 + NA + NC; ++u) LD_S(S3, 3, u);
#pragma unroll
      for (int i = 0; i < 4; ++i) sto128(SCR + (4 * w + i) * 1024 + ((lane ^ ((4 * w + i) & 15)) << 4), t[i]);
    }
    bar();
    const int kb4 = w & 3, hg = w >> 2;
    v4u qf[16];
#pragma unroll
    for (int s = 0; s < 16; ++s) qf[s] = lds128(SCR + (16 * hg + c16) * 1024 + ((((4 * s) & 15) ^ g ^ c16) << 4) + (s >> 2) * 256);
    STAMP(5);
    const unsigned fk = ((c16 & 3) << 2) | ((0x78u >> (2 * (c16 >> 2))) & 3u);
    const unsigned ka = (16 * kb4 + c16) * 1024 + ((g ^ fk) << 4);
    const int h2 = lane >> 5, blk = g & 1, qq = c16 >> 2, pp = lane & 3;
    const unsigned ft0 = (qq << 2) | (h2 ? 3u : 0u), ft1 = (qq << 2) | (h2 ? 1u : 2u);
    const unsigned cbase = 8 * w + 2 * blk + (pp >> 1);
    const unsigned va00 = (8 * h2 + qq) * 1024 + ((cbase ^ ft0) << 4) + (pp & 1) * 8, va01 = (8 * h2 + 4 + qq) * 1024 + ((cbase ^ ft1) << 4) + (pp & 1) * 8;
    const unsigned mxa = opq(B_MX + (16 * hg + c16) * 4), pwa = opq(B_P + kb4 * 1536 + (16 * hg + c16) * 48 + g * 8);
    const unsigned pra = opq(B_P + (lane & 31) * 48 + h2 * 16), ala = opq(B_AL + (lane & 31) * 4);
    float m = -INFINITY, l = 0.f;
    f16v acc0, acc1;
#pragma unroll
    for (int i = 0; i < 16; ++i) { acc0[i] = 0.f; acc1[i] = 0.f; }
#pragma unroll
    for (int rho = 0; rho < 2; ++rho) {
      unsigned kr = ka, vr0 = va00, vr1 = va01;
      asm volatile("" : "+v"(kr), "+v"(vr0), "+v"(vr1));
      bar();
      f4 sa = {0.f, 0.f, 0.f, 0.f}, sb = sa;
      {
        v4u kf[8];
#pragma unroll
        for (int s = 0; s < 8; ++s) kf[s] = lds128((kr ^ ((s & 3) << 6)) + (s >> 2) * 256);
        __builtin_amdgcn_sched_barrier(0);
#pragma unroll
        for (int s = 0; s < 8; ++s) {
          sa = mfma32(kf[s], qf[s], sa);
          kf[s] = lds128((kr ^ ((s & 3) << 6)) + (2 + (s >> 2)) * 256);
          __builtin_amdgcn_sched_barrier(0);
        }
        kr += 65536;
#pragma unroll
        for (int s = 0; s < 8; ++s) {
          sa = mfma32(kf[s], qf[8 + s], sa);
          kf[s] = lds128((kr ^ ((s & 3) << 6)) + (s >> 2) * 256);
          __builtin_amdgcn_sched_barrier(0);
        }
#pragma unroll
        for (int s = 0; s < 8; ++s) {
          sb = mfma32(kf[s], qf[s], sb);
          kf[s] = lds128((kr ^ ((s & 3) << 6)) + (2 + (s >> 2)) * 256);
          __builtin_amdgcn_sched_barrier(0);
        }
#pragma unroll
        for (int s = 0; s < 8; ++s) sb = mfma32(kf[s], qf[8 + s], sb);
      }
      if (rho == 0 && NS3 == 0) { LD_S(S3, 3, 6); LD_S(S3, 3, 7); }
      float mx = fmaxf(fmaxf(fmaxf(sa[0], sa[1]), fmaxf(sa[2], sa[3])), fmaxf(fmaxf(sb[0], sb[1]), fmaxf(sb[2], sb[3])));
      mx = x32max(x16max(mx));
      if (g == 0) stof(mxa + kb4 * 128, mx);
      v4u c0[4], c1[4];
#pragma unroll
      for (int ks = 0; ks < 4; ++ks) {
        c0[ks] = trd2(ks * 16384 + vr0, ks * 16384 + vr1);
        c1[ks] = trd2(ks * 16384 + (vr0 ^ 64), ks * 16384 + (vr1 ^ 64));
      }
      if (rho == 0) STAMP(6); else STAMP(8);
      bar();
      const float mn = fmaxf(fmaxf(m, fmaxf(ldsf(mxa), ldsf(mxa + 128))), fmaxf(ldsf(mxa + 256), ldsf(mxa + 384)));
      const float al = ex2((m - mn) * SC), mc = -mn * SC;
      float p[8];
#pragma unroll
      for (int i = 0; i < 4; ++i) { p[i] = ex2(fmaf(sa[i], SC, mc)); p[4 + i] = ex2(fmaf(sb[i], SC, mc)); }
      l = fmaf(l, al, ((p[0] + p[1]) + (p[2] + p[3])) + ((p[4] + p[5]) + (p[6] + p[7])));
      m = mn;
      sto64(pwa, (v2u){pk(p[0], p[1]), pk(p[2], p[3])});
      sto64(pwa + 6144, (v2u){pk(p[4], p[5]), pk(p[6], p[7])});
      if (kb4 == 0 && g == 0) stof(mxa + (B_AL - B_MX), al);
      if (rho == 1) { l = x32sum(x16sum(l)); if (g == 0) stof(mxa + (B_LS - B_MX) + kb4 * 128, l); }
      bar();
      if (rho == 1 && kb4 == 0 && g == 0) {
        const unsigned la = mxa + (B_LS - B_MX);
        sto64(B_FM + (16 * hg + c16) * 8, (v2u){asu(m * SC), asu((ldsf(la) + ldsf(la + 128)) + (ldsf(la + 256) + ldsf(la + 384)))});
      }
      const float alp = rho ? ldsf(ala) : 0.f;
      v4u pf[4];
#pragma unroll
      for (int ks = 0; ks < 4; ++ks) pf[ks] = lds128(pra + ks * 1536);
      __builtin_amdgcn_sched_barrier(0);
      if (rho) { acc0 *= alp; acc1 *= alp; }
      vr0 += 65536; vr1 += 65536;
#pragma unroll
      for (int ks = 0; ks < 4; ++ks) {
        acc0 = mfmaL(c0[ks], pf[ks], acc0); acc1 = mfmaL(c1[ks], pf[ks], acc1);
        c0[ks] = trd2(ks * 16384 + vr0, ks * 16384 + vr1);
        c1[ks] = trd2(ks * 16384 + (vr0 ^ 64), ks * 16384 + (vr1 ^ 64));
        pf[ks] = lds128(pra + 6144 + ks * 1536);
        __builtin_amdgcn_sched_barrier(0);
      }
      bar();
      if (rho == 0) {
        ST_S(S2, 0);
        vmwait<0>();
        ST_S(S3, 1);
      }
#pragma unroll
      for (int ks = 0; ks < 4; ++ks) { acc0 = mfmaL(c0[ks], pf[ks], acc0); acc1 = mfmaL(c1[ks], pf[ks], acc1); }
      if (rho == 0) STAMP(7); else STAMP(9);
    }
    {
    const int lane = (int)opq((unsigned)lane_), h2 = lane >> 5, tid = wave * 64 + lane;
    {
      const unsigned tb = B_T + w * 4096, n32 = lane & 31;
#pragma unroll
      for (int q4 = 0; q4 < 4; ++q4) {
        sto64(tb + n32 * 128 + ((q4 ^ (n32 & 7)) << 4) + h2 * 8, (v2u){pk(acc0[4 * q4], acc0[4 * q4 + 1]), pk(acc0[4 * q4 + 2], acc0[4 * q4 + 3])});
        sto64(tb + n32 * 128 + (((4 + q4) ^ (n32 & 7)) << 4) + h2 * 8, (v2u){pk(acc1[4 * q4], acc1[4 * q4 + 1]), pk(acc1[4 * q4 + 2], acc1[4 * q4 + 3])});
      }
      lgk0();
      const int hr = lane >> 3, ck = lane & 7;
#pragma unroll
      for (int i = 0; i < 4; ++i)
        gst128(rs, WS_PB + (((8 * i + hr) * 4 + (w >> 1)) * 64 + bid) * 256 + (w & 1) * 128 + ck * 16, lds128(tb + (8 * i + hr) * 128 + ((ck ^ hr) << 4)));
    }
    vmwait<0>();
    bar();
    if (w == 0 && lane < 32) { const v2u fm = *(const LAS v2u*)(B_FM + lane * 8); gst128(rs, WS_PBM + bid * 512 + lane * 16, (v4u){fm.x, tag, fm.y, tag}); }
    STAMP(10);
    if (w == 0) {
      const unsigned off = SKIPA ? WS_F + (64 + bid) * 16384 + (bid & 31) * 128 : PRE2 == 2 ? WS_F + bid * 16384 + ((lane >> 3) * 16 + (bid & 15)) * 128 : PRE2 ? WS_F + bid * 16384 + (2 * bid) * 128 : WS_PAM + (bid * NSPLIT_A + (bid % NSPLIT_A)) * 128;
      _Pragma("nounroll") for (int it = 0; it < SPIN_CAP; ++it) { cfence(); if (__popcll(__ballot(gld64(rs, off).y == tag)) >= PREN) break; __builtin_amdgcn_s_sleep(PRESL); }
    }
    bar();
    STAMP(14);
    v4u f[3];
    if (F2) sweep2<3, F2>(f, rs, tag, [&](int k) { return WS_F + ((k >> 1) * 64 + bid) * 16384 + (k < 2 ? k * 8192 + tid * 16 : (tid & 255) * 16); });
    else sweep<3, FSL, FMASK>(f, rs, tag, [&](int k) { return WS_F + (((SKIPB ? 0 : SKIPA ? 2 : k) >> 1) * 64 + bid) * 16384 + (k < 2 && !SKIPA ? k * 8192 + tid * 16 : (tid & 255) * 16); });
    float r[4] = {asf(f[0].x) + asf(f[1].x), asf(f[0].z) + asf(f[1].z), w < 4 ? asf(f[2].x) : 0.f, w < 4 ? asf(f[2].z) : 0.f};
#pragma unroll
    for (int k = 0; k < 4; ++k) { r[k] += dpp<ROR8>(r[k]); r[k] = x32sum(x16sum(r[k])); }
    if (lane < 8) { sto64(B_FIN + w * 128 + lane * 8, (v2u){asu(r[0]), asu(r[1])}); sto64(B_FIN + w * 128 + 64 + lane * 8, (v2u){asu(r[2]), asu(r[3])}); }
    bar();
    if (tid < 16) {
      float s0 = 0.f, s1 = 0.f;
#pragma unroll
      for (int i = 0; i < 8; ++i) { const f2 t = *(const LAS f2*)(B_FIN + i * 128 + tid * 8); s0 += t.x; s1 += t.y; }
      const int mat = tid >> 3, ob = (tid >> 2) & 1, pr = tid & 3;
      *(GAS unsigned*)((GAS char*)out + (ob * 1024 + mat * 512 + 8 * bid + 2 * pr) * 2) = pk(s0, s1);
      if (!SKIPA && !SKIPB && bid == 0 && tid == 0) gst32(rs, WS_E, tag);
    }
    STAMP(15);
    STAMP_OUT();
    }
  } else {
    const int h = bid - NB_CU, w = wave;
    if (w == 7) {
      if (lane < WS_END / TLBS) touch(ws, lane * TLBS + (bid & 31) * 128, B_DUMMY);
      if (lane < 8) touch(Wuv + h * 256, lane * 524288, B_DUMMY);
      if (lane < 2) touch(Bwo + (size_t)h * 131072, lane * 65536, B_DUMMY);
    }
    GET_TAG();
    if (w == 0) gather_qb(T_QBI, h, lane, rs, tag);
    vmwait<0>();
    STAMP(1);
    v4u OU[16];
    const char* ow = Bwo + (size_t)(h * 128 + 16 * w) * 1024;
    const unsigned vo = g * 4096 + c16 * 16;
#define LD_OU(u) asm volatile("global_load_dwordx4 %0, %1, %2 offset:%3 nt" : "=v"(OU[u]) : "v"(vo), "s"(ow), "n"(((u) >> 2) * 1024 + ((u) & 3) * 256) : "memory")
#pragma unroll
    for (int u = 0; u < TE; ++u) LD_OU(u);
    {
      const unsigned a0 = w * 8192 + c16 * 256 + ((g ^ c16) << 4);
      v4u uf[8];
#pragma unroll
      for (int i = 0; i < 8; ++i) uf[i] = lds128((a0 ^ ((i & 3) << 6)) + (i >> 2) * 4096);
      bar();
      STAMP(2);
      v4u qb[4];
#pragma unroll
      for (int s = 0; s < 4; ++s) qb[s] = lds128(T_QBI + (c16 & 1) * 256 + s * 64 + g * 16);
      __builtin_amdgcn_sched_barrier(0);
      f4 d0 = {0.f, 0.f, 0.f, 0.f}, d1 = d0;
#pragma unroll
      for (int s = 0; s < 4; ++s) { d0 = mfma32(uf[s], qb[s], d0); d1 = mfma32(uf[4 + s], qb[s], d1); }
      const int rb = (c16 >> 1) & 1;
      const f4 e = rb ? d1 : d0;
      if (c16 < 4) sto64(T_QAS + (c16 & 1) * 512 + (32 * w + 16 * rb + 4 * g) * 2, (v2u){pk(e[0], e[1]), pk(e[2], e[3])});
      bar();
      if (w == 1) {
        gst128(rs, WS_QAB + ((lane >> 5) * 32 + h) * 1024 + (lane & 31) * 16, lds128(T_QAS + lane * 16));
        vmwait<0>();
        flag_line(rs, WS_FQ + h * FS, lane, tag);
      }
    }
    bar();
    STAMP(3);
    {
      const char* uv = Wuv + (size_t)(64 * w) * 8192 + h * 256;
      const unsigned vuv = g * 8192 + ((c16 ^ (g << 1)) << 4);
#pragma unroll
      for (int u = 0; u < 16; ++u) { if (u >= WT && !(u & 1)) vmwait<WT - 2>(); dma<0>(uv + (size_t)u * 32768, vuv ^ (((u >> 1) & 1) << 7), w * 16384 + u * 1024); }
#pragma unroll
      for (int u = TE; u < 16; ++u) { if (!(u & 1)) vmwait<WT - 2>(); LD_OU(u); }
    }
    asm volatile("s_waitcnt vmcnt(0)" : "+v"(OU[0]), "+v"(OU[1]), "+v"(OU[2]), "+v"(OU[3]), "+v"(OU[4]), "+v"(OU[5]), "+v"(OU[6]), "+v"(OU[7]),
                 "+v"(OU[8]), "+v"(OU[9]), "+v"(OU[10]), "+v"(OU[11]), "+v"(OU[12]), "+v"(OU[13]), "+v"(OU[14]), "+v"(OU[15]) :: "memory");
    if (tid < 4) sto32(T_RDY + tid * 4, 0u);
    bar();
    STAMP(4);
    v4u wf[16];
    {
      const int q = c16 >> 2, p = lane & 3, f3 = q | ((g & 1) << 2);
      const unsigned a0 = (8 * g + q) * 256 + (((2 * w + (p >> 1)) ^ (f3 << 1)) << 4) + (p & 1) * 8;
      const unsigned aa[2] = {opq(a0), opq(a0 + 65536)};
#pragma unroll
      for (int s = 0; s < 16; ++s) wf[s] = trd2(aa[s >> 3] + (s & 7) * 8192, aa[s >> 3] + (s & 7) * 8192 + 1024);
    }
    STAMP(5);
    {
      const int tb = w >> 2;
      v4u o[8];
#pragma unroll
      for (int i = 0; i < 8; ++i) { o[i] = (v4u){0u, 0u, 0u, 0u}; asm volatile("" : "+v"(o[i])); }
      const char* pb = ws + WS_PB + (h * 4 + (w & 3)) * 16384 + tb * 8192;
      const unsigned vl = lane << 4;
      unsigned done = 0;
      bool all = false;
      v4u ml = {0u, ~tag, 0u, ~tag};
      if (w == 0) {
        _Pragma("nounroll") for (int it = 0; it < SPIN_CAP; ++it) { cfence(); if (__all(gld32(rs, WS_PBM + 2 * h * 512 + h * 16 + 4) == tag)) break; __builtin_amdgcn_s_sleep(PSL1); }
      }
      _Pragma("nounroll") for (int it = 0; it < SPIN_CAP * 8 && !all; ++it) {
        unsigned rdy;
        if (w == 0) {
          cfence();
          const v4u t = gld128(rs, tagok(ml, tag) ? 0xfffffff0u : WS_PBM + lane * 512 + h * 16);
          if (!tagok(ml, tag)) ml = t;
          unsigned long long m = __ballot(tagok(ml, tag));
          all = m == ~0ull;
          m &= m >> 2; m &= m >> 1; m &= 0x1111111111111111ull;
          rdy = (unsigned)m;
          if (lane == 0) *(volatile LAS v2u*)T_RDY = (v2u){rdy, (unsigned)(m >> 32)};
        } else {
          rdy = lds_peek(T_RDY + tb * 4);
          if (rdy == done) { __builtin_amdgcn_s_sleep(1); continue; }
          all = rdy == 0x11111111u;
        }
        const unsigned add = rdy & ~done;
        done = rdy;
#pragma unroll
        for (int i = 0; i < 8; ++i)
          if (add & (1u << (4 * i))) asm volatile("global_load_dwordx4 %0, %1, %2 offset:%3 sc1" : "+v"(o[i]) : "v"(vl), "s"(pb + (i >> 2) * 4096), "n"((i & 3) * 1024) : "memory");
      }
      if (w == 0) {
        const float M = x16max(row16max(asf(ml.x)));
        const float wt = ex2(asf(ml.x) - M), wn = wt / x16sum(row16sum(wt * asf(ml.z)));
        const unsigned hi = pk(wn, wn), wa = T_WN + (lane >> 5) * 128 + (lane & 3) * 32 + ((lane >> 2) & 7) * 4;
        const float lo = wn - asf(hi & 0xffff0000u);
        sto32(wa, hi); sto32(wa + 256, pk(lo, lo));
        lgk0();
        if (lane == 0) *(volatile LAS unsigned*)(T_RDY + 8) = 1u;
      } else {
        lds_wait_ge<0>(T_RDY + 8, 1u);
      }
      const unsigned wa = T_WN + (c16 >> 3) * 256 + tb * 128 + g * 32, half = (c16 & 1) ? 0xffff0000u : 0x0000ffffu;
      const v4u w0_ = lds128(wa), w1_ = lds128(wa + 16);
      const int rr = (c16 >> 1) & 3;
      v4u bo[8];
#pragma unroll
      for (int u = 0; u < 8; ++u) { const unsigned x = (u < 4 ? w0_[u & 3] : w1_[u & 3]) & half; bo[u] = (v4u){rr == 0 ? x : 0u, rr == 1 ? x : 0u, rr == 2 ? x : 0u, rr == 3 ? x : 0u}; }
      asm volatile("s_waitcnt vmcnt(0)" : "+v"(o[0]), "+v"(o[1]), "+v"(o[2]), "+v"(o[3]), "+v"(o[4]), "+v"(o[5]), "+v"(o[6]), "+v"(o[7]),
                   "+v"(bo[0]), "+v"(bo[1]), "+v"(bo[2]), "+v"(bo[3]), "+v"(bo[4]), "+v"(bo[5]), "+v"(bo[6]), "+v"(bo[7]) :: "memory");
      STAMP(8);
      f4 d = {0.f, 0.f, 0.f, 0.f};
#pragma unroll
      for (int u = 0; u < 8; ++u) d = mfma32(o[u], bo[u], d);
#pragma unroll
      for (int r = 0; r < 4; ++r) {
        const float x = d[r] + dpp<ROR8>(d[r]);
        const unsigned pr = pk(x, dpp<XOR1>(x));
        if ((c16 & 9) == 0) sto32(T_OL + tb * 1088 + ((w & 3) * 128 + 32 * g + 8 * r + c16) * 2, pr);
      }
    }
    bar();
    STAMP(6);
    {
      v4u of[16];
      const unsigned oa = opq(T_OL + (c16 >> 3) * 1088 + g * 16);
#pragma unroll
      for (int s = 0; s < 16; ++s) of[s] = lds128(oa + s * 64);
      __builtin_amdgcn_sched_barrier(0);
      f4 d0 = {0.f, 0.f, 0.f, 0.f}, d1 = d0;
#pragma unroll
      for (int s = 0; s < 8; ++s) { d0 = mfma32(wf[2 * s], of[2 * s], d0); d1 = mfma32(wf[2 * s + 1], of[2 * s + 1], d1); }
      d0 += d1;
      const unsigned half = (c16 & 1) ? 0xffff0000u : 0x0000ffffu;
      const int rr = (c16 >> 1) & 3;
      f4 o4[4];
#pragma unroll
      for (int cb = 0; cb < 4; ++cb) o4[cb] = (f4){0.f, 0.f, 0.f, 0.f};
#pragma unroll
      for (int u = 0; u < 4; ++u) {
        const unsigned x = pk(d0[u], d0[u]) & half;
        const v4u bo = {rr == 0 ? x : 0u, rr == 1 ? x : 0u, rr == 2 ? x : 0u, rr == 3 ? x : 0u};
#pragma unroll
        for (int cb = 0; cb < 4; ++cb) o4[cb] = mfma32(OU[4 * u + cb], bo, o4[cb]);
      }
#pragma unroll
      for (int cb = 0; cb < 4; ++cb)
#pragma unroll
        for (int r = 0; r < 4; ++r) stof(T_RED + w * 4096 + cb * 1024 + r * 64 + g * 256 + c16 * 4, o4[cb][r]);
    }
    bar();
    {
      const int j = tid >> 3, pr = tid & 7;
      float s0 = 0.f, s1 = 0.f;
#pragma unroll
      for (int i = 0; i < 8; ++i) { const f2 t = *(const LAS f2*)(T_RED + i * 4096 + tid * 8); s0 += t.x; s1 += t.y; }
      gst128(rs, WS_F + (((64 + j) * 128 + h) * 16 + 2 * pr) * 8, (v4u){asu(s0), tag, asu(s1), tag});
    }
    STAMP(7);
    STAMP_OUT();
  }
}

extern "C" void kernel_launch(void* const* d_in, const int* in_sizes, int n_in, void* d_out, int out_size, void* d_ws, size_t ws_size,
                              hipStream_t stream) {
  (void)in_sizes; (void)n_in; (void)out_size; (void)ws_size;
  (void)hipFuncSetAttribute((const void*)fused_decode, hipFuncAttributeMaxDynamicSharedMemorySize, LDS_BYTES);
  fused_decode<<<256, 512, LDS_BYTES, stream>>>(
      (const char*)d_in[0], (const char*)d_in[1], (const char*)d_in[2], (const char*)d_in[3], (const char*)d_in[4], (const char*)d_in[5],
      (const char*)d_in[6], (const char*)d_in[7], (const char*)d_in[8], (const char*)d_in[9], (const char*)d_in[10], (unsigned short*)d_out,
      (char*)d_ws);
}
```

```cpp
#define DBG_TS
#define PROBE 0
#define PROLE 3
#define PSTAT 0
#define PZERO 0
#include <hip/hip_runtime.h>
#include <stdint.h>
#pragma clang diagnostic ignored "-Wint-to-pointer-cast"

#define GAS __attribute__((address_space(1)))
#define LAS __attribute__((address_space(3)))
typedef unsigned v4u __attribute__((ext_vector_type(4)));
typedef unsigned v2u __attribute__((ext_vector_type(2)));
typedef float f4 __attribute__((ext_vector_type(4)));
typedef float f2 __attribute__((ext_vector_type(2)));
typedef float f16v __attribute__((ext_vector_type(16)));
typedef short s4 __attribute__((ext_vector_type(4)));
typedef __bf16 b8 __attribute__((ext_vector_type(8)));
typedef __bf16 b2 __attribute__((ext_vector_type(2)));

#define W1 12
#define W2 24
#define J1 18
#define ROT 13
#define BROT 3
#define GMASK 0
#define FMASK 0
#define VDL 0
#define TE 6
#define UE 3
#define PREN 32
#define G2 0
#define F2 0
#define EXCL 0
#define VTAIL 0
#define ODL 5
#define BPRIO 0
#define TLBW 1
#define TLBB 0
#define TLBE 0
#define TLBN 2
#define TLBS 2097152
#define DLY2 40
#define DLYP 30
#define DLYB 10
#define NB0 55
#define NB1 52
#define WB 4
#define PRE2 2
#define SKIPA 0
#define SKIPB 0
#define WT 6
#define CODE_KB 25
#define QGO 4

namespace {

constexpr int NB_CU = 64, NT_CU = 32, NSPLIT_A = 10, NSLOT = 14, NCONS = 6;
constexpr float SC = 0.08838834764831845f * 1.4426950408889634f;
constexpr int SPIN_CAP = 1 << 18;

#define NS3 6
#define PSL 4
#define PSL1 8
#define PRESL 8
#define GSL 1
#define FSL 0
constexpr unsigned FS = 128;
constexpr unsigned WS_E = 0;
constexpr unsigned WS_FQ = 4096;
constexpr unsigned WS_FP = WS_FQ + 16384;
constexpr unsigned WS_QG = 65536;
constexpr unsigned WS_QAB = WS_QG + 262144;
constexpr unsigned WS_PBM = WS_QAB + 65536;
constexpr unsigned WS_PAM = WS_PBM + 32768;
constexpr unsigned WS_PAO = WS_PAM + 131072;
constexpr unsigned WS_PB = WS_PAO + 1048576;
constexpr unsigned WS_F = WS_PB + 2097152;
constexpr unsigned WS_END = WS_F + 2097152;
constexpr unsigned WS_TS = 8u << 20;

constexpr unsigned LDS_BYTES = 163840;
constexpr unsigned SCR = 131072;
constexpr unsigned A_MISC = NSLOT * 8192;
constexpr unsigned A_LANDK = A_MISC, A_LANDV = A_MISC + 4, A_QUSED = A_MISC + 8, A_QCNT = A_MISC + 12, A_QB = A_MISC + 16, A_QRDY = A_MISC + 20, A_OUL = A_MISC + 24, A_OUL2 = A_MISC + 28;
constexpr unsigned A_DONE = A_MISC + 64;
constexpr unsigned A_QU2 = 10 * 8192;
constexpr unsigned A_QIMG = A_MISC + 256;
constexpr unsigned A_ML = A_MISC + 1280;
constexpr unsigned A_OIMG = A_MISC + 1792;
constexpr unsigned A_ST = A_MISC + 2048;
constexpr unsigned A_XIMG = A_MISC + 4096, A_DUMMY = A_MISC + 5120, A_QBI = A_MISC + 6144, A_QAS = A_MISC + 6656;
constexpr unsigned SLOT0 = 0, SLOT1 = 65536;
constexpr unsigned B_MX = SCR, B_AL = SCR + 512, B_LS = SCR + 640, B_P = SCR + 2048;
constexpr unsigned B_FM = SCR + 1280, B_FIN = SCR + 16384, B_T = SLOT1, B_DUMMY = SCR + 8192;
constexpr unsigned T_QBI = SCR, T_QAS = SCR + 1024, T_OL = SCR + 4096, T_RDY = SCR + 6400, T_WN = SCR + 6656, T_RED = 0;

__device__ __forceinline__ v4u lds128(unsigned a) { return *(const LAS v4u*)a; }
__device__ __forceinline__ void sto128(unsigned a, v4u v) { *(LAS v4u*)a = v; }
__device__ __forceinline__ void sto64(unsigned a, v2u v) { *(LAS v2u*)a = v; }
__device__ __forceinline__ void sto32(unsigned a, unsigned v) { *(LAS unsigned*)a = v; }
__device__ __forceinline__ float ldsf(unsigned a) { return *(const LAS float*)a; }
__device__ __forceinline__ void stof(unsigned a, float v) { *(LAS float*)a = v; }
__device__ __forceinline__ v2u trd(unsigned a) { return __builtin_bit_cast(v2u, __builtin_amdgcn_ds_read_tr16_b64_v4i16((LAS s4*)a)); }
__device__ __forceinline__ v4u trd2(unsigned a0, unsigned a1) { const v2u x = trd(a0), y = trd(a1); return (v4u){x.x, x.y, y.x, y.y}; }
__device__ __forceinline__ f4 mfma32(v4u a, v4u b, f4 c) {
  return __builtin_amdgcn_mfma_f32_16x16x32_bf16(__builtin_bit_cast(b8, a), __builtin_bit_cast(b8, b), c, 0, 0, 0); }
__device__ __forceinline__ f4 mfma16(v2u a, v2u b, f4 c) {
  return __builtin_amdgcn_mfma_f32_16x16x16bf16_1k(__builtin_bit_cast(s4, a), __builtin_bit_cast(s4, b), c, 0, 0, 0); }
__device__ __forceinline__ f16v mfmaL(v4u a, v4u b, f16v c) {
  return __builtin_amdgcn_mfma_f32_32x32x16_bf16(__builtin_bit_cast(b8, a), __builtin_bit_cast(b8, b), c, 0, 0, 0); }
__device__ __forceinline__ unsigned pk(float lo, float hi) { return __builtin_bit_cast(unsigned, __builtin_convertvector((f2){lo, hi}, b2)); }
__device__ __forceinline__ float asf(unsigned u) { return __builtin_bit_cast(float, u); }
__device__ __forceinline__ unsigned asu(float f) { return __builtin_bit_cast(unsigned, f); }
__device__ __forceinline__ float ex2(float x) { return __builtin_amdgcn_exp2f(x); }
template <int CTRL> __device__ __forceinline__ float dpp(float x) {
  return __builtin_bit_cast(float, __builtin_amdgcn_mov_dpp(__builtin_bit_cast(int, x), CTRL, 0xf, 0xf, true)); }
constexpr int XOR1 = 0xB1, XOR2 = 0x4E, ROR4 = 0x124, ROR8 = 0x128;
__device__ __forceinline__ float x16max(float x) { auto s = __builtin_amdgcn_permlane16_swap(asu(x), asu(x), false, false); return fmaxf(asf(s[0]), asf(s[1])); }
__device__ __forceinline__ float x32max(float x) { auto s = __builtin_amdgcn_permlane32_swap(asu(x), asu(x), false, false); return fmaxf(asf(s[0]), asf(s[1])); }
__device__ __forceinline__ float x16sum(float x) { auto s = __builtin_amdgcn_permlane16_swap(asu(x), asu(x), false, false); return asf(s[0]) + asf(s[1]); }
__device__ __forceinline__ float x32sum(float x) { auto s = __builtin_amdgcn_permlane32_swap(asu(x), asu(x), false, false); return asf(s[0]) + asf(s[1]); }
__device__ __forceinline__ float row16max(float x) { x = fmaxf(x, dpp<XOR1>(x)); x = fmaxf(x, dpp<XOR2>(x)); x = fmaxf(x, dpp<ROR4>(x)); return fmaxf(x, dpp<ROR8>(x)); }
__device__ __forceinline__ float row16sum(float x) { x += dpp<XOR1>(x); x += dpp<XOR2>(x); x += dpp<ROR4>(x); return x + dpp<ROR8>(x); }

template <int IMM> __device__ __forceinline__ void dma(const void* sbase, unsigned voff, unsigned lds) {
  asm volatile("s_mov_b32 m0, %2\n\ts_nop 0\n\tglobal_load_lds_dwordx4 %0, %1 offset:%3 nt" :: "v"(voff), "s"(sbase), "s"(lds), "n"(IMM) : "memory");
}
template <int W> __device__ __forceinline__ void dma4(const void* sbase, unsigned v0, unsigned v1, unsigned v2, unsigned v3, unsigned lds) {
  asm volatile("s_mov_b32 m0, %5\n\ts_waitcnt vmcnt(%6)\n\tglobal_load_lds_dwordx4 %0, %4 nt\n\tglobal_load_lds_dwordx4 %1, %4 offset:1024 nt\n\t"
               "s_waitcnt vmcnt(%6)\n\tglobal_load_lds_dwordx4 %2, %4 offset:2048 nt\n\tglobal_load_lds_dwordx4 %3, %4 offset:3072 nt"
               :: "v"(v0), "v"(v1), "v"(v2), "v"(v3), "s"(sbase), "s"(lds), "n"(W - 2) : "memory");
}
__device__ __forceinline__ void dma_keep(const void* sbase, unsigned voff, unsigned lds) {
  asm volatile("s_mov_b32 m0, %2\n\ts_nop 0\n\tglobal_load_lds_dwordx4 %0, %1" :: "v"(voff), "s"(sbase), "s"(lds) : "memory");
}
__device__ __forceinline__ void touch(const void* sbase, unsigned voff, unsigned lds) {
  asm volatile("s_mov_b32 m0, %2\n\ts_nop 0\n\tglobal_load_lds_dword %0, %1 nt" :: "v"(voff), "s"(sbase), "s"(lds) : "memory");
}
template <int N> __device__ __forceinline__ void vmwait() { asm volatile("s_waitcnt vmcnt(%0)" :: "n"(N) : "memory"); }
__device__ __forceinline__ void bar() { asm volatile("s_waitcnt lgkmcnt(0)\n\ts_barrier" ::: "memory"); }
__device__ __forceinline__ void lgk0() { asm volatile("s_waitcnt lgkmcnt(0)" ::: "memory"); }
__device__ __forceinline__ void cfence() { asm volatile("" ::: "memory"); }
__device__ __forceinline__ unsigned opq(unsigned x) { asm volatile("" : "+v"(x)); return x; }
__device__ __forceinline__ unsigned lds_peek(unsigned a) { return __builtin_amdgcn_readfirstlane(*(volatile LAS unsigned*)a); }
template <int SL = 1> __device__ __forceinline__ void lds_wait_ge(unsigned a, unsigned want) {
  _Pragma("nounroll") for (int it = 0; it < SPIN_CAP * 8; ++it) { if (lds_peek(a) >= want) break; if (SL) __builtin_amdgcn_s_sleep(SL); }
  cfence();
}
typedef __amdgpu_buffer_rsrc_t rsrc_t;
constexpr int SC1 = 16;
__device__ __forceinline__ v4u gld128(rsrc_t r, unsigned off) { return __builtin_amdgcn_raw_buffer_load_b128(r, off, 0, SC1); }
__device__ __forceinline__ v2u gld64(rsrc_t r, unsigned off) { return __builtin_amdgcn_raw_buffer_load_b64(r, off, 0, SC1); }
__device__ __forceinline__ unsigned gld32(rsrc_t r, unsigned off) { return __builtin_amdgcn_raw_buffer_load_b32(r, off, 0, SC1); }
__device__ __forceinline__ void gst128(rsrc_t r, unsigned off, v4u v) { __builtin_amdgcn_raw_buffer_store_b128(v, r, off, 0, SC1); }
__device__ __forceinline__ void gst64(rsrc_t r, unsigned off, v2u v) { __builtin_amdgcn_raw_buffer_store_b64(v, r, off, 0, SC1); }
__device__ __forceinline__ void gst32(rsrc_t r, unsigned off, unsigned v) { __builtin_amdgcn_raw_buffer_store_b32(v, r, off, 0, SC1); }
__device__ __forceinline__ void flag_line(rsrc_t r, unsigned off, int lane, unsigned tag) { if (lane < 8) gst128(r, off + lane * 16, (v4u){tag, tag, tag, tag}); }
__device__ __forceinline__ bool tagok(v4u v, unsigned tag) { return v.y == tag && v.w == tag; }
template <int N, int GAP, class F> __device__ __forceinline__ void sweep2(v4u (&v)[N], rsrc_t rs, unsigned tag, F off) {
  v4u a[N], b[N];
#pragma unroll
  for (int k = 0; k < N; ++k) a[k] = gld128(rs, off(k));
  __builtin_amdgcn_s_sleep(GAP);
  _Pragma("nounroll") for (int it = 0; it < SPIN_CAP; ++it) {
    cfence();
#pragma unroll
    for (int k = 0; k < N; ++k) b[k] = gld128(rs, off(k));
    cfence();
    bool ok = true;
#pragma unroll
    for (int k = 0; k < N; ++k) { ok &= tagok(a[k], tag); v[k] = a[k]; }
    if (__all(ok)) break;
#pragma unroll
    for (int k = 0; k < N; ++k) a[k] = gld128(rs, off(k));
    cfence();
    ok = true;
#pragma unroll
    for (int k = 0; k < N; ++k) { ok &= tagok(b[k], tag); v[k] = b[k]; }
    if (__all(ok)) break;
  }
}
template <int N, int SL, bool MASK, class F> __device__ __forceinline__ void sweep(v4u (&v)[N], rsrc_t rs, unsigned tag, F off) {
  if (MASK) {
#pragma unroll
    for (int k = 0; k < N; ++k) v[k] = (v4u){0u, ~tag, 0u, ~tag};
  }
  _Pragma("nounroll") for (int it = 0; it < SPIN_CAP; ++it) {
    cfence();
    v4u t[N];
#pragma unroll
    for (int k = 0; k < N; ++k) t[k] = gld128(rs, MASK && tagok(v[k], tag) ? 0xfffffff0u : off(k));
    bool ok = true;
#pragma unroll
    for (int k = 0; k < N; ++k) { if (!MASK || !tagok(v[k], tag)) v[k] = t[k]; ok &= tagok(v[k], tag); }
    if (__all(ok)) break;
    if (SL) __builtin_amdgcn_s_sleep(SL);
  }
}

#define STAMP(i) do { unsigned long long t_; asm volatile("s_memrealtime %0\n\ts_waitcnt lgkmcnt(0)" : "=s"(t_) :: "memory"); tsv[i] = (unsigned)t_; } while (0)
#define STAMP_DECL unsigned tsv[16] = {0,0,0,0,0,0,0,0,0,0,0,0,0,0,0,0}
#define STAMP_OUT() do { if (lane == 0) gst128(rs2, WS_TS + (blockIdx.x * 8 + wave) * 16, (v4u){tsv[0], tag, tsv[PROBE], tag}); \
  if (blockIdx.x == 0 && wave == 0) { unsigned mn = 0xffffffffu; float mx = 0.f, sm = 0.f, cnt = 0.f; unsigned ref = tsv[0] - 1000u; \
    for (int i_ = 0; i_ < 32; ++i_) { v4u v_; for (int it = 0; it < SPIN_CAP; ++it) { cfence(); v_ = gld128(rs2, WS_TS + (i_ * 64 + lane) * 16); if (__all(tagok(v_, tag))) break; } \
      mn = min(mn, v_.x - ref); const bool role_ = PROLE == 3 || (PROLE == 1 ? (i_ < 8) : PROLE == 2 ? (i_ >= 8 && i_ < 12) : (i_ >= 12)); if (v_.z != 0u && role_) { const float d_ = (float)(v_.z - ref); mx = fmaxf(mx, d_); sm += d_; cnt += 1.f; } } \
    float mnf = -(float)mn; mnf = -x32max(x16max(row16max(mnf))); mx = x32max(x16max(row16max(mx))); sm = x32sum(x16sum(row16sum(sm))); cnt = x32sum(x16sum(row16sum(cnt))); \
    const float X = PZERO ? 0.f : (PSTAT ? sm / cnt : mx) - mnf; const unsigned until = tsv[15] + (unsigned)(3000.f + 20.f * X); \
    for (int it = 0; it < 100000000; ++it) { unsigned long long t_; asm volatile("s_memrealtime %0\n\ts_waitcnt lgkmcnt(0)" : "=s"(t_) :: "memory"); if ((int)((unsigned)t_ - __builtin_amdgcn_readfirstlane(until)) >= 0) break; } } } while (0)

__device__ __forceinline__ void gather_qb(unsigned img, int h, int lane, rsrc_t rs, unsigned tag) {
  v4u qg[4];
  const unsigned off = WS_QG + (h * 128 + 2 * lane) * 8;
  _Pragma("nounroll") for (int it = 0; it < SPIN_CAP; ++it) {
    cfence();
    bool ok = true;
#pragma unroll
    for (int k = 0; k < 4; ++k) { qg[k] = gld128(rs, off + (k & 1) * 65536 + (k >> 1) * 32768); ok &= tagok(qg[k], tag); }
    if (__all(ok)) break;
    __builtin_amdgcn_s_sleep(1);
  }
  sto32(img + lane * 4, pk(asf(qg[0].x) + asf(qg[1].x), asf(qg[0].z) + asf(qg[1].z)));
  sto32(img + 256 + lane * 4, pk(asf(qg[2].x) + asf(qg[3].x), asf(qg[2].z) + asf(qg[3].z)));
}
__device__ __forceinline__ void lds_inc(unsigned a) { __hip_atomic_fetch_add((LAS unsigned*)a, 1u, __ATOMIC_RELAXED, __HIP_MEMORY_SCOPE_WORKGROUP); }

__device__ __forceinline__ void out_frags(unsigned ou, int w, int lane, v4u (&wf)[4]) {
  const int g = lane >> 4, q = (lane & 15) >> 2, p = lane & 3, f3 = q | ((g & 1) << 2);
#pragma unroll
  for (int tt = 0; tt < 4; ++tt) {
    const unsigned a0 = ou + (8 * g + q) * 1024 + (((2 * (4 * w + tt) + (p >> 1)) ^ (f3 << 1)) << 4) + (p & 1) * 8;
    wf[tt] = trd2(a0, a0 + 4096);
  }
}

}

#define KALIGN 4096
__global__ void __launch_bounds__(512, 2) __attribute__((aligned(KALIGN))) fused_decode(
    const char* __restrict__ Ax, const char* __restrict__ Kc, const char* __restrict__ Vc, const char* __restrict__ Wq, const char* __restrict__ Wo,
    const char* __restrict__ Bx, const char* __restrict__ Ckv, const char* __restrict__ Bwq, const char* __restrict__ Wuk, const char* __restrict__ Wuv,
    const char* __restrict__ Bwo, unsigned short* __restrict__ out, char* __restrict__ ws) {
  const int bid = blockIdx.x, tid = threadIdx.x, lane = tid & 63, wave = __builtin_amdgcn_readfirstlane(tid >> 6);
  const int g = lane >> 4, c16 = lane & 15;
  STAMP_DECL;
  STAMP(0);
  const int iA = bid - (NB_CU + NT_CU);
  const bool isA = iA >= 0, isB = bid < NB_CU;
  const int ty = iA >> 6;
  int dA = 95 - bid, dU = 223 - bid, dB = bid - 64, dQ = 159 - bid, dW = 2 - wave;
  asm("" : "+s"(dA), "+s"(dU), "+s"(dB), "+s"(dQ), "+s"(dW));
  const int mA = dA >> 31, mU = dU >> 31, mB = dB >> 31, mP = mA & ~mU, sh = mP & 1;
  const int hh = (bid >> sh) & 31;
  const unsigned row0 = (mA & ((((bid | mU) & 1) << 8) + 64 * wave - 64)) | (~mA & (32 * wave));
  const unsigned l0 = (mA & (SCR - 16384 + wave * 16384 - ((dW >> 31) & (SCR + 32768 - A_QU2)))) | (~mA & (wave * 8192));
  const unsigned karg = 0x40 - (mP & (8 + ((dQ >> 31) & 32))), more = mA;
  if ((unsigned)(wave + mA + (mB & 8)) < (unsigned)(8 + (mA & -4))) {
    const unsigned b0 = (row0 + g) * 8192 + hh * 256 + ((c16 ^ (g << sh)) << 4), x1 = ~mP & 64;
    unsigned v0 = b0, v1 = (b0 ^ x1) + 32768, v2 = (b0 ^ 128) + 65536, v3 = (b0 ^ x1 ^ 128) + 98304;
    if (DLYP && karg == 0x18) __builtin_amdgcn_s_sleep(DLYP);
    unsigned long long wp;
#define D4 "global_load_lds_dwordx4 %1, %0 nt\n\ts_add_u32 m0, m0, 0x400\n\tv_add_u32 %1, 0x20000, %1\n\t" \
           "global_load_lds_dwordx4 %2, %0 nt\n\ts_add_u32 m0, m0, 0x400\n\tv_add_u32 %2, 0x20000, %2\n\t" \
           "global_load_lds_dwordx4 %3, %0 nt\n\ts_add_u32 m0, m0, 0x400\n\tv_add_u32 %3, 0x20000, %3\n\t" \
           "global_load_lds_dwordx4 %4, %0 nt\n\ts_add_u32 m0, m0, 0x400\n\tv_add_u32 %4, 0x20000, %4\n\t"
    asm volatile("s_load_dwordx2 %0, %5, %6\n\ts_mov_b32 m0, %7\n\ts_waitcnt lgkmcnt(0)\n\t" D4 D4
                 "s_cmp_eq_u32 %8, 0\n\ts_cbranch_scc1 1f\n\t" D4 D4 "1:"
                 : "=&s"(wp), "+v"(v0), "+v"(v1), "+v"(v2), "+v"(v3)
                 : "s"(__builtin_amdgcn_kernarg_segment_ptr()), "s"(karg), "s"(l0), "s"(more) : "memory", "scc");
#undef D4
  }
  {
    unsigned long long pc;
    asm volatile("s_getpc_b64 %0" : "=s"(pc));
    const int kb = ((bid >> 3) + 4 * wave) & 31;
    if (CODE_KB) dma_keep((const char*)((pc & ~1023ull) + (unsigned)((kb < CODE_KB ? kb : CODE_KB - 1) * 1024)), lane * 16, isA ? A_DUMMY : B_DUMMY);
  }
  const rsrc_t rs = __builtin_amdgcn_make_buffer_rsrc(ws, 0, WS_END, 0x00020000);
  const rsrc_t rs2 = __builtin_amdgcn_make_buffer_rsrc(ws, 0, 16u << 20, 0x00020000);
  asm volatile("" :: "s"(Ax), "s"(Kc), "s"(Vc), "s"(Wq), "s"(Wo), "s"(Bx), "s"(Ckv), "s"(Bwq), "s"(Wuk), "s"(Wuv), "s"(Bwo), "s"(out), "s"(ws));
  unsigned tag;
#define GET_TAG() do { asm volatile("s_load_dword %0, %1, 0x0\n\ts_waitcnt lgkmcnt(0)" : "=s"(tag) : "s"(ws) : "memory"); tag += 1u; } while (0)
  const char* cp = Ckv + ((size_t)(bid >> 5) * 8192 + 256 * (bid & 31) + 8 * wave) * 1024;
  const int brot = BROT ? (bid >> BROT) : 0;
  const unsigned vc0 = (lane ^ ((wave & 1) ? 3 : 0)) << 4, vc1 = (lane ^ ((wave & 1) ? 1 : 2)) << 4;
#define LD_C(rho, u) dma<((u) & 3) * 1024>(cp + (size_t)((((rho) + brot) & 3) * 65536) + ((u) >> 2) * 4096, (((u) >> 2) ? vc1 : vc0) ^ (((u) & 3) << 6), ((rho) & 1) * 65536 + wave * 8192 + ((u) >> 2) * 4096)
  if (isA) {
    if (wave == 1 && ty < 2) dma<0>((ty ? Ax : Bx) + 512 * (bid & 1), (lane >> 5) * 1024 + (lane & 31) * 16, A_XIMG);
    if (tid < 32) sto32(A_MISC + tid * 4, 0u);
    bar();
  } else if (isB) {
    __builtin_amdgcn_s_sleep(DLYB);
    LD_C(0, 0); LD_C(0, 1); LD_C(0, 2); LD_C(0, 3); LD_C(0, 4); LD_C(0, 5);
  }

  if (isA) {
    const int split = iA >> 4, pair = (iA + split) & 15, b = pair >> 3, kvh = pair & 7;
    constexpr int NB2 = (512 - 4 * NB0 - 4 * NB1) / 2;
    const int nblk = ty == 0 ? NB0 : ty == 1 ? NB1 : NB2, blk0 = (ty == 0 ? 0 : ty == 1 ? 4 * NB0 : 4 * (NB0 + NB1)) + (split & 3) * nblk;
    if (wave == 0 || wave == 7) {
      __builtin_amdgcn_s_setprio(3);
      static_assert(NSPLIT_A == 10 && W1 >= 4 && W1 % 4 == 0 && W2 % 4 == 0 && W1 <= W2 && W2 <= 24 && J1 <= NB0 && J1 <= NB1 && J1 <= NB2, "window");
      const bool isv = wave == 7;
      int pub = 0, j = 0;
      unsigned sl = 0;
      int pos = (pair * ROT) % nblk;
      const char* gp = (isv ? Vc : Kc) + ((size_t)pair * 8192 + (blk0 + pos) * 16) * 256;
      const unsigned v0 = opq(g * 256 + ((c16 ^ (isv ? g << 1 : g)) << 4));
      const unsigned v1 = opq(v0 ^ (isv ? 128 : 64)), v2 = opq(v0 ^ (isv ? 0 : 128)), v3 = opq(v0 ^ (isv ? 128 : 192));
      const unsigned lo = isv ? 4096 : 0, la = opq(lane ? A_MISC + 1536 + lane * 4 : isv ? A_LANDV : A_LANDK);
      unsigned dn = 0;
#define PUBLISH(val) do { pub = max(pub, (val)); *(volatile LAS unsigned*)la = (unsigned)pub; } while (0)
#define GATE(addr, need) do { if (lds_peek(addr) < (need)) { vmwait<0>(); PUBLISH(j); lds_wait_ge(addr, need); } } while (0)
#define PART(W) do { \
        if (j >= NSLOT) { if ((unsigned)__builtin_amdgcn_readfirstlane(dn) < (unsigned)(j - NSLOT + 1)) GATE(A_DONE + sl, (unsigned)(j - NSLOT + 1)); } \
        else if (j == 10) GATE(A_QUSED, 4u); \
        dma4<W>(gp, v0, v1, v2, v3, sl * 2048 + lo); \
        ++j; gp += 4096; if (ROT && ++pos == nblk) gp -= nblk * 4096; sl = sl == 4 * (NSLOT - 1) ? 0 : sl + 4; \
        PUBLISH(j - W / 4); dn = *(volatile LAS unsigned*)(A_DONE + sl); } while (0)
      STAMP(9);
      GET_TAG();
      if (UE && ty == 2) {
        lds_wait_ge(A_OUL2, 1u);
#pragma nounroll
        while (j < UE) PART(W2);
      }
      lds_wait_ge(A_QCNT, 4u);
      if (VDL && isv) lds_wait_ge(A_LANDK, (unsigned)VDL);
      STAMP(10);
#pragma nounroll
      while (j < J1) PART(W1);
      STAMP(11);
#pragma nounroll
      while (j < nblk - (isv ? VTAIL : 0)) PART(W2);
      if (isv) {
#pragma nounroll
        while (j < nblk) PART(44);
      }
      if (ty < 2 && !isv) {
        const char* op = Wo + (size_t)iA * 32768;
        const unsigned o0 = lane << 4, o1 = o0 ^ 32, o2 = o0 ^ 64, o3 = o0 ^ 96;
        vmwait<20>(); PUBLISH(j - 5); vmwait<16>(); PUBLISH(j - 4); vmwait<12>(); PUBLISH(j - 3); vmwait<8>(); PUBLISH(j - 2); vmwait<4>(); PUBLISH(j - 1); vmwait<0>(); PUBLISH(j);
        if (ODL) lds_wait_ge(A_LANDV, (unsigned)(nblk - ODL));
#pragma unroll
        for (int k = 0; k < 8; ++k) {
          const unsigned x = ((k >> 1) & 1) << 7;
          dma4<62>(op + k * 4096, o0 ^ x, o1 ^ x, o2 ^ x, o3 ^ x, SCR + k * 4096);
        }
      } else {
        vmwait<40>(); PUBLISH(j - 10); vmwait<36>(); PUBLISH(j - 9); vmwait<32>(); PUBLISH(j - 8);
        vmwait<28>(); PUBLISH(j - 7); vmwait<24>(); PUBLISH(j - 6); vmwait<20>(); PUBLISH(j - 5); vmwait<16>(); PUBLISH(j - 4);
        vmwait<12>(); PUBLISH(j - 3); vmwait<8>(); PUBLISH(j - 2); vmwait<4>(); PUBLISH(j - 1); vmwait<0>(); PUBLISH(j);
      }
      __builtin_amdgcn_s_setprio(0);
      STAMP(5);
#undef PUBLISH
#undef GATE
#undef PART
    } else {
      if (wave == 6) {
        const size_t ko = ((size_t)pair * 8192 + blk0 * 16) * 256;
        if (!TLBE && lane < TLBN) { touch(Kc + ko, lane * (unsigned)(nblk * 4096 / TLBN), A_DUMMY); touch(Vc + ko, lane * (unsigned)(nblk * 4096 / TLBN), A_DUMMY); }
        if (lane < 1 && ty < 2) touch(Wo + (size_t)iA * 32768, 0, A_DUMMY);
        if (lane < WS_END / TLBS) touch(ws, lane * TLBS + (bid & 31) * 128, A_DUMMY);
      }
      GET_TAG();
      vmwait<0>();
      if (wave <= 4) {
        const int w1 = wave - 1;
        if (ty < 2) {
          if (lane == 0) lds_inc(A_QCNT);
          const int q = c16 >> 2, p = lane & 3, f3 = q | ((g & 1) << 2);
          const unsigned a0 = (8 * g + q) * 256 + (((4 * w1 + (p >> 1)) ^ (f3 << 1)) << 4) + (p & 1) * 8;
          const unsigned aa[2] = {opq(SCR + a0), opq(A_QU2 + a0)};
          lds_wait_ge(A_QCNT, 4u);
          STAMP(1);
          v4u w0[8], w1f[8], xf[8];
#pragma unroll
          for (int s = 0; s < 8; ++s) {
            const unsigned a = aa[s >> 2] + (s & 3) * 8192;
            w0[s] = trd2(a, a + 1024); w1f[s] = trd2((a ^ 32), (a ^ 32) + 1024);
            xf[s] = lds128(A_XIMG + (c16 & 1) * 512 + g * 16 + s * 64);
          }
          __builtin_amdgcn_sched_barrier(0);
          f4 d0 = {0.f, 0.f, 0.f, 0.f}, d1 = d0;
#pragma unroll
          for (int s = 0; s < 8; ++s) { d0 = mfma32(w0[s], xf[s], d0); d1 = mfma32(w1f[s], xf[s], d1); }
          const int pr = (c16 >> 1) & 1, nb = (c16 >> 2) & 1;
          const f4 e = nb ? d1 : d0;
          if (c16 < 8) gst128(rs, WS_QG + (((ty * 2 + (bid & 1)) * 2 + (c16 & 1)) * 4096 + hh * 128 + 32 * w1 + 16 * nb + 4 * g + 2 * pr) * 8,
                              (v4u){asu(pr ? e[2] : e[0]), tag, asu(pr ? e[3] : e[1]), tag});
          lgk0();
        } else {
          STAMP(1);
          if (w1 == 3 && lane == 0) *(volatile LAS unsigned*)A_OUL2 = 1u;
          const unsigned a0 = opq((w1 < 2 ? SCR : A_QU2 - 32768) + w1 * 16384 + c16 * 256 + ((g ^ c16) << 4));
          v4u uf[16];
#pragma unroll
          for (int i = 0; i < 16; ++i) uf[i] = lds128((a0 ^ ((i & 3) << 6)) + (i >> 2) * 4096);
          lds_wait_ge(A_QB, 1u);
          v4u qb[4];
#pragma unroll
          for (int s = 0; s < 4; ++s) qb[s] = lds128(A_QBI + (c16 & 1) * 256 + s * 64 + g * 16);
          __builtin_amdgcn_sched_barrier(0);
          f4 d[4];
#pragma unroll
          for (int rb = 0; rb < 4; ++rb) d[rb] = (f4){0.f, 0.f, 0.f, 0.f};
#pragma unroll
          for (int s = 0; s < 4; ++s)
#pragma unroll
            for (int rb = 0; rb < 4; ++rb) d[rb] = mfma32(uf[4 * rb + s], qb[s], d[rb]);
          const int rb = (c16 >> 1) & 3;
          const f4 e = rb == 0 ? d[0] : rb == 1 ? d[1] : rb == 2 ? d[2] : d[3];
          if (c16 < 8) sto64(A_QAS + w1 * 256 + (c16 & 1) * 128 + (16 * rb + 4 * g) * 2, (v2u){pk(e[0], e[1]), pk(e[2], e[3])});
          lgk0();
          if (lane < 16) gst128(rs, WS_QAB + ((lane >> 3) * 32 + hh) * 1024 + 512 + w1 * 128 + (lane & 7) * 16, lds128(A_QAS + w1 * 256 + lane * 16));
          vmwait<0>();
        }
        unsigned old = 0;
        if (lane == 0) old = __hip_atomic_fetch_add((LAS unsigned*)A_QUSED, 1u, __ATOMIC_RELAXED, __HIP_MEMORY_SCOPE_WORKGROUP);
        if (ty == 2 && __builtin_amdgcn_readfirstlane(old) == 3u) {
          flag_line(rs, WS_FQ + (32 + hh) * FS, lane, tag);
          cfence();
          if (lane == 0) *(volatile LAS unsigned*)A_QCNT = 4u;
        }
        STAMP(2);
      }
      if (wave == 5) {
        if (ty == 2) {
          lds_wait_ge(A_OUL2, 1u);
          gather_qb(A_QBI, hh, lane, rs, tag);
          lgk0();
          if (lane == 0) *(volatile LAS unsigned*)A_QB = 1u;
          STAMP(1);
        }
        lds_wait_ge(A_LANDK, ty == 2 ? 1u : (unsigned)QGO);
        v4u v[8];
        const unsigned off = WS_QG + 131072 + (b * 4096 + kvh * 512 + 2 * lane) * 8;
        _Pragma("nounroll") for (int it = 0; it < SPIN_CAP; ++it) {
          cfence();
          bool ok = true;
#pragma unroll
          for (int k = 0; k < 8; ++k) { v[k] = gld128(rs, off + (k & 1) * 65536 + (k >> 1) * 1024); ok &= tagok(v[k], tag); }
          if (__all(ok)) break;
          __builtin_amdgcn_s_sleep(4);
        }
#pragma unroll
        for (int k = 0; k < 4; ++k) sto32(A_QIMG + k * 256 + lane * 4, pk(asf(v[2 * k].x) + asf(v[2 * k + 1].x), asf(v[2 * k].z) + asf(v[2 * k + 1].z)));
        lgk0();
        if (lane == 0) *(volatile LAS unsigned*)A_QRDY = 1u;
      }
      lds_wait_ge(A_QRDY, 1u);
      STAMP(3);
      v4u qf[4];
#pragma unroll
      for (int s = 0; s < 4; ++s) qf[s] = lds128(A_QIMG + (c16 & 3) * 256 + s * 64 + g * 16);
      const unsigned ko = c16 * 256 + ((g ^ c16) << 4);
      const int vr = 4 * g + (c16 >> 2);
      const unsigned vo = 4096 + vr * 256 + ((((lane & 3) >> 1) ^ ((vr & 7) << 1)) << 4) + (lane & 1) * 8;
      float m = -INFINITY, l = 0.f;
      f4 acc[8];
#pragma unroll
      for (int t = 0; t < 8; ++t) acc[t] = (f4){0.f, 0.f, 0.f, 0.f};
      int slot = wave - 1;
      for (int j = wave - 1; j < nblk; j += NCONS) {
        const unsigned sb = slot * 8192;
        lds_wait_ge(A_LANDK, (unsigned)(j + 1));
        v4u kf[4];
#pragma unroll
        for (int s = 0; s < 4; ++s) kf[s] = lds128(sb + (ko ^ (s << 6)));
        __builtin_amdgcn_sched_barrier(0);
        f4 s4_ = {0.f, 0.f, 0.f, 0.f};
#pragma unroll
        for (int s = 0; s < 4; ++s) s4_ = mfma32(kf[s], qf[s], s4_);
        float mx = fmaxf(fmaxf(s4_[0], s4_[1]), fmaxf(s4_[2], s4_[3]));
        mx = x32max(x16max(mx));
        const float mn = fmaxf(m, mx), al = ex2((m - mn) * SC), mc = -mn * SC;
        const float p0 = ex2(fmaf(s4_[0], SC, mc)), p1 = ex2(fmaf(s4_[1], SC, mc)), p2 = ex2(fmaf(s4_[2], SC, mc)), p3 = ex2(fmaf(s4_[3], SC, mc));
        l = fmaf(l, al, (p0 + p1) + (p2 + p3));
        m = mn;
        v2u pb = {pk(p0, p1), pk(p2, p3)};
        unsigned va[8];
#pragma unroll
        for (int t = 0; t < 8; ++t) va[t] = sb + (vo ^ (t << 5));
        if (!__all(al == 1.f)) {
#pragma unroll
          for (int t = 0; t < 8; ++t) acc[t] *= al;
        }
        asm volatile("" : "+v"(pb), "+v"(l), "+v"(acc[0]), "+v"(acc[1]), "+v"(acc[2]), "+v"(acc[3]), "+v"(acc[4]), "+v"(acc[5]), "+v"(acc[6]), "+v"(acc[7]),
                     "+v"(va[0]), "+v"(va[1]), "+v"(va[2]), "+v"(va[3]), "+v"(va[4]), "+v"(va[5]), "+v"(va[6]), "+v"(va[7]));
        lds_wait_ge<0>(A_LANDV, (unsigned)(j + 1));
        v2u vf[8];
#pragma unroll
        for (int t = 0; t < 8; ++t) vf[t] = trd(va[t]);
        lgk0();
        if (lane == 0) *(volatile LAS unsigned*)(A_DONE + slot * 4) = (unsigned)(j + 1);
#pragma unroll
        for (int t = 0; t < 8; ++t) acc[t] = mfma16(vf[t], pb, acc[t]);
        slot += NCONS; if (slot >= NSLOT) slot -= NSLOT;
      }
      STAMP(4);
      l = x32sum(x16sum(l));
      if (c16 < 4) {
#pragma unroll
        for (int t = 0; t < 8; ++t) sto128(A_ST + (wave - 1) * 2048 + c16 * 512 + ((t ^ c16) * 64) + g * 16, __builtin_bit_cast(v4u, acc[t]));
        if (g == 0) sto64(A_ML + (c16 * 8 + wave - 1) * 8, (v2u){asu(m * SC), asu(l)});
      }
    }
    bar();
    if (wave >= 3 && wave <= 6) {
      const int hq = wave - 3;
      v2u ml[NCONS];
      f2 ow[NCONS];
      const unsigned sa = opq(A_ST + hq * 512 + ((lane * 8) ^ (hq << 6))), ma = opq(A_ML + hq * 64);
#pragma unroll
      for (int c = 0; c < NCONS; ++c) { ml[c] = *(const LAS v2u*)(ma + c * 8); ow[c] = *(const LAS f2*)(sa + c * 2048); }
      float M = asf(ml[0].x);
#pragma unroll
      for (int c = 1; c < NCONS; ++c) M = fmaxf(M, asf(ml[c].x));
      float L = 0.f, o0 = 0.f, o1 = 0.f;
#pragma unroll
      for (int c = 0; c < NCONS; ++c) { const float wt = ex2(asf(ml[c].x) - M); L = fmaf(wt, asf(ml[c].y), L); o0 = fmaf(wt, ow[c].x, o0); o1 = fmaf(wt, ow[c].y, o1); }
      const int rec = (b * 32 + kvh * 4 + hq) * NSPLIT_A + split;
      gst128(rs, WS_PAO + rec * 1024 + lane * 16, (v4u){asu(o0), tag, asu(o1), tag});
      if (lane < 8) gst128(rs, WS_PAM + rec * 128 + lane * 16, (v4u){asu(M), tag, asu(L), tag});
    }
    STAMP(6);
    if (ty < 2) {
      const int oh = iA >> 2, odq = iA & 3;
      if (wave == 0) { vmwait<0>(); if (lane == 0) lds_inc(A_OUL); }
      lds_wait_ge(A_OUL, 1u);
      v4u wf[4];
      out_frags(SCR, wave, lane, wf);
      const int tt = (c16 >> 1) & 3, pr = c16 >> 3, n = 64 * wave + 16 * tt + 4 * g + 2 * pr;
      unsigned fo = WS_F + ((((n >> 3) * 128 + iA) * 16) + (c16 & 1) * 8 + (n & 7)) * 8, oa = A_OIMG + (c16 & 1) * 64 + g * 16;
      asm volatile("" : "+v"(fo), "+v"(oa));
      if (wave == 1) {
        const int rec = ((lane >> 5) * 32 + oh) * NSPLIT_A + (g & 1) * 5;
        v4u vm[10];
        const unsigned o0_ = EXCL ? WS_PAO + rec * 1536 + odq * 384 + c16 * 16 : WS_PAO + rec * 1024 + (32 * odq + 2 * c16) * 8;
        const unsigned o1_ = EXCL ? WS_PAO + rec * 1536 + odq * 384 + 256 : WS_PAM + rec * 128 + odq * 16;
        if (G2) sweep2<10, G2>(vm, rs, tag, [&](int k) { return k < 5 ? o0_ + k * (EXCL ? 1536 : 1024) : o1_ + (k - 5) * (EXCL ? 1536 : 128); });
        else sweep<10, GSL, GMASK>(vm, rs, tag, [&](int k) { return k < 5 ? o0_ + k * (EXCL ? 1536 : 1024) : o1_ + (k - 5) * (EXCL ? 1536 : 128); });
        const v4u* v = vm; const v4u* ml = vm + 5;
        float M = asf(ml[0].x);
#pragma unroll
        for (int i = 1; i < 5; ++i) M = fmaxf(M, asf(ml[i].x));
        M = x16max(M);
        float L = 0.f, o0 = 0.f, o1 = 0.f;
#pragma unroll
        for (int i = 0; i < 5; ++i) { const float wt = ex2(asf(ml[i].x) - M); L = fmaf(wt, asf(ml[i].z), L); o0 = fmaf(wt, asf(v[i].x), o0); o1 = fmaf(wt, asf(v[i].z), o1); }
        const float inv = 1.f / x16sum(L);
        o0 = x16sum(o0); o1 = x16sum(o1);
        if (!(g & 1)) sto32(A_OIMG + (lane >> 5) * 64 + c16 * 4, pk(o0 * inv, o1 * inv));
      }
      STAMP(7);
      bar();
      const v4u ob = lds128(oa);
      f4 d[4];
#pragma unroll
      for (int t = 0; t < 4; ++t) d[t] = mfma32(wf[t], ob, (f4){0.f, 0.f, 0.f, 0.f});
      const f4 e = tt == 0 ? d[0] : tt == 1 ? d[1] : tt == 2 ? d[2] : d[3];
      gst128(rs, fo, (v4u){asu(pr ? e[2] : e[0]), tag, asu(pr ? e[3] : e[1]), tag});
      STAMP(8);
    }
    STAMP_OUT();
  } else if (isB) {
    const int b = bid >> 5, sp = bid & 31, w = wave, lane_ = lane;
    const unsigned vl = lane << 4;
#define LD_S(S, rho, u) asm volatile("global_load_dwordx4 %0, %1, %2 offset:%3 nt" : "=v"(S[u]) : "v"(vl), "s"(cp + (size_t)((((rho) + brot) & 3) * 65536) + ((u) >> 2) * 4096), "n"(((u) & 3) * 1024) : "memory")
#define ST_S(S, rho) do { unsigned a0_ = ((rho) & 1) * 65536 + w * 8192 + vc0, a1_ = ((rho) & 1) * 65536 + w * 8192 + vc1; asm volatile("" : "+v"(a0_), "+v"(a1_)); \
      _Pragma("unroll") for (int u_ = 0; u_ < 8; ++u_) sto128(u_ * 1024 + (((u_ >> 2) ? a1_ : a0_) ^ ((u_ & 3) << 6)), S[u_]); } while (0)
#define PB() vmwait<WB - 2>()
    if (BPRIO && w >= 4) __builtin_amdgcn_s_setprio(1);
    v4u S2[8], S3[8];
    if (w == 7) {
      if (TLBB && lane < TLBB) touch(Ckv + (size_t)bid * 262144, 65536 + lane * (196608 / (TLBB ? TLBB : 1)), B_DUMMY);
      if (lane < WS_END / TLBS) touch(ws, lane * TLBS + (bid & 31) * 128, B_DUMMY);
      if (lane < 1) touch(out, 0, B_DUMMY);
    }
    GET_TAG();
    PB(); LD_C(0, 6); LD_C(0, 7);
    STAMP(1);
    PB(); LD_C(1, 0); LD_C(1, 1);
    PB(); LD_C(1, 2); LD_C(1, 3);
    PB(); LD_C(1, 4); LD_C(1, 5);
    PB(); LD_C(1, 6); LD_C(1, 7);
    STAMP(2);
    PB(); LD_S(S2, 2, 0); LD_S(S2, 2, 1);
    PB(); LD_S(S2, 2, 2); LD_S(S2, 2, 3);
    PB(); LD_S(S2, 2, 4); LD_S(S2, 2, 5);
    PB(); LD_S(S2, 2, 6); LD_S(S2, 2, 7);
#pragma unroll
    for (int u = 0; u < NS3; u += 2) { PB(); LD_S(S3, 3, u); LD_S(S3, 3, u + 1); }
    STAMP(3);
    if (w == 0) {
      _Pragma("nounroll") for (int it = 0; it < SPIN_CAP; ++it) { cfence(); if (__all(gld32(rs, WS_FQ + lane * FS) == tag)) break; __builtin_amdgcn_s_sleep(PSL); }
    }
    bar();
    STAMP(4);
    {
      v4u t[4];
      const char* qa = ws + WS_QAB + (b * 32 + 4 * w) * 1024;
#pragma unroll
      for (int i = 0; i < 4; ++i) asm volatile("global_load_dwordx4 %0, %1, %2 offset:%3 sc1" : "=v"(t[i]) : "v"(vl), "s"(qa), "n"(i * 1024) : "memory");
      constexpr int NA = NS3 < 8 ? 2 : 0, NC = 8 - NS3 - NA < 4 ? 8 - NS3 - NA : 4;
#pragma unroll
      for (int u = NS3; u < NS3 + NA; ++u) LD_S(S3, 3, u);
      asm volatile("s_waitcnt vmcnt(%12)" : "+v"(t[0]), "+v"(t[1]), "+v"(t[2]), "+v"(t[3]), "+v"(S2[0]), "+v"(S2[1]), "+v"(S2[2]), "+v"(S2[3]),
                   "+v"(S2[4]), "+v"(S2[5]), "+v"(S2[6]), "+v"(S2[7]) : "n"(NA) : "memory");
#pragma unroll
      for (int u = NS3 + NA; u < NS3 + NA + NC; ++u) LD_S(S3, 3, u);
#pragma unroll
      for (int i = 0; i < 4; ++i) sto128(SCR + (4 * w + i) * 1024 + ((lane ^ ((4 * w + i) & 15)) << 4), t[i]);
    }
    bar();
    const int kb4 = w & 3, hg = w >> 2;
    v4u qf[16];
#pragma unroll
    for (int s = 0; s < 16; ++s) qf[s] = lds128(SCR + (16 * hg + c16) * 1024 + ((((4 * s) & 15) ^ g ^ c16) << 4) + (s >> 2) * 256);
    STAMP(5);
    const unsigned fk = ((c16 & 3) << 2) | ((0x78u >> (2 * (c16 >> 2))) & 3u);
    const unsigned ka = (16 * kb4 + c16) * 1024 + ((g ^ fk) << 4);
    const int h2 = lane >> 5, blk = g & 1, qq = c16 >> 2, pp = lane & 3;
    const unsigned ft0 = (qq << 2) | (h2 ? 3u : 0u), ft1 = (qq << 2) | (h2 ? 1u : 2u);
    const unsigned cbase = 8 * w + 2 * blk + (pp >> 1);
    const unsigned va00 = (8 * h2 + qq) * 1024 + ((cbase ^ ft0) << 4) + (pp & 1) * 8, va01 = (8 * h2 + 4 + qq) * 1024 + ((cbase ^ ft1) << 4) + (pp & 1) * 8;
    const unsigned mxa = opq(B_MX + (16 * hg + c16) * 4), pwa = opq(B_P + kb4 * 1536 + (16 * hg + c16) * 48 + g * 8);
    const unsigned pra = opq(B_P + (lane & 31) * 48 + h2 * 16), ala = opq(B_AL + (lane & 31) * 4);
    float m = -INFINITY, l = 0.f;
    f16v acc0, acc1;
#pragma unroll
    for (int i = 0; i < 16; ++i) { acc0[i] = 0.f; acc1[i] = 0.f; }
#pragma unroll
    for (int rho = 0; rho < 2; ++rho) {
      unsigned kr = ka, vr0 = va00, vr1 = va01;
      asm volatile("" : "+v"(kr), "+v"(vr0), "+v"(vr1));
      bar();
      f4 sa = {0.f, 0.f, 0.f, 0.f}, sb = sa;
      {
        v4u kf[8];
#pragma unroll
        for (int s = 0; s < 8; ++s) kf[s] = lds128((kr ^ ((s & 3) << 6)) + (s >> 2) * 256);
        __builtin_amdgcn_sched_barrier(0);
#pragma unroll
        for (int s = 0; s < 8; ++s) {
          sa = mfma32(kf[s], qf[s], sa);
          kf[s] = lds128((kr ^ ((s & 3) << 6)) + (2 + (s >> 2)) * 256);
          __builtin_amdgcn_sched_barrier(0);
        }
        kr += 65536;
#pragma unroll
        for (int s = 0; s < 8; ++s) {
          sa = mfma32(kf[s], qf[8 + s], sa);
          kf[s] = lds128((kr ^ ((s & 3) << 6)) + (s >> 2) * 256);
          __builtin_amdgcn_sched_barrier(0);
        }
#pragma unroll
        for (int s = 0; s < 8; ++s) {
          sb = mfma32(kf[s], qf[s], sb);
          kf[s] = lds128((kr ^ ((s & 3) << 6)) + (2 + (s >> 2)) * 256);
          __builtin_amdgcn_sched_barrier(0);
        }
#pragma unroll
        for (int s = 0; s < 8; ++s) sb = mfma32(kf[s], qf[8 + s], sb);
      }
      if (rho == 0 && NS3 == 0) { LD_S(S3, 3, 6); LD_S(S3, 3, 7); }
      float mx = fmaxf(fmaxf(fmaxf(sa[0], sa[1]), fmaxf(sa[2], sa[3])), fmaxf(fmaxf(sb[0], sb[1]), fmaxf(sb[2], sb[3])));
      mx = x32max(x16max(mx));
      if (g == 0) stof(mxa + kb4 * 128, mx);
      v4u c0[4], c1[4];
#pragma unroll
      for (int ks = 0; ks < 4; ++ks) {
        c0[ks] = trd2(ks * 16384 + vr0, ks * 16384 + vr1);
        c1[ks] = trd2(ks * 16384 + (vr0 ^ 64), ks * 16384 + (vr1 ^ 64));
      }
      if (rho == 0) STAMP(6); else STAMP(8);
      bar();
      const float mn = fmaxf(fmaxf(m, fmaxf(ldsf(mxa), ldsf(mxa + 128))), fmaxf(ldsf(mxa + 256), ldsf(mxa + 384)));
      const float al = ex2((m - mn) * SC), mc = -mn * SC;
      float p[8];
#pragma unroll
      for (int i = 0; i < 4; ++i) { p[i] = ex2(fmaf(sa[i], SC, mc)); p[4 + i] = ex2(fmaf(sb[i], SC, mc)); }
      l = fmaf(l, al, ((p[0] + p[1]) + (p[2] + p[3])) + ((p[4] + p[5]) + (p[6] + p[7])));
      m = mn;
      sto64(pwa, (v2u){pk(p[0], p[1]), pk(p[2], p[3])});
      sto64(pwa + 6144, (v2u){pk(p[4], p[5]), pk(p[6], p[7])});
      if (kb4 == 0 && g == 0) stof(mxa + (B_AL - B_MX), al);
      if (rho == 1) { l = x32sum(x16sum(l)); if (g == 0) stof(mxa + (B_LS - B_MX) + kb4 * 128, l); }
      bar();
      if (rho == 1 && kb4 == 0 && g == 0) {
        const unsigned la = mxa + (B_LS - B_MX);
        sto64(B_FM + (16 * hg + c16) * 8, (v2u){asu(m * SC), asu((ldsf(la) + ldsf(la + 128)) + (ldsf(la + 256) + ldsf(la + 384)))});
      }
      const float alp = rho ? ldsf(ala) : 0.f;
      v4u pf[4];
#pragma unroll
      for (int ks = 0; ks < 4; ++ks) pf[ks] = lds128(pra + ks * 1536);
      __builtin_amdgcn_sched_barrier(0);
      if (rho) { acc0 *= alp; acc1 *= alp; }
      vr0 += 65536; vr1 += 65536;
#pragma unroll
      for (int ks = 0; ks < 4; ++ks) {
        acc0 = mfmaL(c0[ks], pf[ks], acc0); acc1 = mfmaL(c1[ks], pf[ks], acc1);
        c0[ks] = trd2(ks * 16384 + vr0, ks * 16384 + vr1);
        c1[ks] = trd2(ks * 16384 + (vr0 ^ 64), ks * 16384 + (vr1 ^ 64));
        pf[ks] = lds128(pra + 6144 + ks * 1536);
        __builtin_amdgcn_sched_barrier(0);
      }
      bar();
      if (rho == 0) {
        ST_S(S2, 0);
        vmwait<0>();
        ST_S(S3, 1);
      }
#pragma unroll
      for (int ks = 0; ks < 4; ++ks) { acc0 = mfmaL(c0[ks], pf[ks], acc0); acc1 = mfmaL(c1[ks], pf[ks], acc1); }
      if (rho == 0) STAMP(7); else STAMP(9);
    }
    {
    const int lane = (int)opq((unsigned)lane_), h2 = lane >> 5, tid = wave * 64 + lane;
    {
      const unsigned tb = B_T + w * 4096, n32 = lane & 31;
#pragma unroll
      for (int q4 = 0; q4 < 4; ++q4) {
        sto64(tb + n32 * 128 + ((q4 ^ (n32 & 7)) << 4) + h2 * 8, (v2u){pk(acc0[4 * q4], acc0[4 * q4 + 1]), pk(acc0[4 * q4 + 2], acc0[4 * q4 + 3])});
        sto64(tb + n32 * 128 + (((4 + q4) ^ (n32 & 7)) << 4) + h2 * 8, (v2u){pk(acc1[4 * q4], acc1[4 * q4 + 1]), pk(acc1[4 * q4 + 2], acc1[4 * q4 + 3])});
      }
      lgk0();
      const int hr = lane >> 3, ck = lane & 7;
#pragma unroll
      for (int i = 0; i < 4; ++i)
        gst128(rs, WS_PB + (((8 * i + hr) * 4 + (w >> 1)) * 64 + bid) * 256 + (w & 1) * 128 + ck * 16, lds128(tb + (8 * i + hr) * 128 + ((ck ^ hr) << 4)));
    }
    vmwait<0>();
    bar();
    if (w == 0 && lane < 32) { const v2u fm = *(const LAS v2u*)(B_FM + lane * 8); gst128(rs, WS_PBM + bid * 512 + lane * 16, (v4u){fm.x, tag, fm.y, tag}); }
    STAMP(10);
    if (w == 0) {
      const unsigned off = SKIPA ? WS_F + (64 + bid) * 16384 + (bid & 31) * 128 : PRE2 == 2 ? WS_F + bid * 16384 + ((lane >> 3) * 16 + (bid & 15)) * 128 : PRE2 ? WS_F + bid * 16384 + (2 * bid) * 128 : WS_PAM + (bid * NSPLIT_A + (bid % NSPLIT_A)) * 128;
      _Pragma("nounroll") for (int it = 0; it < SPIN_CAP; ++it) { cfence(); if (__popcll(__ballot(gld64(rs, off).y == tag)) >= PREN) break; __builtin_amdgcn_s_sleep(PRESL); }
    }
    bar();
    STAMP(14);
    v4u f[3];
    if (F2) sweep2<3, F2>(f, rs, tag, [&](int k) { return WS_F + ((k >> 1) * 64 + bid) * 16384 + (k < 2 ? k * 8192 + tid * 16 : (tid & 255) * 16); });
    else sweep<3, FSL, FMASK>(f, rs, tag, [&](int k) { return WS_F + (((SKIPB ? 0 : SKIPA ? 2 : k) >> 1) * 64 + bid) * 16384 + (k < 2 && !SKIPA ? k * 8192 + tid * 16 : (tid & 255) * 16); });
    float r[4] = {asf(f[0].x) + asf(f[1].x), asf(f[0].z) + asf(f[1].z), w < 4 ? asf(f[2].x) : 0.f, w < 4 ? asf(f[2].z) : 0.f};
#pragma unroll
    for (int k = 0; k < 4; ++k) { r[k] += dpp<ROR8>(r[k]); r[k] = x32sum(x16sum(r[k])); }
    if (lane < 8) { sto64(B_FIN + w * 128 + lane * 8, (v2u){asu(r[0]), asu(r[1])}); sto64(B_FIN + w * 128 + 64 + lane * 8, (v2u){asu(r[2]), asu(r[3])}); }
    bar();
    if (tid < 16) {
      float s0 = 0.f, s1 = 0.f;
#pragma unroll
      for (int i = 0; i < 8; ++i) { const f2 t = *(const LAS f2*)(B_FIN + i * 128 + tid * 8); s0 += t.x; s1 += t.y; }
      const int mat = tid >> 3, ob = (tid >> 2) & 1, pr = tid & 3;
      *(GAS unsigned*)((GAS char*)out + (ob * 1024 + mat * 512 + 8 * bid + 2 * pr) * 2) = pk(s0, s1);
      if (!SKIPA && !SKIPB && bid == 0 && tid == 0) gst32(rs, WS_E, tag);
    }
    STAMP(15);
    STAMP_OUT();
    }
  } else {
    const int h = bid - NB_CU, w = wave;
    if (w == 7) {
      if (lane < WS_END / TLBS) touch(ws, lane * TLBS + (bid & 31) * 128, B_DUMMY);
      if (lane < 8) touch(Wuv + h * 256, lane * 524288, B_DUMMY);
      if (lane < 2) touch(Bwo + (size_t)h * 131072, lane * 65536, B_DUMMY);
    }
    GET_TAG();
    if (w == 0) gather_qb(T_QBI, h, lane, rs, tag);
    vmwait<0>();
    STAMP(1);
    v4u OU[16];
    const char* ow = Bwo + (size_t)(h * 128 + 16 * w) * 1024;
    const unsigned vo = g * 4096 + c16 * 16;
#define LD_OU(u) asm volatile("global_load_dwordx4 %0, %1, %2 offset:%3 nt" : "=v"(OU[u]) : "v"(vo), "s"(ow), "n"(((u) >> 2) * 1024 + ((u) & 3) * 256) : "memory")
#pragma unroll
    for (int u = 0; u < TE; ++u) LD_OU(u);
    {
      const unsigned a0 = w * 8192 + c16 * 256 + ((g ^ c16) << 4);
      v4u uf[8];
#pragma unroll
      for (int i = 0; i < 8; ++i) uf[i] = lds128((a0 ^ ((i & 3) << 6)) + (i >> 2) * 4096);
      bar();
      STAMP(2);
      v4u qb[4];
#pragma unroll
      for (int s = 0; s < 4; ++s) qb[s] = lds128(T_QBI + (c16 & 1) * 256 + s * 64 + g * 16);
      __builtin_amdgcn_sched_barrier(0);
      f4 d0 = {0.f, 0.f, 0.f, 0.f}, d1 = d0;
#pragma unroll
      for (int s = 0; s < 4; ++s) { d0 = mfma32(uf[s], qb[s], d0); d1 = mfma32(uf[4 + s], qb[s], d1); }
      const int rb = (c16 >> 1) & 1;
      const f4 e = rb ? d1 : d0;
      if (c16 < 4) sto64(T_QAS + (c16 & 1) * 512 + (32 * w + 16 * rb + 4 * g) * 2, (v2u){pk(e[0], e[1]), pk(e[2], e[3])});
      bar();
      if (w == 1) {
        gst128(rs, WS_QAB + ((lane >> 5) * 32 + h) * 1024 + (lane & 31) * 16, lds128(T_QAS + lane * 16));
        vmwait<0>();
        flag_line(rs, WS_FQ + h * FS, lane, tag);
      }
    }
    bar();
    STAMP(3);
    {
      const char* uv = Wuv + (size_t)(64 * w) * 8192 + h * 256;
      const unsigned vuv = g * 8192 + ((c16 ^ (g << 1)) << 4);
#pragma unroll
      for (int u = 0; u < 16; ++u) { if (u >= WT && !(u & 1)) vmwait<WT - 2>(); dma<0>(uv + (size_t)u * 32768, vuv ^ (((u >> 1) & 1) << 7), w * 16384 + u * 1024); }
#pragma unroll
      for (int u = TE; u < 16; ++u) { if (!(u & 1)) vmwait<WT - 2>(); LD_OU(u); }
    }
    asm volatile("s_waitcnt vmcnt(0)" : "+v"(OU[0]), "+v"(OU[1]), "+v"(OU[2]), "+v"(OU[3]), "+v"(OU[4]), "+v"(OU[5]), "+v"(OU[6]), "+v"(OU[7]),
                 "+v"(OU[8]), "+v"(OU[9]), "+v"(OU[10]), "+v"(OU[11]), "+v"(OU[12]), "+v"(OU[13]), "+v"(OU[14]), "+v"(OU[15]) :: "memory");
    if (tid < 4) sto32(T_RDY + tid * 4, 0u);
    bar();
    STAMP(4);
    v4u wf[16];
    {
      const int q = c16 >> 2, p = lane & 3, f3 = q | ((g & 1) << 2);
      const unsigned a0 = (8 * g + q) * 256 + (((2 * w + (p >> 1)) ^ (f3 << 1)) << 4) + (p & 1) * 8;
      const unsigned aa[2] = {opq(a0), opq(a0 + 65536)};
#pragma unroll
      for (int s = 0; s < 16; ++s) wf[s] = trd2(aa[s >> 3] + (s & 7) * 8192, aa[s >> 3] + (s & 7) * 8192 + 1024);
    }
    STAMP(5);
    {
      const int tb = w >> 2;
      v4u o[8];
#pragma unroll
      for (int i = 0; i < 8; ++i) { o[i] = (v4u){0u, 0u, 0u, 0u}; asm volatile("" : "+v"(o[i])); }
      const char* pb = ws + WS_PB + (h * 4 + (w & 3)) * 16384 + tb * 8192;
      const unsigned vl = lane << 4;
      unsigned done = 0;
      bool all = false;
      v4u ml = {0u, ~tag, 0u, ~tag};
      if (w == 0) {
        _Pragma("nounroll") for (int it = 0; it < SPIN_CAP; ++it) { cfence(); if (__all(gld32(rs, WS_PBM + 2 * h * 512 + h * 16 + 4) == tag)) break; __builtin_amdgcn_s_sleep(PSL1); }
      }
      _Pragma("nounroll") for (int it = 0; it < SPIN_CAP * 8 && !all; ++it) {
        unsigned rdy;
        if (w == 0) {
          cfence();
          const v4u t = gld128(rs, tagok(ml, tag) ? 0xfffffff0u : WS_PBM + lane * 512 + h * 16);
          if (!tagok(ml, tag)) ml = t;
          unsigned long long m = __ballot(tagok(ml, tag));
          all = m == ~0ull;
          m &= m >> 2; m &= m >> 1; m &= 0x1111111111111111ull;
          rdy = (unsigned)m;
          if (lane == 0) *(volatile LAS v2u*)T_RDY = (v2u){rdy, (unsigned)(m >> 32)};
        } else {
          rdy = lds_peek(T_RDY + tb * 4);
          if (rdy == done) { __builtin_amdgcn_s_sleep(1); continue; }
          all = rdy == 0x11111111u;
        }
        const unsigned add = rdy & ~done;
        done = rdy;
#pragma unroll
        for (int i = 0; i < 8; ++i)
          if (add & (1u << (4 * i))) asm volatile("global_load_dwordx4 %0, %1, %2 offset:%3 sc1" : "+v"(o[i]) : "v"(vl), "s"(pb + (i >> 2) * 4096), "n"((i & 3) * 1024) : "memory");
      }
      if (w == 0) {
        const float M = x16max(row16max(asf(ml.x)));
        const float wt = ex2(asf(ml.x) - M), wn = wt / x16sum(row16sum(wt * asf(ml.z)));
        const unsigned hi = pk(wn, wn), wa = T_WN + (lane >> 5) * 128 + (lane & 3) * 32 + ((lane >> 2) & 7) * 4;
        const float lo = wn - asf(hi & 0xffff0000u);
        sto32(wa, hi); sto32(wa + 256, pk(lo, lo));
        lgk0();
        if (lane == 0) *(volatile LAS unsigned*)(T_RDY + 8) = 1u;
      } else {
        lds_wait_ge<0>(T_RDY + 8, 1u);
      }
      const unsigned wa = T_WN + (c16 >> 3) * 256 + tb * 128 + g * 32, half = (c16 & 1) ? 0xffff0000u : 0x0000ffffu;
      const v4u w0_ = lds128(wa), w1_ = lds128(wa + 16);
      const int rr = (c16 >> 1) & 3;
      v4u bo[8];
#pragma unroll
      for (int u = 0; u < 8; ++u) { const unsigned x = (u < 4 ? w0_[u & 3] : w1_[u & 3]) & half; bo[u] = (v4u){rr == 0 ? x : 0u, rr == 1 ? x : 0u, rr == 2 ? x : 0u, rr == 3 ? x : 0u}; }
      asm volatile("s_waitcnt vmcnt(0)" : "+v"(o[0]), "+v"(o[1]), "+v"(o[2]), "+v"(o[3]), "+v"(o[4]), "+v"(o[5]), "+v"(o[6]), "+v"(o[7]),
                   "+v"(bo[0]), "+v"(bo[1]), "+v"(bo[2]), "+v"(bo[3]), "+v"(bo[4]), "+v"(bo[5]), "+v"(bo[6]), "+v"(bo[7]) :: "memory");
      STAMP(8);
      f4 d = {0.f, 0.f, 0.f, 0.f};
#pragma unroll
      for (int u = 0; u < 8; ++u) d = mfma32(o[u], bo[u], d);
#pragma unroll
      for (int r = 0; r < 4; ++r) {
        const float x = d[r] + dpp<ROR8>(d[r]);
        const unsigned pr = pk(x, dpp<XOR1>(x));
        if ((c16 & 9) == 0) sto32(T_OL + tb * 1088 + ((w & 3) * 128 + 32 * g + 8 * r + c16) * 2, pr);
      }
    }
    bar();
    STAMP(6);
    {
      v4u of[16];
      const unsigned oa = opq(T_OL + (c16 >> 3) * 1088 + g * 16);
#pragma unroll
      for (int s = 0; s < 16; ++s) of[s] = lds128(oa + s * 64);
      __builtin_amdgcn_sched_barrier(0);
      f4 d0 = {0.f, 0.f, 0.f, 0.f}, d1 = d0;
#pragma unroll
      for (int s = 0; s < 8; ++s) { d0 = mfma32(wf[2 * s], of[2 * s], d0); d1 = mfma32(wf[2 * s + 1], of[2 * s + 1], d1); }
      d0 += d1;
      const unsigned half = (c16 & 1) ? 0xffff0000u : 0x0000ffffu;
      const int rr = (c16 >> 1) & 3;
      f4 o4[4];
#pragma unroll
      for (int cb = 0; cb < 4; ++cb) o4[cb] = (f4){0.f, 0.f, 0.f, 0.f};
#pragma unroll
      for (int u = 0; u < 4; ++u) {
        const unsigned x = pk(d0[u], d0[u]) & half;
        const v4u bo = {rr == 0 ? x : 0u, rr == 1 ? x : 0u, rr == 2 ? x : 0u, rr == 3 ? x : 0u};
#pragma unroll
        for (int cb = 0; cb < 4; ++cb) o4[cb] = mfma32(OU[4 * u + cb], bo, o4[cb]);
      }
#pragma unroll
      for (int cb = 0; cb < 4; ++cb)
#pragma unroll
        for (int r = 0; r < 4; ++r) stof(T_RED + w * 4096 + cb * 1024 + r * 64 + g * 256 + c16 * 4, o4[cb][r]);
    }
    bar();
    {
      const int j = tid >> 3, pr = tid & 7;
      float s0 = 0.f, s1 = 0.f;
#pragma unroll
      for (int i = 0; i < 8; ++i) { const f2 t = *(const LAS f2*)(T_RED + i * 4096 + tid * 8); s0 += t.x; s1 += t.y; }
      gst128(rs, WS_F + (((64 + j) * 128 + h) * 16 + 2 * pr) * 8, (v4u){asu(s0), tag, asu(s1), tag});
    }
    STAMP(7);
    STAMP_OUT();
  }
}

extern "C" void kernel_launch(void* const* d_in, const int* in_sizes, int n_in, void* d_out, int out_size, void* d_ws, size_t ws_size,
                              hipStream_t stream) {
  (void)in_sizes; (void)n_in; (void)out_size; (void)ws_size;
  (void)hipFuncSetAttribute((const void*)fused_decode, hipFuncAttributeMaxDynamicSharedMemorySize, LDS_BYTES);
  fused_decode<<<256, 512, LDS_BYTES, stream>>>(
      (const char*)d_in[0], (const char*)d_in[1], (const char*)d_in[2], (const char*)d_in[3], (const char*)d_in[4], (const char*)d_in[5],
      (const char*)d_in[6], (const char*)d_in[7], (const char*)d_in[8], (const char*)d_in[9], (const char*)d_in[10], (unsigned short*)d_out,
      (char*)d_ws);
}
```

```cpp
#include <hip/hip_runtime.h>
#include <stdint.h>
#pragma clang diagnostic ignored "-Wint-to-pointer-cast"

#define GAS __attribute__((address_space(1)))
#define LAS __attribute__((address_space(3)))
typedef unsigned v4u __attribute__((ext_vector_type(4)));
typedef unsigned v2u __attribute__((ext_vector_type(2)));
typedef float f4 __attribute__((ext_vector_type(4)));
typedef float f2 __attribute__((ext_vector_type(2)));
typedef float f16v __attribute__((ext_vector_type(16)));
typedef short s4 __attribute__((ext_vector_type(4)));
typedef __bf16 b8 __attribute__((ext_vector_type(8)));
typedef __bf16 b2 __attribute__((ext_vector_type(2)));

namespace {

constexpr int NB_CU = 64;
constexpr int NT_CU = 32;
constexpr int NSPLIT_A = 10;
constexpr int NB0 = 55;
constexpr int NB1 = 52;
constexpr int NB2 = (512 - 4 * NB0 - 4 * NB1) / 2;
constexpr int NSLOT = 14, NCONS = 6;
constexpr int W1 = 12;
constexpr int W2 = 24;
constexpr int J1 = 18;
constexpr int WB = 4;
constexpr int WT = 6;
constexpr int DLYP = 30;
constexpr int DLYB = 10;
constexpr int NS3 = 4;
constexpr int TE = 6;
constexpr int QGO = 4;
constexpr int ODL = 5;
constexpr int PREN = 32;
constexpr int CODE_KB = 23;
constexpr float SC = 0.08838834764831845f * 1.4426950408889634f;
constexpr int SPIN_CAP = 1 << 18;

constexpr unsigned FS = 128;
constexpr unsigned WS_E = 0;
constexpr unsigned WS_FQ = 4096;
constexpr unsigned WS_QG = 65536;
constexpr unsigned WS_QAB = WS_QG + 262144;
constexpr unsigned WS_PBM = WS_QAB + 65536;
constexpr unsigned WS_PAM = WS_PBM + 32768;
constexpr unsigned WS_PAO = WS_PAM + 131072;
constexpr unsigned WS_PB = WS_PAO + 786432;
constexpr unsigned WS_F = WS_PB + 2097152;
constexpr unsigned WS_END = WS_F + 2097152;

constexpr unsigned LDS_BYTES = 163840;
constexpr unsigned SCR = 131072;
constexpr unsigned A_MISC = NSLOT * 8192;
constexpr unsigned A_LANDK = A_MISC, A_LANDV = A_MISC + 4, A_QUSED = A_MISC + 8, A_QCNT = A_MISC + 12, A_QB = A_MISC + 16, A_QRDY = A_MISC + 20, A_OUL = A_MISC + 24, A_OUL2 = A_MISC + 28;
constexpr unsigned A_DONE = A_MISC + 64;
constexpr unsigned A_QU2 = 10 * 8192;
constexpr unsigned A_QIMG = A_MISC + 256;
constexpr unsigned A_ML = A_MISC + 1280;
constexpr unsigned A_OIMG = A_MISC + 1792;
constexpr unsigned A_ST = A_MISC + 2048;
constexpr unsigned A_XIMG = A_MISC + 4096, A_DUMMY = A_MISC + 5120, A_QBI = A_MISC + 6144, A_QAS = A_MISC + 6656;
constexpr unsigned SLOT1 = 65536;
constexpr unsigned B_MX = SCR, B_AL = SCR + 512, B_LS = SCR + 640, B_P = SCR + 2048;
constexpr unsigned B_FM = SCR + 1280, B_FIN = SCR + 16384, B_T = SLOT1, B_DUMMY = SCR + 8192;
constexpr unsigned T_QBI = SCR, T_QAS = SCR + 1024, T_OL = SCR + 4096, T_RDY = SCR + 6400, T_WN = SCR + 6656, T_RED = 0;

__device__ __forceinline__ v4u lds128(unsigned a) { return *(const LAS v4u*)a; }
__device__ __forceinline__ void sto128(unsigned a, v4u v) { *(LAS v4u*)a = v; }
__device__ __forceinline__ void sto64(unsigned a, v2u v) { *(LAS v2u*)a = v; }
__device__ __forceinline__ void sto32(unsigned a, unsigned v) { *(LAS unsigned*)a = v; }
__device__ __forceinline__ float ldsf(unsigned a) { return *(const LAS float*)a; }
__device__ __forceinline__ void stof(unsigned a, float v) { *(LAS float*)a = v; }
__device__ __forceinline__ v2u trd(unsigned a) { return __builtin_bit_cast(v2u, __builtin_amdgcn_ds_read_tr16_b64_v4i16((LAS s4*)a)); }
__device__ __forceinline__ v4u trd2(unsigned a0, unsigned a1) { const v2u x = trd(a0), y = trd(a1); return (v4u){x.x, x.y, y.x, y.y}; }
__device__ __forceinline__ f4 mfma32(v4u a, v4u b, f4 c) {
  return __builtin_amdgcn_mfma_f32_16x16x32_bf16(__builtin_bit_cast(b8, a), __builtin_bit_cast(b8, b), c, 0, 0, 0); }
__device__ __forceinline__ f4 mfma16(v2u a, v2u b, f4 c) {
  return __builtin_amdgcn_mfma_f32_16x16x16bf16_1k(__builtin_bit_cast(s4, a), __builtin_bit_cast(s4, b), c, 0, 0, 0); }
__device__ __forceinline__ f16v mfmaL(v4u a, v4u b, f16v c) {
  return __builtin_amdgcn_mfma_f32_32x32x16_bf16(__builtin_bit_cast(b8, a), __builtin_bit_cast(b8, b), c, 0, 0, 0); }
__device__ __forceinline__ unsigned pk(float lo, float hi) { return __builtin_bit_cast(unsigned, __builtin_convertvector((f2){lo, hi}, b2)); }
__device__ __forceinline__ float asf(unsigned u) { return __builtin_bit_cast(float, u); }
__device__ __forceinline__ unsigned asu(float f) { return __builtin_bit_cast(unsigned, f); }
__device__ __forceinline__ float ex2(float x) { return __builtin_amdgcn_exp2f(x); }
template <int CTRL> __device__ __forceinline__ float dpp(float x) {
  return __builtin_bit_cast(float, __builtin_amdgcn_mov_dpp(__builtin_bit_cast(int, x), CTRL, 0xf, 0xf, true)); }
constexpr int XOR1 = 0xB1, XOR2 = 0x4E, ROR4 = 0x124, ROR8 = 0x128;
__device__ __forceinline__ float x16max(float x) { auto s = __builtin_amdgcn_permlane16_swap(asu(x), asu(x), false, false); return fmaxf(asf(s[0]), asf(s[1])); }
__device__ __forceinline__ float x32max(float x) { auto s = __builtin_amdgcn_permlane32_swap(asu(x), asu(x), false, false); return fmaxf(asf(s[0]), asf(s[1])); }
__device__ __forceinline__ float x16sum(float x) { auto s = __builtin_amdgcn_permlane16_swap(asu(x), asu(x), false, false); return asf(s[0]) + asf(s[1]); }
__device__ __forceinline__ float x32sum(float x) { auto s = __builtin_amdgcn_permlane32_swap(asu(x), asu(x), false, false); return asf(s[0]) + asf(s[1]); }
__device__ __forceinline__ float row16max(float x) { x = fmaxf(x, dpp<XOR1>(x)); x = fmaxf(x, dpp<XOR2>(x)); x = fmaxf(x, dpp<ROR4>(x)); return fmaxf(x, dpp<ROR8>(x)); }
__device__ __forceinline__ float row16sum(float x) { x += dpp<XOR1>(x); x += dpp<XOR2>(x); x += dpp<ROR4>(x); return x + dpp<ROR8>(x); }

template <int IMM> __device__ __forceinline__ void dma(const void* sbase, unsigned voff, unsigned lds) {
  asm volatile("s_mov_b32 m0, %2\n\ts_nop 0\n\tglobal_load_lds_dwordx4 %0, %1 offset:%3 nt" :: "v"(voff), "s"(sbase), "s"(lds), "n"(IMM) : "memory");
}
template <int W> __device__ __forceinline__ void dma4(const void* sbase, unsigned v0, unsigned v1, unsigned v2, unsigned v3, unsigned lds) {
  asm volatile("s_mov_b32 m0, %5\n\ts_waitcnt vmcnt(%6)\n\tglobal_load_lds_dwordx4 %0, %4 nt\n\tglobal_load_lds_dwordx4 %1, %4 offset:1024 nt\n\t"
               "s_waitcnt vmcnt(%6)\n\tglobal_load_lds_dwordx4 %2, %4 offset:2048 nt\n\tglobal_load_lds_dwordx4 %3, %4 offset:3072 nt"
               :: "v"(v0), "v"(v1), "v"(v2), "v"(v3), "s"(sbase), "s"(lds), "n"(W - 2) : "memory");
}
__device__ __forceinline__ void dma_keep(const void* sbase, unsigned voff, unsigned lds) {
  asm volatile("s_mov_b32 m0, %2\n\ts_nop 0\n\tglobal_load_lds_dwordx4 %0, %1" :: "v"(voff), "s"(sbase), "s"(lds) : "memory");
}
template <int N> __device__ __forceinline__ void vmwait() { asm volatile("s_waitcnt vmcnt(%0)" :: "n"(N) : "memory"); }
__device__ __forceinline__ void bar() { asm volatile("s_waitcnt lgkmcnt(0)\n\ts_barrier" ::: "memory"); }
__device__ __forceinline__ void lgk0() { asm volatile("s_waitcnt lgkmcnt(0)" ::: "memory"); }
__device__ __forceinline__ void cfence() { asm volatile("" ::: "memory"); }
__device__ __forceinline__ unsigned opq(unsigned x) { asm volatile("" : "+v"(x)); return x; }
__device__ __forceinline__ unsigned lds_peek(unsigned a) { return __builtin_amdgcn_readfirstlane(*(volatile LAS unsigned*)a); }
template <int SL = 1> __device__ __forceinline__ void lds_wait_ge(unsigned a, unsigned want) {
  _Pragma("nounroll") for (int it = 0; it < SPIN_CAP * 8; ++it) { if (lds_peek(a) >= want) break; if (SL) __builtin_amdgcn_s_sleep(SL); }
  cfence();
}
typedef __amdgpu_buffer_rsrc_t rsrc_t;
constexpr int SC1 = 16;
__device__ __forceinline__ v4u gld128(rsrc_t r, unsigned off) { return __builtin_amdgcn_raw_buffer_load_b128(r, off, 0, SC1); }
__device__ __forceinline__ v2u gld64(rsrc_t r, unsigned off) { return __builtin_amdgcn_raw_buffer_load_b64(r, off, 0, SC1); }
__device__ __forceinline__ unsigned gld32(rsrc_t r, unsigned off) { return __builtin_amdgcn_raw_buffer_load_b32(r, off, 0, SC1); }
__device__ __forceinline__ void gst128(rsrc_t r, unsigned off, v4u v) { __builtin_amdgcn_raw_buffer_store_b128(v, r, off, 0, SC1); }
__device__ __forceinline__ void gst32(rsrc_t r, unsigned off, unsigned v) { __builtin_amdgcn_raw_buffer_store_b32(v, r, off, 0, SC1); }
__device__ __forceinline__ void flag_line(rsrc_t r, unsigned off, int lane, unsigned tag) { if (lane < 8) gst128(r, off + lane * 16, (v4u){tag, tag, tag, tag}); }
__device__ __forceinline__ bool tagok(v4u v, unsigned tag) { return v.y == tag && v.w == tag; }
template <int N, int SL, class F> __device__ __forceinline__ void sweep(v4u (&v)[N], rsrc_t rs, unsigned tag, F off) {
  _Pragma("nounroll") for (int it = 0; it < SPIN_CAP; ++it) {
    cfence();
    bool ok = true;
#pragma unroll
    for (int k = 0; k < N; ++k) { v[k] = gld128(rs, off(k)); ok &= tagok(v[k], tag); }
    if (__all(ok)) break;
    if (SL) __builtin_amdgcn_s_sleep(SL);
  }
}

__device__ __forceinline__ void gather_qb(unsigned img, int h, int lane, rsrc_t rs, unsigned tag) {
  v4u qg[4];
  const unsigned off = WS_QG + (h * 128 + 2 * lane) * 8;
  sweep<4, 1>(qg, rs, tag, [&](int k) { return off + (k & 1) * 65536 + (k >> 1) * 32768; });
  sto32(img + lane * 4, pk(asf(qg[0].x) + asf(qg[1].x), asf(qg[0].z) + asf(qg[1].z)));
  sto32(img + 256 + lane * 4, pk(asf(qg[2].x) + asf(qg[3].x), asf(qg[2].z) + asf(qg[3].z)));
}
__device__ __forceinline__ void lds_inc(unsigned a) { __hip_atomic_fetch_add((LAS unsigned*)a, 1u, __ATOMIC_RELAXED, __HIP_MEMORY_SCOPE_WORKGROUP); }

__device__ __forceinline__ void out_frags(unsigned gb, int w, int lane, v4u (&wf)[4]) {
  const int g = lane >> 4, q = (lane & 15) >> 2, p = lane & 3, f3 = q | ((g & 1) << 2);
#pragma unroll
  for (int tt = 0; tt < 4; ++tt) {
    const unsigned a0 = gb + q * 1024 + (((2 * (4 * w + tt) + (p >> 1)) ^ (f3 << 1)) << 4) + (p & 1) * 8;
    wf[tt] = trd2(a0, a0 + 4096);
  }
}

}

__global__ void __launch_bounds__(512, 2) __attribute__((aligned(4096))) fused_decode(
    const char* __restrict__ Ax, const char* __restrict__ Kc, const char* __restrict__ Vc, const char* __restrict__ Wq, const char* __restrict__ Wo,
    const char* __restrict__ Bx, const char* __restrict__ Ckv, const char* __restrict__ Bwq, const char* __restrict__ Wuk, const char* __restrict__ Wuv,
    const char* __restrict__ Bwo, unsigned short* __restrict__ out, char* __restrict__ ws) {
  const int bid = blockIdx.x, tid = threadIdx.x, lane = tid & 63, wave = __builtin_amdgcn_readfirstlane(tid >> 6);
  const int g = lane >> 4, c16 = lane & 15;
  const int iA = bid - (NB_CU + NT_CU);
  const bool isA = iA >= 0, isB = bid < NB_CU;
  const int ty = iA >> 6;
  int dA = 95 - bid, dU = 223 - bid, dB = bid - 64, dQ = 159 - bid, dW = 2 - wave;
  asm("" : "+s"(dA), "+s"(dU), "+s"(dB), "+s"(dQ), "+s"(dW));
  const int mA = dA >> 31, mU = dU >> 31, mB = dB >> 31, mP = mA & ~mU, sh = mP & 1;
  const int hh = (bid >> sh) & 31;
  const unsigned row0 = (mA & ((((bid | mU) & 1) << 8) + 64 * wave - 64)) | (~mA & (32 * wave));
  const unsigned l0 = (mA & (SCR - 16384 + wave * 16384 - ((dW >> 31) & (SCR + 32768 - A_QU2)))) | (~mA & (wave * 8192));
  const unsigned karg = 0x40 - (mP & (8 + ((dQ >> 31) & 32))), more = mA;
  if ((unsigned)(wave + mA + (mB & 8)) < (unsigned)(8 + (mA & -4))) {
    const unsigned b0 = (row0 + g) * 8192 + hh * 256 + ((c16 ^ (g << sh)) << 4), x1 = ~mP & 64;
    unsigned v0 = b0, v1 = (b0 ^ x1) + 32768, v2 = (b0 ^ 128) + 65536, v3 = (b0 ^ x1 ^ 128) + 98304;
    if (karg == 0x18) __builtin_amdgcn_s_sleep(DLYP);
    unsigned long long wp;
#define D4 "global_load_lds_dwordx4 %1, %0 nt\n\ts_add_u32 m0, m0, 0x400\n\tv_add_u32 %1, 0x20000, %1\n\t" \
           "global_load_lds_dwordx4 %2, %0 nt\n\ts_add_u32 m0, m0, 0x400\n\tv_add_u32 %2, 0x20000, %2\n\t" \
           "global_load_lds_dwordx4 %3, %0 nt\n\ts_add_u32 m0, m0, 0x400\n\tv_add_u32 %3, 0x20000, %3\n\t" \
           "global_load_lds_dwordx4 %4, %0 nt\n\ts_add_u32 m0, m0, 0x400\n\tv_add_u32 %4, 0x20000, %4\n\t"
    asm volatile("s_load_dwordx2 %0, %5, %6\n\ts_mov_b32 m0, %7\n\ts_waitcnt lgkmcnt(0)\n\t" D4 D4
                 "s_cmp_eq_u32 %8, 0\n\ts_cbranch_scc1 1f\n\t" D4 D4 "1:"
                 : "=&s"(wp), "+v"(v0), "+v"(v1), "+v"(v2), "+v"(v3)
                 : "s"(__builtin_amdgcn_kernarg_segment_ptr()), "s"(karg), "s"(l0), "s"(more) : "memory", "scc");
#undef D4
  }
  {
    unsigned long long pc;
    asm volatile("s_getpc_b64 %0" : "=s"(pc));
    const int kb = ((bid >> 3) + 4 * wave) & 31;
    dma_keep((const char*)((pc & ~1023ull) + (unsigned)((kb < CODE_KB ? kb : CODE_KB - 1) * 1024)), lane * 16, isA ? A_DUMMY : B_DUMMY);
  }
  const rsrc_t rs = __builtin_amdgcn_make_buffer_rsrc(ws, 0, WS_END, 0x00020000);
  asm volatile("" :: "s"(Ax), "s"(Kc), "s"(Vc), "s"(Wq), "s"(Wo), "s"(Bx), "s"(Ckv), "s"(Bwq), "s"(Wuk), "s"(Wuv), "s"(Bwo), "s"(out), "s"(ws));
  unsigned tag;
#define GET_TAG() do { asm volatile("s_load_dword %0, %1, 0x0\n\ts_waitcnt lgkmcnt(0)" : "=s"(tag) : "s"(ws) : "memory"); tag += 1u; } while (0)
  const char* cp = Ckv + ((size_t)(bid >> 5) * 8192 + 256 * (bid & 31) + 8 * wave) * 1024;
  const unsigned vc0 = (lane ^ ((wave & 1) ? 3 : 0)) << 4, vc1 = (lane ^ ((wave & 1) ? 1 : 2)) << 4;
#define LD_C(rho, u) dma<((u) & 3) * 1024>(cp + (size_t)(rho) * 65536 + ((u) >> 2) * 4096, (((u) >> 2) ? vc1 : vc0) ^ (((u) & 3) << 6), ((rho) & 1) * 65536 + wave * 8192 + ((u) >> 2) * 4096)
  if (isA) {
    if (wave == 1 && ty < 2) dma<0>((ty ? Ax : Bx) + 512 * (bid & 1), (lane >> 5) * 1024 + (lane & 31) * 16, A_XIMG);
    if (tid < 32) sto32(A_MISC + tid * 4, 0u);
    bar();
  } else if (isB) {
    __builtin_amdgcn_s_sleep(DLYB);
    LD_C(0, 0); LD_C(0, 1); LD_C(0, 2); LD_C(0, 3); LD_C(0, 4); LD_C(0, 5);
  }

  if (isA) {
    const int split = iA >> 4, pair = (iA + split) & 15, b = pair >> 3, kvh = pair & 7;
    const int nblk = ty == 0 ? NB0 : ty == 1 ? NB1 : NB2, blk0 = (ty == 0 ? 0 : ty == 1 ? 4 * NB0 : 4 * (NB0 + NB1)) + (split & 3) * nblk;
    if (wave == 0 || wave == 7) {
      __builtin_amdgcn_s_setprio(3);
      static_assert(NSPLIT_A == 10 && W1 >= 4 && W1 % 4 == 0 && W2 % 4 == 0 && W1 <= W2 && W2 <= 24 && J1 <= NB0 && J1 <= NB1 && J1 <= NB2, "window");
      const bool isv = wave == 7;
      int pub = 0, j = 0;
      unsigned sl = 0;
      const char* gp = (isv ? Vc : Kc) + ((size_t)pair * 8192 + blk0 * 16) * 256;
      const unsigned v0 = opq(g * 256 + ((c16 ^ (isv ? g << 1 : g)) << 4));
      const unsigned v1 = opq(v0 ^ (isv ? 128 : 64)), v2 = opq(v0 ^ (isv ? 0 : 128)), v3 = opq(v0 ^ (isv ? 128 : 192));
      const unsigned lo = isv ? 4096 : 0, la = opq(lane ? A_MISC + 1536 + lane * 4 : isv ? A_LANDV : A_LANDK);
      unsigned dn = 0;
#define PUBLISH(val) do { pub = max(pub, (val)); *(volatile LAS unsigned*)la = (unsigned)pub; } while (0)
#define GATE(addr, need) do { if (lds_peek(addr) < (need)) { vmwait<0>(); PUBLISH(j); lds_wait_ge(addr, need); } } while (0)
#define PART(W) do { \
        if (j >= NSLOT) { if ((unsigned)__builtin_amdgcn_readfirstlane(dn) < (unsigned)(j - NSLOT + 1)) GATE(A_DONE + sl, (unsigned)(j - NSLOT + 1)); } \
        else if (j == 10) GATE(A_QUSED, 4u); \
        dma4<W>(gp, v0, v1, v2, v3, sl * 2048 + lo); \
        ++j; gp += 4096; sl = sl == 4 * (NSLOT - 1) ? 0 : sl + 4; \
        PUBLISH(j - W / 4); dn = *(volatile LAS unsigned*)(A_DONE + sl); } while (0)
      GET_TAG();
      lds_wait_ge(A_QCNT, 4u);
#pragma nounroll
      while (j < J1) PART(W1);
#pragma nounroll
      while (j < nblk) PART(W2);
      vmwait<20>(); PUBLISH(j - 5); vmwait<16>(); PUBLISH(j - 4); vmwait<12>(); PUBLISH(j - 3);
      vmwait<8>(); PUBLISH(j - 2); vmwait<4>(); PUBLISH(j - 1); vmwait<0>(); PUBLISH(j);
      if (ty < 2 && !isv) {
        const char* op = Wo + (size_t)iA * 32768;
        const unsigned o0 = lane << 4, o1 = o0 ^ 32, o2 = o0 ^ 64, o3 = o0 ^ 96;
        lds_wait_ge(A_LANDV, (unsigned)(nblk - ODL));
#pragma unroll
        for (int k = 0; k < 8; ++k) {
          const unsigned x = ((k >> 1) & 1) << 7;
          dma4<62>(op + k * 4096, o0 ^ x, o1 ^ x, o2 ^ x, o3 ^ x, SCR + k * 4096);
        }
      }
      __builtin_amdgcn_s_setprio(0);
#undef PUBLISH
#undef GATE
#undef PART
    } else {
      GET_TAG();
      vmwait<0>();
      if (wave <= 4) {
        const int w1 = wave - 1;
        if (ty < 2) {
          if (lane == 0) lds_inc(A_QCNT);
          const int q = c16 >> 2, p = lane & 3, f3 = q | ((g & 1) << 2);
          const unsigned a0 = (8 * g + q) * 256 + (((4 * w1 + (p >> 1)) ^ (f3 << 1)) << 4) + (p & 1) * 8;
          const unsigned aa[2] = {opq(SCR + a0), opq(A_QU2 + a0)};
          lds_wait_ge(A_QCNT, 4u);
          v4u w0[8], w1f[8], xf[8];
#pragma unroll
          for (int s = 0; s < 8; ++s) {
            const unsigned a = aa[s >> 2] + (s & 3) * 8192;
            w0[s] = trd2(a, a + 1024); w1f[s] = trd2((a ^ 32), (a ^ 32) + 1024);
            xf[s] = lds128(A_XIMG + (c16 & 1) * 512 + g * 16 + s * 64);
          }
          __builtin_amdgcn_sched_barrier(0);
          f4 d0 = {0.f, 0.f, 0.f, 0.f}, d1 = d0;
#pragma unroll
          for (int s = 0; s < 8; ++s) { d0 = mfma32(w0[s], xf[s], d0); d1 = mfma32(w1f[s], xf[s], d1); }
          const int pr = (c16 >> 1) & 1, nb = (c16 >> 2) & 1;
          const f4 e = nb ? d1 : d0;
          if (c16 < 8) gst128(rs, WS_QG + (((ty * 2 + (bid & 1)) * 2 + (c16 & 1)) * 4096 + hh * 128 + 32 * w1 + 16 * nb + 4 * g + 2 * pr) * 8,
                              (v4u){asu(pr ? e[2] : e[0]), tag, asu(pr ? e[3] : e[1]), tag});
          lgk0();
        } else {
          if (w1 == 3 && lane == 0) *(volatile LAS unsigned*)A_OUL2 = 1u;
          const unsigned a0 = opq((w1 < 2 ? SCR : A_QU2 - 32768) + w1 * 16384 + c16 * 256 + ((g ^ c16) << 4));
          v4u uf[16];
#pragma unroll
          for (int i = 0; i < 16; ++i) uf[i] = lds128((a0 ^ ((i & 3) << 6)) + (i >> 2) * 4096);
          lds_wait_ge(A_QB, 1u);
          v4u qb[4];
#pragma unroll
          for (int s = 0; s < 4; ++s) qb[s] = lds128(A_QBI + (c16 & 1) * 256 + s * 64 + g * 16);
          __builtin_amdgcn_sched_barrier(0);
          f4 d[4];
#pragma unroll
          for (int rb = 0; rb < 4; ++rb) d[rb] = (f4){0.f, 0.f, 0.f, 0.f};
#pragma unroll
          for (int s = 0; s < 4; ++s)
#pragma unroll
            for (int rb = 0; rb < 4; ++rb) d[rb] = mfma32(uf[4 * rb + s], qb[s], d[rb]);
          const int rb = (c16 >> 1) & 3;
          const f4 e = rb == 0 ? d[0] : rb == 1 ? d[1] : rb == 2 ? d[2] : d[3];
          if (c16 < 8) sto64(A_QAS + w1 * 256 + (c16 & 1) * 128 + (16 * rb + 4 * g) * 2, (v2u){pk(e[0], e[1]), pk(e[2], e[3])});
          lgk0();
          if (lane < 16) gst128(rs, WS_QAB + ((lane >> 3) * 32 + hh) * 1024 + 512 + w1 * 128 + (lane & 7) * 16, lds128(A_QAS + w1 * 256 + lane * 16));
          vmwait<0>();
        }
        unsigned old = 0;
        if (lane == 0) old = __hip_atomic_fetch_add((LAS unsigned*)A_QUSED, 1u, __ATOMIC_RELAXED, __HIP_MEMORY_SCOPE_WORKGROUP);
        if (ty == 2 && __builtin_amdgcn_readfirstlane(old) == 3u) {
          flag_line(rs, WS_FQ + (32 + hh) * FS, lane, tag);
          cfence();
          if (lane == 0) *(volatile LAS unsigned*)A_QCNT = 4u;
        }
      }
      if (wave == 5) {
        if (ty == 2) {
          lds_wait_ge(A_OUL2, 1u);
          gather_qb(A_QBI, hh, lane, rs, tag);
          lgk0();
          if (lane == 0) *(volatile LAS unsigned*)A_QB = 1u;
        }
        lds_wait_ge(A_LANDK, ty == 2 ? 1u : (unsigned)QGO);
        v4u v[8];
        const unsigned off = WS_QG + 131072 + (b * 4096 + kvh * 512 + 2 * lane) * 8;
        sweep<8, 4>(v, rs, tag, [&](int k) { return off + (k & 1) * 65536 + (k >> 1) * 1024; });
#pragma unroll
        for (int k = 0; k < 4; ++k) sto32(A_QIMG + k * 256 + lane * 4, pk(asf(v[2 * k].x) + asf(v[2 * k + 1].x), asf(v[2 * k].z) + asf(v[2 * k + 1].z)));
        lgk0();
        if (lane == 0) *(volatile LAS unsigned*)A_QRDY = 1u;
      }
      lds_wait_ge(A_QRDY, 1u);
      v4u qf[4];
#pragma unroll
      for (int s = 0; s < 4; ++s) qf[s] = lds128(A_QIMG + (c16 & 3) * 256 + s * 64 + g * 16);
      const unsigned ko = c16 * 256 + ((g ^ c16) << 4);
      const int vr = 4 * g + (c16 >> 2);
      const unsigned vo = 4096 + vr * 256 + ((((lane & 3) >> 1) ^ ((vr & 7) << 1)) << 4) + (lane & 1) * 8;
      float m = -INFINITY, l = 0.f;
      f4 acc[8];
#pragma unroll
      for (int t = 0; t < 8; ++t) acc[t] = (f4){0.f, 0.f, 0.f, 0.f};
      int slot = wave - 1;
      for (int j = wave - 1; j < nblk; j += NCONS) {
        const unsigned sb = slot * 8192;
        lds_wait_ge(A_LANDK, (unsigned)(j + 1));
        v4u kf[4];
#pragma unroll
        for (int s = 0; s < 4; ++s) kf[s] = lds128(sb + (ko ^ (s << 6)));
        __builtin_amdgcn_sched_barrier(0);
        f4 s4_ = {0.f, 0.f, 0.f, 0.f};
#pragma unroll
        for (int s = 0; s < 4; ++s) s4_ = mfma32(kf[s], qf[s], s4_);
        float mx = fmaxf(fmaxf(s4_[0], s4_[1]), fmaxf(s4_[2], s4_[3]));
        mx = x32max(x16max(mx));
        const float mn = fmaxf(m, mx), al = ex2((m - mn) * SC), mc = -mn * SC;
        const float p0 = ex2(fmaf(s4_[0], SC, mc)), p1 = ex2(fmaf(s4_[1], SC, mc)), p2 = ex2(fmaf(s4_[2], SC, mc)), p3 = ex2(fmaf(s4_[3], SC, mc));
        l = fmaf(l, al, (p0 + p1) + (p2 + p3));
        m = mn;
        v2u pb = {pk(p0, p1), pk(p2, p3)};
        unsigned va[8];
#pragma unroll
        for (int t = 0; t < 8; ++t) va[t] = sb + (vo ^ (t << 5));
        if (!__all(al == 1.f)) {
#pragma unroll
          for (int t = 0; t < 8; ++t) acc[t] *= al;
        }
        asm volatile("" : "+v"(pb), "+v"(l), "+v"(acc[0]), "+v"(acc[1]), "+v"(acc[2]), "+v"(acc[3]), "+v"(acc[4]), "+v"(acc[5]), "+v"(acc[6]), "+v"(acc[7]),
                     "+v"(va[0]), "+v"(va[1]), "+v"(va[2]), "+v"(va[3]), "+v"(va[4]), "+v"(va[5]), "+v"(va[6]), "+v"(va[7]));
        lds_wait_ge<0>(A_LANDV, (unsigned)(j + 1));
        v2u vf[8];
#pragma unroll
        for (int t = 0; t < 8; ++t) vf[t] = trd(va[t]);
        lgk0();
        if (lane == 0) *(volatile LAS unsigned*)(A_DONE + slot * 4) = (unsigned)(j + 1);
#pragma unroll
        for (int t = 0; t < 8; ++t) acc[t] = mfma16(vf[t], pb, acc[t]);
        slot += NCONS; if (slot >= NSLOT) slot -= NSLOT;
      }
      l = x32sum(x16sum(l));
      if (c16 < 4) {
#pragma unroll
        for (int t = 0; t < 8; ++t) sto128(A_ST + (wave - 1) * 2048 + c16 * 512 + ((t ^ c16) * 64) + g * 16, __builtin_bit_cast(v4u, acc[t]));
        if (g == 0) sto64(A_ML + (c16 * 8 + wave - 1) * 8, (v2u){asu(m * SC), asu(l)});
      }
    }
    bar();
    if (wave >= 3 && wave <= 6) {
      const int hq = wave - 3;
      v2u ml[NCONS];
      f2 ow[NCONS];
      const unsigned sa = opq(A_ST + hq * 512 + ((lane * 8) ^ (hq << 6))), ma = opq(A_ML + hq * 64);
#pragma unroll
      for (int c = 0; c < NCONS; ++c) { ml[c] = *(const LAS v2u*)(ma + c * 8); ow[c] = *(const LAS f2*)(sa + c * 2048); }
      float M = asf(ml[0].x);
#pragma unroll
      for (int c = 1; c < NCONS; ++c) M = fmaxf(M, asf(ml[c].x));
      float L = 0.f, o0 = 0.f, o1 = 0.f;
#pragma unroll
      for (int c = 0; c < NCONS; ++c) { const float wt = ex2(asf(ml[c].x) - M); L = fmaf(wt, asf(ml[c].y), L); o0 = fmaf(wt, ow[c].x, o0); o1 = fmaf(wt, ow[c].y, o1); }
      const int rec = (b * 32 + kvh * 4 + hq) * NSPLIT_A + split;
      gst128(rs, WS_PAO + rec * 1024 + lane * 16, (v4u){asu(o0), tag, asu(o1), tag});
      if (lane < 8) gst128(rs, WS_PAM + rec * 128 + lane * 16, (v4u){asu(M), tag, asu(L), tag});
    }
    if (ty < 2) {
      const int oh = iA >> 2, odq = iA & 3;
      if (wave == 0) { vmwait<0>(); if (lane == 0) lds_inc(A_OUL); }
      lds_wait_ge(A_OUL, 1u);
      v4u wf[4];
      out_frags(SCR + g * 8192, wave, lane, wf);
      const int tt = (c16 >> 1) & 3, pr = c16 >> 3, n = 64 * wave + 16 * tt + 4 * g + 2 * pr;
      unsigned fo = WS_F + ((((n >> 3) * 128 + iA) * 16) + (c16 & 1) * 8 + (n & 7)) * 8, oa = A_OIMG + (c16 & 1) * 64 + g * 16;
      asm volatile("" : "+v"(fo), "+v"(oa));
      if (wave == 1) {
        const int rec = ((lane >> 5) * 32 + oh) * NSPLIT_A + (g & 1) * 5;
        v4u vm[10];
        const unsigned o0_ = WS_PAO + rec * 1024 + (32 * odq + 2 * c16) * 8, o1_ = WS_PAM + rec * 128 + odq * 16;
        sweep<10, 1>(vm, rs, tag, [&](int k) { return k < 5 ? o0_ + k * 1024 : o1_ + (k - 5) * 128; });
        const v4u* v = vm; const v4u* ml = vm + 5;
        float M = asf(ml[0].x);
#pragma unroll
        for (int i = 1; i < 5; ++i) M = fmaxf(M, asf(ml[i].x));
        M = x16max(M);
        float L = 0.f, o0 = 0.f, o1 = 0.f;
#pragma unroll
        for (int i = 0; i < 5; ++i) { const float wt = ex2(asf(ml[i].x) - M); L = fmaf(wt, asf(ml[i].z), L); o0 = fmaf(wt, asf(v[i].x), o0); o1 = fmaf(wt, asf(v[i].z), o1); }
        const float inv = 1.f / x16sum(L);
        o0 = x16sum(o0); o1 = x16sum(o1);
        if (!(g & 1)) sto32(A_OIMG + (lane >> 5) * 64 + c16 * 4, pk(o0 * inv, o1 * inv));
      }
      bar();
      const v4u ob = lds128(oa);
      f4 d[4];
#pragma unroll
      for (int t = 0; t < 4; ++t) d[t] = mfma32(wf[t], ob, (f4){0.f, 0.f, 0.f, 0.f});
      const f4 e = tt == 0 ? d[0] : tt == 1 ? d[1] : tt == 2 ? d[2] : d[3];
      gst128(rs, fo, (v4u){asu(pr ? e[2] : e[0]), tag, asu(pr ? e[3] : e[1]), tag});
    }
  } else if (isB) {
    const int b = bid >> 5, w = wave, lane_ = lane;
    const unsigned vl = lane << 4;
#define LD_S(S, rho, u) asm volatile("global_load_dwordx4 %0, %1, %2 offset:%3 nt" : "=v"(S[u]) : "v"(vl), "s"(cp + (size_t)(rho) * 65536 + ((u) >> 2) * 4096), "n"(((u) & 3) * 1024) : "memory")
#define ST_S(S, rho) do { unsigned a0_ = ((rho) & 1) * 65536 + w * 8192 + vc0, a1_ = ((rho) & 1) * 65536 + w * 8192 + vc1; asm volatile("" : "+v"(a0_), "+v"(a1_)); \
      _Pragma("unroll") for (int u_ = 0; u_ < 8; ++u_) sto128(u_ * 1024 + (((u_ >> 2) ? a1_ : a0_) ^ ((u_ & 3) << 6)), S[u_]); } while (0)
#define PB() vmwait<WB - 2>()
    v4u S2[8], S3[8];
    GET_TAG();
    PB(); LD_C(0, 6); LD_C(0, 7);
    PB(); LD_C(1, 0); LD_C(1, 1);
    PB(); LD_C(1, 2); LD_C(1, 3);
    PB(); LD_C(1, 4); LD_C(1, 5);
    PB(); LD_C(1, 6); LD_C(1, 7);
    PB(); LD_S(S2, 2, 0); LD_S(S2, 2, 1);
    PB(); LD_S(S2, 2, 2); LD_S(S2, 2, 3);
    PB(); LD_S(S2, 2, 4); LD_S(S2, 2, 5);
    PB(); LD_S(S2, 2, 6); LD_S(S2, 2, 7);
#pragma unroll
    for (int u = 0; u < NS3; u += 2) { PB(); LD_S(S3, 3, u); LD_S(S3, 3, u + 1); }
    if (w == 0) {
      _Pragma("nounroll") for (int it = 0; it < SPIN_CAP; ++it) { cfence(); if (__all(gld32(rs, WS_FQ + lane * FS) == tag)) break; __builtin_amdgcn_s_sleep(4); }
    }
    bar();
    {
      v4u t[4];
      const char* qa = ws + WS_QAB + (b * 32 + 4 * w) * 1024;
#pragma unroll
      for (int i = 0; i < 4; ++i) asm volatile("global_load_dwordx4 %0, %1, %2 offset:%3 sc1" : "=v"(t[i]) : "v"(vl), "s"(qa), "n"(i * 1024) : "memory");
      constexpr int NA = 2, NC = 6 - NS3;
      static_assert(NS3 == 2 || NS3 == 4 || NS3 == 6, "");
#pragma unroll
      for (int u = NS3; u < NS3 + NA; ++u) LD_S(S3, 3, u);
      asm volatile("s_waitcnt vmcnt(%12)" : "+v"(t[0]), "+v"(t[1]), "+v"(t[2]), "+v"(t[3]), "+v"(S2[0]), "+v"(S2[1]), "+v"(S2[2]), "+v"(S2[3]),
                   "+v"(S2[4]), "+v"(S2[5]), "+v"(S2[6]), "+v"(S2[7]) : "n"(NA) : "memory");
#pragma unroll
      for (int u = NS3 + NA; u < NS3 + NA + NC; ++u) LD_S(S3, 3, u);
#pragma unroll
      for (int i = 0; i < 4; ++i) sto128(SCR + (4 * w + i) * 1024 + ((lane ^ ((4 * w + i) & 15)) << 4), t[i]);
    }
    bar();
    const int kb4 = w & 3, hg = w >> 2;
    v4u qf[16];
#pragma unroll
    for (int s = 0; s < 16; ++s) qf[s] = lds128(SCR + (16 * hg + c16) * 1024 + ((((4 * s) & 15) ^ g ^ c16) << 4) + (s >> 2) * 256);
    const unsigned fk = ((c16 & 3) << 2) | ((0x78u >> (2 * (c16 >> 2))) & 3u);
    const unsigned ka = (16 * kb4 + c16) * 1024 + ((g ^ fk) << 4);
    const int h2 = lane >> 5, blk = g & 1, qq = c16 >> 2, pp = lane & 3;
    const unsigned ft0 = (qq << 2) | (h2 ? 3u : 0u), ft1 = (qq << 2) | (h2 ? 1u : 2u);
    const unsigned cbase = 8 * w + 2 * blk + (pp >> 1);
    const unsigned va00 = (8 * h2 + qq) * 1024 + ((cbase ^ ft0) << 4) + (pp & 1) * 8, va01 = (8 * h2 + 4 + qq) * 1024 + ((cbase ^ ft1) << 4) + (pp & 1) * 8;
    const unsigned mxa = opq(B_MX + (16 * hg + c16) * 4), pwa = opq(B_P + kb4 * 1536 + (16 * hg + c16) * 48 + g * 8);
    const unsigned pra = opq(B_P + (lane & 31) * 48 + h2 * 16), ala = opq(B_AL + (lane & 31) * 4);
    float m = -INFINITY, l = 0.f;
    f16v acc0, acc1;
#pragma unroll
    for (int i = 0; i < 16; ++i) { acc0[i] = 0.f; acc1[i] = 0.f; }
#pragma unroll
    for (int rho = 0; rho < 2; ++rho) {
      unsigned kr = ka, vr0 = va00, vr1 = va01;
      asm volatile("" : "+v"(kr), "+v"(vr0), "+v"(vr1));
      bar();
      f4 sa = {0.f, 0.f, 0.f, 0.f}, sb = sa;
      {
        v4u kf[8];
#pragma unroll
        for (int s = 0; s < 8; ++s) kf[s] = lds128((kr ^ ((s & 3) << 6)) + (s >> 2) * 256);
        __builtin_amdgcn_sched_barrier(0);
#pragma unroll
        for (int s = 0; s < 8; ++s) {
          sa = mfma32(kf[s], qf[s], sa);
          kf[s] = lds128((kr ^ ((s & 3) << 6)) + (2 + (s >> 2)) * 256);
          __builtin_amdgcn_sched_barrier(0);
        }
        kr += 65536;
#pragma unroll
        for (int s = 0; s < 8; ++s) {
          sa = mfma32(kf[s], qf[8 + s], sa);
          kf[s] = lds128((kr ^ ((s & 3) << 6)) + (s >> 2) * 256);
          __builtin_amdgcn_sched_barrier(0);
        }
#pragma unroll
        for (int s = 0; s < 8; ++s) {
          sb = mfma32(kf[s], qf[s], sb);
          kf[s] = lds128((kr ^ ((s & 3) << 6)) + (2 + (s >> 2)) * 256);
          __builtin_amdgcn_sched_barrier(0);
        }
#pragma unroll
        for (int s = 0; s < 8; ++s) sb = mfma32(kf[s], qf[8 + s], sb);
      }
      float mx = fmaxf(fmaxf(fmaxf(sa[0], sa[1]), fmaxf(sa[2], sa[3])), fmaxf(fmaxf(sb[0], sb[1]), fmaxf(sb[2], sb[3])));
      mx = x32max(x16max(mx));
      if (g == 0) stof(mxa + kb4 * 128, mx);
      v4u c0[4], c1[4];
#pragma unroll
      for (int ks = 0; ks < 4; ++ks) {
        c0[ks] = trd2(ks * 16384 + vr0, ks * 16384 + vr1);
        c1[ks] = trd2(ks * 16384 + (vr0 ^ 64), ks * 16384 + (vr1 ^ 64));
      }
      bar();
      const float mn = fmaxf(fmaxf(m, fmaxf(ldsf(mxa), ldsf(mxa + 128))), fmaxf(ldsf(mxa + 256), ldsf(mxa + 384)));
      const float al = ex2((m - mn) * SC), mc = -mn * SC;
      float p[8];
#pragma unroll
      for (int i = 0; i < 4; ++i) { p[i] = ex2(fmaf(sa[i], SC, mc)); p[4 + i] = ex2(fmaf(sb[i], SC, mc)); }
      l = fmaf(l, al, ((p[0] + p[1]) + (p[2] + p[3])) + ((p[4] + p[5]) + (p[6] + p[7])));
      m = mn;
      sto64(pwa, (v2u){pk(p[0], p[1]), pk(p[2], p[3])});
      sto64(pwa + 6144, (v2u){pk(p[4], p[5]), pk(p[6], p[7])});
      if (kb4 == 0 && g == 0) stof(mxa + (B_AL - B_MX), al);
      if (rho == 1) { l = x32sum(x16sum(l)); if (g == 0) stof(mxa + (B_LS - B_MX) + kb4 * 128, l); }
      bar();
      if (rho == 1 && kb4 == 0 && g == 0) {
        const unsigned la = mxa + (B_LS - B_MX);
        sto64(B_FM + (16 * hg + c16) * 8, (v2u){asu(m * SC), asu((ldsf(la) + ldsf(la + 128)) + (ldsf(la + 256) + ldsf(la + 384)))});
      }
      const float alp = rho ? ldsf(ala) : 0.f;
      v4u pf[4];
#pragma unroll
      for (int ks = 0; ks < 4; ++ks) pf[ks] = lds128(pra + ks * 1536);
      __builtin_amdgcn_sched_barrier(0);
      if (rho) { acc0 *= alp; acc1 *= alp; }
      vr0 += 65536; vr1 += 65536;
#pragma unroll
      for (int ks = 0; ks < 4; ++ks) {
        acc0 = mfmaL(c0[ks], pf[ks], acc0); acc1 = mfmaL(c1[ks], pf[ks], acc1);
        c0[ks] = trd2(ks * 16384 + vr0, ks * 16384 + vr1);
        c1[ks] = trd2(ks * 16384 + (vr0 ^ 64), ks * 16384 + (vr1 ^ 64));
        pf[ks] = lds128(pra + 6144 + ks * 1536);
        __builtin_amdgcn_sched_barrier(0);
      }
      bar();
      if (rho == 0) {
        ST_S(S2, 0);
        vmwait<0>();
        ST_S(S3, 1);
      }
#pragma unroll
      for (int ks = 0; ks < 4; ++ks) { acc0 = mfmaL(c0[ks], pf[ks], acc0); acc1 = mfmaL(c1[ks], pf[ks], acc1); }
    }
    {
    const int lane = (int)opq((unsigned)lane_), h2 = lane >> 5, tid = wave * 64 + lane;
    {
      const unsigned tb = B_T + w * 4096, n32 = lane & 31;
#pragma unroll
      for (int q4 = 0; q4 < 4; ++q4) {
        sto64(tb + n32 * 128 + ((q4 ^ (n32 & 7)) << 4) + h2 * 8, (v2u){pk(acc0[4 * q4], acc0[4 * q4 + 1]), pk(acc0[4 * q4 + 2], acc0[4 * q4 + 3])});
        sto64(tb + n32 * 128 + (((4 + q4) ^ (n32 & 7)) << 4) + h2 * 8, (v2u){pk(acc1[4 * q4], acc1[4 * q4 + 1]), pk(acc1[4 * q4 + 2], acc1[4 * q4 + 3])});
      }
      lgk0();
      const int hr = lane >> 3, ck = lane & 7;
#pragma unroll
      for (int i = 0; i < 4; ++i)
        gst128(rs, WS_PB + (((8 * i + hr) * 4 + (w >> 1)) * 64 + bid) * 256 + (w & 1) * 128 + ck * 16, lds128(tb + (8 * i + hr) * 128 + ((ck ^ hr) << 4)));
    }
    vmwait<0>();
    bar();
    if (w == 0 && lane < 32) { const v2u fm = *(const LAS v2u*)(B_FM + lane * 8); gst128(rs, WS_PBM + bid * 512 + lane * 16, (v4u){fm.x, tag, fm.y, tag}); }
    if (w == 0) {
      const unsigned off = WS_F + bid * 16384 + ((lane >> 3) * 16 + (bid & 15)) * 128;
      _Pragma("nounroll") for (int it = 0; it < SPIN_CAP; ++it) { cfence(); if (__popcll(__ballot(gld64(rs, off).y == tag)) >= PREN) break; __builtin_amdgcn_s_sleep(8); }
    }
    bar();
    v4u f[3];
    sweep<3, 0>(f, rs, tag, [&](int k) { return WS_F + ((k >> 1) * 64 + bid) * 16384 + (k < 2 ? k * 8192 + tid * 16 : (tid & 255) * 16); });
    float r[4] = {asf(f[0].x) + asf(f[1].x), asf(f[0].z) + asf(f[1].z), w < 4 ? asf(f[2].x) : 0.f, w < 4 ? asf(f[2].z) : 0.f};
#pragma unroll
    for (int k = 0; k < 4; ++k) { r[k] += dpp<ROR8>(r[k]); r[k] = x32sum(x16sum(r[k])); }
    if (lane < 8) { sto64(B_FIN + w * 128 + lane * 8, (v2u){asu(r[0]), asu(r[1])}); sto64(B_FIN + w * 128 + 64 + lane * 8, (v2u){asu(r[2]), asu(r[3])}); }
    bar();
    if (tid < 16) {
      float s0 = 0.f, s1 = 0.f;
#pragma unroll
      for (int i = 0; i < 8; ++i) { const f2 t = *(const LAS f2*)(B_FIN + i * 128 + tid * 8); s0 += t.x; s1 += t.y; }
      const int mat = tid >> 3, ob = (tid >> 2) & 1, pr = tid & 3;
      *(GAS unsigned*)((GAS char*)out + (ob * 1024 + mat * 512 + 8 * bid + 2 * pr) * 2) = pk(s0, s1);
      if (bid == 0 && tid == 0) gst32(rs, WS_E, tag);
    }
    }
  } else {
    const int h = bid - NB_CU, w = wave;
    GET_TAG();
    if (w == 0) gather_qb(T_QBI, h, lane, rs, tag);
    vmwait<0>();
    v4u OU[16];
    const char* ow = Bwo + (size_t)(h * 128 + 16 * w) * 1024;
    const unsigned vo = g * 4096 + c16 * 16;
#define LD_OU(u) asm volatile("global_load_dwordx4 %0, %1, %2 offset:%3 nt" : "=v"(OU[u]) : "v"(vo), "s"(ow), "n"(((u) >> 2) * 1024 + ((u) & 3) * 256) : "memory")
#pragma unroll
    for (int u = 0; u < TE; ++u) LD_OU(u);
    {
      const unsigned a0 = w * 8192 + c16 * 256 + ((g ^ c16) << 4);
      v4u uf[8];
#pragma unroll
      for (int i = 0; i < 8; ++i) uf[i] = lds128((a0 ^ ((i & 3) << 6)) + (i >> 2) * 4096);
      bar();
      v4u qb[4];
#pragma unroll
      for (int s = 0; s < 4; ++s) qb[s] = lds128(T_QBI + (c16 & 1) * 256 + s * 64 + g * 16);
      __builtin_amdgcn_sched_barrier(0);
      f4 d0 = {0.f, 0.f, 0.f, 0.f}, d1 = d0;
#pragma unroll
      for (int s = 0; s < 4; ++s) { d0 = mfma32(uf[s], qb[s], d0); d1 = mfma32(uf[4 + s], qb[s], d1); }
      const int rb = (c16 >> 1) & 1;
      const f4 e = rb ? d1 : d0;
      if (c16 < 4) sto64(T_QAS + (c16 & 1) * 512 + (32 * w + 16 * rb + 4 * g) * 2, (v2u){pk(e[0], e[1]), pk(e[2], e[3])});
      bar();
      if (w == 1) {
        gst128(rs, WS_QAB + ((lane >> 5) * 32 + h) * 1024 + (lane & 31) * 16, lds128(T_QAS + lane * 16));
        vmwait<0>();
        flag_line(rs, WS_FQ + h * FS, lane, tag);
      }
    }
    bar();
    {
      const char* uv = Wuv + (size_t)(64 * w) * 8192 + h * 256;
      const unsigned vuv = g * 8192 + ((c16 ^ (g << 1)) << 4);
#pragma unroll
      for (int u = 0; u < 16; ++u) { if (u >= WT && !(u & 1)) vmwait<WT - 2>(); dma<0>(uv + (size_t)u * 32768, vuv ^ (((u >> 1) & 1) << 7), w * 16384 + u * 1024); }
#pragma unroll
      for (int u = TE; u < 16; ++u) { if (!(u & 1)) vmwait<WT - 2>(); LD_OU(u); }
    }
    asm volatile("s_waitcnt vmcnt(0)" : "+v"(OU[0]), "+v"(OU[1]), "+v"(OU[2]), "+v"(OU[3]), "+v"(OU[4]), "+v"(OU[5]), "+v"(OU[6]), "+v"(OU[7]),
                 "+v"(OU[8]), "+v"(OU[9]), "+v"(OU[10]), "+v"(OU[11]), "+v"(OU[12]), "+v"(OU[13]), "+v"(OU[14]), "+v"(OU[15]) :: "memory");
    if (tid < 4) sto32(T_RDY + tid * 4, 0u);
    bar();
    v4u wf[16];
    {
      const int q = c16 >> 2, p = lane & 3, f3 = q | ((g & 1) << 2);
      const unsigned a0 = (8 * g + q) * 256 + (((2 * w + (p >> 1)) ^ (f3 << 1)) << 4) + (p & 1) * 8;
      const unsigned aa[2] = {opq(a0), opq(a0 + 65536)};
#pragma unroll
      for (int s = 0; s < 16; ++s) wf[s] = trd2(aa[s >> 3] + (s & 7) * 8192, aa[s >> 3] + (s & 7) * 8192 + 1024);
    }
    {
      const int tb = w >> 2;
      v4u o[8];
#pragma unroll
      for (int i = 0; i < 8; ++i) { o[i] = (v4u){0u, 0u, 0u, 0u}; asm volatile("" : "+v"(o[i])); }
      const char* pb = ws + WS_PB + (h * 4 + (w & 3)) * 16384 + tb * 8192;
      const unsigned vl = lane << 4;
      unsigned done = 0;
      bool all = false;
      v4u ml = {0u, ~tag, 0u, ~tag};
      if (w == 0) {
        _Pragma("nounroll") for (int it = 0; it < SPIN_CAP; ++it) { cfence(); if (__all(gld32(rs, WS_PBM + 2 * h * 512 + h * 16 + 4) == tag)) break; __builtin_amdgcn_s_sleep(8); }
      }
      _Pragma("nounroll") for (int it = 0; it < SPIN_CAP * 8 && !all; ++it) {
        unsigned rdy;
        if (w == 0) {
          cfence();
          const v4u t = gld128(rs, tagok(ml, tag) ? 0xfffffff0u : WS_PBM + lane * 512 + h * 16);
          if (!tagok(ml, tag)) ml = t;
          unsigned long long m = __ballot(tagok(ml, tag));
          all = m == ~0ull;
          m &= m >> 2; m &= m >> 1; m &= 0x1111111111111111ull;
          rdy = (unsigned)m;
          if (lane == 0) *(volatile LAS v2u*)T_RDY = (v2u){rdy, (unsigned)(m >> 32)};
        } else {
          rdy = lds_peek(T_RDY + tb * 4);
          if (rdy == done) { __builtin_amdgcn_s_sleep(1); continue; }
          all = rdy == 0x11111111u;
        }
        const unsigned add = rdy & ~done;
        done = rdy;
#pragma unroll
        for (int i = 0; i < 8; ++i)
          if (add & (1u << (4 * i))) asm volatile("global_load_dwordx4 %0, %1, %2 offset:%3 sc1" : "+v"(o[i]) : "v"(vl), "s"(pb + (i >> 2) * 4096), "n"((i & 3) * 1024) : "memory");
      }
      if (w == 0) {
        const float M = x16max(row16max(asf(ml.x)));
        const float wt = ex2(asf(ml.x) - M), wn = wt / x16sum(row16sum(wt * asf(ml.z)));
        const unsigned hi = pk(wn, wn), wa = T_WN + (lane >> 5) * 128 + (lane & 3) * 32 + ((lane >> 2) & 7) * 4;
        const float lo = wn - asf(hi & 0xffff0000u);
        sto32(wa, hi); sto32(wa + 256, pk(lo, lo));
        lgk0();
        if (lane == 0) *(volatile LAS unsigned*)(T_RDY + 8) = 1u;
      } else {
        lds_wait_ge<0>(T_RDY + 8, 1u);
      }
      const unsigned wa = T_WN + (c16 >> 3) * 256 + tb * 128 + g * 32, half = (c16 & 1) ? 0xffff0000u : 0x0000ffffu;
      const v4u w0_ = lds128(wa), w1_ = lds128(wa + 16);
      const int rr = (c16 >> 1) & 3;
      v4u bo[8];
#pragma unroll
      for (int u = 0; u < 8; ++u) { const unsigned x = (u < 4 ? w0_[u & 3] : w1_[u & 3]) & half; bo[u] = (v4u){rr == 0 ? x : 0u, rr == 1 ? x : 0u, rr == 2 ? x : 0u, rr == 3 ? x : 0u}; }
      asm volatile("s_waitcnt vmcnt(0)" : "+v"(o[0]), "+v"(o[1]), "+v"(o[2]), "+v"(o[3]), "+v"(o[4]), "+v"(o[5]), "+v"(o[6]), "+v"(o[7]),
                   "+v"(bo[0]), "+v"(bo[1]), "+v"(bo[2]), "+v"(bo[3]), "+v"(bo[4]), "+v"(bo[5]), "+v"(bo[6]), "+v"(bo[7]) :: "memory");
      f4 d = {0.f, 0.f, 0.f, 0.f};
#pragma unroll
      for (int u = 0; u < 8; ++u) d = mfma32(o[u], bo[u], d);
#pragma unroll
      for (int r = 0; r < 4; ++r) {
        const float x = d[r] + dpp<ROR8>(d[r]);
        const unsigned pr = pk(x, dpp<XOR1>(x));
        if ((c16 & 9) == 0) sto32(T_OL + tb * 1088 + ((w & 3) * 128 + 32 * g + 8 * r + c16) * 2, pr);
      }
    }
    bar();
    {
      v4u of[16];
      const unsigned oa = opq(T_OL + (c16 >> 3) * 1088 + g * 16);
#pragma unroll
      for (int s = 0; s < 16; ++s) of[s] = lds128(oa + s * 64);
      __builtin_amdgcn_sched_barrier(0);
      f4 d0 = {0.f, 0.f, 0.f, 0.f}, d1 = d0;
#pragma unroll
      for (int s = 0; s < 8; ++s) { d0 = mfma32(wf[2 * s], of[2 * s], d0); d1 = mfma32(wf[2 * s + 1], of[2 * s + 1], d1); }
      d0 += d1;
      const unsigned half = (c16 & 1) ? 0xffff0000u : 0x0000ffffu;
      const int rr = (c16 >> 1) & 3;
      f4 o4[4];
#pragma unroll
      for (int cb = 0; cb < 4; ++cb) o4[cb] = (f4){0.f, 0.f, 0.f, 0.f};
#pragma unroll
      for (int u = 0; u < 4; ++u) {
        const unsigned x = pk(d0[u], d0[u]) & half;
        const v4u bo = {rr == 0 ? x : 0u, rr == 1 ? x : 0u, rr == 2 ? x : 0u, rr == 3 ? x : 0u};
#pragma unroll
        for (int cb = 0; cb < 4; ++cb) o4[cb] = mfma32(OU[4 * u + cb], bo, o4[cb]);
      }
#pragma unroll
      for (int cb = 0; cb < 4; ++cb)
#pragma unroll
        for (int r = 0; r < 4; ++r) stof(T_RED + w * 4096 + cb * 1024 + r * 64 + g * 256 + c16 * 4, o4[cb][r]);
    }
    bar();
    {
      const int j = tid >> 3, pr = tid & 7;
      float s0 = 0.f, s1 = 0.f;
#pragma unroll
      for (int i = 0; i < 8; ++i) { const f2 t = *(const LAS f2*)(T_RED + i * 4096 + tid * 8); s0 += t.x; s1 += t.y; }
      gst128(rs, WS_F + (((64 + j) * 128 + h) * 16 + 2 * pr) * 8, (v4u){asu(s0), tag, asu(s1), tag});
    }
  }
}

extern "C" void kernel_launch(void* const* d_in, const int* in_sizes, int n_in, void* d_out, int out_size, void* d_ws, size_t ws_size,
                              hipStream_t stream) {
  (void)in_sizes; (void)n_in; (void)out_size; (void)ws_size;
  (void)hipFuncSetAttribute((const void*)fused_decode, hipFuncAttributeMaxDynamicSharedMemorySize, LDS_BYTES);
  fused_decode<<<256, 512, LDS_BYTES, stream>>>(
      (const char*)d_in[0], (const char*)d_in[1], (const char*)d_in[2], (const char*)d_in[3], (const char*)d_in[4], (const char*)d_in[5],
      (const char*)d_in[6], (const char*)d_in[7], (const char*)d_in[8], (const char*)d_in[9], (const char*)d_in[10], (unsigned short*)d_out,
      (char*)d_ws);
}
```

```cpp
#define DBG_TS
#define PROBE 0
#define PROLE 3
#define PSTAT 0
#define PZERO 0
#define XOFF 0
#include <hip/hip_runtime.h>
#include <stdint.h>
#pragma clang diagnostic ignored "-Wint-to-pointer-cast"

#define GAS __attribute__((address_space(1)))
#define LAS __attribute__((address_space(3)))
typedef unsigned v4u __attribute__((ext_vector_type(4)));
typedef unsigned v2u __attribute__((ext_vector_type(2)));
typedef float f4 __attribute__((ext_vector_type(4)));
typedef float f2 __attribute__((ext_vector_type(2)));
typedef float f16v __attribute__((ext_vector_type(16)));
typedef short s4 __attribute__((ext_vector_type(4)));
typedef __bf16 b8 __attribute__((ext_vector_type(8)));
typedef __bf16 b2 __attribute__((ext_vector_type(2)));

#define W1 12
#define W2 24
#define J1 18
#define ROT 0
#define BROT 0
#define GMASK 0
#define FMASK 0
#define VDL 0
#define TE 6
#define UE 0
#define PREN 32
#define G2 0
#define F2 0
#define EXCL 0
#define VTAIL 0
#define ODL 5
#define BPRIO 0
#define TLBW 0
#define TLBB 0
#define TLBE 0
#define TLBN 2
#define TLBS 2097152
#define DLY2 40
#define DLYP 30
#define DLYB 10
#define NB0 55
#define NB1 52
#define WB 4
#define PRE2 2
#define SKIPA 0
#define SKIPB 0
#define WT 6
#define CODE_KB 25
#define QGO 4

namespace {

constexpr int NB_CU = 64, NT_CU = 32, NSPLIT_A = 10, NSLOT = 14, NCONS = 6;
constexpr float SC = 0.08838834764831845f * 1.4426950408889634f;
constexpr int SPIN_CAP = 1 << 18;

#define NS3 4
#define PSL 4
#define PSL1 8
#define PRESL 8
#define GSL 1
#define FSL 0
constexpr unsigned FS = 128;
constexpr unsigned WS_E = 0;
constexpr unsigned WS_FQ = 4096;
constexpr unsigned WS_FP = WS_FQ + 16384;
constexpr unsigned WS_QG = 65536;
constexpr unsigned WS_QAB = WS_QG + 262144;
constexpr unsigned WS_PBM = WS_QAB + 65536;
constexpr unsigned WS_PAM = WS_PBM + 32768;
constexpr unsigned WS_PAO = WS_PAM + 131072;
constexpr unsigned WS_PB = WS_PAO + 1048576;
constexpr unsigned WS_F = WS_PB + 2097152;
constexpr unsigned WS_END = WS_F + 2097152;
constexpr unsigned WS_TS = 8u << 20;

constexpr unsigned LDS_BYTES = 163840;
constexpr unsigned SCR = 131072;
constexpr unsigned A_MISC = NSLOT * 8192;
constexpr unsigned A_LANDK = A_MISC, A_LANDV = A_MISC + 4, A_QUSED = A_MISC + 8, A_QCNT = A_MISC + 12, A_QB = A_MISC + 16, A_QRDY = A_MISC + 20, A_OUL = A_MISC + 24, A_OUL2 = A_MISC + 28;
constexpr unsigned A_DONE = A_MISC + 64;
constexpr unsigned A_QU2 = 10 * 8192;
constexpr unsigned A_QIMG = A_MISC + 256;
constexpr unsigned A_ML = A_MISC + 1280;
constexpr unsigned A_OIMG = A_MISC + 1792;
constexpr unsigned A_ST = A_MISC + 2048;
constexpr unsigned A_XIMG = A_MISC + 4096, A_DUMMY = A_MISC + 5120, A_QBI = A_MISC + 6144, A_QAS = A_MISC + 6656;
constexpr unsigned SLOT0 = 0, SLOT1 = 65536;
constexpr unsigned B_MX = SCR, B_AL = SCR + 512, B_LS = SCR + 640, B_P = SCR + 2048;
constexpr unsigned B_FM = SCR + 1280, B_FIN = SCR + 16384, B_T = SLOT1, B_DUMMY = SCR + 8192;
constexpr unsigned T_QBI = SCR, T_QAS = SCR + 1024, T_OL = SCR + 4096, T_RDY = SCR + 6400, T_WN = SCR + 6656, T_RED = 0;

__device__ __forceinline__ v4u lds128(unsigned a) { return *(const LAS v4u*)a; }
__device__ __forceinline__ void sto128(unsigned a, v4u v) { *(LAS v4u*)a = v; }
__device__ __forceinline__ void sto64(unsigned a, v2u v) { *(LAS v2u*)a = v; }
__device__ __forceinline__ void sto32(unsigned a, unsigned v) { *(LAS unsigned*)a = v; }
__device__ __forceinline__ float ldsf(unsigned a) { return *(const LAS float*)a; }
__device__ __forceinline__ void stof(unsigned a, float v) { *(LAS float*)a = v; }
__device__ __forceinline__ v2u trd(unsigned a) { return __builtin_bit_cast(v2u, __builtin_amdgcn_ds_read_tr16_b64_v4i16((LAS s4*)a)); }
__device__ __forceinline__ v4u trd2(unsigned a0, unsigned a1) { const v2u x = trd(a0), y = trd(a1); return (v4u){x.x, x.y, y.x, y.y}; }
__device__ __forceinline__ f4 mfma32(v4u a, v4u b, f4 c) {
  return __builtin_amdgcn_mfma_f32_16x16x32_bf16(__builtin_bit_cast(b8, a), __builtin_bit_cast(b8, b), c, 0, 0, 0); }
__device__ __forceinline__ f4 mfma16(v2u a, v2u b, f4 c) {
  return __builtin_amdgcn_mfma_f32_16x16x16bf16_1k(__builtin_bit_cast(s4, a), __builtin_bit_cast(s4, b), c, 0, 0, 0); }
__device__ __forceinline__ f16v mfmaL(v4u a, v4u b, f16v c) {
  return __builtin_amdgcn_mfma_f32_32x32x16_bf16(__builtin_bit_cast(b8, a), __builtin_bit_cast(b8, b), c, 0, 0, 0); }
__device__ __forceinline__ unsigned pk(float lo, float hi) { return __builtin_bit_cast(unsigned, __builtin_convertvector((f2){lo, hi}, b2)); }
__device__ __forceinline__ float asf(unsigned u) { return __builtin_bit_cast(float, u); }
__device__ __forceinline__ unsigned asu(float f) { return __builtin_bit_cast(unsigned, f); }
__device__ __forceinline__ float ex2(float x) { return __builtin_amdgcn_exp2f(x); }
template <int CTRL> __device__ __forceinline__ float dpp(float x) {
  return __builtin_bit_cast(float, __builtin_amdgcn_mov_dpp(__builtin_bit_cast(int, x), CTRL, 0xf, 0xf, true)); }
constexpr int XOR1 = 0xB1, XOR2 = 0x4E, ROR4 = 0x124, ROR8 = 0x128;
__device__ __forceinline__ float x16max(float x) { auto s = __builtin_amdgcn_permlane16_swap(asu(x), asu(x), false, false); return fmaxf(asf(s[0]), asf(s[1])); }
__device__ __forceinline__ float x32max(float x) { auto s = __builtin_amdgcn_permlane32_swap(asu(x), asu(x), false, false); return fmaxf(asf(s[0]), asf(s[1])); }
__device__ __forceinline__ float x16sum(float x) { auto s = __builtin_amdgcn_permlane16_swap(asu(x), asu(x), false, false); return asf(s[0]) + asf(s[1]); }
__device__ __forceinline__ float x32sum(float x) { auto s = __builtin_amdgcn_permlane32_swap(asu(x), asu(x), false, false); return asf(s[0]) + asf(s[1]); }
__device__ __forceinline__ float row16max(float x) { x = fmaxf(x, dpp<XOR1>(x)); x = fmaxf(x, dpp<XOR2>(x)); x = fmaxf(x, dpp<ROR4>(x)); return fmaxf(x, dpp<ROR8>(x)); }
__device__ __forceinline__ float row16sum(float x) { x += dpp<XOR1>(x); x += dpp<XOR2>(x); x += dpp<ROR4>(x); return x + dpp<ROR8>(x); }

template <int IMM> __device__ __forceinline__ void dma(const void* sbase, unsigned voff, unsigned lds) {
  asm volatile("s_mov_b32 m0, %2\n\ts_nop 0\n\tglobal_load_lds_dwordx4 %0, %1 offset:%3 nt" :: "v"(voff), "s"(sbase), "s"(lds), "n"(IMM) : "memory");
}
template <int W> __device__ __forceinline__ void dma4(const void* sbase, unsigned v0, unsigned v1, unsigned v2, unsigned v3, unsigned lds) {
  asm volatile("s_mov_b32 m0, %5\n\ts_waitcnt vmcnt(%6)\n\tglobal_load_lds_dwordx4 %0, %4 nt\n\tglobal_load_lds_dwordx4 %1, %4 offset:1024 nt\n\t"
               "s_waitcnt vmcnt(%6)\n\tglobal_load_lds_dwordx4 %2, %4 offset:2048 nt\n\tglobal_load_lds_dwordx4 %3, %4 offset:3072 nt"
               :: "v"(v0), "v"(v1), "v"(v2), "v"(v3), "s"(sbase), "s"(lds), "n"(W - 2) : "memory");
}
__device__ __forceinline__ void dma_keep(const void* sbase, unsigned voff, unsigned lds) {
  asm volatile("s_mov_b32 m0, %2\n\ts_nop 0\n\tglobal_load_lds_dwordx4 %0, %1" :: "v"(voff), "s"(sbase), "s"(lds) : "memory");
}
__device__ __forceinline__ void touch(const void* sbase, unsigned voff, unsigned lds) {
  asm volatile("s_mov_b32 m0, %2\n\ts_nop 0\n\tglobal_load_lds_dword %0, %1 nt" :: "v"(voff), "s"(sbase), "s"(lds) : "memory");
}
template <int N> __device__ __forceinline__ void vmwait() { asm volatile("s_waitcnt vmcnt(%0)" :: "n"(N) : "memory"); }
__device__ __forceinline__ void bar() { asm volatile("s_waitcnt lgkmcnt(0)\n\ts_barrier" ::: "memory"); }
__device__ __forceinline__ void lgk0() { asm volatile("s_waitcnt lgkmcnt(0)" ::: "memory"); }
__device__ __forceinline__ void cfence() { asm volatile("" ::: "memory"); }
__device__ __forceinline__ unsigned opq(unsigned x) { asm volatile("" : "+v"(x)); return x; }
__device__ __forceinline__ unsigned lds_peek(unsigned a) { return __builtin_amdgcn_readfirstlane(*(volatile LAS unsigned*)a); }
template <int SL = 1> __device__ __forceinline__ void lds_wait_ge(unsigned a, unsigned want) {
  _Pragma("nounroll") for (int it = 0; it < SPIN_CAP * 8; ++it) { if (lds_peek(a) >= want) break; if (SL) __builtin_amdgcn_s_sleep(SL); }
  cfence();
}
typedef __amdgpu_buffer_rsrc_t rsrc_t;
constexpr int SC1 = 16;
__device__ __forceinline__ v4u gld128(rsrc_t r, unsigned off) { return __builtin_amdgcn_raw_buffer_load_b128(r, off, 0, SC1); }
__device__ __forceinline__ v2u gld64(rsrc_t r, unsigned off) { return __builtin_amdgcn_raw_buffer_load_b64(r, off, 0, SC1); }
__device__ __forceinline__ unsigned gld32(rsrc_t r, unsigned off) { return __builtin_amdgcn_raw_buffer_load_b32(r, off, 0, SC1); }
__device__ __forceinline__ void gst128(rsrc_t r, unsigned off, v4u v) { __builtin_amdgcn_raw_buffer_store_b128(v, r, off, 0, SC1); }
__device__ __forceinline__ void gst64(rsrc_t r, unsigned off, v2u v) { __builtin_amdgcn_raw_buffer_store_b64(v, r, off, 0, SC1); }
__device__ __forceinline__ void gst32(rsrc_t r, unsigned off, unsigned v) { __builtin_amdgcn_raw_buffer_store_b32(v, r, off, 0, SC1); }
__device__ __forceinline__ void flag_line(rsrc_t r, unsigned off, int lane, unsigned tag) { if (lane < 8) gst128(r, off + lane * 16, (v4u){tag, tag, tag, tag}); }
__device__ __forceinline__ bool tagok(v4u v, unsigned tag) { return v.y == tag && v.w == tag; }
template <int N, int GAP, class F> __device__ __forceinline__ void sweep2(v4u (&v)[N], rsrc_t rs, unsigned tag, F off) {
  v4u a[N], b[N];
#pragma unroll
  for (int k = 0; k < N; ++k) a[k] = gld128(rs, off(k));
  __builtin_amdgcn_s_sleep(GAP);
  _Pragma("nounroll") for (int it = 0; it < SPIN_CAP; ++it) {
    cfence();
#pragma unroll
    for (int k = 0; k < N; ++k) b[k] = gld128(rs, off(k));
    cfence();
    bool ok = true;
#pragma unroll
    for (int k = 0; k < N; ++k) { ok &= tagok(a[k], tag); v[k] = a[k]; }
    if (__all(ok)) break;
#pragma unroll
    for (int k = 0; k < N; ++k) a[k] = gld128(rs, off(k));
    cfence();
    ok = true;
#pragma unroll
    for (int k = 0; k < N; ++k) { ok &= tagok(b[k], tag); v[k] = b[k]; }
    if (__all(ok)) break;
  }
}
template <int N, int SL, bool MASK, class F> __device__ __forceinline__ void sweep(v4u (&v)[N], rsrc_t rs, unsigned tag, F off) {
  if (MASK) {
#pragma unroll
    for (int k = 0; k < N; ++k) v[k] = (v4u){0u, ~tag, 0u, ~tag};
  }
  _Pragma("nounroll") for (int it = 0; it < SPIN_CAP; ++it) {
    cfence();
    v4u t[N];
#pragma unroll
    for (int k = 0; k < N; ++k) t[k] = gld128(rs, MASK && tagok(v[k], tag) ? 0xfffffff0u : off(k));
    bool ok = true;
#pragma unroll
    for (int k = 0; k < N; ++k) { if (!MASK || !tagok(v[k], tag)) v[k] = t[k]; ok &= tagok(v[k], tag); }
    if (__all(ok)) break;
    if (SL) __builtin_amdgcn_s_sleep(SL);
  }
}

#define STAMP(i) do { unsigned long long t_; asm volatile("s_memrealtime %0\n\ts_waitcnt lgkmcnt(0)" : "=s"(t_) :: "memory"); tsv[i] = (unsigned)t_; } while (0)
#define STAMP_DECL unsigned tsv[16] = {0,0,0,0,0,0,0,0,0,0,0,0,0,0,0,0}
#define STAMP_OUT() do { if (lane == 0) gst128(rs2, WS_TS + (blockIdx.x * 8 + wave) * 16, (v4u){tsv[0], tag, tsv[PROBE], tag}); \
  if (blockIdx.x == 0 && wave == 0) { unsigned mn = 0xffffffffu; float mx = 0.f, sm = 0.f, cnt = 0.f; unsigned ref = tsv[0] - 1000u; \
    for (int i_ = 0; i_ < 32; ++i_) { v4u v_; for (int it = 0; it < SPIN_CAP; ++it) { cfence(); v_ = gld128(rs2, WS_TS + (i_ * 64 + lane) * 16); if (__all(tagok(v_, tag))) break; } \
      mn = min(mn, v_.x - ref); const bool role_ = PROLE == 3 || (PROLE == 1 ? (i_ < 8) : PROLE == 2 ? (i_ >= 8 && i_ < 12) : (i_ >= 12)); if (v_.z != 0u && role_) { const float d_ = (float)(v_.z - ref); mx = fmaxf(mx, d_); sm += d_; cnt += 1.f; } } \
    float mnf = -(float)mn; mnf = -x32max(x16max(row16max(mnf))); mx = x32max(x16max(row16max(mx))); sm = x32sum(x16sum(row16sum(sm))); cnt = x32sum(x16sum(row16sum(cnt))); \
    const float X = PZERO ? 0.f : (PSTAT ? sm / cnt : mx) - mnf; const unsigned until = tsv[15] + 3000u + 500u * (unsigned)fmaxf(floorf(X * 0.1f) - XOFF, 0.f); \
    for (int it = 0; it < 100000000; ++it) { unsigned long long t_; asm volatile("s_memrealtime %0\n\ts_waitcnt lgkmcnt(0)" : "=s"(t_) :: "memory"); if ((int)((unsigned)t_ - __builtin_amdgcn_readfirstlane(until)) >= 0) break; } } } while (0)

__device__ __forceinline__ void gather_qb(unsigned img, int h, int lane, rsrc_t rs, unsigned tag) {
  v4u qg[4];
  const unsigned off = WS_QG + (h * 128 + 2 * lane) * 8;
  _Pragma("nounroll") for (int it = 0; it < SPIN_CAP; ++it) {
    cfence();
    bool ok = true;
#pragma unroll
    for (int k = 0; k < 4; ++k) { qg[k] = gld128(rs, off + (k & 1) * 65536 + (k >> 1) * 32768); ok &= tagok(qg[k], tag); }
    if (__all(ok)) break;
    __builtin_amdgcn_s_sleep(1);
  }
  sto32(img + lane * 4, pk(asf(qg[0].x) + asf(qg[1].x), asf(qg[0].z) + asf(qg[1].z)));
  sto32(img + 256 + lane * 4, pk(asf(qg[2].x) + asf(qg[3].x), asf(qg[2].z) + asf(qg[3].z)));
}
__device__ __forceinline__ void lds_inc(unsigned a) { __hip_atomic_fetch_add((LAS unsigned*)a, 1u, __ATOMIC_RELAXED, __HIP_MEMORY_SCOPE_WORKGROUP); }

__device__ __forceinline__ void out_frags(unsigned ou, int w, int lane, v4u (&wf)[4]) {
  const int g = lane >> 4, q = (lane & 15) >> 2, p = lane & 3, f3 = q | ((g & 1) << 2);
#pragma unroll
  for (int tt = 0; tt < 4; ++tt) {
    const unsigned a0 = ou + (8 * g + q) * 1024 + (((2 * (4 * w + tt) + (p >> 1)) ^ (f3 << 1)) << 4) + (p & 1) * 8;
    wf[tt] = trd2(a0, a0 + 4096);
  }
}

}

#define KALIGN 4096
__global__ void __launch_bounds__(512, 2) __attribute__((aligned(KALIGN))) fused_decode(
    const char* __restrict__ Ax, const char* __restrict__ Kc, const char* __restrict__ Vc, const char* __restrict__ Wq, const char* __restrict__ Wo,
    const char* __restrict__ Bx, const char* __restrict__ Ckv, const char* __restrict__ Bwq, const char* __restrict__ Wuk, const char* __restrict__ Wuv,
    const char* __restrict__ Bwo, unsigned short* __restrict__ out, char* __restrict__ ws) {
  const int bid = blockIdx.x, tid = threadIdx.x, lane = tid & 63, wave = __builtin_amdgcn_readfirstlane(tid >> 6);
  const int g = lane >> 4, c16 = lane & 15;
  STAMP_DECL;
  STAMP(0);
  const int iA = bid - (NB_CU + NT_CU);
  const bool isA = iA >= 0, isB = bid < NB_CU;
  const int ty = iA >> 6;
  int dA = 95 - bid, dU = 223 - bid, dB = bid - 64, dQ = 159 - bid, dW = 2 - wave;
  asm("" : "+s"(dA), "+s"(dU), "+s"(dB), "+s"(dQ), "+s"(dW));
  const int mA = dA >> 31, mU = dU >> 31, mB = dB >> 31, mP = mA & ~mU, sh = mP & 1;
  const int hh = (bid >> sh) & 31;
  const unsigned row0 = (mA & ((((bid | mU) & 1) << 8) + 64 * wave - 64)) | (~mA & (32 * wave));
  const unsigned l0 = (mA & (SCR - 16384 + wave * 16384 - ((dW >> 31) & (SCR + 32768 - A_QU2)))) | (~mA & (wave * 8192));
  const unsigned karg = 0x40 - (mP & (8 + ((dQ >> 31) & 32))), more = mA;
  if ((unsigned)(wave + mA + (mB & 8)) < (unsigned)(8 + (mA & -4))) {
    const unsigned b0 = (row0 + g) * 8192 + hh * 256 + ((c16 ^ (g << sh)) << 4), x1 = ~mP & 64;
    unsigned v0 = b0, v1 = (b0 ^ x1) + 32768, v2 = (b0 ^ 128) + 65536, v3 = (b0 ^ x1 ^ 128) + 98304;
    if (DLYP && karg == 0x18) __builtin_amdgcn_s_sleep(DLYP);
    unsigned long long wp;
#define D4 "global_load_lds_dwordx4 %1, %0 nt\n\ts_add_u32 m0, m0, 0x400\n\tv_add_u32 %1, 0x20000, %1\n\t" \
           "global_load_lds_dwordx4 %2, %0 nt\n\ts_add_u32 m0, m0, 0x400\n\tv_add_u32 %2, 0x20000, %2\n\t" \
           "global_load_lds_dwordx4 %3, %0 nt\n\ts_add_u32 m0, m0, 0x400\n\tv_add_u32 %3, 0x20000, %3\n\t" \
           "global_load_lds_dwordx4 %4, %0 nt\n\ts_add_u32 m0, m0, 0x400\n\tv_add_u32 %4, 0x20000, %4\n\t"
    asm volatile("s_load_dwordx2 %0, %5, %6\n\ts_mov_b32 m0, %7\n\ts_waitcnt lgkmcnt(0)\n\t" D4 D4
                 "s_cmp_eq_u32 %8, 0\n\ts_cbranch_scc1 1f\n\t" D4 D4 "1:"
                 : "=&s"(wp), "+v"(v0), "+v"(v1), "+v"(v2), "+v"(v3)
                 : "s"(__builtin_amdgcn_kernarg_segment_ptr()), "s"(karg), "s"(l0), "s"(more) : "memory", "scc");
#undef D4
  }
  {
    unsigned long long pc;
    asm volatile("s_getpc_b64 %0" : "=s"(pc));
    const int kb = ((bid >> 3) + 4 * wave) & 31;
    if (CODE_KB) dma_keep((const char*)((pc & ~1023ull) + (unsigned)((kb < CODE_KB ? kb : CODE_KB - 1) * 1024)), lane * 16, isA ? A_DUMMY : B_DUMMY);
  }
  const rsrc_t rs = __builtin_amdgcn_make_buffer_rsrc(ws, 0, WS_END, 0x00020000);
  const rsrc_t rs2 = __builtin_amdgcn_make_buffer_rsrc(ws, 0, 16u << 20, 0x00020000);
  asm volatile("" :: "s"(Ax), "s"(Kc), "s"(Vc), "s"(Wq), "s"(Wo), "s"(Bx), "s"(Ckv), "s"(Bwq), "s"(Wuk), "s"(Wuv), "s"(Bwo), "s"(out), "s"(ws));
  unsigned tag;
#define GET_TAG() do { asm volatile("s_load_dword %0, %1, 0x0\n\ts_waitcnt lgkmcnt(0)" : "=s"(tag) : "s"(ws) : "memory"); tag += 1u; } while (0)
  const char* cp = Ckv + ((size_t)(bid >> 5) * 8192 + 256 * (bid & 31) + 8 * wave) * 1024;
  const int brot = BROT ? (bid >> BROT) : 0;
  const unsigned vc0 = (lane ^ ((wave & 1) ? 3 : 0)) << 4, vc1 = (lane ^ ((wave & 1) ? 1 : 2)) << 4;
#define LD_C(rho, u) dma<((u) & 3) * 1024>(cp + (size_t)((((rho) + brot) & 3) * 65536) + ((u) >> 2) * 4096, (((u) >> 2) ? vc1 : vc0) ^ (((u) & 3) << 6), ((rho) & 1) * 65536 + wave * 8192 + ((u) >> 2) * 4096)
  if (isA) {
    if (wave == 1 && ty < 2) dma<0>((ty ? Ax : Bx) + 512 * (bid & 1), (lane >> 5) * 1024 + (lane & 31) * 16, A_XIMG);
    if (tid < 32) sto32(A_MISC + tid * 4, 0u);
    bar();
  } else if (isB) {
    __builtin_amdgcn_s_sleep(DLYB);
    LD_C(0, 0); LD_C(0, 1); LD_C(0, 2); LD_C(0, 3); LD_C(0, 4); LD_C(0, 5);
  }

  if (isA) {
    const int split = iA >> 4, pair = (iA + split) & 15, b = pair >> 3, kvh = pair & 7;
    constexpr int NB2 = (512 - 4 * NB0 - 4 * NB1) / 2;
    const int nblk = ty == 0 ? NB0 : ty == 1 ? NB1 : NB2, blk0 = (ty == 0 ? 0 : ty == 1 ? 4 * NB0 : 4 * (NB0 + NB1)) + (split & 3) * nblk;
    if (wave == 0 || wave == 7) {
      __builtin_amdgcn_s_setprio(3);
      static_assert(NSPLIT_A == 10 && W1 >= 4 && W1 % 4 == 0 && W2 % 4 == 0 && W1 <= W2 && W2 <= 24 && J1 <= NB0 && J1 <= NB1 && J1 <= NB2, "window");
      const bool isv = wave == 7;
      int pub = 0, j = 0;
      unsigned sl = 0;
      int pos = (pair * ROT) % nblk;
      const char* gp = (isv ? Vc : Kc) + ((size_t)pair * 8192 + (blk0 + pos) * 16) * 256;
      const unsigned v0 = opq(g * 256 + ((c16 ^ (isv ? g << 1 : g)) << 4));
      const unsigned v1 = opq(v0 ^ (isv ? 128 : 64)), v2 = opq(v0 ^ (isv ? 0 : 128)), v3 = opq(v0 ^ (isv ? 128 : 192));
      const unsigned lo = isv ? 4096 : 0, la = opq(lane ? A_MISC + 1536 + lane * 4 : isv ? A_LANDV : A_LANDK);
      unsigned dn = 0;
#define PUBLISH(val) do { pub = max(pub, (val)); *(volatile LAS unsigned*)la = (unsigned)pub; } while (0)
#define GATE(addr, need) do { if (lds_peek(addr) < (need)) { vmwait<0>(); PUBLISH(j); lds_wait_ge(addr, need); } } while (0)
#define PART(W) do { \
        if (j >= NSLOT) { if ((unsigned)__builtin_amdgcn_readfirstlane(dn) < (unsigned)(j - NSLOT + 1)) GATE(A_DONE + sl, (unsigned)(j - NSLOT + 1)); } \
        else if (j == 10) GATE(A_QUSED, 4u); \
        dma4<W>(gp, v0, v1, v2, v3, sl * 2048 + lo); \
        ++j; gp += 4096; if (ROT && ++pos == nblk) gp -= nblk * 4096; sl = sl == 4 * (NSLOT - 1) ? 0 : sl + 4; \
        PUBLISH(j - W / 4); dn = *(volatile LAS unsigned*)(A_DONE + sl); } while (0)
      STAMP(9);
      GET_TAG();
      if (UE && ty == 2) {
        lds_wait_ge(A_OUL2, 1u);
#pragma nounroll
        while (j < UE) PART(W2);
      }
      lds_wait_ge(A_QCNT, 4u);
      if (VDL && isv) lds_wait_ge(A_LANDK, (unsigned)VDL);
      STAMP(10);
#pragma nounroll
      while (j < J1) PART(W1);
      STAMP(11);
#pragma nounroll
      while (j < nblk - (isv ? VTAIL : 0)) PART(W2);
      if (isv) {
#pragma nounroll
        while (j < nblk) PART(44);
      }
      if (ty < 2 && !isv) {
        const char* op = Wo + (size_t)iA * 32768;
        const unsigned o0 = lane << 4, o1 = o0 ^ 32, o2 = o0 ^ 64, o3 = o0 ^ 96;
        vmwait<20>(); PUBLISH(j - 5); vmwait<16>(); PUBLISH(j - 4); vmwait<12>(); PUBLISH(j - 3); vmwait<8>(); PUBLISH(j - 2); vmwait<4>(); PUBLISH(j - 1); vmwait<0>(); PUBLISH(j);
        if (ODL) lds_wait_ge(A_LANDV, (unsigned)(nblk - ODL));
#pragma unroll
        for (int k = 0; k < 8; ++k) {
          const unsigned x = ((k >> 1) & 1) << 7;
          dma4<62>(op + k * 4096, o0 ^ x, o1 ^ x, o2 ^ x, o3 ^ x, SCR + k * 4096);
        }
      } else {
        vmwait<40>(); PUBLISH(j - 10); vmwait<36>(); PUBLISH(j - 9); vmwait<32>(); PUBLISH(j - 8);
        vmwait<28>(); PUBLISH(j - 7); vmwait<24>(); PUBLISH(j - 6); vmwait<20>(); PUBLISH(j - 5); vmwait<16>(); PUBLISH(j - 4);
        vmwait<12>(); PUBLISH(j - 3); vmwait<8>(); PUBLISH(j - 2); vmwait<4>(); PUBLISH(j - 1); vmwait<0>(); PUBLISH(j);
      }
      __builtin_amdgcn_s_setprio(0);
      STAMP(5);
#undef PUBLISH
#undef GATE
#undef PART
    } else {
      GET_TAG();
      vmwait<0>();
      if (wave <= 4) {
        const int w1 = wave - 1;
        if (ty < 2) {
          if (lane == 0) lds_inc(A_QCNT);
          const int q = c16 >> 2, p = lane & 3, f3 = q | ((g & 1) << 2);
          const unsigned a0 = (8 * g + q) * 256 + (((4 * w1 + (p >> 1)) ^ (f3 << 1)) << 4) + (p & 1) * 8;
          const unsigned aa[2] = {opq(SCR + a0), opq(A_QU2 + a0)};
          lds_wait_ge(A_QCNT, 4u);
          STAMP(1);
          v4u w0[8], w1f[8], xf[8];
#pragma unroll
          for (int s = 0; s < 8; ++s) {
            const unsigned a = aa[s >> 2] + (s & 3) * 8192;
            w0[s] = trd2(a, a + 1024); w1f[s] = trd2((a ^ 32), (a ^ 32) + 1024);
            xf[s] = lds128(A_XIMG + (c16 & 1) * 512 + g * 16 + s * 64);
          }
          __builtin_amdgcn_sched_barrier(0);
          f4 d0 = {0.f, 0.f, 0.f, 0.f}, d1 = d0;
#pragma unroll
          for (int s = 0; s < 8; ++s) { d0 = mfma32(w0[s], xf[s], d0); d1 = mfma32(w1f[s], xf[s], d1); }
          const int pr = (c16 >> 1) & 1, nb = (c16 >> 2) & 1;
          const f4 e = nb ? d1 : d0;
          if (c16 < 8) gst128(rs, WS_QG + (((ty * 2 + (bid & 1)) * 2 + (c16 & 1)) * 4096 + hh * 128 + 32 * w1 + 16 * nb + 4 * g + 2 * pr) * 8,
                              (v4u){asu(pr ? e[2] : e[0]), tag, asu(pr ? e[3] : e[1]), tag});
          lgk0();
        } else {
          STAMP(1);
          if (w1 == 3 && lane == 0) *(volatile LAS unsigned*)A_OUL2 = 1u;
          const unsigned a0 = opq((w1 < 2 ? SCR : A_QU2 - 32768) + w1 * 16384 + c16 * 256 + ((g ^ c16) << 4));
          v4u uf[16];
#pragma unroll
          for (int i = 0; i < 16; ++i) uf[i] = lds128((a0 ^ ((i & 3) << 6)) + (i >> 2) * 4096);
          lds_wait_ge(A_QB, 1u);
          v4u qb[4];
#pragma unroll
          for (int s = 0; s < 4; ++s) qb[s] = lds128(A_QBI + (c16 & 1) * 256 + s * 64 + g * 16);
          __builtin_amdgcn_sched_barrier(0);
          f4 d[4];
#pragma unroll
          for (int rb = 0; rb < 4; ++rb) d[rb] = (f4){0.f, 0.f, 0.f, 0.f};
#pragma unroll
          for (int s = 0; s < 4; ++s)
#pragma unroll
            for (int rb = 0; rb < 4; ++rb) d[rb] = mfma32(uf[4 * rb + s], qb[s], d[rb]);
          const int rb = (c16 >> 1) & 3;
          const f4 e = rb == 0 ? d[0] : rb == 1 ? d[1] : rb == 2 ? d[2] : d[3];
          if (c16 < 8) sto64(A_QAS + w1 * 256 + (c16 & 1) * 128 + (16 * rb + 4 * g) * 2, (v2u){pk(e[0], e[1]), pk(e[2], e[3])});
          lgk0();
          if (lane < 16) gst128(rs, WS_QAB + ((lane >> 3) * 32 + hh) * 1024 + 512 + w1 * 128 + (lane & 7) * 16, lds128(A_QAS + w1 * 256 + lane * 16));
          vmwait<0>();
        }
        unsigned old = 0;
        if (lane == 0) old = __hip_atomic_fetch_add((LAS unsigned*)A_QUSED, 1u, __ATOMIC_RELAXED, __HIP_MEMORY_SCOPE_WORKGROUP);
        if (ty == 2 && __builtin_amdgcn_readfirstlane(old) == 3u) {
          flag_line(rs, WS_FQ + (32 + hh) * FS, lane, tag);
          cfence();
          if (lane == 0) *(volatile LAS unsigned*)A_QCNT = 4u;
        }
        STAMP(2);
      }
      if (wave == 5) {
        if (ty == 2) {
          lds_wait_ge(A_OUL2, 1u);
          gather_qb(A_QBI, hh, lane, rs, tag);
          lgk0();
          if (lane == 0) *(volatile LAS unsigned*)A_QB = 1u;
          STAMP(1);
        }
        lds_wait_ge(A_LANDK, ty == 2 ? 1u : (unsigned)QGO);
        v4u v[8];
        const unsigned off = WS_QG + 131072 + (b * 4096 + kvh * 512 + 2 * lane) * 8;
        _Pragma("nounroll") for (int it = 0; it < SPIN_CAP; ++it) {
          cfence();
          bool ok = true;
#pragma unroll
          for (int k = 0; k < 8; ++k) { v[k] = gld128(rs, off + (k & 1) * 65536 + (k >> 1) * 1024); ok &= tagok(v[k], tag); }
          if (__all(ok)) break;
          __builtin_amdgcn_s_sleep(4);
        }
#pragma unroll
        for (int k = 0; k < 4; ++k) sto32(A_QIMG + k * 256 + lane * 4, pk(asf(v[2 * k].x) + asf(v[2 * k + 1].x), asf(v[2 * k].z) + asf(v[2 * k + 1].z)));
        lgk0();
        if (lane == 0) *(volatile LAS unsigned*)A_QRDY = 1u;
      }
      lds_wait_ge(A_QRDY, 1u);
      STAMP(3);
      v4u qf[4];
#pragma unroll
      for (int s = 0; s < 4; ++s) qf[s] = lds128(A_QIMG + (c16 & 3) * 256 + s * 64 + g * 16);
      const unsigned ko = c16 * 256 + ((g ^ c16) << 4);
      const int vr = 4 * g + (c16 >> 2);
      const unsigned vo = 4096 + vr * 256 + ((((lane & 3) >> 1) ^ ((vr & 7) << 1)) << 4) + (lane & 1) * 8;
      float m = -INFINITY, l = 0.f;
      f4 acc[8];
#pragma unroll
      for (int t = 0; t < 8; ++t) acc[t] = (f4){0.f, 0.f, 0.f, 0.f};
      int slot = wave - 1;
      for (int j = wave - 1; j < nblk; j += NCONS) {
        const unsigned sb = slot * 8192;
        lds_wait_ge(A_LANDK, (unsigned)(j + 1));
        v4u kf[4];
#pragma unroll
        for (int s = 0; s < 4; ++s) kf[s] = lds128(sb + (ko ^ (s << 6)));
        __builtin_amdgcn_sched_barrier(0);
        f4 s4_ = {0.f, 0.f, 0.f, 0.f};
#pragma unroll
        for (int s = 0; s < 4; ++s) s4_ = mfma32(kf[s], qf[s], s4_);
        float mx = fmaxf(fmaxf(s4_[0], s4_[1]), fmaxf(s4_[2], s4_[3]));
        mx = x32max(x16max(mx));
        const float mn = fmaxf(m, mx), al = ex2((m - mn) * SC), mc = -mn * SC;
        const float p0 = ex2(fmaf(s4_[0], SC, mc)), p1 = ex2(fmaf(s4_[1], SC, mc)), p2 = ex2(fmaf(s4_[2], SC, mc)), p3 = ex2(fmaf(s4_[3], SC, mc));
        l = fmaf(l, al, (p0 + p1) + (p2 + p3));
        m = mn;
        v2u pb = {pk(p0, p1), pk(p2, p3)};
        unsigned va[8];
#pragma unroll
        for (int t = 0; t < 8; ++t) va[t] = sb + (vo ^ (t << 5));
        if (!__all(al == 1.f)) {
#pragma unroll
          for (int t = 0; t < 8; ++t) acc[t] *= al;
        }
        asm volatile("" : "+v"(pb), "+v"(l), "+v"(acc[0]), "+v"(acc[1]), "+v"(acc[2]), "+v"(acc[3]), "+v"(acc[4]), "+v"(acc[5]), "+v"(acc[6]), "+v"(acc[7]),
                     "+v"(va[0]), "+v"(va[1]), "+v"(va[2]), "+v"(va[3]), "+v"(va[4]), "+v"(va[5]), "+v"(va[6]), "+v"(va[7]));
        lds_wait_ge<0>(A_LANDV, (unsigned)(j + 1));
        v2u vf[8];
#pragma unroll
        for (int t = 0; t < 8; ++t) vf[t] = trd(va[t]);
        lgk0();
        if (lane == 0) *(volatile LAS unsigned*)(A_DONE + slot * 4) = (unsigned)(j + 1);
#pragma unroll
        for (int t = 0; t < 8; ++t) acc[t] = mfma16(vf[t], pb, acc[t]);
        slot += NCONS; if (slot >= NSLOT) slot -= NSLOT;
      }
      STAMP(4);
      l = x32sum(x16sum(l));
      if (c16 < 4) {
#pragma unroll
        for (int t = 0; t < 8; ++t) sto128(A_ST + (wave - 1) * 2048 + c16 * 512 + ((t ^ c16) * 64) + g * 16, __builtin_bit_cast(v4u, acc[t]));
        if (g == 0) sto64(A_ML + (c16 * 8 + wave - 1) * 8, (v2u){asu(m * SC), asu(l)});
      }
    }
    bar();
    if (wave >= 3 && wave <= 6) {
      const int hq = wave - 3;
      v2u ml[NCONS];
      f2 ow[NCONS];
      const unsigned sa = opq(A_ST + hq * 512 + ((lane * 8) ^ (hq << 6))), ma = opq(A_ML + hq * 64);
#pragma unroll
      for (int c = 0; c < NCONS; ++c) { ml[c] = *(const LAS v2u*)(ma + c * 8); ow[c] = *(const LAS f2*)(sa + c * 2048); }
      float M = asf(ml[0].x);
#pragma unroll
      for (int c = 1; c < NCONS; ++c) M = fmaxf(M, asf(ml[c].x));
      float L = 0.f, o0 = 0.f, o1 = 0.f;
#pragma unroll
      for (int c = 0; c < NCONS; ++c) { const float wt = ex2(asf(ml[c].x) - M); L = fmaf(wt, asf(ml[c].y), L); o0 = fmaf(wt, ow[c].x, o0); o1 = fmaf(wt, ow[c].y, o1); }
      const int rec = (b * 32 + kvh * 4 + hq) * NSPLIT_A + split;
      gst128(rs, WS_PAO + rec * 1024 + lane * 16, (v4u){asu(o0), tag, asu(o1), tag});
      if (lane < 8) gst128(rs, WS_PAM + rec * 128 + lane * 16, (v4u){asu(M), tag, asu(L), tag});
    }
    STAMP(6);
    if (ty < 2) {
      const int oh = iA >> 2, odq = iA & 3;
      if (wave == 0) { vmwait<0>(); if (lane == 0) lds_inc(A_OUL); }
      lds_wait_ge(A_OUL, 1u);
      v4u wf[4];
      out_frags(SCR, wave, lane, wf);
      const int tt = (c16 >> 1) & 3, pr = c16 >> 3, n = 64 * wave + 16 * tt + 4 * g + 2 * pr;
      unsigned fo = WS_F + ((((n >> 3) * 128 + iA) * 16) + (c16 & 1) * 8 + (n & 7)) * 8, oa = A_OIMG + (c16 & 1) * 64 + g * 16;
      asm volatile("" : "+v"(fo), "+v"(oa));
      if (wave == 1) {
        const int rec = ((lane >> 5) * 32 + oh) * NSPLIT_A + (g & 1) * 5;
        v4u vm[10];
        const unsigned o0_ = EXCL ? WS_PAO + rec * 1536 + odq * 384 + c16 * 16 : WS_PAO + rec * 1024 + (32 * odq + 2 * c16) * 8;
        const unsigned o1_ = EXCL ? WS_PAO + rec * 1536 + odq * 384 + 256 : WS_PAM + rec * 128 + odq * 16;
        if (G2) sweep2<10, G2>(vm, rs, tag, [&](int k) { return k < 5 ? o0_ + k * (EXCL ? 1536 : 1024) : o1_ + (k - 5) * (EXCL ? 1536 : 128); });
        else sweep<10, GSL, GMASK>(vm, rs, tag, [&](int k) { return k < 5 ? o0_ + k * (EXCL ? 1536 : 1024) : o1_ + (k - 5) * (EXCL ? 1536 : 128); });
        const v4u* v = vm; const v4u* ml = vm + 5;
        float M = asf(ml[0].x);
#pragma unroll
        for (int i = 1; i < 5; ++i) M = fmaxf(M, asf(ml[i].x));
        M = x16max(M);
        float L = 0.f, o0 = 0.f, o1 = 0.f;
#pragma unroll
        for (int i = 0; i < 5; ++i) { const float wt = ex2(asf(ml[i].x) - M); L = fmaf(wt, asf(ml[i].z), L); o0 = fmaf(wt, asf(v[i].x), o0); o1 = fmaf(wt, asf(v[i].z), o1); }
        const float inv = 1.f / x16sum(L);
        o0 = x16sum(o0); o1 = x16sum(o1);
        if (!(g & 1)) sto32(A_OIMG + (lane >> 5) * 64 + c16 * 4, pk(o0 * inv, o1 * inv));
      }
      STAMP(7);
      bar();
      const v4u ob = lds128(oa);
      f4 d[4];
#pragma unroll
      for (int t = 0; t < 4; ++t) d[t] = mfma32(wf[t], ob, (f4){0.f, 0.f, 0.f, 0.f});
      const f4 e = tt == 0 ? d[0] : tt == 1 ? d[1] : tt == 2 ? d[2] : d[3];
      gst128(rs, fo, (v4u){asu(pr ? e[2] : e[0]), tag, asu(pr ? e[3] : e[1]), tag});
      STAMP(8);
    }
    STAMP_OUT();
  } else if (isB) {
    const int b = bid >> 5, sp = bid & 31, w = wave, lane_ = lane;
    const unsigned vl = lane << 4;
#define LD_S(S, rho, u) asm volatile("global_load_dwordx4 %0, %1, %2 offset:%3 nt" : "=v"(S[u]) : "v"(vl), "s"(cp + (size_t)((((rho) + brot) & 3) * 65536) + ((u) >> 2) * 4096), "n"(((u) & 3) * 1024) : "memory")
#define ST_S(S, rho) do { unsigned a0_ = ((rho) & 1) * 65536 + w * 8192 + vc0, a1_ = ((rho) & 1) * 65536 + w * 8192 + vc1; asm volatile("" : "+v"(a0_), "+v"(a1_)); \
      _Pragma("unroll") for (int u_ = 0; u_ < 8; ++u_) sto128(u_ * 1024 + (((u_ >> 2) ? a1_ : a0_) ^ ((u_ & 3) << 6)), S[u_]); } while (0)
#define PB() vmwait<WB - 2>()
    if (BPRIO && w >= 4) __builtin_amdgcn_s_setprio(1);
    v4u S2[8], S3[8];
    GET_TAG();
    PB(); LD_C(0, 6); LD_C(0, 7);
    STAMP(1);
    PB(); LD_C(1, 0); LD_C(1, 1);
    PB(); LD_C(1, 2); LD_C(1, 3);
    PB(); LD_C(1, 4); LD_C(1, 5);
    PB(); LD_C(1, 6); LD_C(1, 7);
    STAMP(2);
    PB(); LD_S(S2, 2, 0); LD_S(S2, 2, 1);
    PB(); LD_S(S2, 2, 2); LD_S(S2, 2, 3);
    PB(); LD_S(S2, 2, 4); LD_S(S2, 2, 5);
    PB(); LD_S(S2, 2, 6); LD_S(S2, 2, 7);
#pragma unroll
    for (int u = 0; u < NS3; u += 2) { PB(); LD_S(S3, 3, u); LD_S(S3, 3, u + 1); }
    STAMP(3);
    if (w == 0) {
      _Pragma("nounroll") for (int it = 0; it < SPIN_CAP; ++it) { cfence(); if (__all(gld32(rs, WS_FQ + lane * FS) == tag)) break; __builtin_amdgcn_s_sleep(PSL); }
    }
    bar();
    STAMP(4);
    {
      v4u t[4];
      const char* qa = ws + WS_QAB + (b * 32 + 4 * w) * 1024;
#pragma unroll
      for (int i = 0; i < 4; ++i) asm volatile("global_load_dwordx4 %0, %1, %2 offset:%3 sc1" : "=v"(t[i]) : "v"(vl), "s"(qa), "n"(i * 1024) : "memory");
      constexpr int NA = NS3 < 8 ? 2 : 0, NC = 8 - NS3 - NA < 4 ? 8 - NS3 - NA : 4;
#pragma unroll
      for (int u = NS3; u < NS3 + NA; ++u) LD_S(S3, 3, u);
      asm volatile("s_waitcnt vmcnt(%12)" : "+v"(t[0]), "+v"(t[1]), "+v"(t[2]), "+v"(t[3]), "+v"(S2[0]), "+v"(S2[1]), "+v"(S2[2]), "+v"(S2[3]),
                   "+v"(S2[4]), "+v"(S2[5]), "+v"(S2[6]), "+v"(S2[7]) : "n"(NA) : "memory");
#pragma unroll
      for (int u = NS3 + NA; u < NS3 + NA + NC; ++u) LD_S(S3, 3, u);
#pragma unroll
      for (int i = 0; i < 4; ++i) sto128(SCR + (4 * w + i) * 1024 + ((lane ^ ((4 * w + i) & 15)) << 4), t[i]);
    }
    bar();
    const int kb4 = w & 3, hg = w >> 2;
    v4u qf[16];
#pragma unroll
    for (int s = 0; s < 16; ++s) qf[s] = lds128(SCR + (16 * hg + c16) * 1024 + ((((4 * s) & 15) ^ g ^ c16) << 4) + (s >> 2) * 256);
    STAMP(5);
    const unsigned fk = ((c16 & 3) << 2) | ((0x78u >> (2 * (c16 >> 2))) & 3u);
    const unsigned ka = (16 * kb4 + c16) * 1024 + ((g ^ fk) << 4);
    const int h2 = lane >> 5, blk = g & 1, qq = c16 >> 2, pp = lane & 3;
    const unsigned ft0 = (qq << 2) | (h2 ? 3u : 0u), ft1 = (qq << 2) | (h2 ? 1u : 2u);
    const unsigned cbase = 8 * w + 2 * blk + (pp >> 1);
    const unsigned va00 = (8 * h2 + qq) * 1024 + ((cbase ^ ft0) << 4) + (pp & 1) * 8, va01 = (8 * h2 + 4 + qq) * 1024 + ((cbase ^ ft1) << 4) + (pp & 1) * 8;
    const unsigned mxa = opq(B_MX + (16 * hg + c16) * 4), pwa = opq(B_P + kb4 * 1536 + (16 * hg + c16) * 48 + g * 8);
    const unsigned pra = opq(B_P + (lane & 31) * 48 + h2 * 16), ala = opq(B_AL + (lane & 31) * 4);
    float m = -INFINITY, l = 0.f;
    f16v acc0, acc1;
#pragma unroll
    for (int i = 0; i < 16; ++i) { acc0[i] = 0.f; acc1[i] = 0.f; }
#pragma unroll
    for (int rho = 0; rho < 2; ++rho) {
      unsigned kr = ka, vr0 = va00, vr1 = va01;
      asm volatile("" : "+v"(kr), "+v"(vr0), "+v"(vr1));
      bar();
      f4 sa = {0.f, 0.f, 0.f, 0.f}, sb = sa;
      {
        v4u kf[8];
#pragma unroll
        for (int s = 0; s < 8; ++s) kf[s] = lds128((kr ^ ((s & 3) << 6)) + (s >> 2) * 256);
        __builtin_amdgcn_sched_barrier(0);
#pragma unroll
        for (int s = 0; s < 8; ++s) {
          sa = mfma32(kf[s], qf[s], sa);
          kf[s] = lds128((kr ^ ((s & 3) << 6)) + (2 + (s >> 2)) * 256);
          __builtin_amdgcn_sched_barrier(0);
        }
        kr += 65536;
#pragma unroll
        for (int s = 0; s < 8; ++s) {
          sa = mfma32(kf[s], qf[8 + s], sa);
          kf[s] = lds128((kr ^ ((s & 3) << 6)) + (s >> 2) * 256);
          __builtin_amdgcn_sched_barrier(0);
        }
#pragma unroll
        for (int s = 0; s < 8; ++s) {
          sb = mfma32(kf[s], qf[s], sb);
          kf[s] = lds128((kr ^ ((s & 3) << 6)) + (2 + (s >> 2)) * 256);
          __builtin_amdgcn_sched_barrier(0);
        }
#pragma unroll
        for (int s = 0; s < 8; ++s) sb = mfma32(kf[s], qf[8 + s], sb);
      }
      if (rho == 0 && NS3 == 0) { LD_S(S3, 3, 6); LD_S(S3, 3, 7); }
      float mx = fmaxf(fmaxf(fmaxf(sa[0], sa[1]), fmaxf(sa[2], sa[3])), fmaxf(fmaxf(sb[0], sb[1]), fmaxf(sb[2], sb[3])));
      mx = x32max(x16max(mx));
      if (g == 0) stof(mxa + kb4 * 128, mx);
      v4u c0[4], c1[4];
#pragma unroll
      for (int ks = 0; ks < 4; ++ks) {
        c0[ks] = trd2(ks * 16384 + vr0, ks * 16384 + vr1);
        c1[ks] = trd2(ks * 16384 + (vr0 ^ 64), ks * 16384 + (vr1 ^ 64));
      }
      if (rho == 0) STAMP(6); else STAMP(8);
      bar();
      const float mn = fmaxf(fmaxf(m, fmaxf(ldsf(mxa), ldsf(mxa + 128))), fmaxf(ldsf(mxa + 256), ldsf(mxa + 384)));
      const float al = ex2((m - mn) * SC), mc = -mn * SC;
      float p[8];
#pragma unroll
      for (int i = 0; i < 4; ++i) { p[i] = ex2(fmaf(sa[i], SC, mc)); p[4 + i] = ex2(fmaf(sb[i], SC, mc)); }
      l = fmaf(l, al, ((p[0] + p[1]) + (p[2] + p[3])) + ((p[4] + p[5]) + (p[6] + p[7])));
      m = mn;
      sto64(pwa, (v2u){pk(p[0], p[1]), pk(p[2], p[3])});
      sto64(pwa + 6144, (v2u){pk(p[4], p[5]), pk(p[6], p[7])});
      if (kb4 == 0 && g == 0) stof(mxa + (B_AL - B_MX), al);
      if (rho == 1) { l = x32sum(x16sum(l)); if (g == 0) stof(mxa + (B_LS - B_MX) + kb4 * 128, l); }
      bar();
      if (rho == 1 && kb4 == 0 && g == 0) {
        const unsigned la = mxa + (B_LS - B_MX);
        sto64(B_FM + (16 * hg + c16) * 8, (v2u){asu(m * SC), asu((ldsf(la) + ldsf(la + 128)) + (ldsf(la + 256) + ldsf(la + 384)))});
      }
      const float alp = rho ? ldsf(ala) : 0.f;
      v4u pf[4];
#pragma unroll
      for (int ks = 0; ks < 4; ++ks) pf[ks] = lds128(pra + ks * 1536);
      __builtin_amdgcn_sched_barrier(0);
      if (rho) { acc0 *= alp; acc1 *= alp; }
      vr0 += 65536; vr1 += 65536;
#pragma unroll
      for (int ks = 0; ks < 4; ++ks) {
        acc0 = mfmaL(c0[ks], pf[ks], acc0); acc1 = mfmaL(c1[ks], pf[ks], acc1);
        c0[ks] = trd2(ks * 16384 + vr0, ks * 16384 + vr1);
        c1[ks] = trd2(ks * 16384 + (vr0 ^ 64), ks * 16384 + (vr1 ^ 64));
        pf[ks] = lds128(pra + 6144 + ks * 1536);
        __builtin_amdgcn_sched_barrier(0);
      }
      bar();
      if (rho == 0) {
        ST_S(S2, 0);
        vmwait<0>();
        ST_S(S3, 1);
      }
#pragma unroll
      for (int ks = 0; ks < 4; ++ks) { acc0 = mfmaL(c0[ks], pf[ks], acc0); acc1 = mfmaL(c1[ks], pf[ks], acc1); }
      if (rho == 0) STAMP(7); else STAMP(9);
    }
    {
    const int lane = (int)opq((unsigned)lane_), h2 = lane >> 5, tid = wave * 64 + lane;
    {
      const unsigned tb = B_T + w * 4096, n32 = lane & 31;
#pragma unroll
      for (int q4 = 0; q4 < 4; ++q4) {
        sto64(tb + n32 * 128 + ((q4 ^ (n32 & 7)) << 4) + h2 * 8, (v2u){pk(acc0[4 * q4], acc0[4 * q4 + 1]), pk(acc0[4 * q4 + 2], acc0[4 * q4 + 3])});
        sto64(tb + n32 * 128 + (((4 + q4) ^ (n32 & 7)) << 4) + h2 * 8, (v2u){pk(acc1[4 * q4], acc1[4 * q4 + 1]), pk(acc1[4 * q4 + 2], acc1[4 * q4 + 3])});
      }
      lgk0();
      const int hr = lane >> 3, ck = lane & 7;
#pragma unroll
      for (int i = 0; i < 4; ++i)
        gst128(rs, WS_PB + (((8 * i + hr) * 4 + (w >> 1)) * 64 + bid) * 256 + (w & 1) * 128 + ck * 16, lds128(tb + (8 * i + hr) * 128 + ((ck ^ hr) << 4)));
    }
    vmwait<0>();
    bar();
    if (w == 0 && lane < 32) { const v2u fm = *(const LAS v2u*)(B_FM + lane * 8); gst128(rs, WS_PBM + bid * 512 + lane * 16, (v4u){fm.x, tag, fm.y, tag}); }
    STAMP(10);
    if (w == 0) {
      const unsigned off = SKIPA ? WS_F + (64 + bid) * 16384 + (bid & 31) * 128 : PRE2 == 2 ? WS_F + bid * 16384 + ((lane >> 3) * 16 + (bid & 15)) * 128 : PRE2 ? WS_F + bid * 16384 + (2 * bid) * 128 : WS_PAM + (bid * NSPLIT_A + (bid % NSPLIT_A)) * 128;
      _Pragma("nounroll") for (int it = 0; it < SPIN_CAP; ++it) { cfence(); if (__popcll(__ballot(gld64(rs, off).y == tag)) >= PREN) break; __builtin_amdgcn_s_sleep(PRESL); }
    }
    bar();
    STAMP(14);
    v4u f[3];
    if (F2) sweep2<3, F2>(f, rs, tag, [&](int k) { return WS_F + ((k >> 1) * 64 + bid) * 16384 + (k < 2 ? k * 8192 + tid * 16 : (tid & 255) * 16); });
    else sweep<3, FSL, FMASK>(f, rs, tag, [&](int k) { return WS_F + (((SKIPB ? 0 : SKIPA ? 2 : k) >> 1) * 64 + bid) * 16384 + (k < 2 && !SKIPA ? k * 8192 + tid * 16 : (tid & 255) * 16); });
    float r[4] = {asf(f[0].x) + asf(f[1].x), asf(f[0].z) + asf(f[1].z), w < 4 ? asf(f[2].x) : 0.f, w < 4 ? asf(f[2].z) : 0.f};
#pragma unroll
    for (int k = 0; k < 4; ++k) { r[k] += dpp<ROR8>(r[k]); r[k] = x32sum(x16sum(r[k])); }
    if (lane < 8) { sto64(B_FIN + w * 128 + lane * 8, (v2u){asu(r[0]), asu(r[1])}); sto64(B_FIN + w * 128 + 64 + lane * 8, (v2u){asu(r[2]), asu(r[3])}); }
    bar();
    if (tid < 16) {
      float s0 = 0.f, s1 = 0.f;
#pragma unroll
      for (int i = 0; i < 8; ++i) { const f2 t = *(const LAS f2*)(B_FIN + i * 128 + tid * 8); s0 += t.x; s1 += t.y; }
      const int mat = tid >> 3, ob = (tid >> 2) & 1, pr = tid & 3;
      *(GAS unsigned*)((GAS char*)out + (ob * 1024 + mat * 512 + 8 * bid + 2 * pr) * 2) = pk(s0, s1);
      if (!SKIPA && !SKIPB && bid == 0 && tid == 0) gst32(rs, WS_E, tag);
    }
    STAMP(15);
    STAMP_OUT();
    }
  } else {
    const int h = bid - NB_CU, w = wave;
    GET_TAG();
    if (w == 0) gather_qb(T_QBI, h, lane, rs, tag);
    vmwait<0>();
    STAMP(1);
    v4u OU[16];
    const char* ow = Bwo + (size_t)(h * 128 + 16 * w) * 1024;
    const unsigned vo = g * 4096 + c16 * 16;
#define LD_OU(u) asm volatile("global_load_dwordx4 %0, %1, %2 offset:%3 nt" : "=v"(OU[u]) : "v"(vo), "s"(ow), "n"(((u) >> 2) * 1024 + ((u) & 3) * 256) : "memory")
#pragma unroll
    for (int u = 0; u < TE; ++u) LD_OU(u);
    {
      const unsigned a0 = w * 8192 + c16 * 256 + ((g ^ c16) << 4);
      v4u uf[8];
#pragma unroll
      for (int i = 0; i < 8; ++i) uf[i] = lds128((a0 ^ ((i & 3) << 6)) + (i >> 2) * 4096);
      bar();
      STAMP(2);
      v4u qb[4];
#pragma unroll
      for (int s = 0; s < 4; ++s) qb[s] = lds128(T_QBI + (c16 & 1) * 256 + s * 64 + g * 16);
      __builtin_amdgcn_sched_barrier(0);
      f4 d0 = {0.f, 0.f, 0.f, 0.f}, d1 = d0;
#pragma unroll
      for (int s = 0; s < 4; ++s) { d0 = mfma32(uf[s], qb[s], d0); d1 = mfma32(uf[4 + s], qb[s], d1); }
      const int rb = (c16 >> 1) & 1;
      const f4 e = rb ? d1 : d0;
      if (c16 < 4) sto64(T_QAS + (c16 & 1) * 512 + (32 * w + 16 * rb + 4 * g) * 2, (v2u){pk(e[0], e[1]), pk(e[2], e[3])});
      bar();
      if (w == 1) {
        gst128(rs, WS_QAB + ((lane >> 5) * 32 + h) * 1024 + (lane & 31) * 16, lds128(T_QAS + lane * 16));
        vmwait<0>();
        flag_line(rs, WS_FQ + h * FS, lane, tag);
      }
    }
    bar();
    STAMP(3);
    {
      const char* uv = Wuv + (size_t)(64 * w) * 8192 + h * 256;
      const unsigned vuv = g * 8192 + ((c16 ^ (g << 1)) << 4);
#pragma unroll
      for (int u = 0; u < 16; ++u) { if (u >= WT && !(u & 1)) vmwait<WT - 2>(); dma<0>(uv + (size_t)u * 32768, vuv ^ (((u >> 1) & 1) << 7), w * 16384 + u * 1024); }
#pragma unroll
      for (int u = TE; u < 16; ++u) { if (!(u & 1)) vmwait<WT - 2>(); LD_OU(u); }
    }
    asm volatile("s_waitcnt vmcnt(0)" : "+v"(OU[0]), "+v"(OU[1]), "+v"(OU[2]), "+v"(OU[3]), "+v"(OU[4]), "+v"(OU[5]), "+v"(OU[6]), "+v"(OU[7]),
                 "+v"(OU[8]), "+v"(OU[9]), "+v"(OU[10]), "+v"(OU[11]), "+v"(OU[12]), "+v"(OU[13]), "+v"(OU[14]), "+v"(OU[15]) :: "memory");
    if (tid < 4) sto32(T_RDY + tid * 4, 0u);
    bar();
    STAMP(4);
    v4u wf[16];
    {
      const int q = c16 >> 2, p = lane & 3, f3 = q | ((g & 1) << 2);
      const unsigned a0 = (8 * g + q) * 256 + (((2 * w + (p >> 1)) ^ (f3 << 1)) << 4) + (p & 1) * 8;
      const unsigned aa[2] = {opq(a0), opq(a0 + 65536)};
#pragma unroll
      for (int s = 0; s < 16; ++s) wf[s] = trd2(aa[s >> 3] + (s & 7) * 8192, aa[s >> 3] + (s & 7) * 8192 + 1024);
    }
    STAMP(5);
    {
      const int tb = w >> 2;
      v4u o[8];
#pragma unroll
      for (int i = 0; i < 8; ++i) { o[i] = (v4u){0u, 0u, 0u, 0u}; asm volatile("" : "+v"(o[i])); }
      const char* pb = ws + WS_PB + (h * 4 + (w & 3)) * 16384 + tb * 8192;
      const unsigned vl = lane << 4;
      unsigned done = 0;
      bool all = false;
      v4u ml = {0u, ~tag, 0u, ~tag};
      if (w == 0) {
        _Pragma("nounroll") for (int it = 0; it < SPIN_CAP; ++it) { cfence(); if (__all(gld32(rs, WS_PBM + 2 * h * 512 + h * 16 + 4) == tag)) break; __builtin_amdgcn_s_sleep(PSL1); }
      }
      _Pragma("nounroll") for (int it = 0; it < SPIN_CAP * 8 && !all; ++it) {
        unsigned rdy;
        if (w == 0) {
          cfence();
          const v4u t = gld128(rs, tagok(ml, tag) ? 0xfffffff0u : WS_PBM + lane * 512 + h * 16);
          if (!tagok(ml, tag)) ml = t;
          unsigned long long m = __ballot(tagok(ml, tag));
          all = m == ~0ull;
          m &= m >> 2; m &= m >> 1; m &= 0x1111111111111111ull;
          rdy = (unsigned)m;
          if (lane == 0) *(volatile LAS v2u*)T_RDY = (v2u){rdy, (unsigned)(m >> 32)};
        } else {
          rdy = lds_peek(T_RDY + tb * 4);
          if (rdy == done) { __builtin_amdgcn_s_sleep(1); continue; }
          all = rdy == 0x11111111u;
        }
        const unsigned add = rdy & ~done;
        done = rdy;
#pragma unroll
        for (int i = 0; i < 8; ++i)
          if (add & (1u << (4 * i))) asm volatile("global_load_dwordx4 %0, %1, %2 offset:%3 sc1" : "+v"(o[i]) : "v"(vl), "s"(pb + (i >> 2) * 4096), "n"((i & 3) * 1024) : "memory");
      }
      if (w == 0) {
        const float M = x16max(row16max(asf(ml.x)));
        const float wt = ex2(asf(ml.x) - M), wn = wt / x16sum(row16sum(wt * asf(ml.z)));
        const unsigned hi = pk(wn, wn), wa = T_WN + (lane >> 5) * 128 + (lane & 3) * 32 + ((lane >> 2) & 7) * 4;
        const float lo = wn - asf(hi & 0xffff0000u);
        sto32(wa, hi); sto32(wa + 256, pk(lo, lo));
        lgk0();
        if (lane == 0) *(volatile LAS unsigned*)(T_RDY + 8) = 1u;
      } else {
        lds_wait_ge<0>(T_RDY + 8, 1u);
      }
      const unsigned wa = T_WN + (c16 >> 3) * 256 + tb * 128 + g * 32, half = (c16 & 1) ? 0xffff0000u : 0x0000ffffu;
      const v4u w0_ = lds128(wa), w1_ = lds128(wa + 16);
      const int rr = (c16 >> 1) & 3;
      v4u bo[8];
#pragma unroll
      for (int u = 0; u < 8; ++u) { const unsigned x = (u < 4 ? w0_[u & 3] : w1_[u & 3]) & half; bo[u] = (v4u){rr == 0 ? x : 0u, rr == 1 ? x : 0u, rr == 2 ? x : 0u, rr == 3 ? x : 0u}; }
      asm volatile("s_waitcnt vmcnt(0)" : "+v"(o[0]), "+v"(o[1]), "+v"(o[2]), "+v"(o[3]), "+v"(o[4]), "+v"(o[5]), "+v"(o[6]), "+v"(o[7]),
                   "+v"(bo[0]), "+v"(bo[1]), "+v"(bo[2]), "+v"(bo[3]), "+v"(bo[4]), "+v"(bo[5]), "+v"(bo[6]), "+v"(bo[7]) :: "memory");
      STAMP(8);
      f4 d = {0.f, 0.f, 0.f, 0.f};
#pragma unroll
      for (int u = 0; u < 8; ++u) d = mfma32(o[u], bo[u], d);
#pragma unroll
      for (int r = 0; r < 4; ++r) {
        const float x = d[r] + dpp<ROR8>(d[r]);
        const unsigned pr = pk(x, dpp<XOR1>(x));
        if ((c16 & 9) == 0) sto32(T_OL + tb * 1088 + ((w & 3) * 128 + 32 * g + 8 * r + c16) * 2, pr);
      }
    }
    bar();
    STAMP(6);
    {
      v4u of[16];
      const unsigned oa = opq(T_OL + (c16 >> 3) * 1088 + g * 16);
#pragma unroll
      for (int s = 0; s < 16; ++s) of[s] = lds128(oa + s * 64);
      __builtin_amdgcn_sched_barrier(0);
      f4 d0 = {0.f, 0.f, 0.f, 0.f}, d1 = d0;
#pragma unroll
      for (int s = 0; s < 8; ++s) { d0 = mfma32(wf[2 * s], of[2 * s], d0); d1 = mfma32(wf[2 * s + 1], of[2 * s + 1], d1); }
      d0 += d1;
      const unsigned half = (c16 & 1) ? 0xffff0000u : 0x0000ffffu;
      const int rr = (c16 >> 1) & 3;
      f4 o4[4];
#pragma unroll
      for (int cb = 0; cb < 4; ++cb) o4[cb] = (f4){0.f, 0.f, 0.f, 0.f};
#pragma unroll
      for (int u = 0; u < 4; ++u) {
        const unsigned x = pk(d0[u], d0[u]) & half;
        const v4u bo = {rr == 0 ? x : 0u, rr == 1 ? x : 0u, rr == 2 ? x : 0u, rr == 3 ? x : 0u};
#pragma unroll
        for (int cb = 0; cb < 4; ++cb) o4[cb] = mfma32(OU[4 * u + cb], bo, o4[cb]);
      }
#pragma unroll
      for (int cb = 0; cb < 4; ++cb)
#pragma unroll
        for (int r = 0; r < 4; ++r) stof(T_RED + w * 4096 + cb * 1024 + r * 64 + g * 256 + c16 * 4, o4[cb][r]);
    }
    bar();
    {
      const int j = tid >> 3, pr = tid & 7;
      float s0 = 0.f, s1 = 0.f;
#pragma unroll
      for (int i = 0; i < 8; ++i) { const f2 t = *(const LAS f2*)(T_RED + i * 4096 + tid * 8); s0 += t.x; s1 += t.y; }
      gst128(rs, WS_F + (((64 + j) * 128 + h) * 16 + 2 * pr) * 8, (v4u){asu(s0), tag, asu(s1), tag});
    }
    STAMP(7);
    STAMP_OUT();
  }
}

extern "C" void kernel_launch(void* const* d_in, const int* in_sizes, int n_in, void* d_out, int out_size, void* d_ws, size_t ws_size,
                              hipStream_t stream) {
  (void)in_sizes; (void)n_in; (void)out_size; (void)ws_size;
  (void)hipFuncSetAttribute((const void*)fused_decode, hipFuncAttributeMaxDynamicSharedMemorySize, LDS_BYTES);
  fused_decode<<<256, 512, LDS_BYTES, stream>>>(
      (const char*)d_in[0], (const char*)d_in[1], (const char*)d_in[2], (const char*)d_in[3], (const char*)d_in[4], (const char*)d_in[5],
      (const char*)d_in[6], (const char*)d_in[7], (const char*)d_in[8], (const char*)d_in[9], (const char*)d_in[10], (unsigned short*)d_out,
      (char*)d_ws);
}
```

```cpp
#include <hip/hip_runtime.h>
#include <stdint.h>
#pragma clang diagnostic ignored "-Wint-to-pointer-cast"

#define GAS __attribute__((address_space(1)))
#define LAS __attribute__((address_space(3)))
typedef unsigned v4u __attribute__((ext_vector_type(4)));
typedef unsigned v2u __attribute__((ext_vector_type(2)));
typedef float f4 __attribute__((ext_vector_type(4)));
typedef float f2 __attribute__((ext_vector_type(2)));
typedef float f16v __attribute__((ext_vector_type(16)));
typedef short s4 __attribute__((ext_vector_type(4)));
typedef __bf16 b8 __attribute__((ext_vector_type(8)));
typedef __bf16 b2 __attribute__((ext_vector_type(2)));

namespace {

constexpr int NB_CU = 64;
constexpr int NT_CU = 32;
constexpr int NSPLIT_A = 10;
constexpr int NB0 = 55;
constexpr int NB1 = 52;
constexpr int NB2 = (512 - 4 * NB0 - 4 * NB1) / 2;
constexpr int NSLOT = 18, NLOW = 14, NCONS = 6;
constexpr int W1 = 12;
constexpr int W2 = 24;
constexpr int J1 = 18;
constexpr int WB = 4;
constexpr int WT = 6;
constexpr int DLYP = 30;
constexpr int DLYB = 10;
constexpr int NS3 = 4;
constexpr int TE = 6;
constexpr int QGO = 4;
constexpr int ODL = 5;
constexpr int PREN = 32;
constexpr int ROT = 13;
constexpr int BROT = 3;
constexpr int CODE_KB = 24;
constexpr float SC = 0.08838834764831845f * 1.4426950408889634f;
constexpr int SPIN_CAP = 1 << 18;

constexpr unsigned FS = 128;
constexpr unsigned WS_E = 0;
constexpr unsigned WS_FQ = 4096;
constexpr unsigned WS_QG = 65536;
constexpr unsigned WS_QAB = WS_QG + 262144;
constexpr unsigned WS_PBM = WS_QAB + 65536;
constexpr unsigned WS_PAM = WS_PBM + 32768;
constexpr unsigned WS_PAO = WS_PAM + 131072;
constexpr unsigned WS_PB = WS_PAO + 786432;
constexpr unsigned WS_F = WS_PB + 2097152;
constexpr unsigned WS_END = WS_F + 2097152;

constexpr unsigned LDS_BYTES = 163840;
constexpr unsigned SCR = 131072;
constexpr unsigned A_MISC = NLOW * 8192;
constexpr unsigned A_LANDK = A_MISC, A_LANDV = A_MISC + 4, A_QUSED = A_MISC + 8, A_QCNT = A_MISC + 12, A_QB = A_MISC + 16, A_QRDY = A_MISC + 20, A_OUL = A_MISC + 24, A_OUL2 = A_MISC + 28;
constexpr unsigned A_DONE = A_MISC + 64;
constexpr unsigned A_QU2 = 10 * 8192;
constexpr unsigned A_QIMG = A_MISC + 256;
constexpr unsigned A_ML = A_MISC + 1280;
constexpr unsigned A_OIMG = A_MISC + 1792;
constexpr unsigned A_ST = A_MISC + 2048;
constexpr unsigned A_XIMG = A_MISC + 4096, A_DUMMY = A_MISC + 5120, A_QBI = A_MISC + 6144, A_QAS = A_MISC + 6656;
constexpr unsigned SLOT1 = 65536;
constexpr unsigned B_MX = SCR, B_AL = SCR + 512, B_LS = SCR + 640, B_P = SCR + 2048;
constexpr unsigned B_FM = SCR + 1280, B_FIN = SCR + 16384, B_T = SLOT1, B_DUMMY = SCR + 8192;
constexpr unsigned T_QBI = SCR, T_QAS = SCR + 1024, T_OL = SCR + 4096, T_RDY = SCR + 6400, T_WN = SCR + 6656, T_RED = 0;

__device__ __forceinline__ unsigned slot_addr(unsigned s) { return s * 8192 + (s >= NLOW ? 16384u : 0u); }
__device__ __forceinline__ v4u lds128(unsigned a) { return *(const LAS v4u*)a; }
__device__ __forceinline__ void sto128(unsigned a, v4u v) { *(LAS v4u*)a = v; }
__device__ __forceinline__ void sto64(unsigned a, v2u v) { *(LAS v2u*)a = v; }
__device__ __forceinline__ void sto32(unsigned a, unsigned v) { *(LAS unsigned*)a = v; }
__device__ __forceinline__ float ldsf(unsigned a) { return *(const LAS float*)a; }
__device__ __forceinline__ void stof(unsigned a, float v) { *(LAS float*)a = v; }
__device__ __forceinline__ v2u trd(unsigned a) { return __builtin_bit_cast(v2u, __builtin_amdgcn_ds_read_tr16_b64_v4i16((LAS s4*)a)); }
__device__ __forceinline__ v4u trd2(unsigned a0, unsigned a1) { const v2u x = trd(a0), y = trd(a1); return (v4u){x.x, x.y, y.x, y.y}; }
__device__ __forceinline__ f4 mfma32(v4u a, v4u b, f4 c) {
  return __builtin_amdgcn_mfma_f32_16x16x32_bf16(__builtin_bit_cast(b8, a), __builtin_bit_cast(b8, b), c, 0, 0, 0); }
__device__ __forceinline__ f4 mfma16(v2u a, v2u b, f4 c) {
  return __builtin_amdgcn_mfma_f32_16x16x16bf16_1k(__builtin_bit_cast(s4, a), __builtin_bit_cast(s4, b), c, 0, 0, 0); }
__device__ __forceinline__ f16v mfmaL(v4u a, v4u b, f16v c) {
  return __builtin_amdgcn_mfma_f32_32x32x16_bf16(__builtin_bit_cast(b8, a), __builtin_bit_cast(b8, b), c, 0, 0, 0); }
__device__ __forceinline__ unsigned pk(float lo, float hi) { return __builtin_bit_cast(unsigned, __builtin_convertvector((f2){lo, hi}, b2)); }
__device__ __forceinline__ float asf(unsigned u) { return __builtin_bit_cast(float, u); }
__device__ __forceinline__ unsigned asu(float f) { return __builtin_bit_cast(unsigned, f); }
__device__ __forceinline__ float ex2(float x) { return __builtin_amdgcn_exp2f(x); }
template <int CTRL> __device__ __forceinline__ float dpp(float x) {
  return __builtin_bit_cast(float, __builtin_amdgcn_mov_dpp(__builtin_bit_cast(int, x), CTRL, 0xf, 0xf, true)); }
constexpr int XOR1 = 0xB1, XOR2 = 0x4E, ROR4 = 0x124, ROR8 = 0x128;
__device__ __forceinline__ float x16max(float x) { auto s = __builtin_amdgcn_permlane16_swap(asu(x), asu(x), false, false); return fmaxf(asf(s[0]), asf(s[1])); }
__device__ __forceinline__ float x32max(float x) { auto s = __builtin_amdgcn_permlane32_swap(asu(x), asu(x), false, false); return fmaxf(asf(s[0]), asf(s[1])); }
__device__ __forceinline__ float x16sum(float x) { auto s = __builtin_amdgcn_permlane16_swap(asu(x), asu(x), false, false); return asf(s[0]) + asf(s[1]); }
__device__ __forceinline__ float x32sum(float x) { auto s = __builtin_amdgcn_permlane32_swap(asu(x), asu(x), false, false); return asf(s[0]) + asf(s[1]); }
__device__ __forceinline__ float row16max(float x) { x = fmaxf(x, dpp<XOR1>(x)); x = fmaxf(x, dpp<XOR2>(x)); x = fmaxf(x, dpp<ROR4>(x)); return fmaxf(x, dpp<ROR8>(x)); }
__device__ __forceinline__ float row16sum(float x) { x += dpp<XOR1>(x); x += dpp<XOR2>(x); x += dpp<ROR4>(x); return x + dpp<ROR8>(x); }

template <int IMM> __device__ __forceinline__ void dma(const void* sbase, unsigned voff, unsigned lds) {
  asm volatile("s_mov_b32 m0, %2\n\ts_nop 0\n\tglobal_load_lds_dwordx4 %0, %1 offset:%3 nt" :: "v"(voff), "s"(sbase), "s"(lds), "n"(IMM) : "memory");
}
template <int W> __device__ __forceinline__ void dma4(const void* sbase, unsigned v0, unsigned v1, unsigned v2, unsigned v3, unsigned lds) {
  asm volatile("s_mov_b32 m0, %5\n\ts_waitcnt vmcnt(%6)\n\tglobal_load_lds_dwordx4 %0, %4 nt\n\tglobal_load_lds_dwordx4 %1, %4 offset:1024 nt\n\t"
               "s_waitcnt vmcnt(%6)\n\tglobal_load_lds_dwordx4 %2, %4 offset:2048 nt\n\tglobal_load_lds_dwordx4 %3, %4 offset:3072 nt"
               :: "v"(v0), "v"(v1), "v"(v2), "v"(v3), "s"(sbase), "s"(lds), "n"(W - 2) : "memory");
}
__device__ __forceinline__ void dma_keep(const void* sbase, unsigned voff, unsigned lds) {
  asm volatile("s_mov_b32 m0, %2\n\ts_nop 0\n\tglobal_load_lds_dwordx4 %0, %1" :: "v"(voff), "s"(sbase), "s"(lds) : "memory");
}
template <int N> __device__ __forceinline__ void vmwait() { asm volatile("s_waitcnt vmcnt(%0)" :: "n"(N) : "memory"); }
__device__ __forceinline__ void bar() { asm volatile("s_waitcnt lgkmcnt(0)\n\ts_barrier" ::: "memory"); }
__device__ __forceinline__ void lgk0() { asm volatile("s_waitcnt lgkmcnt(0)" ::: "memory"); }
__device__ __forceinline__ void cfence() { asm volatile("" ::: "memory"); }
__device__ __forceinline__ unsigned opq(unsigned x) { asm volatile("" : "+v"(x)); return x; }
__device__ __forceinline__ unsigned lds_peek(unsigned a) { return __builtin_amdgcn_readfirstlane(*(volatile LAS unsigned*)a); }
template <int SL = 1> __device__ __forceinline__ void lds_wait_ge(unsigned a, unsigned want) {
  _Pragma("nounroll") for (int it = 0; it < SPIN_CAP * 8; ++it) { if (lds_peek(a) >= want) break; if (SL) __builtin_amdgcn_s_sleep(SL); }
  cfence();
}
typedef __amdgpu_buffer_rsrc_t rsrc_t;
constexpr int SC1 = 16;
__device__ __forceinline__ v4u gld128(rsrc_t r, unsigned off) { return __builtin_amdgcn_raw_buffer_load_b128(r, off, 0, SC1); }
__device__ __forceinline__ v2u gld64(rsrc_t r, unsigned off) { return __builtin_amdgcn_raw_buffer_load_b64(r, off, 0, SC1); }
__device__ __forceinline__ unsigned gld32(rsrc_t r, unsigned off) { return __builtin_amdgcn_raw_buffer_load_b32(r, off, 0, SC1); }
__device__ __forceinline__ void gst128(rsrc_t r, unsigned off, v4u v) { __builtin_amdgcn_raw_buffer_store_b128(v, r, off, 0, SC1); }
__device__ __forceinline__ void gst32(rsrc_t r, unsigned off, unsigned v) { __builtin_amdgcn_raw_buffer_store_b32(v, r, off, 0, SC1); }
__device__ __forceinline__ void flag_line(rsrc_t r, unsigned off, int lane, unsigned tag) { if (lane < 8) gst128(r, off + lane * 16, (v4u){tag, tag, tag, tag}); }
__device__ __forceinline__ bool tagok(v4u v, unsigned tag) { return v.y == tag && v.w == tag; }
template <int N, int GAP, class F> __device__ __forceinline__ void sweep2(v4u (&v)[N], rsrc_t rs, unsigned tag, F off) {
  v4u a[N], b[N];
#pragma unroll
  for (int k = 0; k < N; ++k) a[k] = gld128(rs, off(k));
  __builtin_amdgcn_s_sleep(GAP);
  _Pragma("nounroll") for (int it = 0; it < SPIN_CAP; ++it) {
    cfence();
#pragma unroll
    for (int k = 0; k < N; ++k) b[k] = gld128(rs, off(k));
    cfence();
    bool ok = true;
#pragma unroll
    for (int k = 0; k < N; ++k) { ok &= tagok(a[k], tag); v[k] = a[k]; }
    if (__all(ok)) break;
#pragma unroll
    for (int k = 0; k < N; ++k) a[k] = gld128(rs, off(k));
    cfence();
    ok = true;
#pragma unroll
    for (int k = 0; k < N; ++k) { ok &= tagok(b[k], tag); v[k] = b[k]; }
    if (__all(ok)) break;
  }
}
template <int N, int SL, class F> __device__ __forceinline__ void sweep(v4u (&v)[N], rsrc_t rs, unsigned tag, F off) {
  _Pragma("nounroll") for (int it = 0; it < SPIN_CAP; ++it) {
    cfence();
    bool ok = true;
#pragma unroll
    for (int k = 0; k < N; ++k) { v[k] = gld128(rs, off(k)); ok &= tagok(v[k], tag); }
    if (__all(ok)) break;
    if (SL) __builtin_amdgcn_s_sleep(SL);
  }
}

__device__ __forceinline__ void gather_qb(unsigned img, int h, int lane, rsrc_t rs, unsigned tag) {
  v4u qg[4];
  const unsigned off = WS_QG + (h * 128 + 2 * lane) * 8;
  sweep<4, 1>(qg, rs, tag, [&](int k) { return off + (k & 1) * 65536 + (k >> 1) * 32768; });
  sto32(img + lane * 4, pk(asf(qg[0].x) + asf(qg[1].x), asf(qg[0].z) + asf(qg[1].z)));
  sto32(img + 256 + lane * 4, pk(asf(qg[2].x) + asf(qg[3].x), asf(qg[2].z) + asf(qg[3].z)));
}
__device__ __forceinline__ void lds_inc(unsigned a) { __hip_atomic_fetch_add((LAS unsigned*)a, 1u, __ATOMIC_RELAXED, __HIP_MEMORY_SCOPE_WORKGROUP); }

__device__ __forceinline__ void out_frags(unsigned gb, int w, int lane, v4u (&wf)[4]) {
  const int g = lane >> 4, q = (lane & 15) >> 2, p = lane & 3, f3 = q | ((g & 1) << 2);
#pragma unroll
  for (int tt = 0; tt < 4; ++tt) {
    const unsigned a0 = gb + q * 1024 + (((2 * (4 * w + tt) + (p >> 1)) ^ (f3 << 1)) << 4) + (p & 1) * 8;
    wf[tt] = trd2(a0, a0 + 4096);
  }
}

}

__global__ void __launch_bounds__(512, 2) __attribute__((aligned(4096))) fused_decode(
    const char* __restrict__ Ax, const char* __restrict__ Kc, const char* __restrict__ Vc, const char* __restrict__ Wq, const char* __restrict__ Wo,
    const char* __restrict__ Bx, const char* __restrict__ Ckv, const char* __restrict__ Bwq, const char* __restrict__ Wuk, const char* __restrict__ Wuv,
    const char* __restrict__ Bwo, unsigned short* __restrict__ out, char* __restrict__ ws) {
  const int bid = blockIdx.x, tid = threadIdx.x, lane = tid & 63, wave = __builtin_amdgcn_readfirstlane(tid >> 6);
  const int g = lane >> 4, c16 = lane & 15;
  const int iA = bid - (NB_CU + NT_CU);
  const bool isA = iA >= 0, isB = bid < NB_CU;
  const int ty = iA >> 6;
  int dA = 95 - bid, dU = 223 - bid, dB = bid - 64, dQ = 159 - bid, dW = 2 - wave;
  asm("" : "+s"(dA), "+s"(dU), "+s"(dB), "+s"(dQ), "+s"(dW));
  const int mA = dA >> 31, mU = dU >> 31, mB = dB >> 31, mP = mA & ~mU, sh = mP & 1;
  const int hh = (bid >> sh) & 31;
  const unsigned row0 = (mA & ((((bid | mU) & 1) << 8) + 64 * wave - 64)) | (~mA & (32 * wave));
  const unsigned l0 = (mA & (SCR - 16384 + wave * 16384 - ((dW >> 31) & (SCR + 32768 - A_QU2)))) | (~mA & (wave * 8192));
  const unsigned karg = 0x40 - (mP & (8 + ((dQ >> 31) & 32))), more = mA;
  if ((unsigned)(wave + mA + (mB & 8)) < (unsigned)(8 + (mA & -4))) {
    const unsigned b0 = (row0 + g) * 8192 + hh * 256 + ((c16 ^ (g << sh)) << 4), x1 = ~mP & 64;
    unsigned v0 = b0, v1 = (b0 ^ x1) + 32768, v2 = (b0 ^ 128) + 65536, v3 = (b0 ^ x1 ^ 128) + 98304;
    if (karg == 0x18) __builtin_amdgcn_s_sleep(DLYP);
    unsigned long long wp;
#define D4 "global_load_lds_dwordx4 %1, %0 nt\n\ts_add_u32 m0, m0, 0x400\n\tv_add_u32 %1, 0x20000, %1\n\t" \
           "global_load_lds_dwordx4 %2, %0 nt\n\ts_add_u32 m0, m0, 0x400\n\tv_add_u32 %2, 0x20000, %2\n\t" \
           "global_load_lds_dwordx4 %3, %0 nt\n\ts_add_u32 m0, m0, 0x400\n\tv_add_u32 %3, 0x20000, %3\n\t" \
           "global_load_lds_dwordx4 %4, %0 nt\n\ts_add_u32 m0, m0, 0x400\n\tv_add_u32 %4, 0x20000, %4\n\t"
    asm volatile("s_load_dwordx2 %0, %5, %6\n\ts_mov_b32 m0, %7\n\ts_waitcnt lgkmcnt(0)\n\t" D4 D4
                 "s_cmp_eq_u32 %8, 0\n\ts_cbranch_scc1 1f\n\t" D4 D4 "1:"
                 : "=&s"(wp), "+v"(v0), "+v"(v1), "+v"(v2), "+v"(v3)
                 : "s"(__builtin_amdgcn_kernarg_segment_ptr()), "s"(karg), "s"(l0), "s"(more) : "memory", "scc");
#undef D4
  }
  {
    unsigned long long pc;
    asm volatile("s_getpc_b64 %0" : "=s"(pc));
    const int kb = ((bid >> 3) + 4 * wave) & 31;
    dma_keep((const char*)((pc & ~1023ull) + (unsigned)((kb < CODE_KB ? kb : CODE_KB - 1) * 1024)), lane * 16, isA ? A_DUMMY : B_DUMMY);
  }
  const rsrc_t rs = __builtin_amdgcn_make_buffer_rsrc(ws, 0, WS_END, 0x00020000);
  asm volatile("" :: "s"(Ax), "s"(Kc), "s"(Vc), "s"(Wq), "s"(Wo), "s"(Bx), "s"(Ckv), "s"(Bwq), "s"(Wuk), "s"(Wuv), "s"(Bwo), "s"(out), "s"(ws));
  unsigned tag;
#define GET_TAG() do { asm volatile("s_load_dword %0, %1, 0x0\n\ts_waitcnt lgkmcnt(0)" : "=s"(tag) : "s"(ws) : "memory"); tag += 1u; } while (0)
  const char* cp = Ckv + ((size_t)(bid >> 5) * 8192 + 256 * (bid & 31) + 8 * wave) * 1024;
  const int brot = bid >> BROT;
  const unsigned vc0 = (lane ^ ((wave & 1) ? 3 : 0)) << 4, vc1 = (lane ^ ((wave & 1) ? 1 : 2)) << 4;
#define LD_C(rho, u) dma<((u) & 3) * 1024>(cp + (size_t)((((rho) + brot) & 3) * 65536) + ((u) >> 2) * 4096, (((u) >> 2) ? vc1 : vc0) ^ (((u) & 3) << 6), ((rho) & 1) * 65536 + wave * 8192 + ((u) >> 2) * 4096)
  if (isA) {
    if (wave == 1 && ty < 2) dma<0>((ty ? Ax : Bx) + 512 * (bid & 1), (lane >> 5) * 1024 + (lane & 31) * 16, A_XIMG);
    if (tid < 64) sto32(A_MISC + tid * 4, 0u);
    bar();
  } else if (isB) {
    __builtin_amdgcn_s_sleep(DLYB);
    LD_C(0, 0); LD_C(0, 1); LD_C(0, 2); LD_C(0, 3); LD_C(0, 4); LD_C(0, 5);
  }

  if (isA) {
    const int split = iA >> 4, pair = (iA + split) & 15, b = pair >> 3, kvh = pair & 7;
    const int nblk = ty == 0 ? NB0 : ty == 1 ? NB1 : NB2, blk0 = (ty == 0 ? 0 : ty == 1 ? 4 * NB0 : 4 * (NB0 + NB1)) + (split & 3) * nblk;
    if (wave == 0 || wave == 7) {
      __builtin_amdgcn_s_setprio(3);
      static_assert(NSPLIT_A == 10 && W1 >= 4 && W1 % 4 == 0 && W2 % 4 == 0 && W1 <= W2 && W2 <= 24 && J1 <= NB0 && J1 <= NB1 && J1 <= NB2, "window");
      const bool isv = wave == 7;
      int pub = 0, j = 0;
      unsigned sl = 0;
      int pos = (pair * ROT) % nblk;
      const char* gp = (isv ? Vc : Kc) + ((size_t)pair * 8192 + (blk0 + pos) * 16) * 256;
      const unsigned v0 = opq(g * 256 + ((c16 ^ (isv ? g << 1 : g)) << 4));
      const unsigned v1 = opq(v0 ^ (isv ? 128 : 64)), v2 = opq(v0 ^ (isv ? 0 : 128)), v3 = opq(v0 ^ (isv ? 128 : 192));
      const unsigned lo = isv ? 4096 : 0, la = opq(lane ? A_MISC + 1536 + lane * 4 : isv ? A_LANDV : A_LANDK);
      unsigned dn = 0;
#define PUBLISH(val) do { pub = max(pub, (val)); *(volatile LAS unsigned*)la = (unsigned)pub; } while (0)
#define GATE(addr, need) do { if (lds_peek(addr) < (need)) { vmwait<0>(); PUBLISH(j); lds_wait_ge(addr, need); } } while (0)
#define PART(W) do { \
        if (j >= NSLOT) { if ((unsigned)__builtin_amdgcn_readfirstlane(dn) < (unsigned)(j - NSLOT + 1)) GATE(A_DONE + sl, (unsigned)(j - NSLOT + 1)); } \
        else if (j == 10) GATE(A_QUSED, 4u); \
        dma4<W>(gp, v0, v1, v2, v3, slot_addr(sl >> 2) + lo); \
        ++j; gp += 4096; if (++pos == nblk) gp -= nblk * 4096; sl = sl == 4 * (NSLOT - 1) ? 0 : sl + 4; \
        PUBLISH(j - W / 4); dn = *(volatile LAS unsigned*)(A_DONE + sl); } while (0)
      GET_TAG();
      lds_wait_ge(A_QCNT, 4u);
#pragma nounroll
      while (j < J1) PART(W1);
#pragma nounroll
      while (j < nblk) PART(W2);
      vmwait<20>(); PUBLISH(j - 5); vmwait<16>(); PUBLISH(j - 4); vmwait<12>(); PUBLISH(j - 3);
      vmwait<8>(); PUBLISH(j - 2); vmwait<4>(); PUBLISH(j - 1); vmwait<0>(); PUBLISH(j);
      if (ty < 2 && !isv) {
        const char* op = Wo + (size_t)iA * 32768;
        const unsigned o0 = lane << 4, o1 = o0 ^ 32, o2 = o0 ^ 64, o3 = o0 ^ 96;
        lds_wait_ge(A_LANDV, (unsigned)(nblk - ODL));
#pragma unroll
        for (int k = 0; k < 8; ++k) {
          const unsigned x = ((k >> 1) & 1) << 7;
          if (!(k & 1)) lds_wait_ge(A_DONE + sl, (unsigned)(j + (k >> 1) - NSLOT + 1));
          dma4<62>(op + k * 4096, o0 ^ x, o1 ^ x, o2 ^ x, o3 ^ x, slot_addr(sl >> 2) + (k & 1) * 4096);
          if (k & 1) sl = sl == 4 * (NSLOT - 1) ? 0 : sl + 4;
        }
      }
      __builtin_amdgcn_s_setprio(0);
#undef PUBLISH
#undef GATE
#undef PART
    } else {
      GET_TAG();
      vmwait<0>();
      if (wave <= 4) {
        const int w1 = wave - 1;
        if (ty < 2) {
          if (lane == 0) lds_inc(A_QCNT);
          const int q = c16 >> 2, p = lane & 3, f3 = q | ((g & 1) << 2);
          const unsigned a0 = (8 * g + q) * 256 + (((4 * w1 + (p >> 1)) ^ (f3 << 1)) << 4) + (p & 1) * 8;
          const unsigned aa[2] = {opq(SCR + a0), opq(A_QU2 + a0)};
          lds_wait_ge(A_QCNT, 4u);
          v4u w0[8], w1f[8], xf[8];
#pragma unroll
          for (int s = 0; s < 8; ++s) {
            const unsigned a = aa[s >> 2] + (s & 3) * 8192;
            w0[s] = trd2(a, a + 1024); w1f[s] = trd2((a ^ 32), (a ^ 32) + 1024);
            xf[s] = lds128(A_XIMG + (c16 & 1) * 512 + g * 16 + s * 64);
          }
          __builtin_amdgcn_sched_barrier(0);
          f4 d0 = {0.f, 0.f, 0.f, 0.f}, d1 = d0;
#pragma unroll
          for (int s = 0; s < 8; ++s) { d0 = mfma32(w0[s], xf[s], d0); d1 = mfma32(w1f[s], xf[s], d1); }
          const int pr = (c16 >> 1) & 1, nb = (c16 >> 2) & 1;
          const f4 e = nb ? d1 : d0;
          if (c16 < 8) gst128(rs, WS_QG + (((ty * 2 + (bid & 1)) * 2 + (c16 & 1)) * 4096 + hh * 128 + 32 * w1 + 16 * nb + 4 * g + 2 * pr) * 8,
                              (v4u){asu(pr ? e[2] : e[0]), tag, asu(pr ? e[3] : e[1]), tag});
          lgk0();
        } else {
          if (w1 == 3 && lane == 0) *(volatile LAS unsigned*)A_OUL2 = 1u;
          const unsigned a0 = opq((w1 < 2 ? SCR : A_QU2 - 32768) + w1 * 16384 + c16 * 256 + ((g ^ c16) << 4));
          v4u uf[16];
#pragma unroll
          for (int i = 0; i < 16; ++i) uf[i] = lds128((a0 ^ ((i & 3) << 6)) + (i >> 2) * 4096);
          lds_wait_ge(A_QB, 1u);
          v4u qb[4];
#pragma unroll
          for (int s = 0; s < 4; ++s) qb[s] = lds128(A_QBI + (c16 & 1) * 256 + s * 64 + g * 16);
          __builtin_amdgcn_sched_barrier(0);
          f4 d[4];
#pragma unroll
          for (int rb = 0; rb < 4; ++rb) d[rb] = (f4){0.f, 0.f, 0.f, 0.f};
#pragma unroll
          for (int s = 0; s < 4; ++s)
#pragma unroll
            for (int rb = 0; rb < 4; ++rb) d[rb] = mfma32(uf[4 * rb + s], qb[s], d[rb]);
          const int rb = (c16 >> 1) & 3;
          const f4 e = rb == 0 ? d[0] : rb == 1 ? d[1] : rb == 2 ? d[2] : d[3];
          if (c16 < 8) sto64(A_QAS + w1 * 256 + (c16 & 1) * 128 + (16 * rb + 4 * g) * 2, (v2u){pk(e[0], e[1]), pk(e[2], e[3])});
          lgk0();
          if (lane < 16) gst128(rs, WS_QAB + ((lane >> 3) * 32 + hh) * 1024 + 512 + w1 * 128 + (lane & 7) * 16, lds128(A_QAS + w1 * 256 + lane * 16));
          vmwait<0>();
        }
        unsigned old = 0;
        if (lane == 0) old = __hip_atomic_fetch_add((LAS unsigned*)A_QUSED, 1u, __ATOMIC_RELAXED, __HIP_MEMORY_SCOPE_WORKGROUP);
        if (ty == 2 && __builtin_amdgcn_readfirstlane(old) == 3u) {
          flag_line(rs, WS_FQ + (32 + hh) * FS, lane, tag);
          cfence();
          if (lane == 0) *(volatile LAS unsigned*)A_QCNT = 4u;
        }
      }
      if (wave == 5) {
        if (ty == 2) {
          lds_wait_ge(A_OUL2, 1u);
          gather_qb(A_QBI, hh, lane, rs, tag);
          lgk0();
          if (lane == 0) *(volatile LAS unsigned*)A_QB = 1u;
        }
        lds_wait_ge(A_LANDK, ty == 2 ? 1u : (unsigned)QGO);
        v4u v[8];
        const unsigned off = WS_QG + 131072 + (b * 4096 + kvh * 512 + 2 * lane) * 8;
        sweep<8, 4>(v, rs, tag, [&](int k) { return off + (k & 1) * 65536 + (k >> 1) * 1024; });
#pragma unroll
        for (int k = 0; k < 4; ++k) sto32(A_QIMG + k * 256 + lane * 4, pk(asf(v[2 * k].x) + asf(v[2 * k + 1].x), asf(v[2 * k].z) + asf(v[2 * k + 1].z)));
        lgk0();
        if (lane == 0) *(volatile LAS unsigned*)A_QRDY = 1u;
      }
      lds_wait_ge(A_QRDY, 1u);
      v4u qf[4];
#pragma unroll
      for (int s = 0; s < 4; ++s) qf[s] = lds128(A_QIMG + (c16 & 3) * 256 + s * 64 + g * 16);
      const unsigned ko = c16 * 256 + ((g ^ c16) << 4);
      const int vr = 4 * g + (c16 >> 2);
      const unsigned vo = 4096 + vr * 256 + ((((lane & 3) >> 1) ^ ((vr & 7) << 1)) << 4) + (lane & 1) * 8;
      float m = -INFINITY, l = 0.f;
      f4 acc[8];
#pragma unroll
      for (int t = 0; t < 8; ++t) acc[t] = (f4){0.f, 0.f, 0.f, 0.f};
      int slot = wave - 1;
      for (int j = wave - 1; j < nblk; j += NCONS) {
        const unsigned sb = slot_addr(slot);
        lds_wait_ge(A_LANDK, (unsigned)(j + 1));
        v4u kf[4];
#pragma unroll
        for (int s = 0; s < 4; ++s) kf[s] = lds128(sb + (ko ^ (s << 6)));
        __builtin_amdgcn_sched_barrier(0);
        f4 s4_ = {0.f, 0.f, 0.f, 0.f};
#pragma unroll
        for (int s = 0; s < 4; ++s) s4_ = mfma32(kf[s], qf[s], s4_);
        float mx = fmaxf(fmaxf(s4_[0], s4_[1]), fmaxf(s4_[2], s4_[3]));
        mx = x32max(x16max(mx));
        const float mn = fmaxf(m, mx), al = ex2((m - mn) * SC), mc = -mn * SC;
        const float p0 = ex2(fmaf(s4_[0], SC, mc)), p1 = ex2(fmaf(s4_[1], SC, mc)), p2 = ex2(fmaf(s4_[2], SC, mc)), p3 = ex2(fmaf(s4_[3], SC, mc));
        l = fmaf(l, al, (p0 + p1) + (p2 + p3));
        m = mn;
        v2u pb = {pk(p0, p1), pk(p2, p3)};
        unsigned va[8];
#pragma unroll
        for (int t = 0; t < 8; ++t) va[t] = sb + (vo ^ (t << 5));
        if (!__all(al == 1.f)) {
#pragma unroll
          for (int t = 0; t < 8; ++t) acc[t] *= al;
        }
        asm volatile("" : "+v"(pb), "+v"(l), "+v"(acc[0]), "+v"(acc[1]), "+v"(acc[2]), "+v"(acc[3]), "+v"(acc[4]), "+v"(acc[5]), "+v"(acc[6]), "+v"(acc[7]),
                     "+v"(va[0]), "+v"(va[1]), "+v"(va[2]), "+v"(va[3]), "+v"(va[4]), "+v"(va[5]), "+v"(va[6]), "+v"(va[7]));
        lds_wait_ge<0>(A_LANDV, (unsigned)(j + 1));
        v2u vf[8];
#pragma unroll
        for (int t = 0; t < 8; ++t) vf[t] = trd(va[t]);
        lgk0();
        if (lane == 0) *(volatile LAS unsigned*)(A_DONE + slot * 4) = (unsigned)(j + 1);
#pragma unroll
        for (int t = 0; t < 8; ++t) acc[t] = mfma16(vf[t], pb, acc[t]);
        slot += NCONS; if (slot >= NSLOT) slot -= NSLOT;
      }
      l = x32sum(x16sum(l));
      if (c16 < 4) {
#pragma unroll
        for (int t = 0; t < 8; ++t) sto128(A_ST + (wave - 1) * 2048 + c16 * 512 + ((t ^ c16) * 64) + g * 16, __builtin_bit_cast(v4u, acc[t]));
        if (g == 0) sto64(A_ML + (c16 * 8 + wave - 1) * 8, (v2u){asu(m * SC), asu(l)});
      }
    }
    bar();
    if (wave >= 3 && wave <= 6) {
      const int hq = wave - 3;
      v2u ml[NCONS];
      f2 ow[NCONS];
      const unsigned sa = opq(A_ST + hq * 512 + ((lane * 8) ^ (hq << 6))), ma = opq(A_ML + hq * 64);
#pragma unroll
      for (int c = 0; c < NCONS; ++c) { ml[c] = *(const LAS v2u*)(ma + c * 8); ow[c] = *(const LAS f2*)(sa + c * 2048); }
      float M = asf(ml[0].x);
#pragma unroll
      for (int c = 1; c < NCONS; ++c) M = fmaxf(M, asf(ml[c].x));
      float L = 0.f, o0 = 0.f, o1 = 0.f;
#pragma unroll
      for (int c = 0; c < NCONS; ++c) { const float wt = ex2(asf(ml[c].x) - M); L = fmaf(wt, asf(ml[c].y), L); o0 = fmaf(wt, ow[c].x, o0); o1 = fmaf(wt, ow[c].y, o1); }
      const int rec = (b * 32 + kvh * 4 + hq) * NSPLIT_A + split;
      gst128(rs, WS_PAO + rec * 1024 + lane * 16, (v4u){asu(o0), tag, asu(o1), tag});
      if (lane < 8) gst128(rs, WS_PAM + rec * 128 + lane * 16, (v4u){asu(M), tag, asu(L), tag});
    }
    if (ty < 2) {
      const int oh = iA >> 2, odq = iA & 3;
      if (wave == 0) { vmwait<0>(); if (lane == 0) lds_inc(A_OUL); }
      lds_wait_ge(A_OUL, 1u);
      v4u wf[4];
      out_frags(slot_addr((nblk + g) % NSLOT), wave, lane, wf);
      const int tt = (c16 >> 1) & 3, pr = c16 >> 3, n = 64 * wave + 16 * tt + 4 * g + 2 * pr;
      unsigned fo = WS_F + ((((n >> 3) * 128 + iA) * 16) + (c16 & 1) * 8 + (n & 7)) * 8, oa = A_OIMG + (c16 & 1) * 64 + g * 16;
      asm volatile("" : "+v"(fo), "+v"(oa));
      if (wave == 1) {
        const int rec = ((lane >> 5) * 32 + oh) * NSPLIT_A + (g & 1) * 5;
        v4u vm[10];
        const unsigned o0_ = WS_PAO + rec * 1024 + (32 * odq + 2 * c16) * 8, o1_ = WS_PAM + rec * 128 + odq * 16;
        sweep2<10, 9>(vm, rs, tag, [&](int k) { return k < 5 ? o0_ + k * 1024 : o1_ + (k - 5) * 128; });
        const v4u* v = vm; const v4u* ml = vm + 5;
        float M = asf(ml[0].x);
#pragma unroll
        for (int i = 1; i < 5; ++i) M = fmaxf(M, asf(ml[i].x));
        M = x16max(M);
        float L = 0.f, o0 = 0.f, o1 = 0.f;
#pragma unroll
        for (int i = 0; i < 5; ++i) { const float wt = ex2(asf(ml[i].x) - M); L = fmaf(wt, asf(ml[i].z), L); o0 = fmaf(wt, asf(v[i].x), o0); o1 = fmaf(wt, asf(v[i].z), o1); }
        const float inv = 1.f / x16sum(L);
        o0 = x16sum(o0); o1 = x16sum(o1);
        if (!(g & 1)) sto32(A_OIMG + (lane >> 5) * 64 + c16 * 4, pk(o0 * inv, o1 * inv));
      }
      bar();
      const v4u ob = lds128(oa);
      f4 d[4];
#pragma unroll
      for (int t = 0; t < 4; ++t) d[t] = mfma32(wf[t], ob, (f4){0.f, 0.f, 0.f, 0.f});
      const f4 e = tt == 0 ? d[0] : tt == 1 ? d[1] : tt == 2 ? d[2] : d[3];
      gst128(rs, fo, (v4u){asu(pr ? e[2] : e[0]), tag, asu(pr ? e[3] : e[1]), tag});
    }
  } else if (isB) {
    const int b = bid >> 5, w = wave, lane_ = lane;
    const unsigned vl = lane << 4;
#define LD_S(S, rho, u) asm volatile("global_load_dwordx4 %0, %1, %2 offset:%3 nt" : "=v"(S[u]) : "v"(vl), "s"(cp + (size_t)((((rho) + brot) & 3) * 65536) + ((u) >> 2) * 4096), "n"(((u) & 3) * 1024) : "memory")
#define ST_S(S, rho) do { unsigned a0_ = ((rho) & 1) * 65536 + w * 8192 + vc0, a1_ = ((rho) & 1) * 65536 + w * 8192 + vc1; asm volatile("" : "+v"(a0_), "+v"(a1_)); \
      _Pragma("unroll") for (int u_ = 0; u_ < 8; ++u_) sto128(u_ * 1024 + (((u_ >> 2) ? a1_ : a0_) ^ ((u_ & 3) << 6)), S[u_]); } while (0)
#define PB() vmwait<WB - 2>()
    v4u S2[8], S3[8];
    GET_TAG();
    PB(); LD_C(0, 6); LD_C(0, 7);
    PB(); LD_C(1, 0); LD_C(1, 1);
    PB(); LD_C(1, 2); LD_C(1, 3);
    PB(); LD_C(1, 4); LD_C(1, 5);
    PB(); LD_C(1, 6); LD_C(1, 7);
    PB(); LD_S(S2, 2, 0); LD_S(S2, 2, 1);
    PB(); LD_S(S2, 2, 2); LD_S(S2, 2, 3);
    PB(); LD_S(S2, 2, 4); LD_S(S2, 2, 5);
    PB(); LD_S(S2, 2, 6); LD_S(S2, 2, 7);
#pragma unroll
    for (int u = 0; u < NS3; u += 2) { PB(); LD_S(S3, 3, u); LD_S(S3, 3, u + 1); }
    if (w == 0) {
      _Pragma("nounroll") for (int it = 0; it < SPIN_CAP; ++it) { cfence(); if (__all(gld32(rs, WS_FQ + lane * FS) == tag)) break; __builtin_amdgcn_s_sleep(4); }
    }
    bar();
    {
      v4u t[4];
      const char* qa = ws + WS_QAB + (b * 32 + 4 * w) * 1024;
#pragma unroll
      for (int i = 0; i < 4; ++i) asm volatile("global_load_dwordx4 %0, %1, %2 offset:%3 sc1" : "=v"(t[i]) : "v"(vl), "s"(qa), "n"(i * 1024) : "memory");
      constexpr int NA = 2, NC = 6 - NS3;
      static_assert(NS3 == 2 || NS3 == 4 || NS3 == 6, "");
#pragma unroll
      for (int u = NS3; u < NS3 + NA; ++u) LD_S(S3, 3, u);
      asm volatile("s_waitcnt vmcnt(%12)" : "+v"(t[0]), "+v"(t[1]), "+v"(t[2]), "+v"(t[3]), "+v"(S2[0]), "+v"(S2[1]), "+v"(S2[2]), "+v"(S2[3]),
                   "+v"(S2[4]), "+v"(S2[5]), "+v"(S2[6]), "+v"(S2[7]) : "n"(NA) : "memory");
#pragma unroll
      for (int u = NS3 + NA; u < NS3 + NA + NC; ++u) LD_S(S3, 3, u);
#pragma unroll
      for (int i = 0; i < 4; ++i) sto128(SCR + (4 * w + i) * 1024 + ((lane ^ ((4 * w + i) & 15)) << 4), t[i]);
    }
    bar();
    const int kb4 = w & 3, hg = w >> 2;
    v4u qf[16];
#pragma unroll
    for (int s = 0; s < 16; ++s) qf[s] = lds128(SCR + (16 * hg + c16) * 1024 + ((((4 * s) & 15) ^ g ^ c16) << 4) + (s >> 2) * 256);
    const unsigned fk = ((c16 & 3) << 2) | ((0x78u >> (2 * (c16 >> 2))) & 3u);
    const unsigned ka = (16 * kb4 + c16) * 1024 + ((g ^ fk) << 4);
    const int h2 = lane >> 5, blk = g & 1, qq = c16 >> 2, pp = lane & 3;
    const unsigned ft0 = (qq << 2) | (h2 ? 3u : 0u), ft1 = (qq << 2) | (h2 ? 1u : 2u);
    const unsigned cbase = 8 * w + 2 * blk + (pp >> 1);
    const unsigned va00 = (8 * h2 + qq) * 1024 + ((cbase ^ ft0) << 4) + (pp & 1) * 8, va01 = (8 * h2 + 4 + qq) * 1024 + ((cbase ^ ft1) << 4) + (pp & 1) * 8;
    const unsigned mxa = opq(B_MX + (16 * hg + c16) * 4), pwa = opq(B_P + kb4 * 1536 + (16 * hg + c16) * 48 + g * 8);
    const unsigned pra = opq(B_P + (lane & 31) * 48 + h2 * 16), ala = opq(B_AL + (lane & 31) * 4);
    float m = -INFINITY, l = 0.f;
    f16v acc0, acc1;
#pragma unroll
    for (int i = 0; i < 16; ++i) { acc0[i] = 0.f; acc1[i] = 0.f; }
#pragma unroll
    for (int rho = 0; rho < 2; ++rho) {
      unsigned kr = ka, vr0 = va00, vr1 = va01;
      asm volatile("" : "+v"(kr), "+v"(vr0), "+v"(vr1));
      bar();
      f4 sa = {0.f, 0.f, 0.f, 0.f}, sb = sa;
      {
        v4u kf[8];
#pragma unroll
        for (int s = 0; s < 8; ++s) kf[s] = lds128((kr ^ ((s & 3) << 6)) + (s >> 2) * 256);
        __builtin_amdgcn_sched_barrier(0);
#pragma unroll
        for (int s = 0; s < 8; ++s) {
          sa = mfma32(kf[s], qf[s], sa);
          kf[s] = lds128((kr ^ ((s & 3) << 6)) + (2 + (s >> 2)) * 256);
          __builtin_amdgcn_sched_barrier(0);
        }
        kr += 65536;
#pragma unroll
        for (int s = 0; s < 8; ++s) {
          sa = mfma32(kf[s], qf[8 + s], sa);
          kf[s] = lds128((kr ^ ((s & 3) << 6)) + (s >> 2) * 256);
          __builtin_amdgcn_sched_barrier(0);
        }
#pragma unroll
        for (int s = 0; s < 8; ++s) {
          sb = mfma32(kf[s], qf[s], sb);
          kf[s] = lds128((kr ^ ((s & 3) << 6)) + (2 + (s >> 2)) * 256);
          __builtin_amdgcn_sched_barrier(0);
        }
#pragma unroll
        for (int s = 0; s < 8; ++s) sb = mfma32(kf[s], qf[8 + s], sb);
      }
      float mx = fmaxf(fmaxf(fmaxf(sa[0], sa[1]), fmaxf(sa[2], sa[3])), fmaxf(fmaxf(sb[0], sb[1]), fmaxf(sb[2], sb[3])));
      mx = x32max(x16max(mx));
      if (g == 0) stof(mxa + kb4 * 128, mx);
      v4u c0[8], c1[8];
#pragma unroll
      for (int ks = 0; ks < 4; ++ks) {
        c0[ks] = trd2(ks * 16384 + vr0, ks * 16384 + vr1);
        c1[ks] = trd2(ks * 16384 + (vr0 ^ 64), ks * 16384 + (vr1 ^ 64));
      }
      bar();
      const float mn = fmaxf(fmaxf(m, fmaxf(ldsf(mxa), ldsf(mxa + 128))), fmaxf(ldsf(mxa + 256), ldsf(mxa + 384)));
      __builtin_amdgcn_sched_barrier(0);
      if (rho == 0) ST_S(S2, 0);
      __builtin_amdgcn_sched_barrier(0);
      vr0 += 65536; vr1 += 65536;
#pragma unroll
      for (int ks = 0; ks < 4; ++ks) {
        c0[4 + ks] = trd2(ks * 16384 + vr0, ks * 16384 + vr1);
        c1[4 + ks] = trd2(ks * 16384 + (vr0 ^ 64), ks * 16384 + (vr1 ^ 64));
      }
      const float al = ex2((m - mn) * SC), mc = -mn * SC;
      float p[8];
#pragma unroll
      for (int i = 0; i < 4; ++i) { p[i] = ex2(fmaf(sa[i], SC, mc)); p[4 + i] = ex2(fmaf(sb[i], SC, mc)); }
      l = fmaf(l, al, ((p[0] + p[1]) + (p[2] + p[3])) + ((p[4] + p[5]) + (p[6] + p[7])));
      m = mn;
      sto64(pwa, (v2u){pk(p[0], p[1]), pk(p[2], p[3])});
      sto64(pwa + 6144, (v2u){pk(p[4], p[5]), pk(p[6], p[7])});
      if (kb4 == 0 && g == 0) stof(mxa + (B_AL - B_MX), al);
      if (rho == 1) { l = x32sum(x16sum(l)); if (g == 0) stof(mxa + (B_LS - B_MX) + kb4 * 128, l); }
      bar();
      if (rho == 1 && kb4 == 0 && g == 0) {
        const unsigned la = mxa + (B_LS - B_MX);
        sto64(B_FM + (16 * hg + c16) * 8, (v2u){asu(m * SC), asu((ldsf(la) + ldsf(la + 128)) + (ldsf(la + 256) + ldsf(la + 384)))});
      }
      const float alp = rho ? ldsf(ala) : 0.f;
      v4u pf[4];
#pragma unroll
      for (int ks = 0; ks < 4; ++ks) pf[ks] = lds128(pra + ks * 1536);
      __builtin_amdgcn_sched_barrier(0);
      if (rho) { acc0 *= alp; acc1 *= alp; }
      unsigned s0_ = 65536 + w * 8192 + vc0, s1_ = 65536 + w * 8192 + vc1;
      if (rho == 0) { asm volatile("" : "+v"(s0_), "+v"(s1_)); vmwait<0>(); }
#pragma unroll
      for (int ks = 0; ks < 8; ++ks) {
        acc0 = mfmaL(c0[ks], pf[ks & 3], acc0); acc1 = mfmaL(c1[ks], pf[ks & 3], acc1);
        if (ks < 4) pf[ks] = lds128(pra + 6144 + ks * 1536);
        if (rho == 0) sto128(ks * 1024 + (((ks >> 2) ? s1_ : s0_) ^ ((ks & 3) << 6)), S3[ks]);
        __builtin_amdgcn_sched_barrier(0);
      }
    }
    {
    const int lane = (int)opq((unsigned)lane_), h2 = lane >> 5, tid = wave * 64 + lane;
    {
      const unsigned tb = B_T + w * 4096, n32 = lane & 31;
#pragma unroll
      for (int q4 = 0; q4 < 4; ++q4) {
        sto64(tb + n32 * 128 + ((q4 ^ (n32 & 7)) << 4) + h2 * 8, (v2u){pk(acc0[4 * q4], acc0[4 * q4 + 1]), pk(acc0[4 * q4 + 2], acc0[4 * q4 + 3])});
        sto64(tb + n32 * 128 + (((4 + q4) ^ (n32 & 7)) << 4) + h2 * 8, (v2u){pk(acc1[4 * q4], acc1[4 * q4 + 1]), pk(acc1[4 * q4 + 2], acc1[4 * q4 + 3])});
      }
      lgk0();
      const int hr = lane >> 3, ck = lane & 7;
#pragma unroll
      for (int i = 0; i < 4; ++i)
        gst128(rs, WS_PB + (((8 * i + hr) * 4 + (w >> 1)) * 64 + bid) * 256 + (w & 1) * 128 + ck * 16, lds128(tb + (8 * i + hr) * 128 + ((ck ^ hr) << 4)));
    }
    vmwait<0>();
    bar();
    if (w == 0 && lane < 32) { const v2u fm = *(const LAS v2u*)(B_FM + lane * 8); gst128(rs, WS_PBM + bid * 512 + lane * 16, (v4u){fm.x, tag, fm.y, tag}); }
    if (w == 0) {
      const unsigned off = WS_F + bid * 16384 + ((lane >> 3) * 16 + (bid & 15)) * 128;
      _Pragma("nounroll") for (int it = 0; it < SPIN_CAP; ++it) { cfence(); if (__popcll(__ballot(gld64(rs, off).y == tag)) >= PREN) break; __builtin_amdgcn_s_sleep(8); }
    }
    bar();
    v4u f[3];
    sweep<3, 0>(f, rs, tag, [&](int k) { return WS_F + ((k >> 1) * 64 + bid) * 16384 + (k < 2 ? k * 8192 + tid * 16 : (tid & 255) * 16); });
    float r[4] = {asf(f[0].x) + asf(f[1].x), asf(f[0].z) + asf(f[1].z), w < 4 ? asf(f[2].x) : 0.f, w < 4 ? asf(f[2].z) : 0.f};
#pragma unroll
    for (int k = 0; k < 4; ++k) { r[k] += dpp<ROR8>(r[k]); r[k] = x32sum(x16sum(r[k])); }
    if (lane < 8) { sto64(B_FIN + w * 128 + lane * 8, (v2u){asu(r[0]), asu(r[1])}); sto64(B_FIN + w * 128 + 64 + lane * 8, (v2u){asu(r[2]), asu(r[3])}); }
    bar();
    if (tid < 16) {
      float s0 = 0.f, s1 = 0.f;
#pragma unroll
      for (int i = 0; i < 8; ++i) { const f2 t = *(const LAS f2*)(B_FIN + i * 128 + tid * 8); s0 += t.x; s1 += t.y; }
      const int mat = tid >> 3, ob = (tid >> 2) & 1, pr = tid & 3;
      *(GAS unsigned*)((GAS char*)out + (ob * 1024 + mat * 512 + 8 * bid + 2 * pr) * 2) = pk(s0, s1);
      if (bid == 0 && tid == 0) gst32(rs, WS_E, tag);
    }
    }
  } else {
    const int h = bid - NB_CU, w = wave;
    GET_TAG();
    if (w == 0) gather_qb(T_QBI, h, lane, rs, tag);
    vmwait<0>();
    v4u OU[16];
    const char* ow = Bwo + (size_t)(h * 128 + 16 * w) * 1024;
    const unsigned vo = g * 4096 + c16 * 16;
#define LD_OU(u) asm volatile("global_load_dwordx4 %0, %1, %2 offset:%3 nt" : "=v"(OU[u]) : "v"(vo), "s"(ow), "n"(((u) >> 2) * 1024 + ((u) & 3) * 256) : "memory")
#pragma unroll
    for (int u = 0; u < TE; ++u) LD_OU(u);
    {
      const unsigned a0 = w * 8192 + c16 * 256 + ((g ^ c16) << 4);
      v4u uf[8];
#pragma unroll
      for (int i = 0; i < 8; ++i) uf[i] = lds128((a0 ^ ((i & 3) << 6)) + (i >> 2) * 4096);
      bar();
      v4u qb[4];
#pragma unroll
      for (int s = 0; s < 4; ++s) qb[s] = lds128(T_QBI + (c16 & 1) * 256 + s * 64 + g * 16);
      __builtin_amdgcn_sched_barrier(0);
      f4 d0 = {0.f, 0.f, 0.f, 0.f}, d1 = d0;
#pragma unroll
      for (int s = 0; s < 4; ++s) { d0 = mfma32(uf[s], qb[s], d0); d1 = mfma32(uf[4 + s], qb[s], d1); }
      const int rb = (c16 >> 1) & 1;
      const f4 e = rb ? d1 : d0;
      if (c16 < 4) sto64(T_QAS + (c16 & 1) * 512 + (32 * w + 16 * rb + 4 * g) * 2, (v2u){pk(e[0], e[1]), pk(e[2], e[3])});
      bar();
      if (w == 1) {
        gst128(rs, WS_QAB + ((lane >> 5) * 32 + h) * 1024 + (lane & 31) * 16, lds128(T_QAS + lane * 16));
        vmwait<0>();
        flag_line(rs, WS_FQ + h * FS, lane, tag);
      }
    }
    bar();
    {
      const char* uv = Wuv + (size_t)(64 * w) * 8192 + h * 256;
      const unsigned vuv = g * 8192 + ((c16 ^ (g << 1)) << 4);
#pragma unroll
      for (int u = 0; u < 16; ++u) { if (u >= WT && !(u & 1)) vmwait<WT - 2>(); dma<0>(uv + (size_t)u * 32768, vuv ^ (((u >> 1) & 1) << 7), w * 16384 + u * 1024); }
#pragma unroll
      for (int u = TE; u < 16; ++u) { if (!(u & 1)) vmwait<WT - 2>(); LD_OU(u); }
    }
    asm volatile("s_waitcnt vmcnt(0)" : "+v"(OU[0]), "+v"(OU[1]), "+v"(OU[2]), "+v"(OU[3]), "+v"(OU[4]), "+v"(OU[5]), "+v"(OU[6]), "+v"(OU[7]),
                 "+v"(OU[8]), "+v"(OU[9]), "+v"(OU[10]), "+v"(OU[11]), "+v"(OU[12]), "+v"(OU[13]), "+v"(OU[14]), "+v"(OU[15]) :: "memory");
    if (tid < 4) sto32(T_RDY + tid * 4, 0u);
    bar();
    v4u wf[16];
    {
      const int q = c16 >> 2, p = lane & 3, f3 = q | ((g & 1) << 2);
      const unsigned a0 = (8 * g + q) * 256 + (((2 * w + (p >> 1)) ^ (f3 << 1)) << 4) + (p & 1) * 8;
      const unsigned aa[2] = {opq(a0), opq(a0 + 65536)};
#pragma unroll
      for (int s = 0; s < 16; ++s) wf[s] = trd2(aa[s >> 3] + (s & 7) * 8192, aa[s >> 3] + (s & 7) * 8192 + 1024);
    }
    {
      const int tb = w >> 2;
      v4u o[8];
#pragma unroll
      for (int i = 0; i < 8; ++i) { o[i] = (v4u){0u, 0u, 0u, 0u}; asm volatile("" : "+v"(o[i])); }
      const char* pb = ws + WS_PB + (h * 4 + (w & 3)) * 16384 + tb * 8192;
      const unsigned vl = lane << 4;
      unsigned done = 0;
      bool all = false;
      v4u ml = {0u, ~tag, 0u, ~tag};
      if (w == 0) {
        _Pragma("nounroll") for (int it = 0; it < SPIN_CAP; ++it) { cfence(); if (__all(gld32(rs, WS_PBM + 2 * h * 512 + h * 16 + 4) == tag)) break; __builtin_amdgcn_s_sleep(8); }
      }
      _Pragma("nounroll") for (int it = 0; it < SPIN_CAP * 8 && !all; ++it) {
        unsigned rdy;
        if (w == 0) {
          cfence();
          const v4u t = gld128(rs, tagok(ml, tag) ? 0xfffffff0u : WS_PBM + lane * 512 + h * 16);
          if (!tagok(ml, tag)) ml = t;
          unsigned long long m = __ballot(tagok(ml, tag));
          all = m == ~0ull;
          m &= m >> 2; m &= m >> 1; m &= 0x1111111111111111ull;
          rdy = (unsigned)m;
          if (lane == 0) *(volatile LAS v2u*)T_RDY = (v2u){rdy, (unsigned)(m >> 32)};
        } else {
          rdy = lds_peek(T_RDY + tb * 4);
          if (rdy == done) { __builtin_amdgcn_s_sleep(1); continue; }
          all = rdy == 0x11111111u;
        }
        const unsigned add = rdy & ~done;
        done = rdy;
#pragma unroll
        for (int i = 0; i < 8; ++i)
          if (add & (1u << (4 * i))) asm volatile("global_load_dwordx4 %0, %1, %2 offset:%3 sc1" : "+v"(o[i]) : "v"(vl), "s"(pb + (i >> 2) * 4096), "n"((i & 3) * 1024) : "memory");
      }
      if (w == 0) {
        const float M = x16max(row16max(asf(ml.x)));
        const float wt = ex2(asf(ml.x) - M), wn = wt / x16sum(row16sum(wt * asf(ml.z)));
        const unsigned hi = pk(wn, wn), wa = T_WN + (lane >> 5) * 128 + (lane & 3) * 32 + ((lane >> 2) & 7) * 4;
        const float lo = wn - asf(hi & 0xffff0000u);
        sto32(wa, hi); sto32(wa + 256, pk(lo, lo));
        lgk0();
        if (lane == 0) *(volatile LAS unsigned*)(T_RDY + 8) = 1u;
      } else {
        lds_wait_ge<0>(T_RDY + 8, 1u);
      }
      const unsigned wa = T_WN + (c16 >> 3) * 256 + tb * 128 + g * 32, half = (c16 & 1) ? 0xffff0000u : 0x0000ffffu;
      const v4u w0_ = lds128(wa), w1_ = lds128(wa + 16);
      const int rr = (c16 >> 1) & 3;
      v4u bo[8];
#pragma unroll
      for (int u = 0; u < 8; ++u) { const unsigned x = (u < 4 ? w0_[u & 3] : w1_[u & 3]) & half; bo[u] = (v4u){rr == 0 ? x : 0u, rr == 1 ? x : 0u, rr == 2 ? x : 0u, rr == 3 ? x : 0u}; }
      asm volatile("s_waitcnt vmcnt(0)" : "+v"(o[0]), "+v"(o[1]), "+v"(o[2]), "+v"(o[3]), "+v"(o[4]), "+v"(o[5]), "+v"(o[6]), "+v"(o[7]),
                   "+v"(bo[0]), "+v"(bo[1]), "+v"(bo[2]), "+v"(bo[3]), "+v"(bo[4]), "+v"(bo[5]), "+v"(bo[6]), "+v"(bo[7]) :: "memory");
      f4 d = {0.f, 0.f, 0.f, 0.f};
#pragma unroll
      for (int u = 0; u < 8; ++u) d = mfma32(o[u], bo[u], d);
#pragma unroll
      for (int r = 0; r < 4; ++r) {
        const float x = d[r] + dpp<ROR8>(d[r]);
        const unsigned pr = pk(x, dpp<XOR1>(x));
        if ((c16 & 9) == 0) sto32(T_OL + tb * 1088 + ((w & 3) * 128 + 32 * g + 8 * r + c16) * 2, pr);
      }
    }
    bar();
    {
      v4u of[16];
      const unsigned oa = opq(T_OL + (c16 >> 3) * 1088 + g * 16);
#pragma unroll
      for (int s = 0; s < 16; ++s) of[s] = lds128(oa + s * 64);
      __builtin_amdgcn_sched_barrier(0);
      f4 d0 = {0.f, 0.f, 0.f, 0.f}, d1 = d0;
#pragma unroll
      for (int s = 0; s < 8; ++s) { d0 = mfma32(wf[2 * s], of[2 * s], d0); d1 = mfma32(wf[2 * s + 1], of[2 * s + 1], d1); }
      d0 += d1;
      const unsigned half = (c16 & 1) ? 0xffff0000u : 0x0000ffffu;
      const int rr = (c16 >> 1) & 3;
      f4 o4[4];
#pragma unroll
      for (int cb = 0; cb < 4; ++cb) o4[cb] = (f4){0.f, 0.f, 0.f, 0.f};
#pragma unroll
      for (int u = 0; u < 4; ++u) {
        const unsigned x = pk(d0[u], d0[u]) & half;
        const v4u bo = {rr == 0 ? x : 0u, rr == 1 ? x : 0u, rr == 2 ? x : 0u, rr == 3 ? x : 0u};
#pragma unroll
        for (int cb = 0; cb < 4; ++cb) o4[cb] = mfma32(OU[4 * u + cb], bo, o4[cb]);
      }
#pragma unroll
      for (int cb = 0; cb < 4; ++cb)
#pragma unroll
        for (int r = 0; r < 4; ++r) stof(T_RED + w * 4096 + cb * 1024 + r * 64 + g * 256 + c16 * 4, o4[cb][r]);
    }
    bar();
    {
      const int j = tid >> 3, pr = tid & 7;
      float s0 = 0.f, s1 = 0.f;
#pragma unroll
      for (int i = 0; i < 8; ++i) { const f2 t = *(const LAS f2*)(T_RED + i * 4096 + tid * 8); s0 += t.x; s1 += t.y; }
      gst128(rs, WS_F + (((64 + j) * 128 + h) * 16 + 2 * pr) * 8, (v4u){asu(s0), tag, asu(s1), tag});
    }
  }
}

extern "C" void kernel_launch(void* const* d_in, const int* in_sizes, int n_in, void* d_out, int out_size, void* d_ws, size_t ws_size,
                              hipStream_t stream) {
  (void)in_sizes; (void)n_in; (void)out_size; (void)ws_size;
  (void)hipFuncSetAttribute((const void*)fused_decode, hipFuncAttributeMaxDynamicSharedMemorySize, LDS_BYTES);
  fused_decode<<<256, 512, LDS_BYTES, stream>>>(
      (const char*)d_in[0], (const char*)d_in[1], (const char*)d_in[2], (const char*)d_in[3], (const char*)d_in[4], (const char*)d_in[5],
      (const char*)d_in[6], (const char*)d_in[7], (const char*)d_in[8], (const char*)d_in[9], (const char*)d_in[10], (unsigned short*)d_out,
      (char*)d_ws);
}
```

```cpp
#include <hip/hip_runtime.h>
#include <stdint.h>
#pragma clang diagnostic ignored "-Wint-to-pointer-cast"

#define GAS __attribute__((address_space(1)))
#define LAS __attribute__((address_space(3)))
typedef unsigned v4u __attribute__((ext_vector_type(4)));
typedef unsigned v2u __attribute__((ext_vector_type(2)));
typedef float f4 __attribute__((ext_vector_type(4)));
typedef float f2 __attribute__((ext_vector_type(2)));
typedef float f16v __attribute__((ext_vector_type(16)));
typedef short s4 __attribute__((ext_vector_type(4)));
typedef __bf16 b8 __attribute__((ext_vector_type(8)));
typedef __bf16 b2 __attribute__((ext_vector_type(2)));

namespace {

constexpr int NB_CU = 64;
constexpr int NT_CU = 32;
constexpr int NSPLIT_A = 10;
constexpr int NB0 = 55;
constexpr int NB1 = 52;
constexpr int NB2 = (512 - 4 * NB0 - 4 * NB1) / 2;
constexpr int NSLOT = 18, NLOW = 14, NCONS = 6;
constexpr int W1 = 12;
constexpr int W2 = 24;
constexpr int J1 = 18;
constexpr int WB = 4;
constexpr int WT = 6;
constexpr int DLYP = 30;
constexpr int DLYB = 10;
constexpr int NS3 = 6;
constexpr int TE = 6;
constexpr int QGO = 4;
constexpr int ODL = 5;
constexpr int PREN = 32;
constexpr int ROT = 13;
constexpr int BROT = 3;
constexpr int CODE_KB = 24;
constexpr float SC = 0.08838834764831845f * 1.4426950408889634f;
constexpr int SPIN_CAP = 1 << 18;

constexpr unsigned FS = 128;
constexpr unsigned WS_E = 0;
constexpr unsigned WS_FQ = 4096;
constexpr unsigned WS_QG = 65536;
constexpr unsigned WS_QAB = WS_QG + 262144;
constexpr unsigned WS_PBM = WS_QAB + 65536;
constexpr unsigned WS_PAM = WS_PBM + 32768;
constexpr unsigned WS_PAO = WS_PAM + 131072;
constexpr unsigned WS_PB = WS_PAO + 786432;
constexpr unsigned WS_F = WS_PB + 2097152;
constexpr unsigned WS_END = WS_F + 2097152;

constexpr unsigned LDS_BYTES = 163840;
constexpr unsigned SCR = 131072;
constexpr unsigned A_MISC = NLOW * 8192;
constexpr unsigned A_LANDK = A_MISC, A_LANDV = A_MISC + 4, A_QUSED = A_MISC + 8, A_QCNT = A_MISC + 12, A_QB = A_MISC + 16, A_QRDY = A_MISC + 20, A_OUL = A_MISC + 24, A_OUL2 = A_MISC + 28;
constexpr unsigned A_DONE = A_MISC + 64;
constexpr unsigned A_QU2 = 10 * 8192;
constexpr unsigned A_QIMG = A_MISC + 256;
constexpr unsigned A_ML = A_MISC + 1280;
constexpr unsigned A_OIMG = A_MISC + 1792;
constexpr unsigned A_ST = A_MISC + 2048;
constexpr unsigned A_XIMG = A_MISC + 4096, A_DUMMY = A_MISC + 5120, A_QBI = A_MISC + 6144, A_QAS = A_MISC + 6656;
constexpr unsigned SLOT1 = 65536;
constexpr unsigned B_MX = SCR, B_AL = SCR + 512, B_LS = SCR + 640, B_P = SCR + 2048;
constexpr unsigned B_FM = SCR + 1280, B_FIN = SCR + 16384, B_T = SLOT1, B_DUMMY = SCR + 8192;
constexpr unsigned T_QBI = SCR, T_QAS = SCR + 1024, T_OL = SCR + 4096, T_RDY = SCR + 6400, T_WN = SCR + 6656, T_RED = 0;

__device__ __forceinline__ unsigned slot_addr(unsigned s) { return s * 8192 + (s >= NLOW ? 16384u : 0u); }
__device__ __forceinline__ v4u lds128(unsigned a) { return *(const LAS v4u*)a; }
__device__ __forceinline__ void sto128(unsigned a, v4u v) { *(LAS v4u*)a = v; }
__device__ __forceinline__ void sto64(unsigned a, v2u v) { *(LAS v2u*)a = v; }
__device__ __forceinline__ void sto32(unsigned a, unsigned v) { *(LAS unsigned*)a = v; }
__device__ __forceinline__ float ldsf(unsigned a) { return *(const LAS float*)a; }
__device__ __forceinline__ void stof(unsigned a, float v) { *(LAS float*)a = v; }
__device__ __forceinline__ v2u trd(unsigned a) { return __builtin_bit_cast(v2u, __builtin_amdgcn_ds_read_tr16_b64_v4i16((LAS s4*)a)); }
__device__ __forceinline__ v4u trd2(unsigned a0, unsigned a1) { const v2u x = trd(a0), y = trd(a1); return (v4u){x.x, x.y, y.x, y.y}; }
__device__ __forceinline__ f4 mfma32(v4u a, v4u b, f4 c) {
  return __builtin_amdgcn_mfma_f32_16x16x32_bf16(__builtin_bit_cast(b8, a), __builtin_bit_cast(b8, b), c, 0, 0, 0); }
__device__ __forceinline__ f4 mfma16(v2u a, v2u b, f4 c) {
  return __builtin_amdgcn_mfma_f32_16x16x16bf16_1k(__builtin_bit_cast(s4, a), __builtin_bit_cast(s4, b), c, 0, 0, 0); }
__device__ __forceinline__ f16v mfmaL(v4u a, v4u b, f16v c) {
  return __builtin_amdgcn_mfma_f32_32x32x16_bf16(__builtin_bit_cast(b8, a), __builtin_bit_cast(b8, b), c, 0, 0, 0); }
__device__ __forceinline__ unsigned pk(float lo, float hi) { return __builtin_bit_cast(unsigned, __builtin_convertvector((f2){lo, hi}, b2)); }
__device__ __forceinline__ float asf(unsigned u) { return __builtin_bit_cast(float, u); }
__device__ __forceinline__ unsigned asu(float f) { return __builtin_bit_cast(unsigned, f); }
__device__ __forceinline__ float ex2(float x) { return __builtin_amdgcn_exp2f(x); }
template <int CTRL> __device__ __forceinline__ float dpp(float x) {
  return __builtin_bit_cast(float, __builtin_amdgcn_mov_dpp(__builtin_bit_cast(int, x), CTRL, 0xf, 0xf, true)); }
constexpr int XOR1 = 0xB1, XOR2 = 0x4E, ROR4 = 0x124, ROR8 = 0x128;
__device__ __forceinline__ float x16max(float x) { auto s = __builtin_amdgcn_permlane16_swap(asu(x), asu(x), false, false); return fmaxf(asf(s[0]), asf(s[1])); }
__device__ __forceinline__ float x32max(float x) { auto s = __builtin_amdgcn_permlane32_swap(asu(x), asu(x), false, false); return fmaxf(asf(s[0]), asf(s[1])); }
__device__ __forceinline__ float x16sum(float x) { auto s = __builtin_amdgcn_permlane16_swap(asu(x), asu(x), false, false); return asf(s[0]) + asf(s[1]); }
__device__ __forceinline__ float x32sum(float x) { auto s = __builtin_amdgcn_permlane32_swap(asu(x), asu(x), false, false); return asf(s[0]) + asf(s[1]); }
__device__ __forceinline__ float row16max(float x) { x = fmaxf(x, dpp<XOR1>(x)); x = fmaxf(x, dpp<XOR2>(x)); x = fmaxf(x, dpp<ROR4>(x)); return fmaxf(x, dpp<ROR8>(x)); }
__device__ __forceinline__ float row16sum(float x) { x += dpp<XOR1>(x); x += dpp<XOR2>(x); x += dpp<ROR4>(x); return x + dpp<ROR8>(x); }

template <int IMM> __device__ __forceinline__ void dma(const void* sbase, unsigned voff, unsigned lds) {
  asm volatile("s_mov_b32 m0, %2\n\ts_nop 0\n\tglobal_load_lds_dwordx4 %0, %1 offset:%3 nt" :: "v"(voff), "s"(sbase), "s"(lds), "n"(IMM) : "memory");
}
template <int W> __device__ __forceinline__ void dma4(const void* sbase, unsigned v0, unsigned v1, unsigned v2, unsigned v3, unsigned lds) {
  asm volatile("s_mov_b32 m0, %5\n\ts_waitcnt vmcnt(%6)\n\tglobal_load_lds_dwordx4 %0, %4 nt\n\tglobal_load_lds_dwordx4 %1, %4 offset:1024 nt\n\t"
               "s_waitcnt vmcnt(%6)\n\tglobal_load_lds_dwordx4 %2, %4 offset:2048 nt\n\tglobal_load_lds_dwordx4 %3, %4 offset:3072 nt"
               :: "v"(v0), "v"(v1), "v"(v2), "v"(v3), "s"(sbase), "s"(lds), "n"(W - 2) : "memory");
}
__device__ __forceinline__ void dma_keep(const void* sbase, unsigned voff, unsigned lds) {
  asm volatile("s_mov_b32 m0, %2\n\ts_nop 0\n\tglobal_load_lds_dwordx4 %0, %1" :: "v"(voff), "s"(sbase), "s"(lds) : "memory");
}
template <int N> __device__ __forceinline__ void vmwait() { asm volatile("s_waitcnt vmcnt(%0)" :: "n"(N) : "memory"); }
__device__ __forceinline__ void bar() { asm volatile("s_waitcnt lgkmcnt(0)\n\ts_barrier" ::: "memory"); }
__device__ __forceinline__ void lgk0() { asm volatile("s_waitcnt lgkmcnt(0)" ::: "memory"); }
__device__ __forceinline__ void cfence() { asm volatile("" ::: "memory"); }
__device__ __forceinline__ unsigned opq(unsigned x) { asm volatile("" : "+v"(x)); return x; }
__device__ __forceinline__ unsigned lds_peek(unsigned a) { return __builtin_amdgcn_readfirstlane(*(volatile LAS unsigned*)a); }
template <int SL = 1> __device__ __forceinline__ void lds_wait_ge(unsigned a, unsigned want) {
  _Pragma("nounroll") for (int it = 0; it < SPIN_CAP * 8; ++it) { if (lds_peek(a) >= want) break; if (SL) __builtin_amdgcn_s_sleep(SL); }
  cfence();
}
typedef __amdgpu_buffer_rsrc_t rsrc_t;
constexpr int SC1 = 16;
__device__ __forceinline__ v4u gld128(rsrc_t r, unsigned off) { return __builtin_amdgcn_raw_buffer_load_b128(r, off, 0, SC1); }
__device__ __forceinline__ v2u gld64(rsrc_t r, unsigned off) { return __builtin_amdgcn_raw_buffer_load_b64(r, off, 0, SC1); }
__device__ __forceinline__ unsigned gld32(rsrc_t r, unsigned off) { return __builtin_amdgcn_raw_buffer_load_b32(r, off, 0, SC1); }
__device__ __forceinline__ void gst128(rsrc_t r, unsigned off, v4u v) { __builtin_amdgcn_raw_buffer_store_b128(v, r, off, 0, SC1); }
__device__ __forceinline__ void gst32(rsrc_t r, unsigned off, unsigned v) { __builtin_amdgcn_raw_buffer_store_b32(v, r, off, 0, SC1); }
__device__ __forceinline__ void flag_line(rsrc_t r, unsigned off, int lane, unsigned tag) { if (lane < 8) gst128(r, off + lane * 16, (v4u){tag, tag, tag, tag}); }
__device__ __forceinline__ bool tagok(v4u v, unsigned tag) { return v.y == tag && v.w == tag; }
template <int N, int GAP, class F> __device__ __forceinline__ void sweep2(v4u (&v)[N], rsrc_t rs, unsigned tag, F off) {
  v4u a[N], b[N];
#pragma unroll
  for (int k = 0; k < N; ++k) a[k] = gld128(rs, off(k));
  __builtin_amdgcn_s_sleep(GAP);
  _Pragma("nounroll") for (int it = 0; it < SPIN_CAP; ++it) {
    cfence();
#pragma unroll
    for (int k = 0; k < N; ++k) b[k] = gld128(rs, off(k));
    cfence();
    bool ok = true;
#pragma unroll
    for (int k = 0; k < N; ++k) { ok &= tagok(a[k], tag); v[k] = a[k]; }
    if (__all(ok)) break;
#pragma unroll
    for (int k = 0; k < N; ++k) a[k] = gld128(rs, off(k));
    cfence();
    ok = true;
#pragma unroll
    for (int k = 0; k < N; ++k) { ok &= tagok(b[k], tag); v[k] = b[k]; }
    if (__all(ok)) break;
  }
}
template <int N, int SL, class F> __device__ __forceinline__ void sweep(v4u (&v)[N], rsrc_t rs, unsigned tag, F off) {
  _Pragma("nounroll") for (int it = 0; it < SPIN_CAP; ++it) {
    cfence();
    bool ok = true;
#pragma unroll
    for (int k = 0; k < N; ++k) { v[k] = gld128(rs, off(k)); ok &= tagok(v[k], tag); }
    if (__all(ok)) break;
    if (SL) __builtin_amdgcn_s_sleep(SL);
  }
}

__device__ __forceinline__ void gather_qb(unsigned img, int h, int lane, rsrc_t rs, unsigned tag) {
  v4u qg[4];
  const unsigned off = WS_QG + (h * 128 + 2 * lane) * 8;
  sweep<4, 1>(qg, rs, tag, [&](int k) { return off + (k & 1) * 65536 + (k >> 1) * 32768; });
  sto32(img + lane * 4, pk(asf(qg[0].x) + asf(qg[1].x), asf(qg[0].z) + asf(qg[1].z)));
  sto32(img + 256 + lane * 4, pk(asf(qg[2].x) + asf(qg[3].x), asf(qg[2].z) + asf(qg[3].z)));
}
__device__ __forceinline__ void lds_inc(unsigned a) { __hip_atomic_fetch_add((LAS unsigned*)a, 1u, __ATOMIC_RELAXED, __HIP_MEMORY_SCOPE_WORKGROUP); }

__device__ __forceinline__ void out_frags(unsigned gb, int w, int lane, v4u (&wf)[4]) {
  const int g = lane >> 4, q = (lane & 15) >> 2, p = lane & 3, f3 = q | ((g & 1) << 2);
#pragma unroll
  for (int tt = 0; tt < 4; ++tt) {
    const unsigned a0 = gb + q * 1024 + (((2 * (4 * w + tt) + (p >> 1)) ^ (f3 << 1)) << 4) + (p & 1) * 8;
    wf[tt] = trd2(a0, a0 + 4096);
  }
}

}

__global__ void __launch_bounds__(512, 2) __attribute__((aligned(4096))) fused_decode(
    const char* __restrict__ Ax, const char* __restrict__ Kc, const char* __restrict__ Vc, const char* __restrict__ Wq, const char* __restrict__ Wo,
    const char* __restrict__ Bx, const char* __restrict__ Ckv, const char* __restrict__ Bwq, const char* __restrict__ Wuk, const char* __restrict__ Wuv,
    const char* __restrict__ Bwo, unsigned short* __restrict__ out, char* __restrict__ ws) {
  const int bid = blockIdx.x, tid = threadIdx.x, lane = tid & 63, wave = __builtin_amdgcn_readfirstlane(tid >> 6);
  const int g = lane >> 4, c16 = lane & 15;
  const int iA = bid - (NB_CU + NT_CU);
  const bool isA = iA >= 0, isB = bid < NB_CU;
  const int ty = iA >> 6;
  int dA = 95 - bid, dU = 223 - bid, dB = bid - 64, dQ = 159 - bid, dW = 2 - wave;
  asm("" : "+s"(dA), "+s"(dU), "+s"(dB), "+s"(dQ), "+s"(dW));
  const int mA = dA >> 31, mU = dU >> 31, mB = dB >> 31, mP = mA & ~mU, sh = mP & 1;
  const int hh = (bid >> sh) & 31;
  const unsigned row0 = (mA & ((((bid | mU) & 1) << 8) + 64 * wave - 64)) | (~mA & (32 * wave));
  const unsigned l0 = (mA & (SCR - 16384 + wave * 16384 - ((dW >> 31) & (SCR + 32768 - A_QU2)))) | (~mA & (wave * 8192));
  const unsigned karg = 0x40 - (mP & (8 + ((dQ >> 31) & 32))), more = mA;
  if ((unsigned)(wave + mA + (mB & 8)) < (unsigned)(8 + (mA & -4))) {
    const unsigned b0 = (row0 + g) * 8192 + hh * 256 + ((c16 ^ (g << sh)) << 4), x1 = ~mP & 64;
    unsigned v0 = b0, v1 = (b0 ^ x1) + 32768, v2 = (b0 ^ 128) + 65536, v3 = (b0 ^ x1 ^ 128) + 98304;
    if (karg == 0x18) __builtin_amdgcn_s_sleep(DLYP);
    unsigned long long wp;
#define D4 "global_load_lds_dwordx4 %1, %0 nt\n\ts_add_u32 m0, m0, 0x400\n\tv_add_u32 %1, 0x20000, %1\n\t" \
           "global_load_lds_dwordx4 %2, %0 nt\n\ts_add_u32 m0, m0, 0x400\n\tv_add_u32 %2, 0x20000, %2\n\t" \
           "global_load_lds_dwordx4 %3, %0 nt\n\ts_add_u32 m0, m0, 0x400\n\tv_add_u32 %3, 0x20000, %3\n\t" \
           "global_load_lds_dwordx4 %4, %0 nt\n\ts_add_u32 m0, m0, 0x400\n\tv_add_u32 %4, 0x20000, %4\n\t"
    asm volatile("s_load_dwordx2 %0, %5, %6\n\ts_mov_b32 m0, %7\n\ts_waitcnt lgkmcnt(0)\n\t" D4 D4
                 "s_cmp_eq_u32 %8, 0\n\ts_cbranch_scc1 1f\n\t" D4 D4 "1:"
                 : "=&s"(wp), "+v"(v0), "+v"(v1), "+v"(v2), "+v"(v3)
                 : "s"(__builtin_amdgcn_kernarg_segment_ptr()), "s"(karg), "s"(l0), "s"(more) : "memory", "scc");
#undef D4
  }
  {
    unsigned long long pc;
    asm volatile("s_getpc_b64 %0" : "=s"(pc));
    const int kb = ((bid >> 3) + 4 * wave) & 31;
    dma_keep((const char*)((pc & ~1023ull) + (unsigned)((kb < CODE_KB ? kb : CODE_KB - 1) * 1024)), lane * 16, isA ? A_DUMMY : B_DUMMY);
  }
  const rsrc_t rs = __builtin_amdgcn_make_buffer_rsrc(ws, 0, WS_END, 0x00020000);
  asm volatile("" :: "s"(Ax), "s"(Kc), "s"(Vc), "s"(Wq), "s"(Wo), "s"(Bx), "s"(Ckv), "s"(Bwq), "s"(Wuk), "s"(Wuv), "s"(Bwo), "s"(out), "s"(ws));
  unsigned tag;
#define GET_TAG() do { asm volatile("s_load_dword %0, %1, 0x0\n\ts_waitcnt lgkmcnt(0)" : "=s"(tag) : "s"(ws) : "memory"); tag += 1u; } while (0)
  const int pid = ((bid & 7) << 3) | ((bid >> 3) & 7);
  const char* cp = Ckv + ((size_t)pid * 256 + 8 * wave) * 1024;
  const int brot = bid >> BROT;
  const unsigned vc0 = (lane ^ ((wave & 1) ? 3 : 0)) << 4, vc1 = (lane ^ ((wave & 1) ? 1 : 2)) << 4;
#define LD_C(rho, u) dma<((u) & 3) * 1024>(cp + (size_t)((((rho) + brot) & 3) * 65536) + ((u) >> 2) * 4096, (((u) >> 2) ? vc1 : vc0) ^ (((u) & 3) << 6), ((rho) & 1) * 65536 + wave * 8192 + ((u) >> 2) * 4096)
  if (isA) {
    if (wave == 1 && ty < 2) dma<0>((ty ? Ax : Bx) + 512 * (bid & 1), (lane >> 5) * 1024 + (lane & 31) * 16, A_XIMG);
    if (tid < 64) sto32(A_MISC + tid * 4, 0u);
    bar();
  } else if (isB) {
    __builtin_amdgcn_s_sleep(DLYB);
    LD_C(0, 0); LD_C(0, 1); LD_C(0, 2); LD_C(0, 3); LD_C(0, 4); LD_C(0, 5);
  }

  if (isA) {
    const int split = iA >> 4, pair = (iA + split) & 15, b = pair >> 3, kvh = pair & 7;
    const int nblk = ty == 0 ? NB0 : ty == 1 ? NB1 : NB2, blk0 = (ty == 0 ? 0 : ty == 1 ? 4 * NB0 : 4 * (NB0 + NB1)) + (split & 3) * nblk;
    if (wave == 0 || wave == 7) {
      __builtin_amdgcn_s_setprio(3);
      static_assert(NSPLIT_A == 10 && W1 >= 4 && W1 % 4 == 0 && W2 % 4 == 0 && W1 <= W2 && W2 <= 24 && J1 <= NB0 && J1 <= NB1 && J1 <= NB2, "window");
      const bool isv = wave == 7;
      int pub = 0, j = 0;
      unsigned sl = 0;
      int pos = (pair * ROT) % nblk;
      const char* gp = (isv ? Vc : Kc) + ((size_t)pair * 8192 + (blk0 + pos) * 16) * 256;
      const unsigned v0 = opq(g * 256 + ((c16 ^ (isv ? g << 1 : g)) << 4));
      const unsigned v1 = opq(v0 ^ (isv ? 128 : 64)), v2 = opq(v0 ^ (isv ? 0 : 128)), v3 = opq(v0 ^ (isv ? 128 : 192));
      const unsigned lo = isv ? 4096 : 0, la = opq(lane ? A_MISC + 1536 + lane * 4 : isv ? A_LANDV : A_LANDK);
      unsigned dn = 0;
#define PUBLISH(val) do { pub = max(pub, (val)); *(volatile LAS unsigned*)la = (unsigned)pub; } while (0)
#define GATE(addr, need) do { if (lds_peek(addr) < (need)) { vmwait<0>(); PUBLISH(j); lds_wait_ge(addr, need); } } while (0)
#define PART(W) do { \
        if (j >= NSLOT) { if ((unsigned)__builtin_amdgcn_readfirstlane(dn) < (unsigned)(j - NSLOT + 1)) GATE(A_DONE + sl, (unsigned)(j - NSLOT + 1)); } \
        else if (j == 10) GATE(A_QUSED, 4u); \
        dma4<W>(gp, v0, v1, v2, v3, slot_addr(sl >> 2) + lo); \
        ++j; gp += 4096; if (++pos == nblk) gp -= nblk * 4096; sl = sl == 4 * (NSLOT - 1) ? 0 : sl + 4; \
        PUBLISH(j - W / 4); dn = *(volatile LAS unsigned*)(A_DONE + sl); } while (0)
      GET_TAG();
      lds_wait_ge(A_QCNT, 4u);
#pragma nounroll
      while (j < J1) PART(W1);
#pragma nounroll
      while (j < nblk) PART(W2);
      vmwait<20>(); PUBLISH(j - 5); vmwait<16>(); PUBLISH(j - 4); vmwait<12>(); PUBLISH(j - 3);
      vmwait<8>(); PUBLISH(j - 2); vmwait<4>(); PUBLISH(j - 1); vmwait<0>(); PUBLISH(j);
      if (ty < 2 && !isv) {
        const char* op = Wo + (size_t)iA * 32768;
        const unsigned o0 = lane << 4, o1 = o0 ^ 32, o2 = o0 ^ 64, o3 = o0 ^ 96;
        lds_wait_ge(A_LANDV, (unsigned)(nblk - ODL));
#pragma unroll
        for (int k = 0; k < 8; ++k) {
          const unsigned x = ((k >> 1) & 1) << 7;
          if (!(k & 1)) lds_wait_ge(A_DONE + sl, (unsigned)(j + (k >> 1) - NSLOT + 1));
          dma4<62>(op + k * 4096, o0 ^ x, o1 ^ x, o2 ^ x, o3 ^ x, slot_addr(sl >> 2) + (k & 1) * 4096);
          if (k & 1) sl = sl == 4 * (NSLOT - 1) ? 0 : sl + 4;
        }
      }
      __builtin_amdgcn_s_setprio(0);
#undef PUBLISH
#undef GATE
#undef PART
    } else {
      GET_TAG();
      vmwait<0>();
      if (wave <= 4) {
        const int w1 = wave - 1;
        if (ty < 2) {
          if (lane == 0) lds_inc(A_QCNT);
          const int q = c16 >> 2, p = lane & 3, f3 = q | ((g & 1) << 2);
          const unsigned a0 = (8 * g + q) * 256 + (((4 * w1 + (p >> 1)) ^ (f3 << 1)) << 4) + (p & 1) * 8;
          const unsigned aa[2] = {opq(SCR + a0), opq(A_QU2 + a0)};
          lds_wait_ge(A_QCNT, 4u);
          v4u w0[8], w1f[8], xf[8];
#pragma unroll
          for (int s = 0; s < 8; ++s) {
            const unsigned a = aa[s >> 2] + (s & 3) * 8192;
            w0[s] = trd2(a, a + 1024); w1f[s] = trd2((a ^ 32), (a ^ 32) + 1024);
            xf[s] = lds128(A_XIMG + (c16 & 1) * 512 + g * 16 + s * 64);
          }
          __builtin_amdgcn_sched_barrier(0);
          f4 d0 = {0.f, 0.f, 0.f, 0.f}, d1 = d0;
#pragma unroll
          for (int s = 0; s < 8; ++s) { d0 = mfma32(w0[s], xf[s], d0); d1 = mfma32(w1f[s], xf[s], d1); }
          const int pr = (c16 >> 1) & 1, nb = (c16 >> 2) & 1;
          const f4 e = nb ? d1 : d0;
          if (c16 < 8) gst128(rs, WS_QG + (((ty * 2 + (bid & 1)) * 2 + (c16 & 1)) * 4096 + hh * 128 + 32 * w1 + 16 * nb + 4 * g + 2 * pr) * 8,
                              (v4u){asu(pr ? e[2] : e[0]), tag, asu(pr ? e[3] : e[1]), tag});
          lgk0();
        } else {
          if (w1 == 3 && lane == 0) *(volatile LAS unsigned*)A_OUL2 = 1u;
          const unsigned a0 = opq((w1 < 2 ? SCR : A_QU2 - 32768) + w1 * 16384 + c16 * 256 + ((g ^ c16) << 4));
          v4u uf[16];
#pragma unroll
          for (int i = 0; i < 16; ++i) uf[i] = lds128((a0 ^ ((i & 3) << 6)) + (i >> 2) * 4096);
          lds_wait_ge(A_QB, 1u);
          v4u qb[4];
#pragma unroll
          for (int s = 0; s < 4; ++s) qb[s] = lds128(A_QBI + (c16 & 1) * 256 + s * 64 + g * 16);
          __builtin_amdgcn_sched_barrier(0);
          f4 d[4];
#pragma unroll
          for (int rb = 0; rb < 4; ++rb) d[rb] = (f4){0.f, 0.f, 0.f, 0.f};
#pragma unroll
          for (int s = 0; s < 4; ++s)
#pragma unroll
            for (int rb = 0; rb < 4; ++rb) d[rb] = mfma32(uf[4 * rb + s], qb[s], d[rb]);
          const int rb = (c16 >> 1) & 3;
          const f4 e = rb == 0 ? d[0] : rb == 1 ? d[1] : rb == 2 ? d[2] : d[3];
          if (c16 < 8) sto64(A_QAS + w1 * 256 + (c16 & 1) * 128 + (16 * rb + 4 * g) * 2, (v2u){pk(e[0], e[1]), pk(e[2], e[3])});
          lgk0();
          if (lane < 16) gst128(rs, WS_QAB + ((lane >> 3) * 32 + hh) * 1024 + 512 + w1 * 128 + (lane & 7) * 16, lds128(A_QAS + w1 * 256 + lane * 16));
          vmwait<0>();
        }
        unsigned old = 0;
        if (lane == 0) old = __hip_atomic_fetch_add((LAS unsigned*)A_QUSED, 1u, __ATOMIC_RELAXED, __HIP_MEMORY_SCOPE_WORKGROUP);
        if (ty == 2 && __builtin_amdgcn_readfirstlane(old) == 3u) {
          flag_line(rs, WS_FQ + (32 + hh) * FS, lane, tag);
          cfence();
          if (lane == 0) *(volatile LAS unsigned*)A_QCNT = 4u;
        }
      }
      if (wave == 5) {
        if (ty == 2) {
          lds_wait_ge(A_OUL2, 1u);
          gather_qb(A_QBI, hh, lane, rs, tag);
          lgk0();
          if (lane == 0) *(volatile LAS unsigned*)A_QB = 1u;
        }
        lds_wait_ge(A_LANDK, ty == 2 ? 1u : (unsigned)QGO);
        v4u v[8];
        const unsigned off = WS_QG + 131072 + (b * 4096 + kvh * 512 + 2 * lane) * 8;
        sweep<8, 4>(v, rs, tag, [&](int k) { return off + (k & 1) * 65536 + (k >> 1) * 1024; });
#pragma unroll
        for (int k = 0; k < 4; ++k) sto32(A_QIMG + k * 256 + lane * 4, pk(asf(v[2 * k].x) + asf(v[2 * k + 1].x), asf(v[2 * k].z) + asf(v[2 * k + 1].z)));
        lgk0();
        if (lane == 0) *(volatile LAS unsigned*)A_QRDY = 1u;
      }
      lds_wait_ge(A_QRDY, 1u);
      v4u qf[4];
#pragma unroll
      for (int s = 0; s < 4; ++s) qf[s] = lds128(A_QIMG + (c16 & 3) * 256 + s * 64 + g * 16);
      const unsigned ko = c16 * 256 + ((g ^ c16) << 4);
      const int vr = 4 * g + (c16 >> 2);
      const unsigned vo = 4096 + vr * 256 + ((((lane & 3) >> 1) ^ ((vr & 7) << 1)) << 4) + (lane & 1) * 8;
      float m = -INFINITY, l = 0.f;
      f4 acc[8];
#pragma unroll
      for (int t = 0; t < 8; ++t) acc[t] = (f4){0.f, 0.f, 0.f, 0.f};
      int slot = wave - 1;
      for (int j = wave - 1; j < nblk; j += NCONS) {
        const unsigned sb = slot_addr(slot);
        lds_wait_ge(A_LANDK, (unsigned)(j + 1));
        v4u kf[4];
#pragma unroll
        for (int s = 0; s < 4; ++s) kf[s] = lds128(sb + (ko ^ (s << 6)));
        __builtin_amdgcn_sched_barrier(0);
        f4 s4_ = {0.f, 0.f, 0.f, 0.f};
#pragma unroll
        for (int s = 0; s < 4; ++s) s4_ = mfma32(kf[s], qf[s], s4_);
        float mx = fmaxf(fmaxf(s4_[0], s4_[1]), fmaxf(s4_[2], s4_[3]));
        mx = x32max(x16max(mx));
        const float mn = fmaxf(m, mx), al = ex2((m - mn) * SC), mc = -mn * SC;
        const float p0 = ex2(fmaf(s4_[0], SC, mc)), p1 = ex2(fmaf(s4_[1], SC, mc)), p2 = ex2(fmaf(s4_[2], SC, mc)), p3 = ex2(fmaf(s4_[3], SC, mc));
        l = fmaf(l, al, (p0 + p1) + (p2 + p3));
        m = mn;
        v2u pb = {pk(p0, p1), pk(p2, p3)};
        unsigned va[8];
#pragma unroll
        for (int t = 0; t < 8; ++t) va[t] = sb + (vo ^ (t << 5));
        if (!__all(al == 1.f)) {
#pragma unroll
          for (int t = 0; t < 8; ++t) acc[t] *= al;
        }
        asm volatile("" : "+v"(pb), "+v"(l), "+v"(acc[0]), "+v"(acc[1]), "+v"(acc[2]), "+v"(acc[3]), "+v"(acc[4]), "+v"(acc[5]), "+v"(acc[6]), "+v"(acc[7]),
                     "+v"(va[0]), "+v"(va[1]), "+v"(va[2]), "+v"(va[3]), "+v"(va[4]), "+v"(va[5]), "+v"(va[6]), "+v"(va[7]));
        lds_wait_ge<0>(A_LANDV, (unsigned)(j + 1));
        v2u vf[8];
#pragma unroll
        for (int t = 0; t < 8; ++t) vf[t] = trd(va[t]);
        lgk0();
        if (lane == 0) *(volatile LAS unsigned*)(A_DONE + slot * 4) = (unsigned)(j + 1);
#pragma unroll
        for (int t = 0; t < 8; ++t) acc[t] = mfma16(vf[t], pb, acc[t]);
        slot += NCONS; if (slot >= NSLOT) slot -= NSLOT;
      }
      l = x32sum(x16sum(l));
      if (c16 < 4) {
#pragma unroll
        for (int t = 0; t < 8; ++t) sto128(A_ST + (wave - 1) * 2048 + c16 * 512 + ((t ^ c16) * 64) + g * 16, __builtin_bit_cast(v4u, acc[t]));
        if (g == 0) sto64(A_ML + (c16 * 8 + wave - 1) * 8, (v2u){asu(m * SC), asu(l)});
      }
    }
    bar();
    if (wave >= 3 && wave <= 6) {
      const int hq = wave - 3;
      v2u ml[NCONS];
      f2 ow[NCONS];
      const unsigned sa = opq(A_ST + hq * 512 + ((lane * 8) ^ (hq << 6))), ma = opq(A_ML + hq * 64);
#pragma unroll
      for (int c = 0; c < NCONS; ++c) { ml[c] = *(const LAS v2u*)(ma + c * 8); ow[c] = *(const LAS f2*)(sa + c * 2048); }
      float M = asf(ml[0].x);
#pragma unroll
      for (int c = 1; c < NCONS; ++c) M = fmaxf(M, asf(ml[c].x));
      float L = 0.f, o0 = 0.f, o1 = 0.f;
#pragma unroll
      for (int c = 0; c < NCONS; ++c) { const float wt = ex2(asf(ml[c].x) - M); L = fmaf(wt, asf(ml[c].y), L); o0 = fmaf(wt, ow[c].x, o0); o1 = fmaf(wt, ow[c].y, o1); }
      const int rec = (b * 32 + kvh * 4 + hq) * NSPLIT_A + split;
      gst128(rs, WS_PAO + rec * 1024 + lane * 16, (v4u){asu(o0), tag, asu(o1), tag});
      if (lane < 8) gst128(rs, WS_PAM + rec * 128 + lane * 16, (v4u){asu(M), tag, asu(L), tag});
    }
    if (ty < 2) {
      const int oh = iA >> 2, odq = iA & 3;
      if (wave == 0) { vmwait<0>(); if (lane == 0) lds_inc(A_OUL); }
      lds_wait_ge(A_OUL, 1u);
      v4u wf[4];
      out_frags(slot_addr((nblk + g) % NSLOT), wave, lane, wf);
      const int tt = (c16 >> 1) & 3, pr = c16 >> 3, n = 64 * wave + 16 * tt + 4 * g + 2 * pr;
      unsigned fo = WS_F + ((((n >> 3) * 128 + iA) * 16) + (c16 & 1) * 8 + (n & 7)) * 8, oa = A_OIMG + (c16 & 1) * 64 + g * 16;
      asm volatile("" : "+v"(fo), "+v"(oa));
      if (wave == 1) {
        const int rec = ((lane >> 5) * 32 + oh) * NSPLIT_A + (g & 1) * 5;
        v4u vm[10];
        const unsigned o0_ = WS_PAO + rec * 1024 + (32 * odq + 2 * c16) * 8, o1_ = WS_PAM + rec * 128 + odq * 16;
        sweep2<10, 9>(vm, rs, tag, [&](int k) { return k < 5 ? o0_ + k * 1024 : o1_ + (k - 5) * 128; });
        const v4u* v = vm; const v4u* ml = vm + 5;
        float M = asf(ml[0].x);
#pragma unroll
        for (int i = 1; i < 5; ++i) M = fmaxf(M, asf(ml[i].x));
        M = x16max(M);
        float L = 0.f, o0 = 0.f, o1 = 0.f;
#pragma unroll
        for (int i = 0; i < 5; ++i) { const float wt = ex2(asf(ml[i].x) - M); L = fmaf(wt, asf(ml[i].z), L); o0 = fmaf(wt, asf(v[i].x), o0); o1 = fmaf(wt, asf(v[i].z), o1); }
        const float inv = 1.f / x16sum(L);
        o0 = x16sum(o0); o1 = x16sum(o1);
        if (!(g & 1)) sto32(A_OIMG + (lane >> 5) * 64 + c16 * 4, pk(o0 * inv, o1 * inv));
      }
      bar();
      const v4u ob = lds128(oa);
      f4 d[4];
#pragma unroll
      for (int t = 0; t < 4; ++t) d[t] = mfma32(wf[t], ob, (f4){0.f, 0.f, 0.f, 0.f});
      const f4 e = tt == 0 ? d[0] : tt == 1 ? d[1] : tt == 2 ? d[2] : d[3];
      gst128(rs, fo, (v4u){asu(pr ? e[2] : e[0]), tag, asu(pr ? e[3] : e[1]), tag});
    }
  } else if (isB) {
    const int b = pid >> 5, w = wave, lane_ = lane;
    const unsigned vl = lane << 4;
#define LD_S(S, rho, u) asm volatile("global_load_dwordx4 %0, %1, %2 offset:%3 nt" : "=v"(S[u]) : "v"(vl), "s"(cp + (size_t)((((rho) + brot) & 3) * 65536) + ((u) >> 2) * 4096), "n"(((u) & 3) * 1024) : "memory")
#define ST_S(S, rho) do { unsigned a0_ = ((rho) & 1) * 65536 + w * 8192 + vc0, a1_ = ((rho) & 1) * 65536 + w * 8192 + vc1; asm volatile("" : "+v"(a0_), "+v"(a1_)); \
      _Pragma("unroll") for (int u_ = 0; u_ < 8; ++u_) sto128(u_ * 1024 + (((u_ >> 2) ? a1_ : a0_) ^ ((u_ & 3) << 6)), S[u_]); } while (0)
#define PB() vmwait<WB - 2>()
    v4u S2[8], S3[8];
    GET_TAG();
    PB(); LD_C(0, 6); LD_C(0, 7);
    PB(); LD_C(1, 0); LD_C(1, 1);
    PB(); LD_C(1, 2); LD_C(1, 3);
    PB(); LD_C(1, 4); LD_C(1, 5);
    PB(); LD_C(1, 6); LD_C(1, 7);
    PB(); LD_S(S2, 2, 0); LD_S(S2, 2, 1);
    PB(); LD_S(S2, 2, 2); LD_S(S2, 2, 3);
    PB(); LD_S(S2, 2, 4); LD_S(S2, 2, 5);
    PB(); LD_S(S2, 2, 6); LD_S(S2, 2, 7);
#pragma unroll
    for (int u = 0; u < NS3; u += 2) { PB(); LD_S(S3, 3, u); LD_S(S3, 3, u + 1); }
    if (w == 0) {
      _Pragma("nounroll") for (int it = 0; it < SPIN_CAP; ++it) { cfence(); if (__all(gld32(rs, WS_FQ + lane * FS) == tag)) break; __builtin_amdgcn_s_sleep(4); }
    }
    bar();
    {
      v4u t[4];
      const char* qa = ws + WS_QAB + (b * 32 + 4 * w) * 1024;
#pragma unroll
      for (int i = 0; i < 4; ++i) asm volatile("global_load_dwordx4 %0, %1, %2 offset:%3 sc1" : "=v"(t[i]) : "v"(vl), "s"(qa), "n"(i * 1024) : "memory");
      constexpr int NA = 2, NC = 6 - NS3;
      static_assert(NS3 == 2 || NS3 == 4 || NS3 == 6, "");
#pragma unroll
      for (int u = NS3; u < NS3 + NA; ++u) LD_S(S3, 3, u);
      asm volatile("s_waitcnt vmcnt(%12)" : "+v"(t[0]), "+v"(t[1]), "+v"(t[2]), "+v"(t[3]), "+v"(S2[0]), "+v"(S2[1]), "+v"(S2[2]), "+v"(S2[3]),
                   "+v"(S2[4]), "+v"(S2[5]), "+v"(S2[6]), "+v"(S2[7]) : "n"(NA) : "memory");
#pragma unroll
      for (int u = NS3 + NA; u < NS3 + NA + NC; ++u) LD_S(S3, 3, u);
#pragma unroll
      for (int i = 0; i < 4; ++i) sto128(SCR + (4 * w + i) * 1024 + ((lane ^ ((4 * w + i) & 15)) << 4), t[i]);
    }
    bar();
    const int kb4 = w & 3, hg = w >> 2;
    v4u qf[16];
#pragma unroll
    for (int s = 0; s < 16; ++s) qf[s] = lds128(SCR + (16 * hg + c16) * 1024 + ((((4 * s) & 15) ^ g ^ c16) << 4) + (s >> 2) * 256);
    const unsigned fk = ((c16 & 3) << 2) | ((0x78u >> (2 * (c16 >> 2))) & 3u);
    const unsigned ka = (16 * kb4 + c16) * 1024 + ((g ^ fk) << 4);
    const int h2 = lane >> 5, blk = g & 1, qq = c16 >> 2, pp = lane & 3;
    const unsigned ft0 = (qq << 2) | (h2 ? 3u : 0u), ft1 = (qq << 2) | (h2 ? 1u : 2u);
    const unsigned cbase = 8 * w + 2 * blk + (pp >> 1);
    const unsigned va00 = (8 * h2 + qq) * 1024 + ((cbase ^ ft0) << 4) + (pp & 1) * 8, va01 = (8 * h2 + 4 + qq) * 1024 + ((cbase ^ ft1) << 4) + (pp & 1) * 8;
    const unsigned mxa = opq(B_MX + (16 * hg + c16) * 4), pwa = opq(B_P + kb4 * 1536 + (16 * hg + c16) * 48 + g * 8);
    const unsigned pra = opq(B_P + (lane & 31) * 48 + h2 * 16), ala = opq(B_AL + (lane & 31) * 4);
    float m = -INFINITY, l = 0.f;
    f16v acc0, acc1;
#pragma unroll
    for (int i = 0; i < 16; ++i) { acc0[i] = 0.f; acc1[i] = 0.f; }
#pragma unroll
    for (int rho = 0; rho < 2; ++rho) {
      unsigned kr = ka, vr0 = va00, vr1 = va01;
      asm volatile("" : "+v"(kr), "+v"(vr0), "+v"(vr1));
      bar();
      f4 sa = {0.f, 0.f, 0.f, 0.f}, sb = sa;
      {
        v4u kf[8];
#pragma unroll
        for (int s = 0; s < 8; ++s) kf[s] = lds128((kr ^ ((s & 3) << 6)) + (s >> 2) * 256);
        __builtin_amdgcn_sched_barrier(0);
#pragma unroll
        for (int s = 0; s < 8; ++s) {
          sa = mfma32(kf[s], qf[s], sa);
          kf[s] = lds128((kr ^ ((s & 3) << 6)) + (2 + (s >> 2)) * 256);
          __builtin_amdgcn_sched_barrier(0);
        }
        kr += 65536;
#pragma unroll
        for (int s = 0; s < 8; ++s) {
          sa = mfma32(kf[s], qf[8 + s], sa);
          kf[s] = lds128((kr ^ ((s & 3) << 6)) + (s >> 2) * 256);
          __builtin_amdgcn_sched_barrier(0);
        }
#pragma unroll
        for (int s = 0; s < 8; ++s) {
          sb = mfma32(kf[s], qf[s], sb);
          kf[s] = lds128((kr ^ ((s & 3) << 6)) + (2 + (s >> 2)) * 256);
          __builtin_amdgcn_sched_barrier(0);
        }
#pragma unroll
        for (int s = 0; s < 8; ++s) sb = mfma32(kf[s], qf[8 + s], sb);
      }
      float mx = fmaxf(fmaxf(fmaxf(sa[0], sa[1]), fmaxf(sa[2], sa[3])), fmaxf(fmaxf(sb[0], sb[1]), fmaxf(sb[2], sb[3])));
      mx = x32max(x16max(mx));
      if (g == 0) stof(mxa + kb4 * 128, mx);
      v4u c0[8], c1[8];
#pragma unroll
      for (int ks = 0; ks < 4; ++ks) {
        c0[ks] = trd2(ks * 16384 + vr0, ks * 16384 + vr1);
        c1[ks] = trd2(ks * 16384 + (vr0 ^ 64), ks * 16384 + (vr1 ^ 64));
      }
      bar();
      const float mn = fmaxf(fmaxf(m, fmaxf(ldsf(mxa), ldsf(mxa + 128))), fmaxf(ldsf(mxa + 256), ldsf(mxa + 384)));
      __builtin_amdgcn_sched_barrier(0);
      if (rho == 0) ST_S(S2, 0);
      __builtin_amdgcn_sched_barrier(0);
      vr0 += 65536; vr1 += 65536;
#pragma unroll
      for (int ks = 0; ks < 4; ++ks) {
        c0[4 + ks] = trd2(ks * 16384 + vr0, ks * 16384 + vr1);
        c1[4 + ks] = trd2(ks * 16384 + (vr0 ^ 64), ks * 16384 + (vr1 ^ 64));
      }
      const float al = ex2((m - mn) * SC), mc = -mn * SC;
      float p[8];
#pragma unroll
      for (int i = 0; i < 4; ++i) { p[i] = ex2(fmaf(sa[i], SC, mc)); p[4 + i] = ex2(fmaf(sb[i], SC, mc)); }
      l = fmaf(l, al, ((p[0] + p[1]) + (p[2] + p[3])) + ((p[4] + p[5]) + (p[6] + p[7])));
      m = mn;
      sto64(pwa, (v2u){pk(p[0], p[1]), pk(p[2], p[3])});
      sto64(pwa + 6144, (v2u){pk(p[4], p[5]), pk(p[6], p[7])});
      if (kb4 == 0 && g == 0) stof(mxa + (B_AL - B_MX), al);
      if (rho == 1) { l = x32sum(x16sum(l)); if (g == 0) stof(mxa + (B_LS - B_MX) + kb4 * 128, l); }
      bar();
      if (rho == 1 && kb4 == 0 && g == 0) {
        const unsigned la = mxa + (B_LS - B_MX);
        sto64(B_FM + (16 * hg + c16) * 8, (v2u){asu(m * SC), asu((ldsf(la) + ldsf(la + 128)) + (ldsf(la + 256) + ldsf(la + 384)))});
      }
      const float alp = rho ? ldsf(ala) : 0.f;
      v4u pf[4];
#pragma unroll
      for (int ks = 0; ks < 4; ++ks) pf[ks] = lds128(pra + ks * 1536);
      __builtin_amdgcn_sched_barrier(0);
      if (rho) { acc0 *= alp; acc1 *= alp; }
      unsigned s0_ = 65536 + w * 8192 + vc0, s1_ = 65536 + w * 8192 + vc1;
      if (rho == 0) { asm volatile("" : "+v"(s0_), "+v"(s1_)); vmwait<0>(); }
#pragma unroll
      for (int ks = 0; ks < 8; ++ks) {
        acc0 = mfmaL(c0[ks], pf[ks & 3], acc0); acc1 = mfmaL(c1[ks], pf[ks & 3], acc1);
        if (ks < 4) pf[ks] = lds128(pra + 6144 + ks * 1536);
        if (rho == 0) sto128(ks * 1024 + (((ks >> 2) ? s1_ : s0_) ^ ((ks & 3) << 6)), S3[ks]);
        __builtin_amdgcn_sched_barrier(0);
      }
    }
    {
    const int lane = (int)opq((unsigned)lane_), h2 = lane >> 5, tid = wave * 64 + lane;
    {
      const unsigned tb = B_T + w * 4096, n32 = lane & 31;
#pragma unroll
      for (int q4 = 0; q4 < 4; ++q4) {
        sto64(tb + n32 * 128 + ((q4 ^ (n32 & 7)) << 4) + h2 * 8, (v2u){pk(acc0[4 * q4], acc0[4 * q4 + 1]), pk(acc0[4 * q4 + 2], acc0[4 * q4 + 3])});
        sto64(tb + n32 * 128 + (((4 + q4) ^ (n32 & 7)) << 4) + h2 * 8, (v2u){pk(acc1[4 * q4], acc1[4 * q4 + 1]), pk(acc1[4 * q4 + 2], acc1[4 * q4 + 3])});
      }
      lgk0();
      const int hr = lane >> 3, ck = lane & 7;
#pragma unroll
      for (int i = 0; i < 4; ++i)
        gst128(rs, WS_PB + (((8 * i + hr) * 4 + (w >> 1)) * 64 + pid) * 256 + (w & 1) * 128 + ck * 16, lds128(tb + (8 * i + hr) * 128 + ((ck ^ hr) << 4)));
    }
    vmwait<0>();
    bar();
    if (w == 0 && lane < 32) { const v2u fm = *(const LAS v2u*)(B_FM + lane * 8); gst128(rs, WS_PBM + pid * 512 + lane * 16, (v4u){fm.x, tag, fm.y, tag}); }
    if (w == 0) {
      const unsigned off = WS_F + bid * 16384 + ((lane >> 3) * 16 + (bid & 15)) * 128;
      _Pragma("nounroll") for (int it = 0; it < SPIN_CAP; ++it) { cfence(); if (__popcll(__ballot(gld64(rs, off).y == tag)) >= PREN) break; __builtin_amdgcn_s_sleep(8); }
    }
    bar();
    v4u f[3];
    sweep<3, 0>(f, rs, tag, [&](int k) { return WS_F + ((k >> 1) * 64 + bid) * 16384 + (k < 2 ? k * 8192 + tid * 16 : (tid & 255) * 16); });
    float r[4] = {asf(f[0].x) + asf(f[1].x), asf(f[0].z) + asf(f[1].z), w < 4 ? asf(f[2].x) : 0.f, w < 4 ? asf(f[2].z) : 0.f};
#pragma unroll
    for (int k = 0; k < 4; ++k) { r[k] += dpp<ROR8>(r[k]); r[k] = x32sum(x16sum(r[k])); }
    if (lane < 8) { sto64(B_FIN + w * 128 + lane * 8, (v2u){asu(r[0]), asu(r[1])}); sto64(B_FIN + w * 128 + 64 + lane * 8, (v2u){asu(r[2]), asu(r[3])}); }
    bar();
    if (tid < 16) {
      float s0 = 0.f, s1 = 0.f;
#pragma unroll
      for (int i = 0; i < 8; ++i) { const f2 t = *(const LAS f2*)(B_FIN + i * 128 + tid * 8); s0 += t.x; s1 += t.y; }
      const int mat = tid >> 3, ob = (tid >> 2) & 1, pr = tid & 3;
      *(GAS unsigned*)((GAS char*)out + (ob * 1024 + mat * 512 + 8 * bid + 2 * pr) * 2) = pk(s0, s1);
      if (bid == 0 && tid == 0) gst32(rs, WS_E, tag);
    }
    }
  } else {
    const int h = bid - NB_CU, w = wave;
    GET_TAG();
    if (w == 0) gather_qb(T_QBI, h, lane, rs, tag);
    vmwait<0>();
    v4u OU[16];
    const char* ow = Bwo + (size_t)(h * 128 + 16 * w) * 1024;
    const unsigned vo = g * 4096 + c16 * 16;
#define LD_OU(u) asm volatile("global_load_dwordx4 %0, %1, %2 offset:%3 nt" : "=v"(OU[u]) : "v"(vo), "s"(ow), "n"(((u) >> 2) * 1024 + ((u) & 3) * 256) : "memory")
#pragma unroll
    for (int u = 0; u < TE; ++u) LD_OU(u);
    {
      const unsigned a0 = w * 8192 + c16 * 256 + ((g ^ c16) << 4);
      v4u uf[8];
#pragma unroll
      for (int i = 0; i < 8; ++i) uf[i] = lds128((a0 ^ ((i & 3) << 6)) + (i >> 2) * 4096);
      bar();
      v4u qb[4];
#pragma unroll
      for (int s = 0; s < 4; ++s) qb[s] = lds128(T_QBI + (c16 & 1) * 256 + s * 64 + g * 16);
      __builtin_amdgcn_sched_barrier(0);
      f4 d0 = {0.f, 0.f, 0.f, 0.f}, d1 = d0;
#pragma unroll
      for (int s = 0; s < 4; ++s) { d0 = mfma32(uf[s], qb[s], d0); d1 = mfma32(uf[4 + s], qb[s], d1); }
      const int rb = (c16 >> 1) & 1;
      const f4 e = rb ? d1 : d0;
      if (c16 < 4) sto64(T_QAS + (c16 & 1) * 512 + (32 * w + 16 * rb + 4 * g) * 2, (v2u){pk(e[0], e[1]), pk(e[2], e[3])});
      bar();
      if (w == 1) {
        gst128(rs, WS_QAB + ((lane >> 5) * 32 + h) * 1024 + (lane & 31) * 16, lds128(T_QAS + lane * 16));
        vmwait<0>();
        flag_line(rs, WS_FQ + h * FS, lane, tag);
      }
    }
    bar();
    {
      const char* uv = Wuv + (size_t)(64 * w) * 8192 + h * 256;
      const unsigned vuv = g * 8192 + ((c16 ^ (g << 1)) << 4);
#pragma unroll
      for (int u = 0; u < 16; ++u) { if (u >= WT && !(u & 1)) vmwait<WT - 2>(); dma<0>(uv + (size_t)u * 32768, vuv ^ (((u >> 1) & 1) << 7), w * 16384 + u * 1024); }
#pragma unroll
      for (int u = TE; u < 16; ++u) { if (!(u & 1)) vmwait<WT - 2>(); LD_OU(u); }
    }
    asm volatile("s_waitcnt vmcnt(0)" : "+v"(OU[0]), "+v"(OU[1]), "+v"(OU[2]), "+v"(OU[3]), "+v"(OU[4]), "+v"(OU[5]), "+v"(OU[6]), "+v"(OU[7]),
                 "+v"(OU[8]), "+v"(OU[9]), "+v"(OU[10]), "+v"(OU[11]), "+v"(OU[12]), "+v"(OU[13]), "+v"(OU[14]), "+v"(OU[15]) :: "memory");
    if (tid < 4) sto32(T_RDY + tid * 4, 0u);
    bar();
    v4u wf[16];
    {
      const int q = c16 >> 2, p = lane & 3, f3 = q | ((g & 1) << 2);
      const unsigned a0 = (8 * g + q) * 256 + (((2 * w + (p >> 1)) ^ (f3 << 1)) << 4) + (p & 1) * 8;
      const unsigned aa[2] = {opq(a0), opq(a0 + 65536)};
#pragma unroll
      for (int s = 0; s < 16; ++s) wf[s] = trd2(aa[s >> 3] + (s & 7) * 8192, aa[s >> 3] + (s & 7) * 8192 + 1024);
    }
    {
      const int tb = w >> 2;
      v4u o[8];
#pragma unroll
      for (int i = 0; i < 8; ++i) { o[i] = (v4u){0u, 0u, 0u, 0u}; asm volatile("" : "+v"(o[i])); }
      const char* pb = ws + WS_PB + (h * 4 + (w & 3)) * 16384 + tb * 8192;
      const unsigned vl = lane << 4;
      unsigned done = 0;
      bool all = false;
      v4u ml = {0u, ~tag, 0u, ~tag};
      if (w == 0) {
        _Pragma("nounroll") for (int it = 0; it < SPIN_CAP; ++it) { cfence(); if (__all(gld32(rs, WS_PBM + 2 * h * 512 + h * 16 + 4) == tag)) break; __builtin_amdgcn_s_sleep(8); }
      }
      _Pragma("nounroll") for (int it = 0; it < SPIN_CAP * 8 && !all; ++it) {
        unsigned rdy;
        if (w == 0) {
          cfence();
          const v4u t = gld128(rs, tagok(ml, tag) ? 0xfffffff0u : WS_PBM + lane * 512 + h * 16);
          if (!tagok(ml, tag)) ml = t;
          unsigned long long m = __ballot(tagok(ml, tag));
          all = m == ~0ull;
          m &= m >> 2; m &= m >> 1; m &= 0x1111111111111111ull;
          rdy = (unsigned)m;
          if (lane == 0) *(volatile LAS v2u*)T_RDY = (v2u){rdy, (unsigned)(m >> 32)};
        } else {
          rdy = lds_peek(T_RDY + tb * 4);
          if (rdy == done) { __builtin_amdgcn_s_sleep(1); continue; }
          all = rdy == 0x11111111u;
        }
        const unsigned add = rdy & ~done;
        done = rdy;
#pragma unroll
        for (int i = 0; i < 8; ++i)
          if (add & (1u << (4 * i))) asm volatile("global_load_dwordx4 %0, %1, %2 offset:%3 sc1" : "+v"(o[i]) : "v"(vl), "s"(pb + (i >> 2) * 4096), "n"((i & 3) * 1024) : "memory");
      }
      if (w == 0) {
        const float M = x16max(row16max(asf(ml.x)));
        const float wt = ex2(asf(ml.x) - M), wn = wt / x16sum(row16sum(wt * asf(ml.z)));
        const unsigned hi = pk(wn, wn), wa = T_WN + (lane >> 5) * 128 + (lane & 3) * 32 + ((lane >> 2) & 7) * 4;
        const float lo = wn - asf(hi & 0xffff0000u);
        sto32(wa, hi); sto32(wa + 256, pk(lo, lo));
        lgk0();
        if (lane == 0) *(volatile LAS unsigned*)(T_RDY + 8) = 1u;
      } else {
        lds_wait_ge<0>(T_RDY + 8, 1u);
      }
      const unsigned wa = T_WN + (c16 >> 3) * 256 + tb * 128 + g * 32, half = (c16 & 1) ? 0xffff0000u : 0x0000ffffu;
      const v4u w0_ = lds128(wa), w1_ = lds128(wa + 16);
      const int rr = (c16 >> 1) & 3;
      v4u bo[8];
#pragma unroll
      for (int u = 0; u < 8; ++u) { const unsigned x = (u < 4 ? w0_[u & 3] : w1_[u & 3]) & half; bo[u] = (v4u){rr == 0 ? x : 0u, rr == 1 ? x : 0u, rr == 2 ? x : 0u, rr == 3 ? x : 0u}; }
      asm volatile("s_waitcnt vmcnt(0)" : "+v"(o[0]), "+v"(o[1]), "+v"(o[2]), "+v"(o[3]), "+v"(o[4]), "+v"(o[5]), "+v"(o[6]), "+v"(o[7]),
                   "+v"(bo[0]), "+v"(bo[1]), "+v"(bo[2]), "+v"(bo[3]), "+v"(bo[4]), "+v"(bo[5]), "+v"(bo[6]), "+v"(bo[7]) :: "memory");
      f4 d = {0.f, 0.f, 0.f, 0.f};
#pragma unroll
      for (int u = 0; u < 8; ++u) d = mfma32(o[u], bo[u], d);
#pragma unroll
      for (int r = 0; r < 4; ++r) {
        const float x = d[r] + dpp<ROR8>(d[r]);
        const unsigned pr = pk(x, dpp<XOR1>(x));
        if ((c16 & 9) == 0) sto32(T_OL + tb * 1088 + ((w & 3) * 128 + 32 * g + 8 * r + c16) * 2, pr);
      }
    }
    bar();
    {
      v4u of[16];
      const unsigned oa = opq(T_OL + (c16 >> 3) * 1088 + g * 16);
#pragma unroll
      for (int s = 0; s < 16; ++s) of[s] = lds128(oa + s * 64);
      __builtin_amdgcn_sched_barrier(0);
      f4 d0 = {0.f, 0.f, 0.f, 0.f}, d1 = d0;
#pragma unroll
      for (int s = 0; s < 8; ++s) { d0 = mfma32(wf[2 * s], of[2 * s], d0); d1 = mfma32(wf[2 * s + 1], of[2 * s + 1], d1); }
      d0 += d1;
      const unsigned half = (c16 & 1) ? 0xffff0000u : 0x0000ffffu;
      const int rr = (c16 >> 1) & 3;
      f4 o4[4];
#pragma unroll
      for (int cb = 0; cb < 4; ++cb) o4[cb] = (f4){0.f, 0.f, 0.f, 0.f};
#pragma unroll
      for (int u = 0; u < 4; ++u) {
        const unsigned x = pk(d0[u], d0[u]) & half;
        const v4u bo = {rr == 0 ? x : 0u, rr == 1 ? x : 0u, rr == 2 ? x : 0u, rr == 3 ? x : 0u};
#pragma unroll
        for (int cb = 0; cb < 4; ++cb) o4[cb] = mfma32(OU[4 * u + cb], bo, o4[cb]);
      }
#pragma unroll
      for (int cb = 0; cb < 4; ++cb)
#pragma unroll
        for (int r = 0; r < 4; ++r) stof(T_RED + w * 4096 + cb * 1024 + r * 64 + g * 256 + c16 * 4, o4[cb][r]);
    }
    bar();
    {
      const int j = tid >> 3, pr = tid & 7;
      float s0 = 0.f, s1 = 0.f;
#pragma unroll
      for (int i = 0; i < 8; ++i) { const f2 t = *(const LAS f2*)(T_RED + i * 4096 + tid * 8); s0 += t.x; s1 += t.y; }
      gst128(rs, WS_F + (((64 + j) * 128 + h) * 16 + 2 * pr) * 8, (v4u){asu(s0), tag, asu(s1), tag});
    }
  }
}

extern "C" void kernel_launch(void* const* d_in, const int* in_sizes, int n_in, void* d_out, int out_size, void* d_ws, size_t ws_size,
                              hipStream_t stream) {
  (void)in_sizes; (void)n_in; (void)out_size; (void)ws_size;
  (void)hipFuncSetAttribute((const void*)fused_decode, hipFuncAttributeMaxDynamicSharedMemorySize, LDS_BYTES);
  fused_decode<<<256, 512, LDS_BYTES, stream>>>(
      (const char*)d_in[0], (const char*)d_in[1], (const char*)d_in[2], (const char*)d_in[3], (const char*)d_in[4], (const char*)d_in[5],
      (const char*)d_in[6], (const char*)d_in[7], (const char*)d_in[8], (const char*)d_in[9], (const char*)d_in[10], (unsigned short*)d_out,
      (char*)d_ws);
}
```

```cpp
#define DBG_TS
#define PROBE 0
#define PROLE 0
#define PSTAT 0
#define PZERO 1
#include <hip/hip_runtime.h>
#include <stdint.h>
#pragma clang diagnostic ignored "-Wint-to-pointer-cast"

#define GAS __attribute__((address_space(1)))
#define LAS __attribute__((address_space(3)))
typedef unsigned v4u __attribute__((ext_vector_type(4)));
typedef unsigned v2u __attribute__((ext_vector_type(2)));
typedef float f4 __attribute__((ext_vector_type(4)));
typedef float f2 __attribute__((ext_vector_type(2)));
typedef float f16v __attribute__((ext_vector_type(16)));
typedef short s4 __attribute__((ext_vector_type(4)));
typedef __bf16 b8 __attribute__((ext_vector_type(8)));
typedef __bf16 b2 __attribute__((ext_vector_type(2)));

namespace {

constexpr int NB_CU = 64;
constexpr int NT_CU = 32;
constexpr int NSPLIT_A = 10;
#define NB0 55
#define NB1 52
constexpr int NB2 = (512 - 4 * NB0 - 4 * NB1) / 2;
constexpr int NSLOT = 18, NLOW = 14, NCONS = 6;
#define W1 12
#define W2 24
#define J1 18
#define WB 4
#define WT 6
#define DLYP 30
#define DLYB 10
#define NS3 6
#define TE 6
#define QGO 4
#define ODL 5
#define PREN 32
#define ROT 13
#define BROT 3
constexpr int CODE_KB = 24;
constexpr float SC = 0.08838834764831845f * 1.4426950408889634f;
constexpr int SPIN_CAP = 1 << 18;

constexpr unsigned FS = 128;
constexpr unsigned WS_E = 0;
constexpr unsigned WS_FQ = 4096;
constexpr unsigned WS_QG = 65536;
constexpr unsigned WS_QAB = WS_QG + 262144;
constexpr unsigned WS_PBM = WS_QAB + 65536;
constexpr unsigned WS_PAM = WS_PBM + 32768;
constexpr unsigned WS_PAO = WS_PAM + 131072;
constexpr unsigned WS_PB = WS_PAO + 786432;
constexpr unsigned WS_F = WS_PB + 2097152;
constexpr unsigned WS_END = WS_F + 2097152;
constexpr unsigned WS_TS = 8u << 20;

constexpr unsigned LDS_BYTES = 163840;
constexpr unsigned SCR = 131072;
constexpr unsigned A_MISC = NLOW * 8192;
constexpr unsigned A_LANDK = A_MISC, A_LANDV = A_MISC + 4, A_QUSED = A_MISC + 8, A_QCNT = A_MISC + 12, A_QB = A_MISC + 16, A_QRDY = A_MISC + 20, A_OUL = A_MISC + 24, A_OUL2 = A_MISC + 28;
constexpr unsigned A_DONE = A_MISC + 64;
constexpr unsigned A_QU2 = 10 * 8192;
constexpr unsigned A_QIMG = A_MISC + 256;
constexpr unsigned A_ML = A_MISC + 1280;
constexpr unsigned A_OIMG = A_MISC + 1792;
constexpr unsigned A_ST = A_MISC + 2048;
constexpr unsigned A_XIMG = A_MISC + 4096, A_DUMMY = A_MISC + 5120, A_QBI = A_MISC + 6144, A_QAS = A_MISC + 6656;
constexpr unsigned SLOT1 = 65536;
constexpr unsigned B_MX = SCR, B_AL = SCR + 512, B_LS = SCR + 640, B_P = SCR + 2048;
constexpr unsigned B_FM = SCR + 1280, B_FIN = SCR + 16384, B_T = SLOT1, B_DUMMY = SCR + 8192;
constexpr unsigned T_QBI = SCR, T_QAS = SCR + 1024, T_OL = SCR + 4096, T_RDY = SCR + 6400, T_WN = SCR + 6656, T_RED = 0;

__device__ __forceinline__ unsigned slot_addr(unsigned s) { return s * 8192 + (s >= NLOW ? 16384u : 0u); }
__device__ __forceinline__ v4u lds128(unsigned a) { return *(const LAS v4u*)a; }
__device__ __forceinline__ void sto128(unsigned a, v4u v) { *(LAS v4u*)a = v; }
__device__ __forceinline__ void sto64(unsigned a, v2u v) { *(LAS v2u*)a = v; }
__device__ __forceinline__ void sto32(unsigned a, unsigned v) { *(LAS unsigned*)a = v; }
__device__ __forceinline__ float ldsf(unsigned a) { return *(const LAS float*)a; }
__device__ __forceinline__ void stof(unsigned a, float v) { *(LAS float*)a = v; }
__device__ __forceinline__ v2u trd(unsigned a) { return __builtin_bit_cast(v2u, __builtin_amdgcn_ds_read_tr16_b64_v4i16((LAS s4*)a)); }
__device__ __forceinline__ v4u trd2(unsigned a0, unsigned a1) { const v2u x = trd(a0), y = trd(a1); return (v4u){x.x, x.y, y.x, y.y}; }
__device__ __forceinline__ f4 mfma32(v4u a, v4u b, f4 c) {
  return __builtin_amdgcn_mfma_f32_16x16x32_bf16(__builtin_bit_cast(b8, a), __builtin_bit_cast(b8, b), c, 0, 0, 0); }
__device__ __forceinline__ f4 mfma16(v2u a, v2u b, f4 c) {
  return __builtin_amdgcn_mfma_f32_16x16x16bf16_1k(__builtin_bit_cast(s4, a), __builtin_bit_cast(s4, b), c, 0, 0, 0); }
__device__ __forceinline__ f16v mfmaL(v4u a, v4u b, f16v c) {
  return __builtin_amdgcn_mfma_f32_32x32x16_bf16(__builtin_bit_cast(b8, a), __builtin_bit_cast(b8, b), c, 0, 0, 0); }
__device__ __forceinline__ unsigned pk(float lo, float hi) { return __builtin_bit_cast(unsigned, __builtin_convertvector((f2){lo, hi}, b2)); }
__device__ __forceinline__ float asf(unsigned u) { return __builtin_bit_cast(float, u); }
__device__ __forceinline__ unsigned asu(float f) { return __builtin_bit_cast(unsigned, f); }
__device__ __forceinline__ float ex2(float x) { return __builtin_amdgcn_exp2f(x); }
template <int CTRL> __device__ __forceinline__ float dpp(float x) {
  return __builtin_bit_cast(float, __builtin_amdgcn_mov_dpp(__builtin_bit_cast(int, x), CTRL, 0xf, 0xf, true)); }
constexpr int XOR1 = 0xB1, XOR2 = 0x4E, ROR4 = 0x124, ROR8 = 0x128;
__device__ __forceinline__ float x16max(float x) { auto s = __builtin_amdgcn_permlane16_swap(asu(x), asu(x), false, false); return fmaxf(asf(s[0]), asf(s[1])); }
__device__ __forceinline__ float x32max(float x) { auto s = __builtin_amdgcn_permlane32_swap(asu(x), asu(x), false, false); return fmaxf(asf(s[0]), asf(s[1])); }
__device__ __forceinline__ float x16sum(float x) { auto s = __builtin_amdgcn_permlane16_swap(asu(x), asu(x), false, false); return asf(s[0]) + asf(s[1]); }
__device__ __forceinline__ float x32sum(float x) { auto s = __builtin_amdgcn_permlane32_swap(asu(x), asu(x), false, false); return asf(s[0]) + asf(s[1]); }
__device__ __forceinline__ float row16max(float x) { x = fmaxf(x, dpp<XOR1>(x)); x = fmaxf(x, dpp<XOR2>(x)); x = fmaxf(x, dpp<ROR4>(x)); return fmaxf(x, dpp<ROR8>(x)); }
__device__ __forceinline__ float row16sum(float x) { x += dpp<XOR1>(x); x += dpp<XOR2>(x); x += dpp<ROR4>(x); return x + dpp<ROR8>(x); }

template <int IMM> __device__ __forceinline__ void dma(const void* sbase, unsigned voff, unsigned lds) {
  asm volatile("s_mov_b32 m0, %2\n\ts_nop 0\n\tglobal_load_lds_dwordx4 %0, %1 offset:%3 nt" :: "v"(voff), "s"(sbase), "s"(lds), "n"(IMM) : "memory");
}
template <int W> __device__ __forceinline__ void dma4(const void* sbase, unsigned v0, unsigned v1, unsigned v2, unsigned v3, unsigned lds) {
  asm volatile("s_mov_b32 m0, %5\n\ts_waitcnt vmcnt(%6)\n\tglobal_load_lds_dwordx4 %0, %4 nt\n\tglobal_load_lds_dwordx4 %1, %4 offset:1024 nt\n\t"
               "s_waitcnt vmcnt(%6)\n\tglobal_load_lds_dwordx4 %2, %4 offset:2048 nt\n\tglobal_load_lds_dwordx4 %3, %4 offset:3072 nt"
               :: "v"(v0), "v"(v1), "v"(v2), "v"(v3), "s"(sbase), "s"(lds), "n"(W - 2) : "memory");
}
__device__ __forceinline__ void dma_keep(const void* sbase, unsigned voff, unsigned lds) {
  asm volatile("s_mov_b32 m0, %2\n\ts_nop 0\n\tglobal_load_lds_dwordx4 %0, %1" :: "v"(voff), "s"(sbase), "s"(lds) : "memory");
}
template <int N> __device__ __forceinline__ void vmwait() { asm volatile("s_waitcnt vmcnt(%0)" :: "n"(N) : "memory"); }
__device__ __forceinline__ void bar() { asm volatile("s_waitcnt lgkmcnt(0)\n\ts_barrier" ::: "memory"); }
__device__ __forceinline__ void lgk0() { asm volatile("s_waitcnt lgkmcnt(0)" ::: "memory"); }
__device__ __forceinline__ void cfence() { asm volatile("" ::: "memory"); }
__device__ __forceinline__ unsigned opq(unsigned x) { asm volatile("" : "+v"(x)); return x; }
__device__ __forceinline__ unsigned lds_peek(unsigned a) { return __builtin_amdgcn_readfirstlane(*(volatile LAS unsigned*)a); }
template <int SL = 1> __device__ __forceinline__ void lds_wait_ge(unsigned a, unsigned want) {
  _Pragma("nounroll") for (int it = 0; it < SPIN_CAP * 8; ++it) { if (lds_peek(a) >= want) break; if (SL) __builtin_amdgcn_s_sleep(SL); }
  cfence();
}
typedef __amdgpu_buffer_rsrc_t rsrc_t;
constexpr int SC1 = 16;
__device__ __forceinline__ v4u gld128(rsrc_t r, unsigned off) { return __builtin_amdgcn_raw_buffer_load_b128(r, off, 0, SC1); }
__device__ __forceinline__ v2u gld64(rsrc_t r, unsigned off) { return __builtin_amdgcn_raw_buffer_load_b64(r, off, 0, SC1); }
__device__ __forceinline__ unsigned gld32(rsrc_t r, unsigned off) { return __builtin_amdgcn_raw_buffer_load_b32(r, off, 0, SC1); }
__device__ __forceinline__ void gst128(rsrc_t r, unsigned off, v4u v) { __builtin_amdgcn_raw_buffer_store_b128(v, r, off, 0, SC1); }
__device__ __forceinline__ void gst32(rsrc_t r, unsigned off, unsigned v) { __builtin_amdgcn_raw_buffer_store_b32(v, r, off, 0, SC1); }
__device__ __forceinline__ void flag_line(rsrc_t r, unsigned off, int lane, unsigned tag) { if (lane < 8) gst128(r, off + lane * 16, (v4u){tag, tag, tag, tag}); }
__device__ __forceinline__ bool tagok(v4u v, unsigned tag) { return v.y == tag && v.w == tag; }
template <int N, int GAP, class F> __device__ __forceinline__ void sweep2(v4u (&v)[N], rsrc_t rs, unsigned tag, F off) {
  v4u a[N], b[N];
#pragma unroll
  for (int k = 0; k < N; ++k) a[k] = gld128(rs, off(k));
  __builtin_amdgcn_s_sleep(GAP);
  _Pragma("nounroll") for (int it = 0; it < SPIN_CAP; ++it) {
    cfence();
#pragma unroll
    for (int k = 0; k < N; ++k) b[k] = gld128(rs, off(k));
    cfence();
    bool ok = true;
#pragma unroll
    for (int k = 0; k < N; ++k) { ok &= tagok(a[k], tag); v[k] = a[k]; }
    if (__all(ok)) break;
#pragma unroll
    for (int k = 0; k < N; ++k) a[k] = gld128(rs, off(k));
    cfence();
    ok = true;
#pragma unroll
    for (int k = 0; k < N; ++k) { ok &= tagok(b[k], tag); v[k] = b[k]; }
    if (__all(ok)) break;
  }
}
template <int N, int SL, class F> __device__ __forceinline__ void sweep(v4u (&v)[N], rsrc_t rs, unsigned tag, F off) {
  _Pragma("nounroll") for (int it = 0; it < SPIN_CAP; ++it) {
    cfence();
    bool ok = true;
#pragma unroll
    for (int k = 0; k < N; ++k) { v[k] = gld128(rs, off(k)); ok &= tagok(v[k], tag); }
    if (__all(ok)) break;
    if (SL) __builtin_amdgcn_s_sleep(SL);
  }
}

#define STAMP(i) do { unsigned long long t_; asm volatile("s_memrealtime %0\n\ts_waitcnt lgkmcnt(0)" : "=s"(t_) :: "memory"); tsv[i] = (unsigned)t_; } while (0)
#define STAMP_DECL unsigned tsv[16] = {0,0,0,0,0,0,0,0,0,0,0,0,0,0,0,0}
#define STAMP_OUT() do { if (lane == 0) gst128(rs2, WS_TS + (blockIdx.x * 8 + wave) * 16, (v4u){tsv[0], tag, tsv[PROBE], tag}); \
  if (blockIdx.x == 0 && wave == 0) { unsigned mn = 0xffffffffu; float mx = 0.f, sm = 0.f, cnt = 0.f; unsigned ref = tsv[0] - 1000u; \
    for (int i_ = 0; i_ < 32; ++i_) { v4u v_; for (int it = 0; it < SPIN_CAP; ++it) { cfence(); v_ = gld128(rs2, WS_TS + (i_ * 64 + lane) * 16); if (__all(tagok(v_, tag))) break; } \
      mn = min(mn, v_.x - ref); const bool role_ = PROLE == 3 || (PROLE == 1 ? (i_ < 8) : PROLE == 2 ? (i_ >= 8 && i_ < 12) : (i_ >= 12)); if (v_.z != 0u && role_) { const float d_ = (float)(v_.z - ref); mx = fmaxf(mx, d_); sm += d_; cnt += 1.f; } } \
    float mnf = -(float)mn; mnf = -x32max(x16max(row16max(mnf))); mx = x32max(x16max(row16max(mx))); sm = x32sum(x16sum(row16sum(sm))); cnt = x32sum(x16sum(row16sum(cnt))); \
    const float X = PZERO ? 0.f : (PSTAT ? sm / cnt : mx) - mnf; const unsigned until = ref + (unsigned)(mnf + 6000.f + 8.f * X); \
    for (int it = 0; it < 100000000; ++it) { unsigned long long t_; asm volatile("s_memrealtime %0\n\ts_waitcnt lgkmcnt(0)" : "=s"(t_) :: "memory"); if ((int)((unsigned)t_ - __builtin_amdgcn_readfirstlane(until)) >= 0) break; } } } while (0)

__device__ __forceinline__ void gather_qb(unsigned img, int h, int lane, rsrc_t rs, unsigned tag) {
  v4u qg[4];
  const unsigned off = WS_QG + (h * 128 + 2 * lane) * 8;
  sweep<4, 1>(qg, rs, tag, [&](int k) { return off + (k & 1) * 65536 + (k >> 1) * 32768; });
  sto32(img + lane * 4, pk(asf(qg[0].x) + asf(qg[1].x), asf(qg[0].z) + asf(qg[1].z)));
  sto32(img + 256 + lane * 4, pk(asf(qg[2].x) + asf(qg[3].x), asf(qg[2].z) + asf(qg[3].z)));
}
__device__ __forceinline__ void lds_inc(unsigned a) { __hip_atomic_fetch_add((LAS unsigned*)a, 1u, __ATOMIC_RELAXED, __HIP_MEMORY_SCOPE_WORKGROUP); }

__device__ __forceinline__ void out_frags(unsigned gb, int w, int lane, v4u (&wf)[4]) {
  const int g = lane >> 4, q = (lane & 15) >> 2, p = lane & 3, f3 = q | ((g & 1) << 2);
#pragma unroll
  for (int tt = 0; tt < 4; ++tt) {
    const unsigned a0 = gb + q * 1024 + (((2 * (4 * w + tt) + (p >> 1)) ^ (f3 << 1)) << 4) + (p & 1) * 8;
    wf[tt] = trd2(a0, a0 + 4096);
  }
}

}

__global__ void __launch_bounds__(512, 2) __attribute__((aligned(4096))) fused_decode(
    const char* __restrict__ Ax, const char* __restrict__ Kc, const char* __restrict__ Vc, const char* __restrict__ Wq, const char* __restrict__ Wo,
    const char* __restrict__ Bx, const char* __restrict__ Ckv, const char* __restrict__ Bwq, const char* __restrict__ Wuk, const char* __restrict__ Wuv,
    const char* __restrict__ Bwo, unsigned short* __restrict__ out, char* __restrict__ ws) {
  const int bid = blockIdx.x, tid = threadIdx.x, lane = tid & 63, wave = __builtin_amdgcn_readfirstlane(tid >> 6);
  const int g = lane >> 4, c16 = lane & 15;
  STAMP_DECL;
  STAMP(0);
  const int iA = bid - (NB_CU + NT_CU);
  const bool isA = iA >= 0, isB = bid < NB_CU;
  const int ty = iA >> 6;
  int dA = 95 - bid, dU = 223 - bid, dB = bid - 64, dQ = 159 - bid, dW = 2 - wave;
  asm("" : "+s"(dA), "+s"(dU), "+s"(dB), "+s"(dQ), "+s"(dW));
  const int mA = dA >> 31, mU = dU >> 31, mB = dB >> 31, mP = mA & ~mU, sh = mP & 1;
  const int hh = (bid >> sh) & 31;
  const unsigned row0 = (mA & ((((bid | mU) & 1) << 8) + 64 * wave - 64)) | (~mA & (32 * wave));
  const unsigned l0 = (mA & (SCR - 16384 + wave * 16384 - ((dW >> 31) & (SCR + 32768 - A_QU2)))) | (~mA & (wave * 8192));
  const unsigned karg = 0x40 - (mP & (8 + ((dQ >> 31) & 32))), more = mA;
  if ((unsigned)(wave + mA + (mB & 8)) < (unsigned)(8 + (mA & -4))) {
    const unsigned b0 = (row0 + g) * 8192 + hh * 256 + ((c16 ^ (g << sh)) << 4), x1 = ~mP & 64;
    unsigned v0 = b0, v1 = (b0 ^ x1) + 32768, v2 = (b0 ^ 128) + 65536, v3 = (b0 ^ x1 ^ 128) + 98304;
    if (DLYP && karg == 0x18) __builtin_amdgcn_s_sleep(DLYP);
    unsigned long long wp;
#define D4 "global_load_lds_dwordx4 %1, %0 nt\n\ts_add_u32 m0, m0, 0x400\n\tv_add_u32 %1, 0x20000, %1\n\t" \
           "global_load_lds_dwordx4 %2, %0 nt\n\ts_add_u32 m0, m0, 0x400\n\tv_add_u32 %2, 0x20000, %2\n\t" \
           "global_load_lds_dwordx4 %3, %0 nt\n\ts_add_u32 m0, m0, 0x400\n\tv_add_u32 %3, 0x20000, %3\n\t" \
           "global_load_lds_dwordx4 %4, %0 nt\n\ts_add_u32 m0, m0, 0x400\n\tv_add_u32 %4, 0x20000, %4\n\t"
    asm volatile("s_load_dwordx2 %0, %5, %6\n\ts_mov_b32 m0, %7\n\ts_waitcnt lgkmcnt(0)\n\t" D4 D4
                 "s_cmp_eq_u32 %8, 0\n\ts_cbranch_scc1 1f\n\t" D4 D4 "1:"
                 : "=&s"(wp), "+v"(v0), "+v"(v1), "+v"(v2), "+v"(v3)
                 : "s"(__builtin_amdgcn_kernarg_segment_ptr()), "s"(karg), "s"(l0), "s"(more) : "memory", "scc");
#undef D4
  }
  {
    unsigned long long pc;
    asm volatile("s_getpc_b64 %0" : "=s"(pc));
    const int kb = ((bid >> 3) + 4 * wave) & 31;
    dma_keep((const char*)((pc & ~1023ull) + (unsigned)((kb < CODE_KB ? kb : CODE_KB - 1) * 1024)), lane * 16, isA ? A_DUMMY : B_DUMMY);
  }
  const rsrc_t rs = __builtin_amdgcn_make_buffer_rsrc(ws, 0, WS_END, 0x00020000);
  const rsrc_t rs2 = __builtin_amdgcn_make_buffer_rsrc(ws, 0, 16u << 20, 0x00020000);
  asm volatile("" :: "s"(Ax), "s"(Kc), "s"(Vc), "s"(Wq), "s"(Wo), "s"(Bx), "s"(Ckv), "s"(Bwq), "s"(Wuk), "s"(Wuv), "s"(Bwo), "s"(out), "s"(ws));
  unsigned tag;
#define GET_TAG() do { asm volatile("s_load_dword %0, %1, 0x0\n\ts_waitcnt lgkmcnt(0)" : "=s"(tag) : "s"(ws) : "memory"); tag += 1u; } while (0)
  const int pid = ((bid & 7) << 3) | ((bid >> 3) & 7);
  const char* cp = Ckv + ((size_t)pid * 256 + 8 * wave) * 1024;
  const int brot = BROT ? bid >> BROT : 0;
  const unsigned vc0 = (lane ^ ((wave & 1) ? 3 : 0)) << 4, vc1 = (lane ^ ((wave & 1) ? 1 : 2)) << 4;
#define LD_C(rho, u) dma<((u) & 3) * 1024>(cp + (size_t)((((rho) + brot) & 3) * 65536) + ((u) >> 2) * 4096, (((u) >> 2) ? vc1 : vc0) ^ (((u) & 3) << 6), ((rho) & 1) * 65536 + wave * 8192 + ((u) >> 2) * 4096)
  if (isA) {
    if (wave == 1 && ty < 2) dma<0>((ty ? Ax : Bx) + 512 * (bid & 1), (lane >> 5) * 1024 + (lane & 31) * 16, A_XIMG);
    if (tid < 64) sto32(A_MISC + tid * 4, 0u);
    bar();
  } else if (isB) {
    __builtin_amdgcn_s_sleep(DLYB);
    LD_C(0, 0); LD_C(0, 1); LD_C(0, 2); LD_C(0, 3); LD_C(0, 4); LD_C(0, 5);
  }

  if (isA) {
    const int split = iA >> 4, pair = (iA + split) & 15, b = pair >> 3, kvh = pair & 7;
    const int nblk = ty == 0 ? NB0 : ty == 1 ? NB1 : NB2, blk0 = (ty == 0 ? 0 : ty == 1 ? 4 * NB0 : 4 * (NB0 + NB1)) + (split & 3) * nblk;
    if (wave == 0 || wave == 7) {
      __builtin_amdgcn_s_setprio(3);
      static_assert(NSPLIT_A == 10 && W1 >= 4 && W1 % 4 == 0 && W2 % 4 == 0 && W1 <= W2 && W2 <= 24 && J1 <= NB0 && J1 <= NB1 && J1 <= NB2, "window");
      const bool isv = wave == 7;
      int pub = 0, j = 0;
      unsigned sl = 0;
      int pos = (pair * ROT) % nblk;
      const char* gp = (isv ? Vc : Kc) + ((size_t)pair * 8192 + (blk0 + pos) * 16) * 256;
      const unsigned v0 = opq(g * 256 + ((c16 ^ (isv ? g << 1 : g)) << 4));
      const unsigned v1 = opq(v0 ^ (isv ? 128 : 64)), v2 = opq(v0 ^ (isv ? 0 : 128)), v3 = opq(v0 ^ (isv ? 128 : 192));
      const unsigned lo = isv ? 4096 : 0, la = opq(lane ? A_MISC + 1536 + lane * 4 : isv ? A_LANDV : A_LANDK);
      unsigned dn = 0;
#define PUBLISH(val) do { pub = max(pub, (val)); *(volatile LAS unsigned*)la = (unsigned)pub; } while (0)
#define GATE(addr, need) do { if (lds_peek(addr) < (need)) { vmwait<0>(); PUBLISH(j); lds_wait_ge(addr, need); } } while (0)
#define PART(W) do { \
        if (j >= NSLOT) { if ((unsigned)__builtin_amdgcn_readfirstlane(dn) < (unsigned)(j - NSLOT + 1)) GATE(A_DONE + sl, (unsigned)(j - NSLOT + 1)); } \
        else if (j == 10) GATE(A_QUSED, 4u); \
        dma4<W>(gp, v0, v1, v2, v3, slot_addr(sl >> 2) + lo); \
        ++j; gp += 4096; if (++pos == nblk) gp -= nblk * 4096; sl = sl == 4 * (NSLOT - 1) ? 0 : sl + 4; \
        PUBLISH(j - W / 4); dn = *(volatile LAS unsigned*)(A_DONE + sl); } while (0)
      STAMP(9);
      GET_TAG();
      lds_wait_ge(A_QCNT, 4u);
      STAMP(10);
#pragma nounroll
      while (j < J1) PART(W1);
      STAMP(11);
#pragma nounroll
      while (j < nblk) PART(W2);
      vmwait<20>(); PUBLISH(j - 5); vmwait<16>(); PUBLISH(j - 4); vmwait<12>(); PUBLISH(j - 3);
      vmwait<8>(); PUBLISH(j - 2); vmwait<4>(); PUBLISH(j - 1); vmwait<0>(); PUBLISH(j);
      if (ty < 2 && !isv) {
        const char* op = Wo + (size_t)iA * 32768;
        const unsigned o0 = lane << 4, o1 = o0 ^ 32, o2 = o0 ^ 64, o3 = o0 ^ 96;
        lds_wait_ge(A_LANDV, (unsigned)(nblk - ODL));
#pragma unroll
        for (int k = 0; k < 8; ++k) {
          const unsigned x = ((k >> 1) & 1) << 7;
          if (!(k & 1)) lds_wait_ge(A_DONE + sl, (unsigned)(j + (k >> 1) - NSLOT + 1));
          dma4<62>(op + k * 4096, o0 ^ x, o1 ^ x, o2 ^ x, o3 ^ x, slot_addr(sl >> 2) + (k & 1) * 4096);
          if (k & 1) sl = sl == 4 * (NSLOT - 1) ? 0 : sl + 4;
        }
      }
      __builtin_amdgcn_s_setprio(0);
      STAMP(5);
#undef PUBLISH
#undef GATE
#undef PART
    } else {
      GET_TAG();
      vmwait<0>();
      if (wave <= 4) {
        const int w1 = wave - 1;
        if (ty < 2) {
          if (lane == 0) lds_inc(A_QCNT);
          const int q = c16 >> 2, p = lane & 3, f3 = q | ((g & 1) << 2);
          const unsigned a0 = (8 * g + q) * 256 + (((4 * w1 + (p >> 1)) ^ (f3 << 1)) << 4) + (p & 1) * 8;
          const unsigned aa[2] = {opq(SCR + a0), opq(A_QU2 + a0)};
          lds_wait_ge(A_QCNT, 4u);
          STAMP(1);
          v4u w0[8], w1f[8], xf[8];
#pragma unroll
          for (int s = 0; s < 8; ++s) {
            const unsigned a = aa[s >> 2] + (s & 3) * 8192;
            w0[s] = trd2(a, a + 1024); w1f[s] = trd2((a ^ 32), (a ^ 32) + 1024);
            xf[s] = lds128(A_XIMG + (c16 & 1) * 512 + g * 16 + s * 64);
          }
          __builtin_amdgcn_sched_barrier(0);
          f4 d0 = {0.f, 0.f, 0.f, 0.f}, d1 = d0;
#pragma unroll
          for (int s = 0; s < 8; ++s) { d0 = mfma32(w0[s], xf[s], d0); d1 = mfma32(w1f[s], xf[s], d1); }
          const int pr = (c16 >> 1) & 1, nb = (c16 >> 2) & 1;
          const f4 e = nb ? d1 : d0;
          if (c16 < 8) gst128(rs, WS_QG + (((ty * 2 + (bid & 1)) * 2 + (c16 & 1)) * 4096 + hh * 128 + 32 * w1 + 16 * nb + 4 * g + 2 * pr) * 8,
                              (v4u){asu(pr ? e[2] : e[0]), tag, asu(pr ? e[3] : e[1]), tag});
          lgk0();
        } else {
          STAMP(1);
          if (w1 == 3 && lane == 0) *(volatile LAS unsigned*)A_OUL2 = 1u;
          const unsigned a0 = opq((w1 < 2 ? SCR : A_QU2 - 32768) + w1 * 16384 + c16 * 256 + ((g ^ c16) << 4));
          v4u uf[16];
#pragma unroll
          for (int i = 0; i < 16; ++i) uf[i] = lds128((a0 ^ ((i & 3) << 6)) + (i >> 2) * 4096);
          lds_wait_ge(A_QB, 1u);
          v4u qb[4];
#pragma unroll
          for (int s = 0; s < 4; ++s) qb[s] = lds128(A_QBI + (c16 & 1) * 256 + s * 64 + g * 16);
          __builtin_amdgcn_sched_barrier(0);
          f4 d[4];
#pragma unroll
          for (int rb = 0; rb < 4; ++rb) d[rb] = (f4){0.f, 0.f, 0.f, 0.f};
#pragma unroll
          for (int s = 0; s < 4; ++s)
#pragma unroll
            for (int rb = 0; rb < 4; ++rb) d[rb] = mfma32(uf[4 * rb + s], qb[s], d[rb]);
          const int rb = (c16 >> 1) & 3;
          const f4 e = rb == 0 ? d[0] : rb == 1 ? d[1] : rb == 2 ? d[2] : d[3];
          if (c16 < 8) sto64(A_QAS + w1 * 256 + (c16 & 1) * 128 + (16 * rb + 4 * g) * 2, (v2u){pk(e[0], e[1]), pk(e[2], e[3])});
          lgk0();
          if (lane < 16) gst128(rs, WS_QAB + ((lane >> 3) * 32 + hh) * 1024 + 512 + w1 * 128 + (lane & 7) * 16, lds128(A_QAS + w1 * 256 + lane * 16));
          vmwait<0>();
        }
        unsigned old = 0;
        if (lane == 0) old = __hip_atomic_fetch_add((LAS unsigned*)A_QUSED, 1u, __ATOMIC_RELAXED, __HIP_MEMORY_SCOPE_WORKGROUP);
        if (ty == 2 && __builtin_amdgcn_readfirstlane(old) == 3u) {
          flag_line(rs, WS_FQ + (32 + hh) * FS, lane, tag);
          cfence();
          if (lane == 0) *(volatile LAS unsigned*)A_QCNT = 4u;
        }
        STAMP(2);
      }
      if (wave == 5) {
        if (ty == 2) {
          lds_wait_ge(A_OUL2, 1u);
          gather_qb(A_QBI, hh, lane, rs, tag);
          lgk0();
          if (lane == 0) *(volatile LAS unsigned*)A_QB = 1u;
          STAMP(1);
        }
        lds_wait_ge(A_LANDK, ty == 2 ? 1u : (unsigned)QGO);
        v4u v[8];
        const unsigned off = WS_QG + 131072 + (b * 4096 + kvh * 512 + 2 * lane) * 8;
        sweep<8, 4>(v, rs, tag, [&](int k) { return off + (k & 1) * 65536 + (k >> 1) * 1024; });
#pragma unroll
        for (int k = 0; k < 4; ++k) sto32(A_QIMG + k * 256 + lane * 4, pk(asf(v[2 * k].x) + asf(v[2 * k + 1].x), asf(v[2 * k].z) + asf(v[2 * k + 1].z)));
        lgk0();
        if (lane == 0) *(volatile LAS unsigned*)A_QRDY = 1u;
      }
      lds_wait_ge(A_QRDY, 1u);
      STAMP(3);
      v4u qf[4];
#pragma unroll
      for (int s = 0; s < 4; ++s) qf[s] = lds128(A_QIMG + (c16 & 3) * 256 + s * 64 + g * 16);
      const unsigned ko = c16 * 256 + ((g ^ c16) << 4);
      const int vr = 4 * g + (c16 >> 2);
      const unsigned vo = 4096 + vr * 256 + ((((lane & 3) >> 1) ^ ((vr & 7) << 1)) << 4) + (lane & 1) * 8;
      float m = -INFINITY, l = 0.f;
      f4 acc[8];
#pragma unroll
      for (int t = 0; t < 8; ++t) acc[t] = (f4){0.f, 0.f, 0.f, 0.f};
      int slot = wave - 1;
      for (int j = wave - 1; j < nblk; j += NCONS) {
        const unsigned sb = slot_addr(slot);
        lds_wait_ge(A_LANDK, (unsigned)(j + 1));
        v4u kf[4];
#pragma unroll
        for (int s = 0; s < 4; ++s) kf[s] = lds128(sb + (ko ^ (s << 6)));
        __builtin_amdgcn_sched_barrier(0);
        f4 s4_ = {0.f, 0.f, 0.f, 0.f};
#pragma unroll
        for (int s = 0; s < 4; ++s) s4_ = mfma32(kf[s], qf[s], s4_);
        float mx = fmaxf(fmaxf(s4_[0], s4_[1]), fmaxf(s4_[2], s4_[3]));
        mx = x32max(x16max(mx));
        const float mn = fmaxf(m, mx), al = ex2((m - mn) * SC), mc = -mn * SC;
        const float p0 = ex2(fmaf(s4_[0], SC, mc)), p1 = ex2(fmaf(s4_[1], SC, mc)), p2 = ex2(fmaf(s4_[2], SC, mc)), p3 = ex2(fmaf(s4_[3], SC, mc));
        l = fmaf(l, al, (p0 + p1) + (p2 + p3));
        m = mn;
        v2u pb = {pk(p0, p1), pk(p2, p3)};
        unsigned va[8];
#pragma unroll
        for (int t = 0; t < 8; ++t) va[t] = sb + (vo ^ (t << 5));
        if (!__all(al == 1.f)) {
#pragma unroll
          for (int t = 0; t < 8; ++t) acc[t] *= al;
        }
        asm volatile("" : "+v"(pb), "+v"(l), "+v"(acc[0]), "+v"(acc[1]), "+v"(acc[2]), "+v"(acc[3]), "+v"(acc[4]), "+v"(acc[5]), "+v"(acc[6]), "+v"(acc[7]),
                     "+v"(va[0]), "+v"(va[1]), "+v"(va[2]), "+v"(va[3]), "+v"(va[4]), "+v"(va[5]), "+v"(va[6]), "+v"(va[7]));
        lds_wait_ge<0>(A_LANDV, (unsigned)(j + 1));
        v2u vf[8];
#pragma unroll
        for (int t = 0; t < 8; ++t) vf[t] = trd(va[t]);
        lgk0();
        if (lane == 0) *(volatile LAS unsigned*)(A_DONE + slot * 4) = (unsigned)(j + 1);
#pragma unroll
        for (int t = 0; t < 8; ++t) acc[t] = mfma16(vf[t], pb, acc[t]);
        slot += NCONS; if (slot >= NSLOT) slot -= NSLOT;
      }
      STAMP(4);
      l = x32sum(x16sum(l));
      if (c16 < 4) {
#pragma unroll
        for (int t = 0; t < 8; ++t) sto128(A_ST + (wave - 1) * 2048 + c16 * 512 + ((t ^ c16) * 64) + g * 16, __builtin_bit_cast(v4u, acc[t]));
        if (g == 0) sto64(A_ML + (c16 * 8 + wave - 1) * 8, (v2u){asu(m * SC), asu(l)});
      }
    }
    bar();
    if (wave >= 3 && wave <= 6) {
      const int hq = wave - 3;
      v2u ml[NCONS];
      f2 ow[NCONS];
      const unsigned sa = opq(A_ST + hq * 512 + ((lane * 8) ^ (hq << 6))), ma = opq(A_ML + hq * 64);
#pragma unroll
      for (int c = 0; c < NCONS; ++c) { ml[c] = *(const LAS v2u*)(ma + c * 8); ow[c] = *(const LAS f2*)(sa + c * 2048); }
      float M = asf(ml[0].x);
#pragma unroll
      for (int c = 1; c < NCONS; ++c) M = fmaxf(M, asf(ml[c].x));
      float L = 0.f, o0 = 0.f, o1 = 0.f;
#pragma unroll
      for (int c = 0; c < NCONS; ++c) { const float wt = ex2(asf(ml[c].x) - M); L = fmaf(wt, asf(ml[c].y), L); o0 = fmaf(wt, ow[c].x, o0); o1 = fmaf(wt, ow[c].y, o1); }
      const int rec = (b * 32 + kvh * 4 + hq) * NSPLIT_A + split;
      gst128(rs, WS_PAO + rec * 1024 + lane * 16, (v4u){asu(o0), tag, asu(o1), tag});
      if (lane < 8) gst128(rs, WS_PAM + rec * 128 + lane * 16, (v4u){asu(M), tag, asu(L), tag});
    }
    STAMP(6);
    if (ty < 2) {
      const int oh = iA >> 2, odq = iA & 3;
      if (wave == 0) { vmwait<0>(); if (lane == 0) lds_inc(A_OUL); }
      lds_wait_ge(A_OUL, 1u);
      v4u wf[4];
      out_frags(slot_addr((nblk + g) % NSLOT), wave, lane, wf);
      const int tt = (c16 >> 1) & 3, pr = c16 >> 3, n = 64 * wave + 16 * tt + 4 * g + 2 * pr;
      unsigned fo = WS_F + ((((n >> 3) * 128 + iA) * 16) + (c16 & 1) * 8 + (n & 7)) * 8, oa = A_OIMG + (c16 & 1) * 64 + g * 16;
      asm volatile("" : "+v"(fo), "+v"(oa));
      if (wave == 1) {
        const int rec = ((lane >> 5) * 32 + oh) * NSPLIT_A + (g & 1) * 5;
        v4u vm[10];
        const unsigned o0_ = WS_PAO + rec * 1024 + (32 * odq + 2 * c16) * 8, o1_ = WS_PAM + rec * 128 + odq * 16;
        sweep2<10, 9>(vm, rs, tag, [&](int k) { return k < 5 ? o0_ + k * 1024 : o1_ + (k - 5) * 128; });
        const v4u* v = vm; const v4u* ml = vm + 5;
        float M = asf(ml[0].x);
#pragma unroll
        for (int i = 1; i < 5; ++i) M = fmaxf(M, asf(ml[i].x));
        M = x16max(M);
        float L = 0.f, o0 = 0.f, o1 = 0.f;
#pragma unroll
        for (int i = 0; i < 5; ++i) { const float wt = ex2(asf(ml[i].x) - M); L = fmaf(wt, asf(ml[i].z), L); o0 = fmaf(wt, asf(v[i].x), o0); o1 = fmaf(wt, asf(v[i].z), o1); }
        const float inv = 1.f / x16sum(L);
        o0 = x16sum(o0); o1 = x16sum(o1);
        if (!(g & 1)) sto32(A_OIMG + (lane >> 5) * 64 + c16 * 4, pk(o0 * inv, o1 * inv));
      }
      STAMP(7);
      bar();
      const v4u ob = lds128(oa);
      f4 d[4];
#pragma unroll
      for (int t = 0; t < 4; ++t) d[t] = mfma32(wf[t], ob, (f4){0.f, 0.f, 0.f, 0.f});
      const f4 e = tt == 0 ? d[0] : tt == 1 ? d[1] : tt == 2 ? d[2] : d[3];
      gst128(rs, fo, (v4u){asu(pr ? e[2] : e[0]), tag, asu(pr ? e[3] : e[1]), tag});
      STAMP(8);
    }
    STAMP_OUT();
  } else if (isB) {
    const int b = pid >> 5, w = wave, lane_ = lane;
    const unsigned vl = lane << 4;
#define LD_S(S, rho, u) asm volatile("global_load_dwordx4 %0, %1, %2 offset:%3 nt" : "=v"(S[u]) : "v"(vl), "s"(cp + (size_t)((((rho) + brot) & 3) * 65536) + ((u) >> 2) * 4096), "n"(((u) & 3) * 1024) : "memory")
#define ST_S(S, rho) do { unsigned a0_ = ((rho) & 1) * 65536 + w * 8192 + vc0, a1_ = ((rho) & 1) * 65536 + w * 8192 + vc1; asm volatile("" : "+v"(a0_), "+v"(a1_)); \
      _Pragma("unroll") for (int u_ = 0; u_ < 8; ++u_) sto128(u_ * 1024 + (((u_ >> 2) ? a1_ : a0_) ^ ((u_ & 3) << 6)), S[u_]); } while (0)
#define PB() vmwait<WB - 2>()
    v4u S2[8], S3[8];
    GET_TAG();
    PB(); LD_C(0, 6); LD_C(0, 7);
    STAMP(1);
    PB(); LD_C(1, 0); LD_C(1, 1);
    PB(); LD_C(1, 2); LD_C(1, 3);
    PB(); LD_C(1, 4); LD_C(1, 5);
    PB(); LD_C(1, 6); LD_C(1, 7);
    STAMP(2);
    PB(); LD_S(S2, 2, 0); LD_S(S2, 2, 1);
    PB(); LD_S(S2, 2, 2); LD_S(S2, 2, 3);
    PB(); LD_S(S2, 2, 4); LD_S(S2, 2, 5);
    PB(); LD_S(S2, 2, 6); LD_S(S2, 2, 7);
#pragma unroll
    for (int u = 0; u < NS3; u += 2) { PB(); LD_S(S3, 3, u); LD_S(S3, 3, u + 1); }
    STAMP(3);
    if (w == 0) {
      _Pragma("nounroll") for (int it = 0; it < SPIN_CAP; ++it) { cfence(); if (__all(gld32(rs, WS_FQ + lane * FS) == tag)) break; __builtin_amdgcn_s_sleep(4); }
    }
    bar();
    STAMP(4);
    {
      v4u t[4];
      const char* qa = ws + WS_QAB + (b * 32 + 4 * w) * 1024;
#pragma unroll
      for (int i = 0; i < 4; ++i) asm volatile("global_load_dwordx4 %0, %1, %2 offset:%3 sc1" : "=v"(t[i]) : "v"(vl), "s"(qa), "n"(i * 1024) : "memory");
      constexpr int NA = 2, NC = 6 - NS3;
      static_assert(NS3 == 2 || NS3 == 4 || NS3 == 6, "");
#pragma unroll
      for (int u = NS3; u < NS3 + NA; ++u) LD_S(S3, 3, u);
      asm volatile("s_waitcnt vmcnt(%12)" : "+v"(t[0]), "+v"(t[1]), "+v"(t[2]), "+v"(t[3]), "+v"(S2[0]), "+v"(S2[1]), "+v"(S2[2]), "+v"(S2[3]),
                   "+v"(S2[4]), "+v"(S2[5]), "+v"(S2[6]), "+v"(S2[7]) : "n"(NA) : "memory");
#pragma unroll
      for (int u = NS3 + NA; u < NS3 + NA + NC; ++u) LD_S(S3, 3, u);
#pragma unroll
      for (int i = 0; i < 4; ++i) sto128(SCR + (4 * w + i) * 1024 + ((lane ^ ((4 * w + i) & 15)) << 4), t[i]);
    }
    bar();
    const int kb4 = w & 3, hg = w >> 2;
    v4u qf[16];
#pragma unroll
    for (int s = 0; s < 16; ++s) qf[s] = lds128(SCR + (16 * hg + c16) * 1024 + ((((4 * s) & 15) ^ g ^ c16) << 4) + (s >> 2) * 256);
    STAMP(5);
    const unsigned fk = ((c16 & 3) << 2) | ((0x78u >> (2 * (c16 >> 2))) & 3u);
    const unsigned ka = (16 * kb4 + c16) * 1024 + ((g ^ fk) << 4);
    const int h2 = lane >> 5, blk = g & 1, qq = c16 >> 2, pp = lane & 3;
    const unsigned ft0 = (qq << 2) | (h2 ? 3u : 0u), ft1 = (qq << 2) | (h2 ? 1u : 2u);
    const unsigned cbase = 8 * w + 2 * blk + (pp >> 1);
    const unsigned va00 = (8 * h2 + qq) * 1024 + ((cbase ^ ft0) << 4) + (pp & 1) * 8, va01 = (8 * h2 + 4 + qq) * 1024 + ((cbase ^ ft1) << 4) + (pp & 1) * 8;
    const unsigned mxa = opq(B_MX + (16 * hg + c16) * 4), pwa = opq(B_P + kb4 * 1536 + (16 * hg + c16) * 48 + g * 8);
    const unsigned pra = opq(B_P + (lane & 31) * 48 + h2 * 16), ala = opq(B_AL + (lane & 31) * 4);
    float m = -INFINITY, l = 0.f;
    f16v acc0, acc1;
#pragma unroll
    for (int i = 0; i < 16; ++i) { acc0[i] = 0.f; acc1[i] = 0.f; }
#pragma unroll
    for (int rho = 0; rho < 2; ++rho) {
      unsigned kr = ka, vr0 = va00, vr1 = va01;
      asm volatile("" : "+v"(kr), "+v"(vr0), "+v"(vr1));
      bar();
      f4 sa = {0.f, 0.f, 0.f, 0.f}, sb = sa;
      {
        v4u kf[8];
#pragma unroll
        for (int s = 0; s < 8; ++s) kf[s] = lds128((kr ^ ((s & 3) << 6)) + (s >> 2) * 256);
        __builtin_amdgcn_sched_barrier(0);
#pragma unroll
        for (int s = 0; s < 8; ++s) {
          sa = mfma32(kf[s], qf[s], sa);
          kf[s] = lds128((kr ^ ((s & 3) << 6)) + (2 + (s >> 2)) * 256);
          __builtin_amdgcn_sched_barrier(0);
        }
        kr += 65536;
#pragma unroll
        for (int s = 0; s < 8; ++s) {
          sa = mfma32(kf[s], qf[8 + s], sa);
          kf[s] = lds128((kr ^ ((s & 3) << 6)) + (s >> 2) * 256);
          __builtin_amdgcn_sched_barrier(0);
        }
#pragma unroll
        for (int s = 0; s < 8; ++s) {
          sb = mfma32(kf[s], qf[s], sb);
          kf[s] = lds128((kr ^ ((s & 3) << 6)) + (2 + (s >> 2)) * 256);
          __builtin_amdgcn_sched_barrier(0);
        }
#pragma unroll
        for (int s = 0; s < 8; ++s) sb = mfma32(kf[s], qf[8 + s], sb);
      }
      float mx = fmaxf(fmaxf(fmaxf(sa[0], sa[1]), fmaxf(sa[2], sa[3])), fmaxf(fmaxf(sb[0], sb[1]), fmaxf(sb[2], sb[3])));
      mx = x32max(x16max(mx));
      if (g == 0) stof(mxa + kb4 * 128, mx);
      v4u c0[8], c1[8];
#pragma unroll
      for (int ks = 0; ks < 4; ++ks) {
        c0[ks] = trd2(ks * 16384 + vr0, ks * 16384 + vr1);
        c1[ks] = trd2(ks * 16384 + (vr0 ^ 64), ks * 16384 + (vr1 ^ 64));
      }
      if (rho == 0) STAMP(6); else STAMP(8);
      bar();
      const float mn = fmaxf(fmaxf(m, fmaxf(ldsf(mxa), ldsf(mxa + 128))), fmaxf(ldsf(mxa + 256), ldsf(mxa + 384)));
      __builtin_amdgcn_sched_barrier(0);
      if (rho == 0) ST_S(S2, 0);
      __builtin_amdgcn_sched_barrier(0);
      vr0 += 65536; vr1 += 65536;
#pragma unroll
      for (int ks = 0; ks < 4; ++ks) {
        c0[4 + ks] = trd2(ks * 16384 + vr0, ks * 16384 + vr1);
        c1[4 + ks] = trd2(ks * 16384 + (vr0 ^ 64), ks * 16384 + (vr1 ^ 64));
      }
      const float al = ex2((m - mn) * SC), mc = -mn * SC;
      float p[8];
#pragma unroll
      for (int i = 0; i < 4; ++i) { p[i] = ex2(fmaf(sa[i], SC, mc)); p[4 + i] = ex2(fmaf(sb[i], SC, mc)); }
      l = fmaf(l, al, ((p[0] + p[1]) + (p[2] + p[3])) + ((p[4] + p[5]) + (p[6] + p[7])));
      m = mn;
      sto64(pwa, (v2u){pk(p[0], p[1]), pk(p[2], p[3])});
      sto64(pwa + 6144, (v2u){pk(p[4], p[5]), pk(p[6], p[7])});
      if (kb4 == 0 && g == 0) stof(mxa + (B_AL - B_MX), al);
      if (rho == 1) { l = x32sum(x16sum(l)); if (g == 0) stof(mxa + (B_LS - B_MX) + kb4 * 128, l); }
      bar();
      if (rho == 1 && kb4 == 0 && g == 0) {
        const unsigned la = mxa + (B_LS - B_MX);
        sto64(B_FM + (16 * hg + c16) * 8, (v2u){asu(m * SC), asu((ldsf(la) + ldsf(la + 128)) + (ldsf(la + 256) + ldsf(la + 384)))});
      }
      const float alp = rho ? ldsf(ala) : 0.f;
      v4u pf[4];
#pragma unroll
      for (int ks = 0; ks < 4; ++ks) pf[ks] = lds128(pra + ks * 1536);
      __builtin_amdgcn_sched_barrier(0);
      if (rho) { acc0 *= alp; acc1 *= alp; }
      unsigned s0_ = 65536 + w * 8192 + vc0, s1_ = 65536 + w * 8192 + vc1;
      if (rho == 0) { asm volatile("" : "+v"(s0_), "+v"(s1_)); vmwait<0>(); }
#pragma unroll
      for (int ks = 0; ks < 8; ++ks) {
        acc0 = mfmaL(c0[ks], pf[ks & 3], acc0); acc1 = mfmaL(c1[ks], pf[ks & 3], acc1);
        if (ks < 4) pf[ks] = lds128(pra + 6144 + ks * 1536);
        if (rho == 0) sto128(ks * 1024 + (((ks >> 2) ? s1_ : s0_) ^ ((ks & 3) << 6)), S3[ks]);
        __builtin_amdgcn_sched_barrier(0);
      }
      if (rho == 0) STAMP(7); else STAMP(9);
    }
    {
    const int lane = (int)opq((unsigned)lane_), h2 = lane >> 5, tid = wave * 64 + lane;
    {
      const unsigned tb = B_T + w * 4096, n32 = lane & 31;
#pragma unroll
      for (int q4 = 0; q4 < 4; ++q4) {
        sto64(tb + n32 * 128 + ((q4 ^ (n32 & 7)) << 4) + h2 * 8, (v2u){pk(acc0[4 * q4], acc0[4 * q4 + 1]), pk(acc0[4 * q4 + 2], acc0[4 * q4 + 3])});
        sto64(tb + n32 * 128 + (((4 + q4) ^ (n32 & 7)) << 4) + h2 * 8, (v2u){pk(acc1[4 * q4], acc1[4 * q4 + 1]), pk(acc1[4 * q4 + 2], acc1[4 * q4 + 3])});
      }
      lgk0();
      const int hr = lane >> 3, ck = lane & 7;
#pragma unroll
      for (int i = 0; i < 4; ++i)
        gst128(rs, WS_PB + (((8 * i + hr) * 4 + (w >> 1)) * 64 + pid) * 256 + (w & 1) * 128 + ck * 16, lds128(tb + (8 * i + hr) * 128 + ((ck ^ hr) << 4)));
    }
    vmwait<0>();
    bar();
    if (w == 0 && lane < 32) { const v2u fm = *(const LAS v2u*)(B_FM + lane * 8); gst128(rs, WS_PBM + pid * 512 + lane * 16, (v4u){fm.x, tag, fm.y, tag}); }
    STAMP(10);
    if (w == 0) {
      const unsigned off = WS_F + bid * 16384 + ((lane >> 3) * 16 + (bid & 15)) * 128;
      _Pragma("nounroll") for (int it = 0; it < SPIN_CAP; ++it) { cfence(); if (__popcll(__ballot(gld64(rs, off).y == tag)) >= PREN) break; __builtin_amdgcn_s_sleep(8); }
    }
    bar();
    STAMP(14);
    v4u f[3];
    sweep<3, 0>(f, rs, tag, [&](int k) { return WS_F + ((k >> 1) * 64 + bid) * 16384 + (k < 2 ? k * 8192 + tid * 16 : (tid & 255) * 16); });
    float r[4] = {asf(f[0].x) + asf(f[1].x), asf(f[0].z) + asf(f[1].z), w < 4 ? asf(f[2].x) : 0.f, w < 4 ? asf(f[2].z) : 0.f};
#pragma unroll
    for (int k = 0; k < 4; ++k) { r[k] += dpp<ROR8>(r[k]); r[k] = x32sum(x16sum(r[k])); }
    if (lane < 8) { sto64(B_FIN + w * 128 + lane * 8, (v2u){asu(r[0]), asu(r[1])}); sto64(B_FIN + w * 128 + 64 + lane * 8, (v2u){asu(r[2]), asu(r[3])}); }
    bar();
    if (tid < 16) {
      float s0 = 0.f, s1 = 0.f;
#pragma unroll
      for (int i = 0; i < 8; ++i) { const f2 t = *(const LAS f2*)(B_FIN + i * 128 + tid * 8); s0 += t.x; s1 += t.y; }
      const int mat = tid >> 3, ob = (tid >> 2) & 1, pr = tid & 3;
      *(GAS unsigned*)((GAS char*)out + (ob * 1024 + mat * 512 + 8 * bid + 2 * pr) * 2) = pk(s0, s1);
      if (bid == 0 && tid == 0) gst32(rs, WS_E, tag);
    }
    STAMP(15);
    STAMP_OUT();
    }
  } else {
    const int h = bid - NB_CU, w = wave;
    GET_TAG();
    if (w == 0) gather_qb(T_QBI, h, lane, rs, tag);
    vmwait<0>();
    STAMP(1);
    v4u OU[16];
    const char* ow = Bwo + (size_t)(h * 128 + 16 * w) * 1024;
    const unsigned vo = g * 4096 + c16 * 16;
#define LD_OU(u) asm volatile("global_load_dwordx4 %0, %1, %2 offset:%3 nt" : "=v"(OU[u]) : "v"(vo), "s"(ow), "n"(((u) >> 2) * 1024 + ((u) & 3) * 256) : "memory")
#pragma unroll
    for (int u = 0; u < TE; ++u) LD_OU(u);
    {
      const unsigned a0 = w * 8192 + c16 * 256 + ((g ^ c16) << 4);
      v4u uf[8];
#pragma unroll
      for (int i = 0; i < 8; ++i) uf[i] = lds128((a0 ^ ((i & 3) << 6)) + (i >> 2) * 4096);
      bar();
      STAMP(2);
      v4u qb[4];
#pragma unroll
      for (int s = 0; s < 4; ++s) qb[s] = lds128(T_QBI + (c16 & 1) * 256 + s * 64 + g * 16);
      __builtin_amdgcn_sched_barrier(0);
      f4 d0 = {0.f, 0.f, 0.f, 0.f}, d1 = d0;
#pragma unroll
      for (int s = 0; s < 4; ++s) { d0 = mfma32(uf[s], qb[s], d0); d1 = mfma32(uf[4 + s], qb[s], d1); }
      const int rb = (c16 >> 1) & 1;
      const f4 e = rb ? d1 : d0;
      if (c16 < 4) sto64(T_QAS + (c16 & 1) * 512 + (32 * w + 16 * rb + 4 * g) * 2, (v2u){pk(e[0], e[1]), pk(e[2], e[3])});
      bar();
      if (w == 1) {
        gst128(rs, WS_QAB + ((lane >> 5) * 32 + h) * 1024 + (lane & 31) * 16, lds128(T_QAS + lane * 16));
        vmwait<0>();
        flag_line(rs, WS_FQ + h * FS, lane, tag);
      }
    }
    bar();
    STAMP(3);
    {
      const char* uv = Wuv + (size_t)(64 * w) * 8192 + h * 256;
      const unsigned vuv = g * 8192 + ((c16 ^ (g << 1)) << 4);
#pragma unroll
      for (int u = 0; u < 16; ++u) { if (u >= WT && !(u & 1)) vmwait<WT - 2>(); dma<0>(uv + (size_t)u * 32768, vuv ^ (((u >> 1) & 1) << 7), w * 16384 + u * 1024); }
#pragma unroll
      for (int u = TE; u < 16; ++u) { if (!(u & 1)) vmwait<WT - 2>(); LD_OU(u); }
    }
    asm volatile("s_waitcnt vmcnt(0)" : "+v"(OU[0]), "+v"(OU[1]), "+v"(OU[2]), "+v"(OU[3]), "+v"(OU[4]), "+v"(OU[5]), "+v"(OU[6]), "+v"(OU[7]),
                 "+v"(OU[8]), "+v"(OU[9]), "+v"(OU[10]), "+v"(OU[11]), "+v"(OU[12]), "+v"(OU[13]), "+v"(OU[14]), "+v"(OU[15]) :: "memory");
    if (tid < 4) sto32(T_RDY + tid * 4, 0u);
    bar();
    STAMP(4);
    v4u wf[16];
    {
      const int q = c16 >> 2, p = lane & 3, f3 = q | ((g & 1) << 2);
      const unsigned a0 = (8 * g + q) * 256 + (((2 * w + (p >> 1)) ^ (f3 << 1)) << 4) + (p & 1) * 8;
      const unsigned aa[2] = {opq(a0), opq(a0 + 65536)};
#pragma unroll
      for (int s = 0; s < 16; ++s) wf[s] = trd2(aa[s >> 3] + (s & 7) * 8192, aa[s >> 3] + (s & 7) * 8192 + 1024);
    }
    STAMP(5);
    {
      const int tb = w >> 2;
      v4u o[8];
#pragma unroll
      for (int i = 0; i < 8; ++i) { o[i] = (v4u){0u, 0u, 0u, 0u}; asm volatile("" : "+v"(o[i])); }
      const char* pb = ws + WS_PB + (h * 4 + (w & 3)) * 16384 + tb * 8192;
      const unsigned vl = lane << 4;
      unsigned done = 0;
      bool all = false;
      v4u ml = {0u, ~tag, 0u, ~tag};
      if (w == 0) {
        _Pragma("nounroll") for (int it = 0; it < SPIN_CAP; ++it) { cfence(); if (__all(gld32(rs, WS_PBM + 2 * h * 512 + h * 16 + 4) == tag)) break; __builtin_amdgcn_s_sleep(8); }
      }
      _Pragma("nounroll") for (int it = 0; it < SPIN_CAP * 8 && !all; ++it) {
        unsigned rdy;
        if (w == 0) {
          cfence();
          const v4u t = gld128(rs, tagok(ml, tag) ? 0xfffffff0u : WS_PBM + lane * 512 + h * 16);
          if (!tagok(ml, tag)) ml = t;
          unsigned long long m = __ballot(tagok(ml, tag));
          all = m == ~0ull;
          m &= m >> 2; m &= m >> 1; m &= 0x1111111111111111ull;
          rdy = (unsigned)m;
          if (lane == 0) *(volatile LAS v2u*)T_RDY = (v2u){rdy, (unsigned)(m >> 32)};
        } else {
          rdy = lds_peek(T_RDY + tb * 4);
          if (rdy == done) { __builtin_amdgcn_s_sleep(1); continue; }
          all = rdy == 0x11111111u;
        }
        const unsigned add = rdy & ~done;
        done = rdy;
#pragma unroll
        for (int i = 0; i < 8; ++i)
          if (add & (1u << (4 * i))) asm volatile("global_load_dwordx4 %0, %1, %2 offset:%3 sc1" : "+v"(o[i]) : "v"(vl), "s"(pb + (i >> 2) * 4096), "n"((i & 3) * 1024) : "memory");
      }
      if (w == 0) {
        const float M = x16max(row16max(asf(ml.x)));
        const float wt = ex2(asf(ml.x) - M), wn = wt / x16sum(row16sum(wt * asf(ml.z)));
        const unsigned hi = pk(wn, wn), wa = T_WN + (lane >> 5) * 128 + (lane & 3) * 32 + ((lane >> 2) & 7) * 4;
        const float lo = wn - asf(hi & 0xffff0000u);
        sto32(wa, hi); sto32(wa + 256, pk(lo, lo));
        lgk0();
        if (lane == 0) *(volatile LAS unsigned*)(T_RDY + 8) = 1u;
      } else {
        lds_wait_ge<0>(T_RDY + 8, 1u);
      }
      const unsigned wa = T_WN + (c16 >> 3) * 256 + tb * 128 + g * 32, half = (c16 & 1) ? 0xffff0000u : 0x0000ffffu;
      const v4u w0_ = lds128(wa), w1_ = lds128(wa + 16);
      const int rr = (c16 >> 1) & 3;
      v4u bo[8];
#pragma unroll
      for (int u = 0; u < 8; ++u) { const unsigned x = (u < 4 ? w0_[u & 3] : w1_[u & 3]) & half; bo[u] = (v4u){rr == 0 ? x : 0u, rr == 1 ? x : 0u, rr == 2 ? x : 0u, rr == 3 ? x : 0u}; }
      asm volatile("s_waitcnt vmcnt(0)" : "+v"(o[0]), "+v"(o[1]), "+v"(o[2]), "+v"(o[3]), "+v"(o[4]), "+v"(o[5]), "+v"(o[6]), "+v"(o[7]),
                   "+v"(bo[0]), "+v"(bo[1]), "+v"(bo[2]), "+v"(bo[3]), "+v"(bo[4]), "+v"(bo[5]), "+v"(bo[6]), "+v"(bo[7]) :: "memory");
      STAMP(8);
      f4 d = {0.f, 0.f, 0.f, 0.f};
#pragma unroll
      for (int u = 0; u < 8; ++u) d = mfma32(o[u], bo[u], d);
#pragma unroll
      for (int r = 0; r < 4; ++r) {
        const float x = d[r] + dpp<ROR8>(d[r]);
        const unsigned pr = pk(x, dpp<XOR1>(x));
        if ((c16 & 9) == 0) sto32(T_OL + tb * 1088 + ((w & 3) * 128 + 32 * g + 8 * r + c16) * 2, pr);
      }
    }
    bar();
    STAMP(6);
    {
      v4u of[16];
      const unsigned oa = opq(T_OL + (c16 >> 3) * 1088 + g * 16);
#pragma unroll
      for (int s = 0; s < 16; ++s) of[s] = lds128(oa + s * 64);
      __builtin_amdgcn_sched_barrier(0);
      f4 d0 = {0.f, 0.f, 0.f, 0.f}, d1 = d0;
#pragma unroll
      for (int s = 0; s < 8; ++s) { d0 = mfma32(wf[2 * s], of[2 * s], d0); d1 = mfma32(wf[2 * s + 1], of[2 * s + 1], d1); }
      d0 += d1;
      const unsigned half = (c16 & 1) ? 0xffff0000u : 0x0000ffffu;
      const int rr = (c16 >> 1) & 3;
      f4 o4[4];
#pragma unroll
      for (int cb = 0; cb < 4; ++cb) o4[cb] = (f4){0.f, 0.f, 0.f, 0.f};
#pragma unroll
      for (int u = 0; u < 4; ++u) {
        const unsigned x = pk(d0[u], d0[u]) & half;
        const v4u bo = {rr == 0 ? x : 0u, rr == 1 ? x : 0u, rr == 2 ? x : 0u, rr == 3 ? x : 0u};
#pragma unroll
        for (int cb = 0; cb < 4; ++cb) o4[cb] = mfma32(OU[4 * u + cb], bo, o4[cb]);
      }
#pragma unroll
      for (int cb = 0; cb < 4; ++cb)
#pragma unroll
        for (int r = 0; r < 4; ++r) stof(T_RED + w * 4096 + cb * 1024 + r * 64 + g * 256 + c16 * 4, o4[cb][r]);
    }
    bar();
    {
      const int j = tid >> 3, pr = tid & 7;
      float s0 = 0.f, s1 = 0.f;
#pragma unroll
      for (int i = 0; i < 8; ++i) { const f2 t = *(const LAS f2*)(T_RED + i * 4096 + tid * 8); s0 += t.x; s1 += t.y; }
      gst128(rs, WS_F + (((64 + j) * 128 + h) * 16 + 2 * pr) * 8, (v4u){asu(s0), tag, asu(s1), tag});
    }
    STAMP(7);
    STAMP_OUT();
  }
}

extern "C" void kernel_launch(void* const* d_in, const int* in_sizes, int n_in, void* d_out, int out_size, void* d_ws, size_t ws_size,
                              hipStream_t stream) {
  (void)in_sizes; (void)n_in; (void)out_size; (void)ws_size;
  (void)hipFuncSetAttribute((const void*)fused_decode, hipFuncAttributeMaxDynamicSharedMemorySize, LDS_BYTES);
  fused_decode<<<256, 512, LDS_BYTES, stream>>>(
      (const char*)d_in[0], (const char*)d_in[1], (const char*)d_in[2], (const char*)d_in[3], (const char*)d_in[4], (const char*)d_in[5],
      (const char*)d_in[6], (const char*)d_in[7], (const char*)d_in[8], (const char*)d_in[9], (const char*)d_in[10], (unsigned short*)d_out,
      (char*)d_ws);
}
```

```cpp
#include <hip/hip_runtime.h>
#include <stdint.h>
#pragma clang diagnostic ignored "-Wint-to-pointer-cast"

#define GAS __attribute__((address_space(1)))
#define LAS __attribute__((address_space(3)))
typedef unsigned v4u __attribute__((ext_vector_type(4)));
typedef unsigned v2u __attribute__((ext_vector_type(2)));
typedef float f4 __attribute__((ext_vector_type(4)));
typedef float f2 __attribute__((ext_vector_type(2)));
typedef float f16v __attribute__((ext_vector_type(16)));
typedef short s4 __attribute__((ext_vector_type(4)));
typedef __bf16 b8 __attribute__((ext_vector_type(8)));
typedef __bf16 b2 __attribute__((ext_vector_type(2)));

namespace {

constexpr int NB_CU = 64;
constexpr int NT_CU = 32;
constexpr int NSPLIT_A = 10;
constexpr int NB0 = 55;
constexpr int NB1 = 52;
constexpr int NB2 = (512 - 4 * NB0 - 4 * NB1) / 2;
constexpr int NSLOT = 18, NLOW = 14, NCONS = 6;
constexpr int W1 = 12;
constexpr int W2 = 24;
constexpr int J1 = 18;
constexpr int WB = 4;
constexpr int WT = 6;
constexpr int DLYP = 30;
constexpr int DLYB = 10;
constexpr int NS3 = 6;
constexpr int TE = 6;
constexpr int QGO = 4;
constexpr int ODL = 5;
constexpr int PREN = 32;
constexpr int ROT = 13, BROT = 3;
constexpr int CODE_KB = 24;
constexpr float SC = 0.08838834764831845f * 1.4426950408889634f;
constexpr int SPIN_CAP = 1 << 18;

constexpr unsigned FS = 128;
constexpr unsigned WS_E = 0;
constexpr unsigned WS_FQ = 4096;
constexpr unsigned WS_QG = 65536;
constexpr unsigned WS_QAB = WS_QG + 262144;
constexpr unsigned WS_PBM = WS_QAB + 65536;
constexpr unsigned WS_PAM = WS_PBM + 32768;
constexpr unsigned WS_PAO = WS_PAM + 131072;
constexpr unsigned WS_PB = WS_PAO + 786432;
constexpr unsigned WS_F = WS_PB + 2097152;
constexpr unsigned WS_END = WS_F + 2097152;

constexpr unsigned LDS_BYTES = 163840;
constexpr unsigned SCR = 131072;
constexpr unsigned A_MISC = NLOW * 8192;
constexpr unsigned A_LANDK = A_MISC, A_LANDV = A_MISC + 4, A_QUSED = A_MISC + 8, A_QCNT = A_MISC + 12, A_QB = A_MISC + 16, A_QRDY = A_MISC + 20, A_OUL = A_MISC + 24, A_OUL2 = A_MISC + 28;
constexpr unsigned A_DONE = A_MISC + 64;
constexpr unsigned A_QU2 = 10 * 8192;
constexpr unsigned A_QIMG = A_MISC + 256;
constexpr unsigned A_ML = A_MISC + 1280;
constexpr unsigned A_OIMG = A_MISC + 1792;
constexpr unsigned A_ST = A_MISC + 2048;
constexpr unsigned A_XIMG = A_MISC + 4096, A_DUMMY = A_MISC + 5120, A_QBI = A_MISC + 6144, A_QAS = A_MISC + 6656;
constexpr unsigned SLOT1 = 65536;
constexpr unsigned B_MX = SCR, B_AL = SCR + 512, B_LS = SCR + 640, B_P = SCR + 2048;
constexpr unsigned B_FM = SCR + 1280, B_FIN = SCR + 16384, B_T = SLOT1, B_DUMMY = SCR + 8192;
constexpr unsigned T_QBI = SCR, T_QAS = SCR + 1024, T_OL = SCR + 4096, T_RDY = SCR + 6400, T_WN = SCR + 6656, T_RED = 0;

__device__ __forceinline__ unsigned slot_addr(unsigned s) { return s * 8192 + (s >= NLOW ? 16384u : 0u); }
__device__ __forceinline__ v4u lds128(unsigned a) { return *(const LAS v4u*)a; }
__device__ __forceinline__ void sto128(unsigned a, v4u v) { *(LAS v4u*)a = v; }
__device__ __forceinline__ void sto64(unsigned a, v2u v) { *(LAS v2u*)a = v; }
__device__ __forceinline__ void sto32(unsigned a, unsigned v) { *(LAS unsigned*)a = v; }
__device__ __forceinline__ float ldsf(unsigned a) { return *(const LAS float*)a; }
__device__ __forceinline__ void stof(unsigned a, float v) { *(LAS float*)a = v; }
__device__ __forceinline__ v2u trd(unsigned a) { return __builtin_bit_cast(v2u, __builtin_amdgcn_ds_read_tr16_b64_v4i16((LAS s4*)a)); }
__device__ __forceinline__ v4u trd2(unsigned a0, unsigned a1) { const v2u x = trd(a0), y = trd(a1); return (v4u){x.x, x.y, y.x, y.y}; }
__device__ __forceinline__ f4 mfma32(v4u a, v4u b, f4 c) {
  return __builtin_amdgcn_mfma_f32_16x16x32_bf16(__builtin_bit_cast(b8, a), __builtin_bit_cast(b8, b), c, 0, 0, 0); }
__device__ __forceinline__ f4 mfma16(v2u a, v2u b, f4 c) {
  return __builtin_amdgcn_mfma_f32_16x16x16bf16_1k(__builtin_bit_cast(s4, a), __builtin_bit_cast(s4, b), c, 0, 0, 0); }
__device__ __forceinline__ f16v mfmaL(v4u a, v4u b, f16v c) {
  return __builtin_amdgcn_mfma_f32_32x32x16_bf16(__builtin_bit_cast(b8, a), __builtin_bit_cast(b8, b), c, 0, 0, 0); }
__device__ __forceinline__ unsigned pk(float lo, float hi) { return __builtin_bit_cast(unsigned, __builtin_convertvector((f2){lo, hi}, b2)); }
__device__ __forceinline__ float asf(unsigned u) { return __builtin_bit_cast(float, u); }
__device__ __forceinline__ unsigned asu(float f) { return __builtin_bit_cast(unsigned, f); }
__device__ __forceinline__ float ex2(float x) { return __builtin_amdgcn_exp2f(x); }
template <int CTRL> __device__ __forceinline__ float dpp(float x) {
  return __builtin_bit_cast(float, __builtin_amdgcn_mov_dpp(__builtin_bit_cast(int, x), CTRL, 0xf, 0xf, true)); }
constexpr int XOR1 = 0xB1, XOR2 = 0x4E, ROR4 = 0x124, ROR8 = 0x128;
__device__ __forceinline__ float x16max(float x) { auto s = __builtin_amdgcn_permlane16_swap(asu(x), asu(x), false, false); return fmaxf(asf(s[0]), asf(s[1])); }
__device__ __forceinline__ float x32max(float x) { auto s = __builtin_amdgcn_permlane32_swap(asu(x), asu(x), false, false); return fmaxf(asf(s[0]), asf(s[1])); }
__device__ __forceinline__ float x16sum(float x) { auto s = __builtin_amdgcn_permlane16_swap(asu(x), asu(x), false, false); return asf(s[0]) + asf(s[1]); }
__device__ __forceinline__ float x32sum(float x) { auto s = __builtin_amdgcn_permlane32_swap(asu(x), asu(x), false, false); return asf(s[0]) + asf(s[1]); }
__device__ __forceinline__ float row16max(float x) { x = fmaxf(x, dpp<XOR1>(x)); x = fmaxf(x, dpp<XOR2>(x)); x = fmaxf(x, dpp<ROR4>(x)); return fmaxf(x, dpp<ROR8>(x)); }
__device__ __forceinline__ float row16sum(float x) { x += dpp<XOR1>(x); x += dpp<XOR2>(x); x += dpp<ROR4>(x); return x + dpp<ROR8>(x); }

template <int IMM> __device__ __forceinline__ void dma(const void* sbase, unsigned voff, unsigned lds) {
  asm volatile("s_mov_b32 m0, %2\n\ts_nop 0\n\tglobal_load_lds_dwordx4 %0, %1 offset:%3 nt" :: "v"(voff), "s"(sbase), "s"(lds), "n"(IMM) : "memory");
}
template <int W> __device__ __forceinline__ void dma4(const void* sbase, unsigned v0, unsigned v1, unsigned v2, unsigned v3, unsigned lds) {
  asm volatile("s_mov_b32 m0, %5\n\ts_waitcnt vmcnt(%6)\n\tglobal_load_lds_dwordx4 %0, %4 nt\n\tglobal_load_lds_dwordx4 %1, %4 offset:1024 nt\n\t"
               "s_waitcnt vmcnt(%6)\n\tglobal_load_lds_dwordx4 %2, %4 offset:2048 nt\n\tglobal_load_lds_dwordx4 %3, %4 offset:3072 nt"
               :: "v"(v0), "v"(v1), "v"(v2), "v"(v3), "s"(sbase), "s"(lds), "n"(W - 2) : "memory");
}
__device__ __forceinline__ void dma_keep(const void* sbase, unsigned voff, unsigned lds) {
  asm volatile("s_mov_b32 m0, %2\n\ts_nop 0\n\tglobal_load_lds_dwordx4 %0, %1" :: "v"(voff), "s"(sbase), "s"(lds) : "memory");
}
template <int N> __device__ __forceinline__ void vmwait() { asm volatile("s_waitcnt vmcnt(%0)" :: "n"(N) : "memory"); }
__device__ __forceinline__ void bar() { asm volatile("s_waitcnt lgkmcnt(0)\n\ts_barrier" ::: "memory"); }
__device__ __forceinline__ void lgk0() { asm volatile("s_waitcnt lgkmcnt(0)" ::: "memory"); }
__device__ __forceinline__ void cfence() { asm volatile("" ::: "memory"); }
__device__ __forceinline__ unsigned opq(unsigned x) { asm volatile("" : "+v"(x)); return x; }
__device__ __forceinline__ unsigned lds_peek(unsigned a) { return __builtin_amdgcn_readfirstlane(*(volatile LAS unsigned*)a); }
template <int SL = 1> __device__ __forceinline__ void lds_wait_ge(unsigned a, unsigned want) {
  _Pragma("nounroll") for (int it = 0; it < SPIN_CAP * 8; ++it) { if (lds_peek(a) >= want) break; if (SL) __builtin_amdgcn_s_sleep(SL); }
  cfence();
}
typedef __amdgpu_buffer_rsrc_t rsrc_t;
constexpr int SC1 = 16;
__device__ __forceinline__ v4u gld128(rsrc_t r, unsigned off) { return __builtin_amdgcn_raw_buffer_load_b128(r, off, 0, SC1); }
__device__ __forceinline__ v2u gld64(rsrc_t r, unsigned off) { return __builtin_amdgcn_raw_buffer_load_b64(r, off, 0, SC1); }
__device__ __forceinline__ unsigned gld32(rsrc_t r, unsigned off) { return __builtin_amdgcn_raw_buffer_load_b32(r, off, 0, SC1); }
__device__ __forceinline__ void gst128(rsrc_t r, unsigned off, v4u v) { __builtin_amdgcn_raw_buffer_store_b128(v, r, off, 0, SC1); }
__device__ __forceinline__ void gst32(rsrc_t r, unsigned off, unsigned v) { __builtin_amdgcn_raw_buffer_store_b32(v, r, off, 0, SC1); }
__device__ __forceinline__ void flag_line(rsrc_t r, unsigned off, int lane, unsigned tag) { if (lane < 8) gst128(r, off + lane * 16, (v4u){tag, tag, tag, tag}); }
__device__ __forceinline__ bool tagok(v4u v, unsigned tag) { return v.y == tag && v.w == tag; }
template <int N, int GAP, class F> __device__ __forceinline__ void sweep2(v4u (&v)[N], rsrc_t rs, unsigned tag, F off) {
  v4u a[N], b[N];
#pragma unroll
  for (int k = 0; k < N; ++k) a[k] = gld128(rs, off(k));
  __builtin_amdgcn_s_sleep(GAP);
  _Pragma("nounroll") for (int it = 0; it < SPIN_CAP; ++it) {
    cfence();
#pragma unroll
    for (int k = 0; k < N; ++k) b[k] = gld128(rs, off(k));
    cfence();
    bool ok = true;
#pragma unroll
    for (int k = 0; k < N; ++k) { ok &= tagok(a[k], tag); v[k] = a[k]; }
    if (__all(ok)) break;
#pragma unroll
    for (int k = 0; k < N; ++k) a[k] = gld128(rs, off(k));
    cfence();
    ok = true;
#pragma unroll
    for (int k = 0; k < N; ++k) { ok &= tagok(b[k], tag); v[k] = b[k]; }
    if (__all(ok)) break;
  }
}
template <int N, int SL, class F> __device__ __forceinline__ void sweep(v4u (&v)[N], rsrc_t rs, unsigned tag, F off) {
  _Pragma("nounroll") for (int it = 0; it < SPIN_CAP; ++it) {
    cfence();
    bool ok = true;
#pragma unroll
    for (int k = 0; k < N; ++k) { v[k] = gld128(rs, off(k)); ok &= tagok(v[k], tag); }
    if (__all(ok)) break;
    if (SL) __builtin_amdgcn_s_sleep(SL);
  }
}

__device__ __forceinline__ void gather_qb(unsigned img, int h, int lane, rsrc_t rs, unsigned tag) {
  v4u qg[4];
  const unsigned off = WS_QG + (h * 128 + 2 * lane) * 8;
  sweep<4, 1>(qg, rs, tag, [&](int k) { return off + (k & 1) * 65536 + (k >> 1) * 32768; });
  sto32(img + lane * 4, pk(asf(qg[0].x) + asf(qg[1].x), asf(qg[0].z) + asf(qg[1].z)));
  sto32(img + 256 + lane * 4, pk(asf(qg[2].x) + asf(qg[3].x), asf(qg[2].z) + asf(qg[3].z)));
}
__device__ __forceinline__ void lds_inc(unsigned a) { __hip_atomic_fetch_add((LAS unsigned*)a, 1u, __ATOMIC_RELAXED, __HIP_MEMORY_SCOPE_WORKGROUP); }

__device__ __forceinline__ void out_frags(unsigned gb, int w, int lane, v4u (&wf)[4]) {
  const int g = lane >> 4, q = (lane & 15) >> 2, p = lane & 3, f3 = q | ((g & 1) << 2);
#pragma unroll
  for (int tt = 0; tt < 4; ++tt) {
    const unsigned a0 = gb + q * 1024 + (((2 * (4 * w + tt) + (p >> 1)) ^ (f3 << 1)) << 4) + (p & 1) * 8;
    wf[tt] = trd2(a0, a0 + 4096);
  }
}

}

__global__ void __launch_bounds__(512, 2) __attribute__((aligned(4096))) fused_decode(
    const char* __restrict__ Ax, const char* __restrict__ Kc, const char* __restrict__ Vc, const char* __restrict__ Wq, const char* __restrict__ Wo,
    const char* __restrict__ Bx, const char* __restrict__ Ckv, const char* __restrict__ Bwq, const char* __restrict__ Wuk, const char* __restrict__ Wuv,
    const char* __restrict__ Bwo, unsigned short* __restrict__ out, char* __restrict__ ws) {
  const int bid = blockIdx.x, tid = threadIdx.x, lane = tid & 63, wave = __builtin_amdgcn_readfirstlane(tid >> 6);
  const int g = lane >> 4, c16 = lane & 15;
  const int iA = bid - (NB_CU + NT_CU);
  const bool isA = iA >= 0, isB = bid < NB_CU;
  const int ty = iA >> 6;
  int dA = 95 - bid, dU = 223 - bid, dB = bid - 64, dQ = 159 - bid, dW = 2 - wave;
  asm("" : "+s"(dA), "+s"(dU), "+s"(dB), "+s"(dQ), "+s"(dW));
  const int mA = dA >> 31, mU = dU >> 31, mB = dB >> 31, mP = mA & ~mU, sh = mP & 1;
  const int hh = (bid >> sh) & 31;
  const unsigned row0 = (mA & ((((bid | mU) & 1) << 8) + 64 * wave - 64)) | (~mA & (32 * wave));
  const unsigned l0 = (mA & (SCR - 16384 + wave * 16384 - ((dW >> 31) & (SCR + 32768 - A_QU2)))) | (~mA & (wave * 8192));
  const unsigned karg = 0x40 - (mP & (8 + ((dQ >> 31) & 32))), more = mA;
  if ((unsigned)(wave + mA + (mB & 8)) < (unsigned)(8 + (mA & -4))) {
    const unsigned b0 = (row0 + g) * 8192 + hh * 256 + ((c16 ^ (g << sh)) << 4), x1 = ~mP & 64;
    unsigned v0 = b0, v1 = (b0 ^ x1) + 32768, v2 = (b0 ^ 128) + 65536, v3 = (b0 ^ x1 ^ 128) + 98304;
    if (karg == 0x18) __builtin_amdgcn_s_sleep(DLYP);
    unsigned long long wp;
#define D4 "global_load_lds_dwordx4 %1, %0 nt\n\ts_add_u32 m0, m0, 0x400\n\tv_add_u32 %1, 0x20000, %1\n\t" \
           "global_load_lds_dwordx4 %2, %0 nt\n\ts_add_u32 m0, m0, 0x400\n\tv_add_u32 %2, 0x20000, %2\n\t" \
           "global_load_lds_dwordx4 %3, %0 nt\n\ts_add_u32 m0, m0, 0x400\n\tv_add_u32 %3, 0x20000, %3\n\t" \
           "global_load_lds_dwordx4 %4, %0 nt\n\ts_add_u32 m0, m0, 0x400\n\tv_add_u32 %4, 0x20000, %4\n\t"
    asm volatile("s_load_dwordx2 %0, %5, %6\n\ts_mov_b32 m0, %7\n\ts_waitcnt lgkmcnt(0)\n\t" D4 D4
                 "s_cmp_eq_u32 %8, 0\n\ts_cbranch_scc1 1f\n\t" D4 D4 "1:"
                 : "=&s"(wp), "+v"(v0), "+v"(v1), "+v"(v2), "+v"(v3)
                 : "s"(__builtin_amdgcn_kernarg_segment_ptr()), "s"(karg), "s"(l0), "s"(more) : "memory", "scc");
#undef D4
  }
  {
    unsigned long long pc;
    asm volatile("s_getpc_b64 %0" : "=s"(pc));
    const int kb = ((bid >> 3) + 4 * wave) & 31;
    dma_keep((const char*)((pc & ~1023ull) + (unsigned)((kb < CODE_KB ? kb : CODE_KB - 1) * 1024)), lane * 16, isA ? A_DUMMY : B_DUMMY);
  }
  const rsrc_t rs = __builtin_amdgcn_make_buffer_rsrc(ws, 0, WS_END, 0x00020000);
  asm volatile("" :: "s"(Ax), "s"(Kc), "s"(Vc), "s"(Wq), "s"(Wo), "s"(Bx), "s"(Ckv), "s"(Bwq), "s"(Wuk), "s"(Wuv), "s"(Bwo), "s"(out), "s"(ws));
  unsigned tag;
#define GET_TAG() do { asm volatile("s_load_dword %0, %1, 0x0\n\ts_waitcnt lgkmcnt(0)" : "=s"(tag) : "s"(ws) : "memory"); tag += 1u; } while (0)
  const int pid = ((bid & 7) << 3) | ((bid >> 3) & 7);
  const char* cp = Ckv + ((size_t)pid * 256 + 8 * wave) * 1024;
  const int brot = bid >> BROT;
  const unsigned vc0 = (lane ^ ((wave & 1) ? 3 : 0)) << 4, vc1 = (lane ^ ((wave & 1) ? 1 : 2)) << 4;
#define LD_C(rho, u) dma<((u) & 3) * 1024>(cp + (size_t)((((rho) + brot) & 3) * 65536) + ((u) >> 2) * 4096, (((u) >> 2) ? vc1 : vc0) ^ (((u) & 3) << 6), ((rho) & 1) * 65536 + wave * 8192 + ((u) >> 2) * 4096)
  if (isA) {
    if (wave == 1 && ty < 2) dma<0>((ty ? Ax : Bx) + 512 * (bid & 1), (lane >> 5) * 1024 + (lane & 31) * 16, A_XIMG);
    if (tid < 64) sto32(A_MISC + tid * 4, 0u);
    bar();
  } else if (isB) {
    __builtin_amdgcn_s_sleep(DLYB);
    LD_C(0, 0); LD_C(0, 1); LD_C(0, 2); LD_C(0, 3); LD_C(0, 4); LD_C(0, 5);
  }

  if (isA) {
    const int split = iA >> 4, pair = (iA + split) & 15, b = pair >> 3, kvh = pair & 7;
    const int nblk = ty == 0 ? NB0 : ty == 1 ? NB1 : NB2, blk0 = (ty == 0 ? 0 : ty == 1 ? 4 * NB0 : 4 * (NB0 + NB1)) + (split & 3) * nblk;
    if (wave == 0 || wave == 7) {
      __builtin_amdgcn_s_setprio(3);
      static_assert(NSPLIT_A == 10 && W1 >= 4 && W1 % 4 == 0 && W2 % 4 == 0 && W1 <= W2 && W2 <= 24 && J1 <= NB0 && J1 <= NB1 && J1 <= NB2, "window");
      const bool isv = wave == 7;
      int pub = 0, j = 0;
      unsigned sl = 0;
      int pos = (pair * ROT) % nblk;
      const char* gp = (isv ? Vc : Kc) + ((size_t)pair * 8192 + (blk0 + pos) * 16) * 256;
      const unsigned v0 = opq(g * 256 + ((c16 ^ (isv ? g << 1 : g)) << 4));
      const unsigned v1 = opq(v0 ^ (isv ? 128 : 64)), v2 = opq(v0 ^ (isv ? 0 : 128)), v3 = opq(v0 ^ (isv ? 128 : 192));
      const unsigned lo = isv ? 4096 : 0, la = opq(lane ? A_MISC + 1536 + lane * 4 : isv ? A_LANDV : A_LANDK);
      unsigned dn = 0;
#define PUBLISH(val) do { pub = max(pub, (val)); *(volatile LAS unsigned*)la = (unsigned)pub; } while (0)
#define GATE(addr, need) do { if (lds_peek(addr) < (need)) { vmwait<0>(); PUBLISH(j); lds_wait_ge(addr, need); } } while (0)
#define PART(W) do { \
        if (j >= NSLOT) { if ((unsigned)__builtin_amdgcn_readfirstlane(dn) < (unsigned)(j - NSLOT + 1)) GATE(A_DONE + sl, (unsigned)(j - NSLOT + 1)); } \
        else if (j == 10) GATE(A_QUSED, 4u); \
        dma4<W>(gp, v0, v1, v2, v3, slot_addr(sl >> 2) + lo); \
        ++j; gp += 4096; if (++pos == nblk) gp -= nblk * 4096; sl = sl == 4 * (NSLOT - 1) ? 0 : sl + 4; \
        PUBLISH(j - W / 4); dn = *(volatile LAS unsigned*)(A_DONE + sl); } while (0)
      GET_TAG();
      lds_wait_ge(A_QCNT, 4u);
#pragma nounroll
      while (j < J1) PART(W1);
#pragma nounroll
      while (j < nblk) PART(W2);
      vmwait<20>(); PUBLISH(j - 5); vmwait<16>(); PUBLISH(j - 4); vmwait<12>(); PUBLISH(j - 3);
      vmwait<8>(); PUBLISH(j - 2); vmwait<4>(); PUBLISH(j - 1); vmwait<0>(); PUBLISH(j);
      if (ty < 2 && !isv) {
        const char* op = Wo + (size_t)iA * 32768;
        const unsigned o0 = lane << 4, o1 = o0 ^ 32, o2 = o0 ^ 64, o3 = o0 ^ 96;
        lds_wait_ge(A_LANDV, (unsigned)(nblk - ODL));
#pragma unroll
        for (int k = 0; k < 8; ++k) {
          const unsigned x = ((k >> 1) & 1) << 7;
          if (!(k & 1)) lds_wait_ge(A_DONE + sl, (unsigned)(j + (k >> 1) - NSLOT + 1));
          dma4<62>(op + k * 4096, o0 ^ x, o1 ^ x, o2 ^ x, o3 ^ x, slot_addr(sl >> 2) + (k & 1) * 4096);
          if (k & 1) sl = sl == 4 * (NSLOT - 1) ? 0 : sl + 4;
        }
      }
      __builtin_amdgcn_s_setprio(0);
#undef PUBLISH
#undef GATE
#undef PART
    } else {
      GET_TAG();
      vmwait<0>();
      if (wave <= 4) {
        const int w1 = wave - 1;
        if (ty < 2) {
          if (lane == 0) lds_inc(A_QCNT);
          const int q = c16 >> 2, p = lane & 3, f3 = q | ((g & 1) << 2);
          const unsigned a0 = (8 * g + q) * 256 + (((4 * w1 + (p >> 1)) ^ (f3 << 1)) << 4) + (p & 1) * 8;
          const unsigned aa[2] = {opq(SCR + a0), opq(A_QU2 + a0)};
          lds_wait_ge(A_QCNT, 4u);
          v4u w0[8], w1f[8], xf[8];
#pragma unroll
          for (int s = 0; s < 8; ++s) {
            const unsigned a = aa[s >> 2] + (s & 3) * 8192;
            w0[s] = trd2(a, a + 1024); w1f[s] = trd2((a ^ 32), (a ^ 32) + 1024);
            xf[s] = lds128(A_XIMG + (c16 & 1) * 512 + g * 16 + s * 64);
          }
          __builtin_amdgcn_sched_barrier(0);
          f4 d0 = {0.f, 0.f, 0.f, 0.f}, d1 = d0;
#pragma unroll
          for (int s = 0; s < 8; ++s) { d0 = mfma32(w0[s], xf[s], d0); d1 = mfma32(w1f[s], xf[s], d1); }
          const int pr = (c16 >> 1) & 1, nb = (c16 >> 2) & 1;
          const f4 e = nb ? d1 : d0;
          if (c16 < 8) gst128(rs, WS_QG + (((ty * 2 + (bid & 1)) * 2 + (c16 & 1)) * 4096 + hh * 128 + 32 * w1 + 16 * nb + 4 * g + 2 * pr) * 8,
                              (v4u){asu(pr ? e[2] : e[0]), tag, asu(pr ? e[3] : e[1]), tag});
          lgk0();
        } else {
          if (w1 == 3 && lane == 0) *(volatile LAS unsigned*)A_OUL2 = 1u;
          const unsigned a0 = opq((w1 < 2 ? SCR : A_QU2 - 32768) + w1 * 16384 + c16 * 256 + ((g ^ c16) << 4));
          v4u uf[16];
#pragma unroll
          for (int i = 0; i < 16; ++i) uf[i] = lds128((a0 ^ ((i & 3) << 6)) + (i >> 2) * 4096);
          lds_wait_ge(A_QB, 1u);
          v4u qb[4];
#pragma unroll
          for (int s = 0; s < 4; ++s) qb[s] = lds128(A_QBI + (c16 & 1) * 256 + s * 64 + g * 16);
          __builtin_amdgcn_sched_barrier(0);
          f4 d[4];
#pragma unroll
          for (int rb = 0; rb < 4; ++rb) d[rb] = (f4){0.f, 0.f, 0.f, 0.f};
#pragma unroll
          for (int s = 0; s < 4; ++s)
#pragma unroll
            for (int rb = 0; rb < 4; ++rb) d[rb] = mfma32(uf[4 * rb + s], qb[s], d[rb]);
          const int rb = (c16 >> 1) & 3;
          const f4 e = rb == 0 ? d[0] : rb == 1 ? d[1] : rb == 2 ? d[2] : d[3];
          if (c16 < 8) sto64(A_QAS + w1 * 256 + (c16 & 1) * 128 + (16 * rb + 4 * g) * 2, (v2u){pk(e[0], e[1]), pk(e[2], e[3])});
          lgk0();
          if (lane < 16) gst128(rs, WS_QAB + ((lane >> 3) * 32 + hh) * 1024 + 512 + w1 * 128 + (lane & 7) * 16, lds128(A_QAS + w1 * 256 + lane * 16));
          vmwait<0>();
        }
        unsigned old = 0;
        if (lane == 0) old = __hip_atomic_fetch_add((LAS unsigned*)A_QUSED, 1u, __ATOMIC_RELAXED, __HIP_MEMORY_SCOPE_WORKGROUP);
        if (ty == 2 && __builtin_amdgcn_readfirstlane(old) == 3u) {
          flag_line(rs, WS_FQ + (32 + hh) * FS, lane, tag);
          cfence();
          if (lane == 0) *(volatile LAS unsigned*)A_QCNT = 4u;
        }
      }
      if (wave == 5) {
        if (ty == 2) {
          lds_wait_ge(A_OUL2, 1u);
          gather_qb(A_QBI, hh, lane, rs, tag);
          lgk0();
          if (lane == 0) *(volatile LAS unsigned*)A_QB = 1u;
        }
        lds_wait_ge(A_LANDK, ty == 2 ? 1u : (unsigned)QGO);
        v4u v[8];
        const unsigned off = WS_QG + 131072 + (b * 4096 + kvh * 512 + 2 * lane) * 8;
        sweep<8, 4>(v, rs, tag, [&](int k) { return off + (k & 1) * 65536 + (k >> 1) * 1024; });
#pragma unroll
        for (int k = 0; k < 4; ++k) sto32(A_QIMG + k * 256 + lane * 4, pk(asf(v[2 * k].x) + asf(v[2 * k + 1].x), asf(v[2 * k].z) + asf(v[2 * k + 1].z)));
        lgk0();
        if (lane == 0) *(volatile LAS unsigned*)A_QRDY = 1u;
      }
      lds_wait_ge(A_QRDY, 1u);
      v4u qf[4];
#pragma unroll
      for (int s = 0; s < 4; ++s) qf[s] = lds128(A_QIMG + (c16 & 3) * 256 + s * 64 + g * 16);
      const unsigned ko = c16 * 256 + ((g ^ c16) << 4);
      const int vr = 4 * g + (c16 >> 2);
      const unsigned vo = 4096 + vr * 256 + ((((lane & 3) >> 1) ^ ((vr & 7) << 1)) << 4) + (lane & 1) * 8;
      float m = -INFINITY, l = 0.f;
      f4 acc[8];
#pragma unroll
      for (int t = 0; t < 8; ++t) acc[t] = (f4){0.f, 0.f, 0.f, 0.f};
      int slot = wave - 1;
      for (int j = wave - 1; j < nblk; j += NCONS) {
        const unsigned sb = slot_addr(slot);
        lds_wait_ge(A_LANDK, (unsigned)(j + 1));
        v4u kf[4];
#pragma unroll
        for (int s = 0; s < 4; ++s) kf[s] = lds128(sb + (ko ^ (s << 6)));
        __builtin_amdgcn_sched_barrier(0);
        f4 s4_ = {0.f, 0.f, 0.f, 0.f};
#pragma unroll
        for (int s = 0; s < 4; ++s) s4_ = mfma32(kf[s], qf[s], s4_);
        float mx = fmaxf(fmaxf(s4_[0], s4_[1]), fmaxf(s4_[2], s4_[3]));
        mx = x32max(x16max(mx));
        const float mn = fmaxf(m, mx), al = ex2((m - mn) * SC), mc = -mn * SC;
        const float p0 = ex2(fmaf(s4_[0], SC, mc)), p1 = ex2(fmaf(s4_[1], SC, mc)), p2 = ex2(fmaf(s4_[2], SC, mc)), p3 = ex2(fmaf(s4_[3], SC, mc));
        l = fmaf(l, al, (p0 + p1) + (p2 + p3));
        m = mn;
        v2u pb = {pk(p0, p1), pk(p2, p3)};
        unsigned va[8];
#pragma unroll
        for (int t = 0; t < 8; ++t) va[t] = sb + (vo ^ (t << 5));
        if (!__all(al == 1.f)) {
#pragma unroll
          for (int t = 0; t < 8; ++t) acc[t] *= al;
        }
        asm volatile("" : "+v"(pb), "+v"(l), "+v"(acc[0]), "+v"(acc[1]), "+v"(acc[2]), "+v"(acc[3]), "+v"(acc[4]), "+v"(acc[5]), "+v"(acc[6]), "+v"(acc[7]),
                     "+v"(va[0]), "+v"(va[1]), "+v"(va[2]), "+v"(va[3]), "+v"(va[4]), "+v"(va[5]), "+v"(va[6]), "+v"(va[7]));
        lds_wait_ge<0>(A_LANDV, (unsigned)(j + 1));
        v2u vf[8];
#pragma unroll
        for (int t = 0; t < 8; ++t) vf[t] = trd(va[t]);
        lgk0();
        if (lane == 0) *(volatile LAS unsigned*)(A_DONE + slot * 4) = (unsigned)(j + 1);
#pragma unroll
        for (int t = 0; t < 8; ++t) acc[t] = mfma16(vf[t], pb, acc[t]);
        slot += NCONS; if (slot >= NSLOT) slot -= NSLOT;
      }
      l = x32sum(x16sum(l));
      if (c16 < 4) {
#pragma unroll
        for (int t = 0; t < 8; ++t) sto128(A_ST + (wave - 1) * 2048 + c16 * 512 + ((t ^ c16) * 64) + g * 16, __builtin_bit_cast(v4u, acc[t]));
        if (g == 0) sto64(A_ML + (c16 * 8 + wave - 1) * 8, (v2u){asu(m * SC), asu(l)});
      }
    }
    bar();
    if (wave >= 3 && wave <= 6) {
      const int hq = wave - 3;
      v2u ml[NCONS];
      f2 ow[NCONS];
      const unsigned sa = opq(A_ST + hq * 512 + ((lane * 8) ^ (hq << 6))), ma = opq(A_ML + hq * 64);
#pragma unroll
      for (int c = 0; c < NCONS; ++c) { ml[c] = *(const LAS v2u*)(ma + c * 8); ow[c] = *(const LAS f2*)(sa + c * 2048); }
      float M = asf(ml[0].x);
#pragma unroll
      for (int c = 1; c < NCONS; ++c) M = fmaxf(M, asf(ml[c].x));
      float L = 0.f, o0 = 0.f, o1 = 0.f;
#pragma unroll
      for (int c = 0; c < NCONS; ++c) { const float wt = ex2(asf(ml[c].x) - M); L = fmaf(wt, asf(ml[c].y), L); o0 = fmaf(wt, ow[c].x, o0); o1 = fmaf(wt, ow[c].y, o1); }
      const int rec = (b * 32 + kvh * 4 + hq) * NSPLIT_A + split;
      gst128(rs, WS_PAO + rec * 1024 + lane * 16, (v4u){asu(o0), tag, asu(o1), tag});
      if (lane < 8) gst128(rs, WS_PAM + rec * 128 + lane * 16, (v4u){asu(M), tag, asu(L), tag});
    }
    if (ty < 2) {
      const int oh = iA >> 2, odq = iA & 3;
      if (wave == 0) { vmwait<0>(); if (lane == 0) lds_inc(A_OUL); }
      lds_wait_ge(A_OUL, 1u);
      v4u wf[4];
      out_frags(slot_addr((nblk + g) % NSLOT), wave, lane, wf);
      const int tt = (c16 >> 1) & 3, pr = c16 >> 3, n = 64 * wave + 16 * tt + 4 * g + 2 * pr;
      unsigned fo = WS_F + ((((n >> 3) * 128 + iA) * 16) + (c16 & 1) * 8 + (n & 7)) * 8, oa = A_OIMG + (c16 & 1) * 64 + g * 16;
      asm volatile("" : "+v"(fo), "+v"(oa));
      if (wave == 1) {
        const int rec = ((lane >> 5) * 32 + oh) * NSPLIT_A + (g & 1) * 5;
        v4u vm[10];
        const unsigned o0_ = WS_PAO + rec * 1024 + (32 * odq + 2 * c16) * 8, o1_ = WS_PAM + rec * 128 + odq * 16;
        sweep2<10, 9>(vm, rs, tag, [&](int k) { return k < 5 ? o0_ + k * 1024 : o1_ + (k - 5) * 128; });
        const v4u* v = vm; const v4u* ml = vm + 5;
        float M = asf(ml[0].x);
#pragma unroll
        for (int i = 1; i < 5; ++i) M = fmaxf(M, asf(ml[i].x));
        M = x16max(M);
        float L = 0.f, o0 = 0.f, o1 = 0.f;
#pragma unroll
        for (int i = 0; i < 5; ++i) { const float wt = ex2(asf(ml[i].x) - M); L = fmaf(wt, asf(ml[i].z), L); o0 = fmaf(wt, asf(v[i].x), o0); o1 = fmaf(wt, asf(v[i].z), o1); }
        const float inv = 1.f / x16sum(L);
        o0 = x16sum(o0); o1 = x16sum(o1);
        if (!(g & 1)) sto32(A_OIMG + (lane >> 5) * 64 + c16 * 4, pk(o0 * inv, o1 * inv));
      }
      bar();
      const v4u ob = lds128(oa);
      f4 d[4];
#pragma unroll
      for (int t = 0; t < 4; ++t) d[t] = mfma32(wf[t], ob, (f4){0.f, 0.f, 0.f, 0.f});
      const f4 e = tt == 0 ? d[0] : tt == 1 ? d[1] : tt == 2 ? d[2] : d[3];
      gst128(rs, fo, (v4u){asu(pr ? e[2] : e[0]), tag, asu(pr ? e[3] : e[1]), tag});
    }
  } else if (isB) {
    const int b = pid >> 5, w = wave, lane_ = lane;
    const unsigned vl = lane << 4;
#define LD_S(S, rho, u) asm volatile("global_load_dwordx4 %0, %1, %2 offset:%3 nt" : "=v"(S[u]) : "v"(vl), "s"(cp + (size_t)((((rho) + brot) & 3) * 65536) + ((u) >> 2) * 4096), "n"(((u) & 3) * 1024) : "memory")
#define ST_S(S, rho) do { unsigned a0_ = ((rho) & 1) * 65536 + w * 8192 + vc0, a1_ = ((rho) & 1) * 65536 + w * 8192 + vc1; asm volatile("" : "+v"(a0_), "+v"(a1_)); \
      _Pragma("unroll") for (int u_ = 0; u_ < 8; ++u_) sto128(u_ * 1024 + (((u_ >> 2) ? a1_ : a0_) ^ ((u_ & 3) << 6)), S[u_]); } while (0)
#define PB() vmwait<WB - 2>()
    v4u S2[8], S3[8];
    GET_TAG();
    PB(); LD_C(0, 6); LD_C(0, 7);
    PB(); LD_C(1, 0); LD_C(1, 1);
    PB(); LD_C(1, 2); LD_C(1, 3);
    PB(); LD_C(1, 4); LD_C(1, 5);
    PB(); LD_C(1, 6); LD_C(1, 7);
    PB(); LD_S(S2, 2, 0); LD_S(S2, 2, 1);
    PB(); LD_S(S2, 2, 2); LD_S(S2, 2, 3);
    PB(); LD_S(S2, 2, 4); LD_S(S2, 2, 5);
    PB(); LD_S(S2, 2, 6); LD_S(S2, 2, 7);
#pragma unroll
    for (int u = 0; u < NS3; u += 2) { PB(); LD_S(S3, 3, u); LD_S(S3, 3, u + 1); }
    if (w == 0) {
      _Pragma("nounroll") for (int it = 0; it < SPIN_CAP; ++it) { cfence(); if (__all(gld32(rs, WS_FQ + lane * FS) == tag)) break; __builtin_amdgcn_s_sleep(4); }
    }
    bar();
    {
      v4u t[4];
      const char* qa = ws + WS_QAB + (b * 32 + 4 * w) * 1024;
#pragma unroll
      for (int i = 0; i < 4; ++i) asm volatile("global_load_dwordx4 %0, %1, %2 offset:%3 sc1" : "=v"(t[i]) : "v"(vl), "s"(qa), "n"(i * 1024) : "memory");
      constexpr int NA = 2, NC = 6 - NS3;
      static_assert(NS3 == 2 || NS3 == 4 || NS3 == 6, "");
#pragma unroll
      for (int u = NS3; u < NS3 + NA; ++u) LD_S(S3, 3, u);
      asm volatile("s_waitcnt vmcnt(%12)" : "+v"(t[0]), "+v"(t[1]), "+v"(t[2]), "+v"(t[3]), "+v"(S2[0]), "+v"(S2[1]), "+v"(S2[2]), "+v"(S2[3]),
                   "+v"(S2[4]), "+v"(S2[5]), "+v"(S2[6]), "+v"(S2[7]) : "n"(NA) : "memory");
#pragma unroll
      for (int u = NS3 + NA; u < NS3 + NA + NC; ++u) LD_S(S3, 3, u);
#pragma unroll
      for (int i = 0; i < 4; ++i) sto128(SCR + (4 * w + i) * 1024 + ((lane ^ ((4 * w + i) & 15)) << 4), t[i]);
    }
    bar();
    const int kb4 = w & 3, hg = w >> 2;
    v4u qf[16];
#pragma unroll
    for (int s = 0; s < 16; ++s) qf[s] = lds128(SCR + (16 * hg + c16) * 1024 + ((((4 * s) & 15) ^ g ^ c16) << 4) + (s >> 2) * 256);
    const unsigned fk = ((c16 & 3) << 2) | ((0x78u >> (2 * (c16 >> 2))) & 3u);
    const unsigned ka = (16 * kb4 + c16) * 1024 + ((g ^ fk) << 4);
    const int h2 = lane >> 5, blk = g & 1, qq = c16 >> 2, pp = lane & 3;
    const unsigned ft0 = (qq << 2) | (h2 ? 3u : 0u), ft1 = (qq << 2) | (h2 ? 1u : 2u);
    const unsigned cbase = 8 * w + 2 * blk + (pp >> 1);
    const unsigned va00 = (8 * h2 + qq) * 1024 + ((cbase ^ ft0) << 4) + (pp & 1) * 8, va01 = (8 * h2 + 4 + qq) * 1024 + ((cbase ^ ft1) << 4) + (pp & 1) * 8;
    const unsigned mxa = opq(B_MX + (16 * hg + c16) * 4), pwa = opq(B_P + kb4 * 1536 + (16 * hg + c16) * 48 + g * 8);
    const unsigned pra = opq(B_P + (lane & 31) * 48 + h2 * 16), ala = opq(B_AL + (lane & 31) * 4);
    float m = -INFINITY, l = 0.f;
    f16v acc0, acc1;
#pragma unroll
    for (int i = 0; i < 16; ++i) { acc0[i] = 0.f; acc1[i] = 0.f; }
#pragma unroll
    for (int rho = 0; rho < 2; ++rho) {
      unsigned kr = ka, vr0 = va00, vr1 = va01;
      asm volatile("" : "+v"(kr), "+v"(vr0), "+v"(vr1));
      bar();
      f4 sa = {0.f, 0.f, 0.f, 0.f}, sb = sa;
      {
        v4u kf[8];
#pragma unroll
        for (int s = 0; s < 8; ++s) kf[s] = lds128((kr ^ ((s & 3) << 6)) + (s >> 2) * 256);
        __builtin_amdgcn_sched_barrier(0);
#pragma unroll
        for (int s = 0; s < 8; ++s) {
          sa = mfma32(kf[s], qf[s], sa);
          kf[s] = lds128((kr ^ ((s & 3) << 6)) + (2 + (s >> 2)) * 256);
          __builtin_amdgcn_sched_barrier(0);
        }
        kr += 65536;
#pragma unroll
        for (int s = 0; s < 8; ++s) {
          sa = mfma32(kf[s], qf[8 + s], sa);
          kf[s] = lds128((kr ^ ((s & 3) << 6)) + (s >> 2) * 256);
          __builtin_amdgcn_sched_barrier(0);
        }
#pragma unroll
        for (int s = 0; s < 8; ++s) {
          sb = mfma32(kf[s], qf[s], sb);
          kf[s] = lds128((kr ^ ((s & 3) << 6)) + (2 + (s >> 2)) * 256);
          __builtin_amdgcn_sched_barrier(0);
        }
#pragma unroll
        for (int s = 0; s < 8; ++s) sb = mfma32(kf[s], qf[8 + s], sb);
      }
      float mx = fmaxf(fmaxf(fmaxf(sa[0], sa[1]), fmaxf(sa[2], sa[3])), fmaxf(fmaxf(sb[0], sb[1]), fmaxf(sb[2], sb[3])));
      mx = x32max(x16max(mx));
      if (g == 0) stof(mxa + kb4 * 128, mx);
      v4u c0[8], c1[8];
#pragma unroll
      for (int ks = 0; ks < 4; ++ks) {
        c0[ks] = trd2(ks * 16384 + vr0, ks * 16384 + vr1);
        c1[ks] = trd2(ks * 16384 + (vr0 ^ 64), ks * 16384 + (vr1 ^ 64));
      }
      bar();
      const float mn = fmaxf(fmaxf(m, fmaxf(ldsf(mxa), ldsf(mxa + 128))), fmaxf(ldsf(mxa + 256), ldsf(mxa + 384)));
      __builtin_amdgcn_sched_barrier(0);
      if (rho == 0) ST_S(S2, 0);
      __builtin_amdgcn_sched_barrier(0);
      vr0 += 65536; vr1 += 65536;
#pragma unroll
      for (int ks = 0; ks < 4; ++ks) {
        c0[4 + ks] = trd2(ks * 16384 + vr0, ks * 16384 + vr1);
        c1[4 + ks] = trd2(ks * 16384 + (vr0 ^ 64), ks * 16384 + (vr1 ^ 64));
      }
      const float al = ex2((m - mn) * SC), mc = -mn * SC;
      float p[8];
#pragma unroll
      for (int i = 0; i < 4; ++i) { p[i] = ex2(fmaf(sa[i], SC, mc)); p[4 + i] = ex2(fmaf(sb[i], SC, mc)); }
      l = fmaf(l, al, ((p[0] + p[1]) + (p[2] + p[3])) + ((p[4] + p[5]) + (p[6] + p[7])));
      m = mn;
      sto64(pwa, (v2u){pk(p[0], p[1]), pk(p[2], p[3])});
      sto64(pwa + 6144, (v2u){pk(p[4], p[5]), pk(p[6], p[7])});
      if (kb4 == 0 && g == 0) stof(mxa + (B_AL - B_MX), al);
      if (rho == 1) { l = x32sum(x16sum(l)); if (g == 0) stof(mxa + (B_LS - B_MX) + kb4 * 128, l); }
      bar();
      if (rho == 1 && kb4 == 0 && g == 0) {
        const unsigned la = mxa + (B_LS - B_MX);
        sto64(B_FM + (16 * hg + c16) * 8, (v2u){asu(m * SC), asu((ldsf(la) + ldsf(la + 128)) + (ldsf(la + 256) + ldsf(la + 384)))});
      }
      const float alp = rho ? ldsf(ala) : 0.f;
      v4u pf[4];
#pragma unroll
      for (int ks = 0; ks < 4; ++ks) pf[ks] = lds128(pra + ks * 1536);
      __builtin_amdgcn_sched_barrier(0);
      if (rho) { acc0 *= alp; acc1 *= alp; }
      unsigned s0_ = 65536 + w * 8192 + vc0, s1_ = 65536 + w * 8192 + vc1;
      if (rho == 0) { asm volatile("" : "+v"(s0_), "+v"(s1_)); vmwait<0>(); }
#pragma unroll
      for (int ks = 0; ks < 8; ++ks) {
        acc0 = mfmaL(c0[ks], pf[ks & 3], acc0); acc1 = mfmaL(c1[ks], pf[ks & 3], acc1);
        if (ks < 4) pf[ks] = lds128(pra + 6144 + ks * 1536);
        if (rho == 0) sto128(ks * 1024 + (((ks >> 2) ? s1_ : s0_) ^ ((ks & 3) << 6)), S3[ks]);
        __builtin_amdgcn_sched_barrier(0);
      }
    }
    {
    const int lane = (int)opq((unsigned)lane_), h2 = lane >> 5, tid = wave * 64 + lane;
    {
      const unsigned tb = B_T + w * 4096, n32 = lane & 31;
#pragma unroll
      for (int q4 = 0; q4 < 4; ++q4) {
        sto64(tb + n32 * 128 + ((q4 ^ (n32 & 7)) << 4) + h2 * 8, (v2u){pk(acc0[4 * q4], acc0[4 * q4 + 1]), pk(acc0[4 * q4 + 2], acc0[4 * q4 + 3])});
        sto64(tb + n32 * 128 + (((4 + q4) ^ (n32 & 7)) << 4) + h2 * 8, (v2u){pk(acc1[4 * q4], acc1[4 * q4 + 1]), pk(acc1[4 * q4 + 2], acc1[4 * q4 + 3])});
      }
      lgk0();
      const int hr = lane >> 3, ck = lane & 7;
#pragma unroll
      for (int i = 0; i < 4; ++i)
        gst128(rs, WS_PB + (((8 * i + hr) * 4 + (w >> 1)) * 64 + pid) * 256 + (w & 1) * 128 + ck * 16, lds128(tb + (8 * i + hr) * 128 + ((ck ^ hr) << 4)));
    }
    vmwait<0>();
    bar();
    if (w == 0 && lane < 32) { const v2u fm = *(const LAS v2u*)(B_FM + lane * 8); gst128(rs, WS_PBM + pid * 512 + lane * 16, (v4u){fm.x, tag, fm.y, tag}); }
    if (w == 0) {
      const unsigned off = WS_F + bid * 16384 + ((lane >> 3) * 16 + (bid & 15)) * 128;
      _Pragma("nounroll") for (int it = 0; it < SPIN_CAP; ++it) { cfence(); if (__popcll(__ballot(gld64(rs, off).y == tag)) >= PREN) break; __builtin_amdgcn_s_sleep(8); }
    }
    bar();
    v4u f[3];
    sweep<3, 0>(f, rs, tag, [&](int k) { return WS_F + ((k >> 1) * 64 + bid) * 16384 + (k < 2 ? k * 8192 + tid * 16 : (tid & 255) * 16); });
    float r[4] = {asf(f[0].x) + asf(f[1].x), asf(f[0].z) + asf(f[1].z), w < 4 ? asf(f[2].x) : 0.f, w < 4 ? asf(f[2].z) : 0.f};
#pragma unroll
    for (int k = 0; k < 4; ++k) { r[k] += dpp<ROR8>(r[k]); r[k] = x32sum(x16sum(r[k])); }
    if (lane < 8) { sto64(B_FIN + w * 128 + lane * 8, (v2u){asu(r[0]), asu(r[1])}); sto64(B_FIN + w * 128 + 64 + lane * 8, (v2u){asu(r[2]), asu(r[3])}); }
    bar();
    if (tid < 16) {
      float s0 = 0.f, s1 = 0.f;
#pragma unroll
      for (int i = 0; i < 8; ++i) { const f2 t = *(const LAS f2*)(B_FIN + i * 128 + tid * 8); s0 += t.x; s1 += t.y; }
      const int mat = tid >> 3, ob = (tid >> 2) & 1, pr = tid & 3;
      *(GAS unsigned*)((GAS char*)out + (ob * 1024 + mat * 512 + 8 * bid + 2 * pr) * 2) = pk(s0, s1);
      if (bid == 0 && tid == 0) gst32(rs, WS_E, tag);
    }
    }
  } else {
    const int h = bid - NB_CU, w = wave;
    GET_TAG();
    if (w == 0) gather_qb(T_QBI, h, lane, rs, tag);
    vmwait<0>();
    v4u OU[16];
    const char* ow = Bwo + (size_t)(h * 128 + 16 * w) * 1024;
    const unsigned vo = g * 4096 + c16 * 16;
#define LD_OU(u) asm volatile("global_load_dwordx4 %0, %1, %2 offset:%3 nt" : "=v"(OU[u]) : "v"(vo), "s"(ow), "n"(((u) >> 2) * 1024 + ((u) & 3) * 256) : "memory")
#pragma unroll
    for (int u = 0; u < TE; ++u) LD_OU(u);
    {
      const unsigned a0 = w * 8192 + c16 * 256 + ((g ^ c16) << 4);
      v4u uf[8];
#pragma unroll
      for (int i = 0; i < 8; ++i) uf[i] = lds128((a0 ^ ((i & 3) << 6)) + (i >> 2) * 4096);
      bar();
      v4u qb[4];
#pragma unroll
      for (int s = 0; s < 4; ++s) qb[s] = lds128(T_QBI + (c16 & 1) * 256 + s * 64 + g * 16);
      __builtin_amdgcn_sched_barrier(0);
      f4 d0 = {0.f, 0.f, 0.f, 0.f}, d1 = d0;
#pragma unroll
      for (int s = 0; s < 4; ++s) { d0 = mfma32(uf[s], qb[s], d0); d1 = mfma32(uf[4 + s], qb[s], d1); }
      const int rb = (c16 >> 1) & 1;
      const f4 e = rb ? d1 : d0;
      if (c16 < 4) sto64(T_QAS + (c16 & 1) * 512 + (32 * w + 16 * rb + 4 * g) * 2, (v2u){pk(e[0], e[1]), pk(e[2], e[3])});
      bar();
      if (w == 1) {
        gst128(rs, WS_QAB + ((lane >> 5) * 32 + h) * 1024 + (lane & 31) * 16, lds128(T_QAS + lane * 16));
        vmwait<0>();
        flag_line(rs, WS_FQ + h * FS, lane, tag);
      }
    }
    bar();
    {
      const char* uv = Wuv + (size_t)(64 * w) * 8192 + h * 256;
      const unsigned vuv = g * 8192 + ((c16 ^ (g << 1)) << 4);
#pragma unroll
      for (int u = 0; u < 16; ++u) { if (u >= WT && !(u & 1)) vmwait<WT - 2>(); dma<0>(uv + (size_t)u * 32768, vuv ^ (((u >> 1) & 1) << 7), w * 16384 + u * 1024); }
#pragma unroll
      for (int u = TE; u < 16; ++u) { if (!(u & 1)) vmwait<WT - 2>(); LD_OU(u); }
    }
    asm volatile("s_waitcnt vmcnt(0)" : "+v"(OU[0]), "+v"(OU[1]), "+v"(OU[2]), "+v"(OU[3]), "+v"(OU[4]), "+v"(OU[5]), "+v"(OU[6]), "+v"(OU[7]),
                 "+v"(OU[8]), "+v"(OU[9]), "+v"(OU[10]), "+v"(OU[11]), "+v"(OU[12]), "+v"(OU[13]), "+v"(OU[14]), "+v"(OU[15]) :: "memory");
    if (tid < 4) sto32(T_RDY + tid * 4, 0u);
    bar();
    v4u wf[16];
    {
      const int q = c16 >> 2, p = lane & 3, f3 = q | ((g & 1) << 2);
      const unsigned a0 = (8 * g + q) * 256 + (((2 * w + (p >> 1)) ^ (f3 << 1)) << 4) + (p & 1) * 8;
      const unsigned aa[2] = {opq(a0), opq(a0 + 65536)};
#pragma unroll
      for (int s = 0; s < 16; ++s) wf[s] = trd2(aa[s >> 3] + (s & 7) * 8192, aa[s >> 3] + (s & 7) * 8192 + 1024);
    }
    {
      const int tb = w >> 2;
      v4u o[8];
#pragma unroll
      for (int i = 0; i < 8; ++i) { o[i] = (v4u){0u, 0u, 0u, 0u}; asm volatile("" : "+v"(o[i])); }
      const char* pb = ws + WS_PB + (h * 4 + (w & 3)) * 16384 + tb * 8192;
      const unsigned vl = lane << 4;
      unsigned done = 0;
      bool all = false;
      v4u ml = {0u, ~tag, 0u, ~tag};
      if (w == 0) {
        _Pragma("nounroll") for (int it = 0; it < SPIN_CAP; ++it) { cfence(); if (__all(gld32(rs, WS_PBM + 2 * h * 512 + h * 16 + 4) == tag)) break; __builtin_amdgcn_s_sleep(8); }
      }
      _Pragma("nounroll") for (int it = 0; it < SPIN_CAP * 8 && !all; ++it) {
        unsigned rdy;
        if (w == 0) {
          cfence();
          const v4u t = gld128(rs, tagok(ml, tag) ? 0xfffffff0u : WS_PBM + lane * 512 + h * 16);
          if (!tagok(ml, tag)) ml = t;
          unsigned long long m = __ballot(tagok(ml, tag));
          all = m == ~0ull;
          m &= m >> 2; m &= m >> 1; m &= 0x1111111111111111ull;
          rdy = (unsigned)m;
          if (lane == 0) *(volatile LAS v2u*)T_RDY = (v2u){rdy, (unsigned)(m >> 32)};
        } else {
          rdy = lds_peek(T_RDY + tb * 4);
          if (rdy == done) { __builtin_amdgcn_s_sleep(1); continue; }
          all = rdy == 0x11111111u;
        }
        const unsigned add = rdy & ~done;
        done = rdy;
#pragma unroll
        for (int i = 0; i < 8; ++i)
          if (add & (1u << (4 * i))) asm volatile("global_load_dwordx4 %0, %1, %2 offset:%3 sc1" : "+v"(o[i]) : "v"(vl), "s"(pb + (i >> 2) * 4096), "n"((i & 3) * 1024) : "memory");
      }
      if (w == 0) {
        const float M = x16max(row16max(asf(ml.x)));
        const float wt = ex2(asf(ml.x) - M), wn = wt / x16sum(row16sum(wt * asf(ml.z)));
        const unsigned hi = pk(wn, wn), wa = T_WN + (lane >> 5) * 128 + (lane & 3) * 32 + ((lane >> 2) & 7) * 4;
        const float lo = wn - asf(hi & 0xffff0000u);
        sto32(wa, hi); sto32(wa + 256, pk(lo, lo));
        lgk0();
        if (lane == 0) *(volatile LAS unsigned*)(T_RDY + 8) = 1u;
      } else {
        lds_wait_ge<0>(T_RDY + 8, 1u);
      }
      const unsigned wa = T_WN + (c16 >> 3) * 256 + tb * 128 + g * 32, half = (c16 & 1) ? 0xffff0000u : 0x0000ffffu;
      const v4u w0_ = lds128(wa), w1_ = lds128(wa + 16);
      const int rr = (c16 >> 1) & 3;
      v4u bo[8];
#pragma unroll
      for (int u = 0; u < 8; ++u) { const unsigned x = (u < 4 ? w0_[u & 3] : w1_[u & 3]) & half; bo[u] = (v4u){rr == 0 ? x : 0u, rr == 1 ? x : 0u, rr == 2 ? x : 0u, rr == 3 ? x : 0u}; }
      asm volatile("s_waitcnt vmcnt(0)" : "+v"(o[0]), "+v"(o[1]), "+v"(o[2]), "+v"(o[3]), "+v"(o[4]), "+v"(o[5]), "+v"(o[6]), "+v"(o[7]),
                   "+v"(bo[0]), "+v"(bo[1]), "+v"(bo[2]), "+v"(bo[3]), "+v"(bo[4]), "+v"(bo[5]), "+v"(bo[6]), "+v"(bo[7]) :: "memory");
      f4 d = {0.f, 0.f, 0.f, 0.f};
#pragma unroll
      for (int u = 0; u < 8; ++u) d = mfma32(o[u], bo[u], d);
#pragma unroll
      for (int r = 0; r < 4; ++r) {
        const float x = d[r] + dpp<ROR8>(d[r]);
        const unsigned pr = pk(x, dpp<XOR1>(x));
        if ((c16 & 9) == 0) sto32(T_OL + tb * 1088 + ((w & 3) * 128 + 32 * g + 8 * r + c16) * 2, pr);
      }
    }
    bar();
    {
      v4u of[16];
      const unsigned oa = opq(T_OL + (c16 >> 3) * 1088 + g * 16);
#pragma unroll
      for (int s = 0; s < 16; ++s) of[s] = lds128(oa + s * 64);
      __builtin_amdgcn_sched_barrier(0);
      f4 d0 = {0.f, 0.f, 0.f, 0.f}, d1 = d0;
#pragma unroll
      for (int s = 0; s < 8; ++s) { d0 = mfma32(wf[2 * s], of[2 * s], d0); d1 = mfma32(wf[2 * s + 1], of[2 * s + 1], d1); }
      d0 += d1;
      const unsigned half = (c16 & 1) ? 0xffff0000u : 0x0000ffffu;
      const int rr = (c16 >> 1) & 3;
      f4 o4[4];
#pragma unroll
      for (int cb = 0; cb < 4; ++cb) o4[cb] = (f4){0.f, 0.f, 0.f, 0.f};
#pragma unroll
      for (int u = 0; u < 4; ++u) {
        const unsigned x = pk(d0[u], d0[u]) & half;
        const v4u bo = {rr == 0 ? x : 0u, rr == 1 ? x : 0u, rr == 2 ? x : 0u, rr == 3 ? x : 0u};
#pragma unroll
        for (int cb = 0; cb < 4; ++cb) o4[cb] = mfma32(OU[4 * u + cb], bo, o4[cb]);
      }
#pragma unroll
      for (int cb = 0; cb < 4; ++cb)
#pragma unroll
        for (int r = 0; r < 4; ++r) stof(T_RED + w * 4096 + cb * 1024 + r * 64 + g * 256 + c16 * 4, o4[cb][r]);
    }
    bar();
    {
      const int j = tid >> 3, pr = tid & 7;
      float s0 = 0.f, s1 = 0.f;
#pragma unroll
      for (int i = 0; i < 8; ++i) { const f2 t = *(const LAS f2*)(T_RED + i * 4096 + tid * 8); s0 += t.x; s1 += t.y; }
      gst128(rs, WS_F + (((64 + j) * 128 + h) * 16 + 2 * pr) * 8, (v4u){asu(s0), tag, asu(s1), tag});
    }
  }
}

extern "C" void kernel_launch(void* const* d_in, const int* in_sizes, int n_in, void* d_out, int out_size, void* d_ws, size_t ws_size,
                              hipStream_t stream) {
  (void)in_sizes; (void)n_in; (void)out_size; (void)ws_size;
  (void)hipFuncSetAttribute((const void*)fused_decode, hipFuncAttributeMaxDynamicSharedMemorySize, LDS_BYTES);
  fused_decode<<<256, 512, LDS_BYTES, stream>>>(
      (const char*)d_in[0], (const char*)d_in[1], (const char*)d_in[2], (const char*)d_in[3], (const char*)d_in[4], (const char*)d_in[5],
      (const char*)d_in[6], (const char*)d_in[7], (const char*)d_in[8], (const char*)d_in[9], (const char*)d_in[10], (unsigned short*)d_out,
      (char*)d_ws);
}
```
